```python
import torch

def _softmax(x, axis=-1):
    return torch.softmax(x, dim=axis)
def _gelu(x):
    return torch.nn.functional.gelu(x, approximate="tanh")
def _silu(x):
    return torch.nn.functional.silu(x)
def _sigmoid(x):
    return torch.sigmoid(x)
def _layernorm(x, axis=-1, eps=1e-5):
    m = x.mean(dim=axis, keepdim=True)
    v = x.var(dim=axis, keepdim=True, unbiased=False)
    return (x - m) / torch.sqrt(v + eps)
def _scatter_add(tgt, idx, src, axis=0):
    return tgt.clone().index_add_(axis, idx, src)
def _segment_sum(src, seg, n):
    out = torch.zeros((n,) + src.shape[1:], dtype=src.dtype)
    return out.index_add_(0, seg, src)
def _pad(x, pw):
    flat = tuple(v for pair in reversed(pw) for v in pair)
    return torch.nn.functional.pad(x, flat)

kernel_name = 'recurrent_scan__compressed_kv_cla__swiglu_ffn__prompt'

def setup_inputs():

    _w = (lambda a: (lambda t: t.to(torch.bfloat16) if t.is_floating_point() else t)(torch.as_tensor(a)))
    def _A_setup():
        B, H, Sq, Sk, D, M = 1, 16, 2048, 2048, 256, 448
        Dc = 4 * D
        return {
            "x": _w((lambda *s: torch.randn(*s, dtype=torch.bfloat16) * 0.05)(B, Sq, M)),
            "c_kv": _w((lambda *s: torch.randn(*s, dtype=torch.bfloat16) * 0.2)(B, Sk, Dc)),
            "w_q": _w((lambda *s: torch.randn(*s, dtype=torch.bfloat16) * 3.2)(M, H, D) * (M ** -0.5)),
            "w_uk": _w((lambda *s: torch.randn(*s, dtype=torch.bfloat16) * 3.2)(Dc, H, D) * (Dc ** -0.5)),
            "w_uv": _w((lambda *s: torch.randn(*s, dtype=torch.bfloat16) * 0.2)(Dc, H, D) * (Dc ** -0.5)),
            "w_o": _w((lambda *s: torch.randn(*s, dtype=torch.bfloat16) * 0.2)(H, D, M) * ((H * D) ** -0.5)),
        }
    _A_in = _A_setup()

    def _B_setup():
        B, S, D, F = 1, 2048, 448, 1792
        return {
            "x": _w((lambda *s: torch.randn(*s, dtype=torch.bfloat16) * 0.05)(B, S, D)),
            "w_gate": _w((lambda *s: torch.randn(*s, dtype=torch.bfloat16) * 0.1)(D, F)),
            "w_up": _w((lambda *s: torch.randn(*s, dtype=torch.bfloat16) * 0.1)(D, F)),
            "w_down": _w((lambda *s: torch.randn(*s, dtype=torch.bfloat16) * 0.1)(F, D)),
        }
    _B_in = _B_setup()

    return {
        "A_x": _A_in["x"],
        "A_c_kv": _A_in["c_kv"],
        "A_w_q": _A_in["w_q"],
        "A_w_uk": _A_in["w_uk"],
        "A_w_uv": _A_in["w_uv"],
        "A_w_o": _A_in["w_o"],
        "B_x": _B_in["x"],
        "B_w_gate": _B_in["w_gate"],
        "B_w_up": _B_in["w_up"],
        "B_w_down": _B_in["w_down"],
    }

def reference(A_x, A_c_kv, A_w_q, A_w_uk, A_w_uv, A_w_o, B_x, B_w_gate, B_w_up, B_w_down):
    def _A_ref(x, c_kv, w_q, w_uk, w_uv, w_o):
        B, Sq, M = x.shape[0], x.shape[1], x.shape[2]
        Sk = c_kv.shape[1]
        H, D = w_q.shape[1], w_q.shape[2]
        q = torch.einsum("bqm,mhd->bhqd", x, w_q)
        k = torch.einsum("bkc,chd->bhkd", c_kv, w_uk)
        v = torch.einsum("bkc,chd->bhkd", c_kv, w_uv)
        scores = torch.einsum("bhqd,bhkd->bhqk", q, k) * (D ** -0.5)
        off = Sk - Sq
        qi = torch.arange(Sq)[:, None]
        kj = torch.arange(Sk)[None, :]
        cmask = kj <= (qi + off)
        scores = torch.where(cmask[None, None], scores, -1e9)
        p = _softmax(scores, axis=-1)
        ctx = torch.einsum("bhqk,bhkd->bhqd", p, v)
        return torch.einsum("bhqd,hdm->bqm", ctx, w_o)

    def _B_ref(x, w_gate, w_up, w_down):
        g = _silu(x @ w_gate)
        u = x @ w_up
        return (g * u) @ w_down

    x = A_x
    xn = _layernorm(x, axis=-1)
    s = _A_ref(xn, A_c_kv, A_w_q, A_w_uk, A_w_uv, A_w_o)
    g = _silu(_B_ref(xn, B_w_gate, B_w_up, B_w_down))
    return x + s * g
```

```cpp
#include <hip/hip_runtime.h>
#include <hip/hip_bf16.h>
typedef unsigned short u16;
__device__ __forceinline__ float bf2f(u16 b){ return __uint_as_float(((unsigned)b)<<16); }
__device__ __forceinline__ u16 f2bf(float f){ unsigned u=__float_as_uint(f); u += 0x7FFFu + ((u>>16)&1u); return (u16)(u>>16); }
__device__ __forceinline__ float rbf(float f){ return bf2f(f2bf(f)); }
constexpr int S=2048, M=448, H=16, D=256, DC=1024, F=1792;

__global__ void k_ln(const u16* x, u16* xn){
  int row=blockIdx.x; int t=threadIdx.x;
  float v[7]; float s=0;
  for(int i=0;i<7;i++){ v[i]=bf2f(x[row*M+t+64*i]); s+=v[i]; }
  for(int o=32;o>=1;o>>=1) s+=__shfl_xor(s,o);
  float mean=s/M; float q=0;
  for(int i=0;i<7;i++){ float d=v[i]-mean; q+=d*d; }
  for(int o=32;o>=1;o>>=1) q+=__shfl_xor(q,o);
  float var=rbf(q/M); float m=rbf(mean);
  float den=rbf(sqrtf(rbf(var+1e-5f)));
  for(int i=0;i<7;i++){ xn[row*M+t+64*i]=f2bf(rbf(v[i]-m)/den); }
}
__global__ void k_gemm(const u16* A, const u16* B, u16* C, int Mm, int N, int K, int lda, int ldb, int ldc){
  __shared__ float As[16][65], Bs[16][65];
  int tx=threadIdx.x&15, ty=threadIdx.x>>4;
  int r0=blockIdx.y*64, c0=blockIdx.x*64;
  float acc[4][4]={};
  for(int k0=0;k0<K;k0+=16){
    for(int i=threadIdx.x;i<16*64;i+=256){ int r=i>>4,k=i&15; As[k][r]=bf2f(A[(size_t)(r0+r)*lda+k0+k]); }
    for(int i=threadIdx.x;i<16*64;i+=256){ int k=i>>6,c=i&63; Bs[k][c]=bf2f(B[(size_t)(k0+k)*ldb+c0+c]); }
    __syncthreads();
    for(int k=0;k<16;k++){ float a[4],b[4];
      for(int i=0;i<4;i++){a[i]=As[k][ty*4+i]; b[i]=Bs[k][tx*4+i];}
      for(int i=0;i<4;i++)for(int j=0;j<4;j++)acc[i][j]+=a[i]*b[j]; }
    __syncthreads();
  }
  for(int i=0;i<4;i++)for(int j=0;j<4;j++) C[(size_t)(r0+ty*4+i)*ldc+c0+tx*4+j]=f2bf(acc[i][j]);
}
__global__ void k_h(const u16* g, const u16* u, u16* h, int n){ int i=blockIdx.x*256+threadIdx.x; if(i<n){ float gg=bf2f(g[i]); float s=rbf(gg/(1.f+__expf(-gg))); h[i]=f2bf(s*bf2f(u[i])); } }
__global__ void k_attn(const u16* q, const u16* k, const u16* v, u16* ctx){
  int row=blockIdx.x, h=blockIdx.y, t=threadIdx.x;
  __shared__ float qs[256]; __shared__ float p[2048]; __shared__ float red[256];
  qs[t]=bf2f(q[(size_t)row*4096+h*256+t]); __syncthreads();
  float mx=-1e30f;
  for(int j=t;j<=row;j+=256){ float s=0; const u16* kp=k+(size_t)j*4096+h*256; for(int d=0;d<256;d++) s+=qs[d]*bf2f(kp[d]); s=rbf(rbf(s)*0.0625f); p[j]=s; mx=fmaxf(mx,s);}
  red[t]=mx; __syncthreads(); for(int o=128;o>=1;o>>=1){ if(t<o) red[t]=fmaxf(red[t],red[t+o]); __syncthreads(); } mx=red[0]; __syncthreads();
  float sum=0; for(int j=t;j<=row;j+=256){ float e=__expf(p[j]-mx); p[j]=e; sum+=e; }
  red[t]=sum; __syncthreads(); for(int o=128;o>=1;o>>=1){ if(t<o) red[t]+=red[t+o]; __syncthreads(); } sum=red[0]; __syncthreads();
  float inv=1.f/sum; float o=0; for(int j=0;j<=row;j++){ o+=rbf(p[j]*inv)*bf2f(v[(size_t)j*4096+h*256+t]); }
  ctx[(size_t)row*4096+h*256+t]=f2bf(o);
}
__global__ void k_fin(const u16* x,const u16* s,const u16* g,u16* out,int n){ int i=blockIdx.x*256+threadIdx.x; if(i<n){ float gg=bf2f(g[i]); float sg=rbf(gg/(1.f+__expf(-gg))); out[i]=f2bf(bf2f(x[i])+rbf(bf2f(s[i])*sg)); } }

extern "C" void kernel_launch(void* const* d_in, const int* in_sizes, int n_in, void* d_out, int out_size, void* d_ws, size_t ws_size, hipStream_t stream) {
  const u16* x=(const u16*)d_in[0]; const u16* ckv=(const u16*)d_in[1]; const u16* wq=(const u16*)d_in[2]; const u16* wuk=(const u16*)d_in[3];
  const u16* wuv=(const u16*)d_in[4]; const u16* wo=(const u16*)d_in[5]; const u16* wg=(const u16*)d_in[7]; const u16* wu=(const u16*)d_in[8]; const u16* wd=(const u16*)d_in[9];
  u16* ws=(u16*)d_ws; u16* xn=ws; u16* q=xn+S*M; u16* k=q+S*4096; u16* v=k+S*4096; u16* ctx=v+S*4096; u16* g=ctx+S*4096; u16* u=g+S*F; u16* hh=u+S*F; u16* s=hh+S*F; u16* gd=s+S*M;
  k_ln<<<S,64,0,stream>>>(x,xn);
  k_gemm<<<dim3(4096/64,S/64),256,0,stream>>>(xn,wq,q,S,4096,M,M,4096,4096);
  k_gemm<<<dim3(4096/64,S/64),256,0,stream>>>(ckv,wuk,k,S,4096,DC,DC,4096,4096);
  k_gemm<<<dim3(4096/64,S/64),256,0,stream>>>(ckv,wuv,v,S,4096,DC,DC,4096,4096);
  k_gemm<<<dim3(F/64,S/64),256,0,stream>>>(xn,wg,g,S,F,M,M,F,F);
  k_gemm<<<dim3(F/64,S/64),256,0,stream>>>(xn,wu,u,S,F,M,M,F,F);
  k_h<<<(S*F+255)/256,256,0,stream>>>(g,u,hh,S*F);
  k_gemm<<<dim3(M/64,S/64),256,0,stream>>>(hh,wd,gd,S,M,F,F,M,M);
  k_attn<<<dim3(S,H),256,0,stream>>>(q,k,v,ctx);
  k_gemm<<<dim3(M/64,S/64),256,0,stream>>>(ctx,wo,s,S,M,4096,4096,M,M);
  k_fin<<<(S*M+255)/256,256,0,stream>>>(x,s,gd,(u16*)d_out,S*M);
}
```

```cpp
#include <hip/hip_runtime.h>
#include <hip/hip_bf16.h>

extern "C" __global__ __launch_bounds__(256, 1) void k_F(const void* x, const void* ckv, const void* wq, const void* wuk, const void* wuv, const void* wo, const void* wg, const void* wu, const void* wd, void* out, void* ws) {
  asm volatile(
".ifndef ZMACROS\n.set ZMACROS,1\n"
".macro zm a:vararg\nv_mfma_f32_16x16x32_bf16 \\a\n.endm\n"
".macro zr a:vararg\nv_accvgpr_read_b32 \\a\n.endm\n"
".macro zw a:vararg\nv_accvgpr_write_b32 \\a\n.endm\n"
".macro zt a:vararg\nds_read_b64_tr_b16 \\a\n.endm\n"
".macro zc a:vararg\nv_cvt_pk_bf16_f32 \\a\n.endm\n"
".macro zl a:vararg\nbuffer_load_dwordx4 \\a\n.endm\n"
".macro zd a:vararg\nds_read_b128 \\a\n.endm\n"
".macro zs a:vararg\nbuffer_store_dwordx4 \\a\n.endm\n"
".macro zx a:vararg\nv_mul_f32 \\a\n.endm\n"
".macro za a:vararg\nv_add_f32 \\a\n.endm\n"
".macro ze a:vararg\nv_exp_f32 \\a\n.endm\n"
".macro zh a:vararg\nv_lshlrev_b32 \\a\n.endm\n"
".macro zo a:vararg\nv_xor_b32 \\a\n.endm\n"
".macro zu a:vararg\nv_sub_f32 \\a\n.endm\n"
".macro zp a:vararg\ns_add_u32 \\a\n.endm\n"
".macro zv a:vararg\ns_waitcnt \\a\n.endm\n"
".macro zz a:vararg\nv_dot2c_f32_bf16 \\a\n.endm\n"
".macro zk a:vararg\nv_cndmask_b32 \\a\n.endm\n"
".macro zf a:vararg\nv_fmac_f32 \\a\n.endm\n"
".macro zq a:vararg\nv_rcp_f32 \\a\n.endm\n"
".endif\n"
"L_F_codestart:\ns_mov_b64 s[20:21],%[karg]\ns_mov_b32 s22,%[bid]\nv_mov_b32 v7,%[tid]\ns_load_dwordx2 s[94:95],s[20:21],80\ns_load_dwordx16 s[4:19],s[20:21],0\ns_load_dwordx2 s[96:97],s[20:21],64\nv_lshrrev_b32 v26,6,v7\n"
"s_nop 0\nv_readfirstlane_b32 s23,v26\nv_and_b32 v6,63,v7\ns_lshr_b32 s60,s23,1\ns_and_b32 s61,s23,1\ns_and_b32 s76,s22,7\ns_lshr_b32 s77,s22,3\ns_and_b32 s68,s77,1\n"
"s_lshr_b32 s78,s77,1\ns_and_b32 s78,s78,1\ns_lshl_b32 s79,s76,1\nzp s69,s79,s78\ns_lshr_b32 s70,s77,2\nv_lshrrev_b32 v26,3,v6\nv_and_b32 v27,7,v6\nzo v27,v27,v26\n"
"zh v27,4,v27\nv_mov_b32 v28,2048\nv_mad_u32_u24 v27,v26,v28,v27\ns_mul_i32 s76,s23,131072\nv_add_u32 v27,s76,v27\nv_add_u32 v8,0,v27\nv_add_u32 v9,15360,v27\nv_add_u32 v10,30720,v27\n"
"v_add_u32 v11,46080,v27\nv_lshrrev_b32 v26,4,v6\nv_and_b32 v27,15,v6\nzh v28,1,v26\nzo v27,v27,v28\ns_mov_b32 s77,8192\nv_mul_lo_u32 v29,v26,s77\ns_mul_i32 s76,s23,s77\n"
"s_lshl_b32 s76,s76,4\nv_add_u32 v29,s76,v29\ns_lshl_b32 s78,s77,2\ns_sub_u32 s78,s78,1024\ns_mov_b32 s79,0\nzo v30,0,v27\nzh v30,4,v30\nv_add_u32 v30,v30,v29\n"
"v_add_u32 v12,s79,v30\nzp s79,s79,s78\nzo v30,0,v27\nzh v30,4,v30\nv_add_u32 v30,v30,v29\nv_add_u32 v13,s79,v30\nzp s79,s79,s78\nzo v30,8,v27\n"
"zh v30,4,v30\nv_add_u32 v30,v30,v29\nv_add_u32 v14,s79,v30\nzp s79,s79,s78\nzo v30,8,v27\nzh v30,4,v30\nv_add_u32 v30,v30,v29\nv_add_u32 v15,s79,v30\n"
"s_mov_b32 s62,0\ns_mov_b32 s63,65536\ns_mov_b32 s64,0\ns_mov_b32 s74,524288\ns_lshl_b32 s65,s23,13\ns_lshl_b32 s66,s23,12\nzp s66,s66,0x10000\ns_lshl_b32 s76,s22,3\n"
"s_lshl_b32 s77,s23,1\nzp s76,s76,s77\ns_mul_i32 s80,s76,896\nzh v201,4,v6\nv_add_u32 v201,s80,v201\nv_mov_b32 v26,2147483632\nv_cmp_gt_u32 vcc,56,v6\nv_mov_b32 v200,0\n"
"s_nop 0\nzk v201,v26,v201,vcc\nzk v200,0,1.0,vcc\nzv lgkmcnt(0)\nv_mov_b32 v228,s96\nv_mov_b32 v229,s97\ns_load_dword s34,s[94:95],0\ns_mul_i32 s76,s70,524288\n"
"s_sub_u32 s77,4194304,s76\nzp s24,s6,s76\ns_addc_u32 s25,s7,0\ns_and_b32 s25,s25,0xffff\ns_mov_b32 s26,s77\ns_mov_b32 s27,0x20000\ns_cmp_eq_u32 s68,0\ns_cselect_b32 s78,s10,s12\n"
"s_cselect_b32 s79,s11,s13\ns_lshl_b32 s76,s69,9\ns_sub_u32 s77,8388608,s76\nzp s28,s78,s76\ns_addc_u32 s29,s79,0\ns_and_b32 s29,s29,0xffff\ns_mov_b32 s30,s77\ns_mov_b32 s31,0x20000\n"
"zp s76,s76,256\ns_sub_u32 s77,s77,256\nzp s36,s78,s76\ns_addc_u32 s37,s79,0\ns_and_b32 s37,s37,0xffff\ns_mov_b32 s38,s77\ns_mov_b32 s39,0x20000\nzp s84,s4,0\n"
"s_addc_u32 s85,s5,0\ns_and_b32 s85,s85,0xffff\ns_mov_b32 s86,1835008\ns_mov_b32 s87,0x20000\nzl v[160:163],v201,s[84:87],0 offen\nzl v[164:167],v201,s[84:87],0 offen offset:896\ns_mov_b32 m0,s65\ns_nop 0\n"
"zl v8,s[24:27],s62 offen offset:0 sc1 lds\ns_lshr_b32 s76,s22,3\ns_and_b32 s76,s76,3\ns_lshl_b32 s76,s76,6\ns_lshl_b32 s77,s23,4\nzp s76,s76,s77\nv_add_u32 v26,s76,v6\nv_mul_u32_u24 v4,2048,v26\n"
"v_mov_b32 v29,2147483632\nv_cmp_gt_u32 vcc,16,v6\ns_nop 1\nzk v4,v29,v4,vcc\nzl v9,s[24:27],s62 offen offset:1024 sc1 lds\ns_lshl_b32 s76,s23,3\nv_add_u32 v26,s76,v6\nv_lshrrev_b32 v27,2,v26\n"
"s_lshl_b32 s76,s70,3\nv_add_u32 v27,s76,v27\nv_mul_u32_u24 v5,8192,v27\nv_and_b32 v26,3,v26\nv_lshl_add_u32 v5,v26,7,v5\nzl v10,s[24:27],s62 offen offset:2048 sc1 lds\nv_cmp_gt_u32 vcc,8,v6\ns_nop 1\n"
"zk v5,v29,v5,vcc\nv_and_b32 v26,15,v6\nzh v26,7,v26\ns_lshl_b32 s76,s60,14\nv_add_u32 v26,s76,v26\nv_lshrrev_b32 v27,4,v6\nv_and_b32 v28,7,v6\nv_add_u32 v29,0,v27\n"
"zl v11,s[24:27],s62 offen offset:3072 sc1 lds\nzo v29,v29,v28\nzh v29,4,v29\nv_add_u32 v16,v26,v29\nv_add_u32 v29,4,v27\nzo v29,v29,v28\nzh v29,4,v29\nv_add_u32 v17,v26,v29\n"
"v_lshrrev_b32 v27,4,v6\nzp m0,s65,4096\ns_nop 0\nzl v8,s[24:27],s63 offen offset:0 sc1 lds\nv_bfe_u32 v28,v6,2,2\nv_and_b32 v29,3,v6\nv_lshl_add_u32 v30,v27,3,v28\nzh v30,8,v30\n"
"v_lshl_add_u32 v30,v29,3,v30\nv_add_u32 v30,0x10000,v30\nv_and_b32 v27,1,v27\nv_lshl_or_b32 v28,v27,2,v28\nzl v9,s[24:27],s63 offen offset:1024 sc1 lds\ns_lshl_b32 s76,s61,14\ns_mov_b32 s77,0\nzo v29,s77,v28\n"
"v_lshl_add_u32 v29,v29,5,v30\nv_add_u32 v18,s76,v29\ns_lshl_b32 s76,s61,14\ns_mov_b32 s77,1\nzo v29,s77,v28\nzl v10,s[24:27],s63 offen offset:2048 sc1 lds\nv_lshl_add_u32 v29,v29,5,v30\nv_add_u32 v19,s76,v29\n"
"s_lshl_b32 s76,s61,14\ns_mov_b32 s77,2\nzo v29,s77,v28\nv_lshl_add_u32 v29,v29,5,v30\nv_add_u32 v20,s76,v29\ns_lshl_b32 s76,s61,14\nzl v11,s[24:27],s63 offen offset:3072 sc1 lds\ns_mov_b32 s77,3\n"
"zo v29,s77,v28\nv_lshl_add_u32 v29,v29,5,v30\nv_add_u32 v21,s76,v29\ns_lshl_b32 s76,s61,14\ns_mov_b32 s77,4\nzo v29,s77,v28\nv_lshl_add_u32 v29,v29,5,v30\ns_mov_b32 m0,s66\n"
"s_nop 0\nzl v12,s[28:31],s64 offen offset:0 lds\nv_add_u32 v22,s76,v29\ns_lshl_b32 s76,s61,14\ns_mov_b32 s77,5\nzo v29,s77,v28\nv_lshl_add_u32 v29,v29,5,v30\nv_add_u32 v23,s76,v29\n"
"s_lshl_b32 s76,s61,14\ns_mov_b32 s77,6\nzl v13,s[28:31],s64 offen offset:1024 lds\nzo v29,s77,v28\nv_lshl_add_u32 v29,v29,5,v30\nv_add_u32 v24,s76,v29\ns_lshl_b32 s76,s61,14\ns_mov_b32 s77,7\n"
"zo v29,s77,v28\nv_lshl_add_u32 v29,v29,5,v30\nv_add_u32 v25,s76,v29\nzl v14,s[28:31],s64 offen offset:2048 lds\ns_and_b32 s76,s22,7\ns_lshr_b32 s77,s22,3\ns_lshr_b32 s80,s77,1\ns_lshr_b32 s78,s80,3\n"
"s_lshl_b32 s79,s76,1\nzp s72,s79,s78\ns_and_b32 s73,s80,7\ns_and_b32 s78,s77,1\nzl v15,s[28:31],s64 offen offset:3072 lds\ns_cmp_eq_u32 s78,1\ns_cselect_b32 s72,s80,s72\ns_cselect_b32 s73,s76,s73\n"
"s_cselect_b32 s71,1,0\ns_lshl_b32 s78,s78,5\nzp s78,s78,s80\ns_cmp_ge_u32 s78,46\ns_cselect_b32 s71,2,s71\nv_lshrrev_b32 v27,4,v6\nv_bfe_u32 v28,v6,2,2\nv_and_b32 v29,3,v6\n"
"v_lshl_add_u32 v30,v27,3,v28\nzp m0,s66,16384\ns_nop 0\nzl v12,s[36:39],s64 offen offset:0 lds\nzh v30,8,v30\nv_lshl_add_u32 v30,v29,3,v30\nv_add_u32 v30,0x10000,v30\nv_and_b32 v27,1,v27\n"
"v_lshl_or_b32 v28,v27,2,v28\ns_lshl_b32 s76,s61,14\ns_mov_b32 s77,0\ns_lshl_b32 s78,s61,2\nzl v13,s[36:39],s64 offen offset:1024 lds\nzp s78,s78,0\ns_cmp_eq_u32 s71,1\ns_cselect_b32 s76,0,s76\n"
"s_cselect_b32 s77,s78,s77\nzo v29,s77,v28\nv_lshl_add_u32 v29,v29,5,v30\nv_add_u32 v216,s76,v29\ns_lshl_b32 s76,s61,14\ns_mov_b32 s77,1\ns_lshl_b32 s78,s61,2\nzl v14,s[36:39],s64 offen offset:2048 lds\n"
"zp s78,s78,1\ns_cmp_eq_u32 s71,1\ns_cselect_b32 s76,0,s76\ns_cselect_b32 s77,s78,s77\nzo v29,s77,v28\nv_lshl_add_u32 v29,v29,5,v30\nv_add_u32 v217,s76,v29\ns_lshl_b32 s76,s61,14\n"
"s_mov_b32 s77,2\ns_lshl_b32 s78,s61,2\nzl v15,s[36:39],s64 offen offset:3072 lds\nzp s78,s78,2\ns_cmp_eq_u32 s71,1\ns_cselect_b32 s76,0,s76\ns_cselect_b32 s77,s78,s77\nzo v29,s77,v28\n"
"v_lshl_add_u32 v29,v29,5,v30\nv_add_u32 v218,s76,v29\ns_lshl_b32 s76,s61,14\ns_mov_b32 s77,3\ns_lshl_b32 s78,s61,2\nzp s78,s78,3\nzp s62,s62,128\nzp s63,s63,128\n"
"zp s64,s64,s74\ns_xor_b32 s65,s65,0x8000\ns_xor_b32 s66,s66,0x8000\ns_mov_b32 m0,s65\ns_nop 0\nzl v8,s[24:27],s62 offen offset:0 sc1 lds\ns_cmp_eq_u32 s71,1\ns_cselect_b32 s76,0,s76\n"
"s_cselect_b32 s77,s78,s77\nzo v29,s77,v28\nv_lshl_add_u32 v29,v29,5,v30\nv_add_u32 v219,s76,v29\ns_lshl_b32 s76,s61,14\ns_mov_b32 s77,4\ns_lshl_b32 s78,s61,2\nzp s78,s78,0\n"
"zl v9,s[24:27],s62 offen offset:1024 sc1 lds\ns_cmp_eq_u32 s71,1\ns_cselect_b32 s76,16384,s76\ns_cselect_b32 s77,s78,s77\nzo v29,s77,v28\nv_lshl_add_u32 v29,v29,5,v30\nv_add_u32 v220,s76,v29\ns_lshl_b32 s76,s61,14\n"
"s_mov_b32 s77,5\ns_lshl_b32 s78,s61,2\nzp s78,s78,1\nzl v10,s[24:27],s62 offen offset:2048 sc1 lds\ns_cmp_eq_u32 s71,1\ns_cselect_b32 s76,16384,s76\ns_cselect_b32 s77,s78,s77\nzo v29,s77,v28\n"
"v_lshl_add_u32 v29,v29,5,v30\nv_add_u32 v221,s76,v29\ns_lshl_b32 s76,s61,14\ns_mov_b32 s77,6\ns_lshl_b32 s78,s61,2\nzp s78,s78,2\nzl v11,s[24:27],s62 offen offset:3072 sc1 lds\ns_cmp_eq_u32 s71,1\n"
"s_cselect_b32 s76,16384,s76\ns_cselect_b32 s77,s78,s77\nzo v29,s77,v28\nv_lshl_add_u32 v29,v29,5,v30\nv_add_u32 v222,s76,v29\ns_lshl_b32 s76,s61,14\ns_mov_b32 s77,7\ns_lshl_b32 s78,s61,2\n"
"zp s78,s78,3\nzp m0,s65,4096\ns_nop 0\nzl v8,s[24:27],s63 offen offset:0 sc1 lds\ns_cmp_eq_u32 s71,1\ns_cselect_b32 s76,16384,s76\ns_cselect_b32 s77,s78,s77\nzo v29,s77,v28\n"
"v_lshl_add_u32 v29,v29,5,v30\nv_add_u32 v223,s76,v29\ns_cmp_eq_u32 s71,1\ns_mov_b32 s81,8192\ns_cselect_b32 s81,3584,s81\ns_sub_u32 s82,15,s73\ns_cmp_lt_u32 s23,2\ns_cselect_b32 s82,s82,s73\n"
"s_lshl_b32 s82,s82,7\nzl v9,s[24:27],s63 offen offset:1024 sc1 lds\ns_and_b32 s83,s23,1\ns_lshl_b32 s83,s83,6\nzp s82,s82,s83\ns_lshl_b32 s83,s73,8\ns_lshl_b32 s80,s23,6\nzp s83,s83,s80\n"
"s_cmp_eq_u32 s71,1\ns_cselect_b32 s82,s83,s82\nv_lshrrev_b32 v26,3,v6\nzl v10,s[24:27],s63 offen offset:2048 sc1 lds\nv_and_b32 v27,7,v6\nzo v27,v27,v26\nzh v27,4,v27\nv_mov_b32 v28,896\n"
"v_mad_u32_u24 v27,v26,v28,v27\ns_mul_i32 s76,s82,896\nv_add_u32 v27,s76,v27\nv_add_u32 v208,0,v27\nzl v11,s[24:27],s63 offen offset:3072 sc1 lds\nv_add_u32 v209,6144,v27\nv_add_u32 v210,12288,v27\nv_add_u32 v211,18432,v27\n"
"v_lshrrev_b32 v26,4,v6\nv_and_b32 v27,15,v6\nzh v28,1,v26\nzo v27,v27,v28\ns_mov_b32 s77,s81\ns_mov_b32 m0,s66\ns_nop 0\nzl v12,s[28:31],s64 offen offset:0 lds\n"
"v_mul_lo_u32 v29,v26,s77\ns_mul_i32 s76,s23,s77\ns_lshl_b32 s76,s76,4\nv_add_u32 v29,s76,v29\ns_lshl_b32 s78,s77,2\ns_sub_u32 s78,s78,1024\ns_mov_b32 s79,0\nzo v30,0,v27\n"
"zl v13,s[28:31],s64 offen offset:1024 lds\nzh v30,4,v30\nv_add_u32 v30,v30,v29\nv_add_u32 v212,s79,v30\nzp s79,s79,s78\nzo v30,0,v27\nzh v30,4,v30\nv_add_u32 v30,v30,v29\n"
"v_add_u32 v213,s79,v30\nzl v14,s[28:31],s64 offen offset:2048 lds\nzp s79,s79,s78\nzo v30,8,v27\nzh v30,4,v30\nv_add_u32 v30,v30,v29\nv_add_u32 v214,s79,v30\nzp s79,s79,s78\n"
"zo v30,8,v27\nzh v30,4,v30\nzl v15,s[28:31],s64 offen offset:3072 lds\nv_add_u32 v30,v30,v29\nv_add_u32 v215,s79,v30\ns_cmp_eq_u32 s71,1\ns_mov_b32 s78,3145728\ns_cselect_b32 s78,1048576,s78\n"
"zp s44,s94,s78\ns_addc_u32 s45,s95,0\ns_and_b32 s45,s45,0xffff\ns_mov_b32 s46,1835008\ns_mov_b32 s47,0x20000\ns_lshl_b32 s76,s72,9\ns_lshl_b32 s77,s72,8\ns_cmp_eq_u32 s71,1\n"
"s_cselect_b32 s76,s77,s76\ns_mov_b32 s77,3670016\ns_cselect_b32 s77,1605632,s77\ns_cselect_b32 s78,s16,s8\ns_cselect_b32 s79,s17,s9\ns_sub_u32 s80,s77,s76\nzp m0,s66,16384\ns_nop 0\n"
"zl v12,s[36:39],s64 offen offset:0 lds\nzp s48,s78,s76\ns_addc_u32 s49,s79,0\ns_and_b32 s49,s49,0xffff\ns_mov_b32 s50,s80\ns_mov_b32 s51,0x20000\nzp s80,s76,256\ns_cmp_eq_u32 s71,1\n"
"s_cselect_b32 s76,s76,s80\ns_cselect_b32 s78,s18,s8\ns_cselect_b32 s79,s19,s9\ns_sub_u32 s80,s77,s76\nzp s52,s78,s76\ns_addc_u32 s53,s79,0\ns_and_b32 s53,s53,0xffff\ns_mov_b32 s54,s80\n"
"s_mov_b32 s55,0x20000\ns_lshl_b32 s76,s72,20\nzp s76,s76,8388608\ns_cmp_eq_u32 s71,1\ns_cselect_b32 s76,75497472,s76\ns_mov_b32 s77,1048576\ns_cselect_b32 s77,7340032,s77\nzl v13,s[36:39],s64 offen offset:1024 lds\n"
"zp s56,s94,s76\ns_addc_u32 s57,s95,0\ns_and_b32 s57,s57,0xffff\ns_mov_b32 s58,s77\ns_mov_b32 s59,0x20000\ns_cmp_eq_u32 s68,0\ns_mov_b32 s78,41943040\ns_cselect_b32 s78,25165824,s78\n"
"s_lshl_b32 s76,s69,20\nzp s76,s76,s78\nzp s40,s94,s76\ns_addc_u32 s41,s95,0\ns_and_b32 s41,s41,0xffff\ns_mov_b32 s42,1048576\ns_mov_b32 s43,0x20000\nzp s84,s94,1024\n"
"s_addc_u32 s85,s95,0\ns_and_b32 s85,s85,0xffff\ns_mov_b32 s86,1024\ns_mov_b32 s87,0x20000\ns_sub_u32 s76,15,s73\ns_lshl_b32 s76,s76,4\nzl v14,s[36:39],s64 offen offset:2048 lds\ns_lshl_b32 s77,s73,4\n"
"s_sub_u32 s77,s77,16\ns_lshl_b32 s78,s73,5\ns_cmp_eq_u32 s71,1\ns_cselect_b32 s76,s78,s76\ns_cselect_b32 s77,s78,s77\nv_mov_b32 v26,s76\nv_mov_b32 v27,s77\nv_cmp_gt_u32 vcc,16,v6\n"
"s_nop 1\nzk v26,v27,v26,vcc\nv_add_u32 v26,v26,v6\nzh v225,2,v26\nv_mov_b32 v224,0\nzl v15,s[36:39],s64 offen offset:3072 lds\ns_lshl_b32 s76,s23,8\nzp s76,s76,s22\n"
"s_mul_i32 s76,s76,13\nv_min_u32 v26,12,v6\nv_add_u32 v26,s76,v26\nv_min_u32 v26,12543,v26\nzh v231,7,v26\nv_add_co_u32 v228,vcc,v228,v231\ns_nop 1\nv_addc_co_u32 v229,vcc,0,v229,vcc\n"
"s_mov_b64 s[4:5],s[8:9]\ns_mov_b64 s[10:11],s[16:17]\ns_mov_b64 s[12:13],s[18:19]\nzp s6,s8,1605632\ns_addc_u32 s7,s9,0\nzp s8,s8,2064384\ns_addc_u32 s9,s9,0\nzp s16,s14,1605632\n"
"s_addc_u32 s17,s15,0\nzp s18,s14,2064384\ns_addc_u32 s19,s15,0\nzp s62,s62,128\nzp s63,s63,128\nzp s64,s64,s74\ns_xor_b32 s65,s65,0x8000\ns_xor_b32 s66,s66,0x8000\n"
"buffer_load_dword v31,v4,s[24:27],s62 offen\nbuffer_load_dword v31,v5,s[28:31],s64 offen\ns_getpc_b64 s[76:77]\nL_cpf1:\ns_sub_u32 s76,s76,L_cpf1-L_F_codestart\ns_subb_u32 s77,s77,0\ns_lshr_b32 s78,s22,3\ns_lshl_b32 s78,s78,5\n"
"s_lshl_b32 s79,s23,3\nzp s78,s78,s79\nv_and_b32 v26,7,v6\nv_add_u32 v26,s78,v26\nzh v26,7,v26\nv_min_u32 v26,L_F_codeend-L_F_codestart-4,v26\nglobal_load_dword v31,v26,s[76:77]\nzv lgkmcnt(0)\n"
"zp s34,s34,1\nzp s88,s94,1048576\ns_addc_u32 s89,s95,0\ns_and_b32 s89,s89,0xffff\ns_mov_b32 s90,3932160\ns_mov_b32 s91,0x20000\ns_mov_b32 s92,2097152\nzv vmcnt(19)\n"
"s_barrier\nzt v[64:65],v18 offset:0\nzt v[66:67],v18 offset:1024\nzd v[32:35],v16 offset:0\nzt v[68:69],v19 offset:0\nzt v[70:71],v19 offset:1024\nzd v[36:39],v16 offset:2048\nzt v[72:73],v20 offset:0\n"
"zt v[74:75],v20 offset:1024\nzd v[40:43],v16 offset:4096\nzt v[76:77],v21 offset:0\nzt v[78:79],v21 offset:1024\nzd v[44:47],v16 offset:6144\nzt v[80:81],v22 offset:0\nzt v[82:83],v22 offset:1024\nzd v[48:51],v16 offset:8192\n"
"zt v[84:85],v23 offset:0\nzt v[86:87],v23 offset:1024\nzd v[52:55],v16 offset:10240\nzt v[88:89],v24 offset:0\nzt v[90:91],v24 offset:1024\nzd v[56:59],v16 offset:12288\nzt v[92:93],v25 offset:0\nzt v[94:95],v25 offset:1024\n"
"zd v[60:63],v16 offset:14336\nzv lgkmcnt(0)\ns_cmp_eq_u32 s68,0\ns_cbranch_scc0 L_P_v0\nL_P_k0:\nzm a[0:3],v[64:67],v[32:35],0\nzh v168,16,v160\nzm a[4:7],v[64:67],v[36:39],0\n"
"zt v[128:129],v18 offset:8192\nzh v180,16,v164\nzm a[8:11],v[64:67],v[40:43],0\nzm a[12:15],v[64:67],v[44:47],0\nzt v[130:131],v18 offset:9216\nv_and_b32 v169,0xffff0000,v160\nzm a[16:19],v[64:67],v[48:51],0\nzm a[20:23],v[64:67],v[52:55],0\n"
"zd v[96:99],v17 offset:0\nv_and_b32 v181,0xffff0000,v164\nzm a[24:27],v[64:67],v[56:59],0\nzm a[28:31],v[64:67],v[60:63],0\nzt v[132:133],v19 offset:8192\nzh v170,16,v161\nzm a[32:35],v[68:71],v[32:35],0\nzm a[36:39],v[68:71],v[36:39],0\n"
"zt v[134:135],v19 offset:9216\nzh v182,16,v165\nzm a[40:43],v[68:71],v[40:43],0\nv_and_b32 v171,0xffff0000,v161\nzm a[44:47],v[68:71],v[44:47],0\nzd v[100:103],v17 offset:2048\nzm a[48:51],v[68:71],v[48:51],0\nv_and_b32 v183,0xffff0000,v165\n"
"zm a[52:55],v[68:71],v[52:55],0\nzt v[136:137],v20 offset:8192\nzm a[56:59],v[68:71],v[56:59],0\nzh v172,16,v162\nzm a[60:63],v[68:71],v[60:63],0\nzt v[138:139],v20 offset:9216\nzm a[64:67],v[72:75],v[32:35],0\nzh v184,16,v166\n"
"zm a[68:71],v[72:75],v[36:39],0\nzd v[104:107],v17 offset:4096\nzm a[72:75],v[72:75],v[40:43],0\nv_and_b32 v173,0xffff0000,v162\nzm a[76:79],v[72:75],v[44:47],0\nzt v[140:141],v21 offset:8192\nv_and_b32 v185,0xffff0000,v166\nzm a[80:83],v[72:75],v[48:51],0\n"
"zm a[84:87],v[72:75],v[52:55],0\nzt v[142:143],v21 offset:9216\nzh v174,16,v163\nzm a[88:91],v[72:75],v[56:59],0\nzm a[92:95],v[72:75],v[60:63],0\nzd v[108:111],v17 offset:6144\nzh v186,16,v167\nzm a[96:99],v[76:79],v[32:35],0\n"
"zm a[100:103],v[76:79],v[36:39],0\nzt v[144:145],v22 offset:8192\nv_and_b32 v175,0xffff0000,v163\nzm a[104:107],v[76:79],v[40:43],0\nzm a[108:111],v[76:79],v[44:47],0\nzt v[146:147],v22 offset:9216\nv_and_b32 v187,0xffff0000,v167\nzm a[112:115],v[76:79],v[48:51],0\n"
"za v192,v168,v169\nzm a[116:119],v[76:79],v[52:55],0\nzd v[112:115],v17 offset:8192\nzm a[120:123],v[76:79],v[56:59],0\nza v196,v180,v181\nzm a[124:127],v[76:79],v[60:63],0\nzt v[148:149],v23 offset:8192\nzm a[128:131],v[80:83],v[32:35],0\n"
"za v192,v192,v170\nzm a[132:135],v[80:83],v[36:39],0\nzt v[150:151],v23 offset:9216\nzm a[136:139],v[80:83],v[40:43],0\nza v196,v196,v182\nzm a[140:143],v[80:83],v[44:47],0\nzd v[116:119],v17 offset:10240\nzm a[144:147],v[80:83],v[48:51],0\n"
"za v192,v192,v171\nzm a[148:151],v[80:83],v[52:55],0\nzt v[152:153],v24 offset:8192\nza v196,v196,v183\nzm a[152:155],v[80:83],v[56:59],0\nzm a[156:159],v[80:83],v[60:63],0\nzt v[154:155],v24 offset:9216\nza v192,v192,v172\n"
"zm a[160:163],v[84:87],v[32:35],0\nzm a[164:167],v[84:87],v[36:39],0\nzd v[120:123],v17 offset:12288\nza v196,v196,v184\nzm a[168:171],v[84:87],v[40:43],0\nzm a[172:175],v[84:87],v[44:47],0\nzt v[156:157],v25 offset:8192\nza v192,v192,v173\n"
"zm a[176:179],v[84:87],v[48:51],0\nzm a[180:183],v[84:87],v[52:55],0\nzt v[158:159],v25 offset:9216\nza v196,v196,v185\nzm a[184:187],v[84:87],v[56:59],0\nza v192,v192,v174\nzm a[188:191],v[84:87],v[60:63],0\nzd v[124:127],v17 offset:14336\n"
"zm a[192:195],v[88:91],v[32:35],0\nza v196,v196,v186\nzm a[196:199],v[88:91],v[36:39],0\nzm a[200:203],v[88:91],v[40:43],0\nza v192,v192,v175\nzm a[204:207],v[88:91],v[44:47],0\nzm a[208:211],v[88:91],v[48:51],0\nzo v16,0x8000,v16\n"
"za v196,v196,v187\nzm a[212:215],v[88:91],v[52:55],0\nzo v17,0x8000,v17\nzm a[216:219],v[88:91],v[56:59],0\nzo v18,0x8000,v18\ns_nop 1\nzm a[220:223],v[88:91],v[60:63],0\nzo v19,0x8000,v19\n"
"v_add_f32_dpp v192,v192,v192 row_ror:8 row_mask:0xf bank_mask:0xf\nzm a[224:227],v[92:95],v[32:35],0\nzo v20,0x8000,v20\nzm a[228:231],v[92:95],v[36:39],0\nzo v21,0x8000,v21\nv_add_f32_dpp v196,v196,v196 row_ror:8 row_mask:0xf bank_mask:0xf\nzm a[232:235],v[92:95],v[40:43],0\nzo v22,0x8000,v22\n"
"zm a[236:239],v[92:95],v[44:47],0\nzo v23,0x8000,v23\ns_nop 1\nzm a[240:243],v[92:95],v[48:51],0\nzo v24,0x8000,v24\nzm a[244:247],v[92:95],v[52:55],0\nzo v25,0x8000,v25\nv_add_f32_dpp v192,v192,v192 row_ror:4 row_mask:0xf bank_mask:0xf\n"
"zm a[248:251],v[92:95],v[56:59],0\nzm a[252:255],v[92:95],v[60:63],0\nzv vmcnt(3) lgkmcnt(0)\ns_barrier\nzm a[0:3],v[128:131],v[96:99],a[0:3]\nzt v[64:65],v18 offset:0\ns_mov_b32 m0,s65\nzm a[4:7],v[128:131],v[100:103],a[4:7]\n"
"zl v8,s[24:27],s62 offen offset:0 sc1 lds\nzm a[8:11],v[128:131],v[104:107],a[8:11]\nzt v[66:67],v18 offset:1024\nzm a[12:15],v[128:131],v[108:111],a[12:15]\nzm a[16:19],v[128:131],v[112:115],a[16:19]\nzl v9,s[24:27],s62 offen offset:1024 sc1 lds\nzm a[20:23],v[128:131],v[116:119],a[20:23]\nzd v[32:35],v16 offset:0\n"
"zm a[24:27],v[128:131],v[120:123],a[24:27]\nzt v[68:69],v19 offset:0\nzm a[28:31],v[128:131],v[124:127],a[28:31]\nzm a[32:35],v[132:135],v[96:99],a[32:35]\nzl v10,s[24:27],s62 offen offset:2048 sc1 lds\nv_add_f32_dpp v196,v196,v196 row_ror:4 row_mask:0xf bank_mask:0xf\nzm a[36:39],v[132:135],v[100:103],a[36:39]\nzt v[70:71],v19 offset:1024\n"
"s_nop 1\nzm a[40:43],v[132:135],v[104:107],a[40:43]\nzd v[36:39],v16 offset:2048\nv_add_f32_dpp v192,v192,v192 row_ror:2 row_mask:0xf bank_mask:0xf\nzm a[44:47],v[132:135],v[108:111],a[44:47]\nzm a[48:51],v[132:135],v[112:115],a[48:51]\nzl v11,s[24:27],s62 offen offset:3072 sc1 lds\nv_add_f32_dpp v196,v196,v196 row_ror:2 row_mask:0xf bank_mask:0xf\n"
"zm a[52:55],v[132:135],v[116:119],a[52:55]\nzt v[72:73],v20 offset:0\ns_nop 1\nzm a[56:59],v[132:135],v[120:123],a[56:59]\nzm a[60:63],v[132:135],v[124:127],a[60:63]\nzt v[74:75],v20 offset:1024\nzp m0,s65,4096\nv_add_f32_dpp v192,v192,v192 row_ror:1 row_mask:0xf bank_mask:0xf\n"
"zm a[64:67],v[136:139],v[96:99],a[64:67]\nzl v8,s[24:27],s63 offen offset:0 sc1 lds\nv_add_f32_dpp v196,v196,v196 row_ror:1 row_mask:0xf bank_mask:0xf\nzm a[68:71],v[136:139],v[100:103],a[68:71]\nzd v[40:43],v16 offset:4096\nv_mov_b32 v193,v192\nzm a[72:75],v[136:139],v[104:107],a[72:75]\nzm a[76:79],v[136:139],v[108:111],a[76:79]\n"
"zt v[76:77],v21 offset:0\nv_mov_b32 v197,v196\nzm a[80:83],v[136:139],v[112:115],a[80:83]\nzl v9,s[24:27],s63 offen offset:1024 sc1 lds\ns_nop 1\nzm a[84:87],v[136:139],v[116:119],a[84:87]\nzt v[78:79],v21 offset:1024\nzm a[88:91],v[136:139],v[120:123],a[88:91]\n"
"v_permlane16_swap_b32 v192,v193\nzm a[92:95],v[136:139],v[124:127],a[92:95]\nzl v10,s[24:27],s63 offen offset:2048 sc1 lds\nv_permlane16_swap_b32 v196,v197\nzm a[96:99],v[140:143],v[96:99],a[96:99]\nzd v[44:47],v16 offset:6144\nzm a[100:103],v[140:143],v[100:103],a[100:103]\ns_nop 1\n"
"zm a[104:107],v[140:143],v[104:107],a[104:107]\nzt v[80:81],v22 offset:0\nza v192,v192,v193\nzm a[108:111],v[140:143],v[108:111],a[108:111]\nzl v11,s[24:27],s63 offen offset:3072 sc1 lds\nza v196,v196,v197\nzm a[112:115],v[140:143],v[112:115],a[112:115]\nzt v[82:83],v22 offset:1024\n"
"zm a[116:119],v[140:143],v[116:119],a[116:119]\nv_mov_b32 v193,v192\nzm a[120:123],v[140:143],v[120:123],a[120:123]\nzd v[48:51],v16 offset:8192\ns_mov_b32 m0,s66\nv_mov_b32 v197,v196\nzm a[124:127],v[140:143],v[124:127],a[124:127]\nzl v12,s[28:31],s64 offen offset:0 lds\n"
"zm a[128:131],v[144:147],v[96:99],a[128:131]\nzt v[84:85],v23 offset:0\ns_nop 1\nzm a[132:135],v[144:147],v[100:103],a[132:135]\nv_permlane32_swap_b32 v192,v193\nzm a[136:139],v[144:147],v[104:107],a[136:139]\nzt v[86:87],v23 offset:1024\nv_permlane32_swap_b32 v196,v197\n"
"zm a[140:143],v[144:147],v[108:111],a[140:143]\nzl v13,s[28:31],s64 offen offset:1024 lds\nzm a[144:147],v[144:147],v[112:115],a[144:147]\nzd v[52:55],v16 offset:10240\ns_nop 1\nzm a[148:151],v[144:147],v[116:119],a[148:151]\nza v192,v192,v193\nzm a[152:155],v[144:147],v[120:123],a[152:155]\n"
"zt v[88:89],v24 offset:0\nzm a[156:159],v[144:147],v[124:127],a[156:159]\nzl v14,s[28:31],s64 offen offset:2048 lds\nza v196,v196,v197\nzm a[160:163],v[148:151],v[96:99],a[160:163]\nzx v192,0x3b124925,v192\nzm a[164:167],v[148:151],v[100:103],a[164:167]\nzt v[90:91],v24 offset:1024\n"
"zm a[168:171],v[148:151],v[104:107],a[168:171]\nzl v15,s[28:31],s64 offen offset:3072 lds\nzx v196,0x3b124925,v196\nzm a[172:175],v[148:151],v[108:111],a[172:175]\nzd v[56:59],v16 offset:12288\nzu v195,v168,v192\nzm a[176:179],v[148:151],v[112:115],a[176:179]\nzu v199,v180,v196\n"
"zm a[180:183],v[148:151],v[116:119],a[180:183]\nzt v[92:93],v25 offset:0\nzp m0,s66,16384\nzm a[184:187],v[148:151],v[120:123],a[184:187]\nzl v12,s[36:39],s64 offen offset:0 lds\nzx v194,v195,v195\nzm a[188:191],v[148:151],v[124:127],a[188:191]\nzt v[94:95],v25 offset:1024\n"
"zx v198,v199,v199\nzm a[192:195],v[152:155],v[96:99],a[192:195]\nzm a[196:199],v[152:155],v[100:103],a[196:199]\nzd v[60:63],v16 offset:14336\nzu v195,v169,v192\nzm a[200:203],v[152:155],v[104:107],a[200:203]\nzl v13,s[36:39],s64 offen offset:1024 lds\nzu v199,v181,v196\n"
"zm a[204:207],v[152:155],v[108:111],a[204:207]\nzf v194,v195,v195\nzm a[208:211],v[152:155],v[112:115],a[208:211]\nzm a[212:215],v[152:155],v[116:119],a[212:215]\nzf v198,v199,v199\nzm a[216:219],v[152:155],v[120:123],a[216:219]\nzl v14,s[36:39],s64 offen offset:2048 lds\nzu v195,v170,v192\n"
"zm a[220:223],v[152:155],v[124:127],a[220:223]\nzm a[224:227],v[156:159],v[96:99],a[224:227]\nzu v199,v182,v196\nzm a[228:231],v[156:159],v[100:103],a[228:231]\nzf v194,v195,v195\nzm a[232:235],v[156:159],v[104:107],a[232:235]\nzl v15,s[36:39],s64 offen offset:3072 lds\nzm a[236:239],v[156:159],v[108:111],a[236:239]\n"
"zp s93,s64,s74\nzm a[240:243],v[156:159],v[112:115],a[240:243]\nbuffer_load_dword v31,v4,s[24:27],s62 offen offset:128\nzm a[244:247],v[156:159],v[116:119],a[244:247]\nbuffer_load_dword v31,v5,s[28:31],s93 offen\nzm a[248:251],v[156:159],v[120:123],a[248:251]\nglobal_load_dword v31,v231,s[4:5]\nzp s62,s62,128\n"
"zp s63,s63,128\nzp s64,s64,s74\nzm a[252:255],v[156:159],v[124:127],a[252:255]\ns_xor_b32 s65,s65,0x8000\ns_xor_b32 s66,s66,0x8000\nzv lgkmcnt(0)\nzm a[0:3],v[64:67],v[32:35],a[0:3]\nzf v198,v199,v199\n"
"zm a[4:7],v[64:67],v[36:39],a[4:7]\nzt v[128:129],v18 offset:8192\nzu v195,v171,v192\nzm a[8:11],v[64:67],v[40:43],a[8:11]\ns_mov_b64 s[4:5],s[6:7]\nzm a[12:15],v[64:67],v[44:47],a[12:15]\nzt v[130:131],v18 offset:9216\nzu v199,v183,v196\n"
"zm a[16:19],v[64:67],v[48:51],a[16:19]\ns_mov_b64 s[6:7],s[8:9]\nzm a[20:23],v[64:67],v[52:55],a[20:23]\nzd v[96:99],v17 offset:0\nzf v194,v195,v195\nzm a[24:27],v[64:67],v[56:59],a[24:27]\ns_mov_b64 s[8:9],s[10:11]\nzm a[28:31],v[64:67],v[60:63],a[28:31]\n"
"zt v[132:133],v19 offset:8192\nzf v198,v199,v199\nzm a[32:35],v[68:71],v[32:35],a[32:35]\ns_mov_b64 s[10:11],s[12:13]\nzu v195,v172,v192\nzm a[36:39],v[68:71],v[36:39],a[36:39]\nzt v[134:135],v19 offset:9216\nzm a[40:43],v[68:71],v[40:43],a[40:43]\n"
"s_mov_b64 s[12:13],s[14:15]\nzu v199,v184,v196\nzm a[44:47],v[68:71],v[44:47],a[44:47]\nzd v[100:103],v17 offset:2048\nzm a[48:51],v[68:71],v[48:51],a[48:51]\ns_mov_b64 s[14:15],s[16:17]\nzf v194,v195,v195\nzm a[52:55],v[68:71],v[52:55],a[52:55]\n"
"zt v[136:137],v20 offset:8192\nzm a[56:59],v[68:71],v[56:59],a[56:59]\ns_mov_b64 s[16:17],s[18:19]\nzf v198,v199,v199\nzm a[60:63],v[68:71],v[60:63],a[60:63]\nzt v[138:139],v20 offset:9216\nzu v195,v173,v192\nzm a[64:67],v[72:75],v[32:35],a[64:67]\n"
"zm a[68:71],v[72:75],v[36:39],a[68:71]\nzd v[104:107],v17 offset:4096\nzu v199,v185,v196\nzm a[72:75],v[72:75],v[40:43],a[72:75]\nzm a[76:79],v[72:75],v[44:47],a[76:79]\nzt v[140:141],v21 offset:8192\nzf v194,v195,v195\nzm a[80:83],v[72:75],v[48:51],a[80:83]\n"
"zm a[84:87],v[72:75],v[52:55],a[84:87]\nzt v[142:143],v21 offset:9216\nzf v198,v199,v199\nzm a[88:91],v[72:75],v[56:59],a[88:91]\nzu v195,v174,v192\nzm a[92:95],v[72:75],v[60:63],a[92:95]\nzd v[108:111],v17 offset:6144\nzm a[96:99],v[76:79],v[32:35],a[96:99]\n"
"zu v199,v186,v196\nzm a[100:103],v[76:79],v[36:39],a[100:103]\nzt v[144:145],v22 offset:8192\nzm a[104:107],v[76:79],v[40:43],a[104:107]\nzf v194,v195,v195\nzm a[108:111],v[76:79],v[44:47],a[108:111]\nzt v[146:147],v22 offset:9216\nzm a[112:115],v[76:79],v[48:51],a[112:115]\n"
"zf v198,v199,v199\nzm a[116:119],v[76:79],v[52:55],a[116:119]\nzd v[112:115],v17 offset:8192\nzu v195,v175,v192\nzm a[120:123],v[76:79],v[56:59],a[120:123]\nzm a[124:127],v[76:79],v[60:63],a[124:127]\nzt v[148:149],v23 offset:8192\nzu v199,v187,v196\n"
"zm a[128:131],v[80:83],v[32:35],a[128:131]\nzm a[132:135],v[80:83],v[36:39],a[132:135]\nzt v[150:151],v23 offset:9216\nzf v194,v195,v195\nzm a[136:139],v[80:83],v[40:43],a[136:139]\nzm a[140:143],v[80:83],v[44:47],a[140:143]\nzd v[116:119],v17 offset:10240\nzf v198,v199,v199\n"
"zm a[144:147],v[80:83],v[48:51],a[144:147]\nzx v194,v194,v200\nzm a[148:151],v[80:83],v[52:55],a[148:151]\nzt v[152:153],v24 offset:8192\nzm a[152:155],v[80:83],v[56:59],a[152:155]\nzx v198,v198,v200\nzm a[156:159],v[80:83],v[60:63],a[156:159]\nzt v[154:155],v24 offset:9216\n"
"zm a[160:163],v[84:87],v[32:35],a[160:163]\ns_nop 1\nzm a[164:167],v[84:87],v[36:39],a[164:167]\nzd v[120:123],v17 offset:12288\nzm a[168:171],v[84:87],v[40:43],a[168:171]\nv_add_f32_dpp v194,v194,v194 row_ror:8 row_mask:0xf bank_mask:0xf\nzm a[172:175],v[84:87],v[44:47],a[172:175]\nzt v[156:157],v25 offset:8192\n"
"v_add_f32_dpp v198,v198,v198 row_ror:8 row_mask:0xf bank_mask:0xf\nzm a[176:179],v[84:87],v[48:51],a[176:179]\nzm a[180:183],v[84:87],v[52:55],a[180:183]\nzt v[158:159],v25 offset:9216\ns_nop 1\nzm a[184:187],v[84:87],v[56:59],a[184:187]\nzm a[188:191],v[84:87],v[60:63],a[188:191]\nzd v[124:127],v17 offset:14336\n"
"v_add_f32_dpp v194,v194,v194 row_ror:4 row_mask:0xf bank_mask:0xf\nzm a[192:195],v[88:91],v[32:35],a[192:195]\nzm a[196:199],v[88:91],v[36:39],a[196:199]\nv_add_f32_dpp v198,v198,v198 row_ror:4 row_mask:0xf bank_mask:0xf\nzm a[200:203],v[88:91],v[40:43],a[200:203]\ns_nop 1\nzm a[204:207],v[88:91],v[44:47],a[204:207]\nzm a[208:211],v[88:91],v[48:51],a[208:211]\n"
"zo v16,0x8000,v16\nv_add_f32_dpp v194,v194,v194 row_ror:2 row_mask:0xf bank_mask:0xf\nzm a[212:215],v[88:91],v[52:55],a[212:215]\nzo v17,0x8000,v17\nzm a[216:219],v[88:91],v[56:59],a[216:219]\nzo v18,0x8000,v18\nv_add_f32_dpp v198,v198,v198 row_ror:2 row_mask:0xf bank_mask:0xf\nzm a[220:223],v[88:91],v[60:63],a[220:223]\n"
"zo v19,0x8000,v19\nzm a[224:227],v[92:95],v[32:35],a[224:227]\nzo v20,0x8000,v20\ns_nop 1\nzm a[228:231],v[92:95],v[36:39],a[228:231]\nzo v21,0x8000,v21\nv_add_f32_dpp v194,v194,v194 row_ror:1 row_mask:0xf bank_mask:0xf\nzm a[232:235],v[92:95],v[40:43],a[232:235]\n"
"zo v22,0x8000,v22\nzm a[236:239],v[92:95],v[44:47],a[236:239]\nzo v23,0x8000,v23\nv_add_f32_dpp v198,v198,v198 row_ror:1 row_mask:0xf bank_mask:0xf\nzm a[240:243],v[92:95],v[48:51],a[240:243]\nzo v24,0x8000,v24\nzm a[244:247],v[92:95],v[52:55],a[244:247]\nzo v25,0x8000,v25\n"
"v_mov_b32 v193,v194\nzm a[248:251],v[92:95],v[56:59],a[248:251]\nzm a[252:255],v[92:95],v[60:63],a[252:255]\nzv vmcnt(3) lgkmcnt(0)\ns_barrier\nzm a[0:3],v[128:131],v[96:99],a[0:3]\nzt v[64:65],v18 offset:0\ns_mov_b32 m0,s65\n"
"zm a[4:7],v[128:131],v[100:103],a[4:7]\nzl v8,s[24:27],s62 offen offset:0 sc1 lds\nzm a[8:11],v[128:131],v[104:107],a[8:11]\nzt v[66:67],v18 offset:1024\nzm a[12:15],v[128:131],v[108:111],a[12:15]\nzm a[16:19],v[128:131],v[112:115],a[16:19]\nzl v9,s[24:27],s62 offen offset:1024 sc1 lds\nzm a[20:23],v[128:131],v[116:119],a[20:23]\n"
"zd v[32:35],v16 offset:0\nzm a[24:27],v[128:131],v[120:123],a[24:27]\nzt v[68:69],v19 offset:0\nzm a[28:31],v[128:131],v[124:127],a[28:31]\nzm a[32:35],v[132:135],v[96:99],a[32:35]\nzl v10,s[24:27],s62 offen offset:2048 sc1 lds\nv_mov_b32 v197,v198\nzm a[36:39],v[132:135],v[100:103],a[36:39]\n"
"zt v[70:71],v19 offset:1024\ns_nop 1\nzm a[40:43],v[132:135],v[104:107],a[40:43]\nzd v[36:39],v16 offset:2048\nv_permlane16_swap_b32 v194,v193\nzm a[44:47],v[132:135],v[108:111],a[44:47]\nzm a[48:51],v[132:135],v[112:115],a[48:51]\nzl v11,s[24:27],s62 offen offset:3072 sc1 lds\n"
"v_permlane16_swap_b32 v198,v197\nzm a[52:55],v[132:135],v[116:119],a[52:55]\nzt v[72:73],v20 offset:0\ns_nop 1\nzm a[56:59],v[132:135],v[120:123],a[56:59]\nzm a[60:63],v[132:135],v[124:127],a[60:63]\nzt v[74:75],v20 offset:1024\nzp m0,s65,4096\n"
"za v194,v194,v193\nzm a[64:67],v[136:139],v[96:99],a[64:67]\nzl v8,s[24:27],s63 offen offset:0 sc1 lds\nza v198,v198,v197\nzm a[68:71],v[136:139],v[100:103],a[68:71]\nzd v[40:43],v16 offset:4096\nv_mov_b32 v193,v194\nzm a[72:75],v[136:139],v[104:107],a[72:75]\n"
"zm a[76:79],v[136:139],v[108:111],a[76:79]\nzt v[76:77],v21 offset:0\nv_mov_b32 v197,v198\nzm a[80:83],v[136:139],v[112:115],a[80:83]\nzl v9,s[24:27],s63 offen offset:1024 sc1 lds\ns_nop 1\nzm a[84:87],v[136:139],v[116:119],a[84:87]\nzt v[78:79],v21 offset:1024\n"
"zm a[88:91],v[136:139],v[120:123],a[88:91]\nv_permlane32_swap_b32 v194,v193\nzm a[92:95],v[136:139],v[124:127],a[92:95]\nzl v10,s[24:27],s63 offen offset:2048 sc1 lds\nv_permlane32_swap_b32 v198,v197\nzm a[96:99],v[140:143],v[96:99],a[96:99]\nzd v[44:47],v16 offset:6144\nzm a[100:103],v[140:143],v[100:103],a[100:103]\n"
"s_nop 1\nzm a[104:107],v[140:143],v[104:107],a[104:107]\nzt v[80:81],v22 offset:0\nza v194,v194,v193\nzm a[108:111],v[140:143],v[108:111],a[108:111]\nzl v11,s[24:27],s63 offen offset:3072 sc1 lds\nza v198,v198,v197\nzm a[112:115],v[140:143],v[112:115],a[112:115]\n"
"zt v[82:83],v22 offset:1024\nzm a[116:119],v[140:143],v[116:119],a[116:119]\nzx v194,0x3b124925,v194\nzm a[120:123],v[140:143],v[120:123],a[120:123]\nzd v[48:51],v16 offset:8192\ns_mov_b32 m0,s66\nzx v198,0x3b124925,v198\nzm a[124:127],v[140:143],v[124:127],a[124:127]\n"
"zl v12,s[28:31],s64 offen offset:0 lds\nzm a[128:131],v[144:147],v[96:99],a[128:131]\nzt v[84:85],v23 offset:0\nzc v192,v192,v192\nzm a[132:135],v[144:147],v[100:103],a[132:135]\nzc v196,v196,v196\nzm a[136:139],v[144:147],v[104:107],a[136:139]\nzt v[86:87],v23 offset:1024\n"
"zh v192,16,v192\nzm a[140:143],v[144:147],v[108:111],a[140:143]\nzl v13,s[28:31],s64 offen offset:1024 lds\nzm a[144:147],v[144:147],v[112:115],a[144:147]\nzd v[52:55],v16 offset:10240\nzh v196,16,v196\nzm a[148:151],v[144:147],v[116:119],a[148:151]\nzc v194,v194,v194\n"
"zm a[152:155],v[144:147],v[120:123],a[152:155]\nzt v[88:89],v24 offset:0\nzm a[156:159],v[144:147],v[124:127],a[156:159]\nzl v14,s[28:31],s64 offen offset:2048 lds\nzc v198,v198,v198\nzm a[160:163],v[148:151],v[96:99],a[160:163]\nzh v194,16,v194\nzm a[164:167],v[148:151],v[100:103],a[164:167]\n"
"zt v[90:91],v24 offset:1024\nzm a[168:171],v[148:151],v[104:107],a[168:171]\nzl v15,s[28:31],s64 offen offset:3072 lds\nzh v198,16,v198\nzm a[172:175],v[148:151],v[108:111],a[172:175]\nzd v[56:59],v16 offset:12288\nza v194,0x3727c5ac,v194\nzm a[176:179],v[148:151],v[112:115],a[176:179]\n"
"za v198,0x3727c5ac,v198\nzm a[180:183],v[148:151],v[116:119],a[180:183]\nzt v[92:93],v25 offset:0\nzp m0,s66,16384\nzm a[184:187],v[148:151],v[120:123],a[184:187]\nzl v12,s[36:39],s64 offen offset:0 lds\nzc v194,v194,v194\nzm a[188:191],v[148:151],v[124:127],a[188:191]\n"
"zt v[94:95],v25 offset:1024\nzc v198,v198,v198\nzm a[192:195],v[152:155],v[96:99],a[192:195]\nzm a[196:199],v[152:155],v[100:103],a[196:199]\nzd v[60:63],v16 offset:14336\nzh v194,16,v194\nzm a[200:203],v[152:155],v[104:107],a[200:203]\nzl v13,s[36:39],s64 offen offset:1024 lds\n"
"zh v198,16,v198\nzm a[204:207],v[152:155],v[108:111],a[204:207]\nv_sqrt_f32 v194,v194\nzm a[208:211],v[152:155],v[112:115],a[208:211]\nzm a[212:215],v[152:155],v[116:119],a[212:215]\nv_sqrt_f32 v198,v198\nzm a[216:219],v[152:155],v[120:123],a[216:219]\nzl v14,s[36:39],s64 offen offset:2048 lds\n"
"s_nop 0\nzm a[220:223],v[152:155],v[124:127],a[220:223]\nzm a[224:227],v[156:159],v[96:99],a[224:227]\nzc v194,v194,v194\nzm a[228:231],v[156:159],v[100:103],a[228:231]\nzc v198,v198,v198\nzm a[232:235],v[156:159],v[104:107],a[232:235]\nzl v15,s[36:39],s64 offen offset:3072 lds\n"
"zm a[236:239],v[156:159],v[108:111],a[236:239]\nzp s93,s64,s74\nzm a[240:243],v[156:159],v[112:115],a[240:243]\nbuffer_load_dword v31,v4,s[24:27],s62 offen offset:128\nzm a[244:247],v[156:159],v[116:119],a[244:247]\nbuffer_load_dword v31,v5,s[28:31],s93 offen\nzm a[248:251],v[156:159],v[120:123],a[248:251]\nglobal_load_dword v31,v231,s[4:5]\n"
"zp s62,s62,128\nzp s63,s63,128\nzp s64,s64,s74\nzm a[252:255],v[156:159],v[124:127],a[252:255]\ns_xor_b32 s65,s65,0x8000\ns_xor_b32 s66,s66,0x8000\nzv lgkmcnt(0)\nzm a[0:3],v[64:67],v[32:35],a[0:3]\n"
"zh v194,16,v194\nzm a[4:7],v[64:67],v[36:39],a[4:7]\nzt v[128:129],v18 offset:8192\nzh v198,16,v198\nzm a[8:11],v[64:67],v[40:43],a[8:11]\ns_mov_b64 s[4:5],s[6:7]\nzm a[12:15],v[64:67],v[44:47],a[12:15]\nzt v[130:131],v18 offset:9216\n"
"zq v194,v194\nzm a[16:19],v[64:67],v[48:51],a[16:19]\ns_mov_b64 s[6:7],s[8:9]\nzm a[20:23],v[64:67],v[52:55],a[20:23]\nzd v[96:99],v17 offset:0\nzq v198,v198\nzm a[24:27],v[64:67],v[56:59],a[24:27]\ns_mov_b64 s[8:9],s[10:11]\n"
"zm a[28:31],v[64:67],v[60:63],a[28:31]\nzt v[132:133],v19 offset:8192\nzu v168,v168,v192\nzm a[32:35],v[68:71],v[32:35],a[32:35]\ns_mov_b64 s[10:11],s[12:13]\nzu v180,v180,v196\nzm a[36:39],v[68:71],v[36:39],a[36:39]\nzt v[134:135],v19 offset:9216\n"
"zm a[40:43],v[68:71],v[40:43],a[40:43]\ns_mov_b64 s[12:13],s[14:15]\nzu v169,v169,v192\nzm a[44:47],v[68:71],v[44:47],a[44:47]\nzd v[100:103],v17 offset:2048\nzm a[48:51],v[68:71],v[48:51],a[48:51]\ns_mov_b64 s[14:15],s[16:17]\nzu v181,v181,v196\n"
"zm a[52:55],v[68:71],v[52:55],a[52:55]\nzt v[136:137],v20 offset:8192\nzm a[56:59],v[68:71],v[56:59],a[56:59]\ns_mov_b64 s[16:17],s[18:19]\nzu v170,v170,v192\nzm a[60:63],v[68:71],v[60:63],a[60:63]\nzt v[138:139],v20 offset:9216\nzu v182,v182,v196\n"
"zm a[64:67],v[72:75],v[32:35],a[64:67]\nzm a[68:71],v[72:75],v[36:39],a[68:71]\nzd v[104:107],v17 offset:4096\nzu v171,v171,v192\nzm a[72:75],v[72:75],v[40:43],a[72:75]\nzm a[76:79],v[72:75],v[44:47],a[76:79]\nzt v[140:141],v21 offset:8192\nzu v183,v183,v196\n"
"zm a[80:83],v[72:75],v[48:51],a[80:83]\nzm a[84:87],v[72:75],v[52:55],a[84:87]\nzt v[142:143],v21 offset:9216\nzu v172,v172,v192\nzm a[88:91],v[72:75],v[56:59],a[88:91]\nzu v184,v184,v196\nzm a[92:95],v[72:75],v[60:63],a[92:95]\nzd v[108:111],v17 offset:6144\n"
"zm a[96:99],v[76:79],v[32:35],a[96:99]\nzu v173,v173,v192\nzm a[100:103],v[76:79],v[36:39],a[100:103]\nzt v[144:145],v22 offset:8192\nzm a[104:107],v[76:79],v[40:43],a[104:107]\nzu v185,v185,v196\nzm a[108:111],v[76:79],v[44:47],a[108:111]\nzt v[146:147],v22 offset:9216\n"
"zm a[112:115],v[76:79],v[48:51],a[112:115]\nzu v174,v174,v192\nzm a[116:119],v[76:79],v[52:55],a[116:119]\nzd v[112:115],v17 offset:8192\nzu v186,v186,v196\nzm a[120:123],v[76:79],v[56:59],a[120:123]\nzm a[124:127],v[76:79],v[60:63],a[124:127]\nzt v[148:149],v23 offset:8192\n"
"zu v175,v175,v192\nzm a[128:131],v[80:83],v[32:35],a[128:131]\nzm a[132:135],v[80:83],v[36:39],a[132:135]\nzt v[150:151],v23 offset:9216\nzu v187,v187,v196\nzm a[136:139],v[80:83],v[40:43],a[136:139]\nzm a[140:143],v[80:83],v[44:47],a[140:143]\nzd v[116:119],v17 offset:10240\n"
"zc v176,v168,v169\nzm a[144:147],v[80:83],v[48:51],a[144:147]\nzc v188,v180,v181\nzm a[148:151],v[80:83],v[52:55],a[148:151]\nzt v[152:153],v24 offset:8192\nzm a[152:155],v[80:83],v[56:59],a[152:155]\nzc v177,v170,v171\nzm a[156:159],v[80:83],v[60:63],a[156:159]\n"
"zt v[154:155],v24 offset:9216\nzm a[160:163],v[84:87],v[32:35],a[160:163]\nzc v189,v182,v183\nzm a[164:167],v[84:87],v[36:39],a[164:167]\nzd v[120:123],v17 offset:12288\nzm a[168:171],v[84:87],v[40:43],a[168:171]\nzc v178,v172,v173\nzm a[172:175],v[84:87],v[44:47],a[172:175]\n"
"zt v[156:157],v25 offset:8192\nzc v190,v184,v185\nzm a[176:179],v[84:87],v[48:51],a[176:179]\nzm a[180:183],v[84:87],v[52:55],a[180:183]\nzt v[158:159],v25 offset:9216\nzc v179,v174,v175\nzm a[184:187],v[84:87],v[56:59],a[184:187]\nzm a[188:191],v[84:87],v[60:63],a[188:191]\n"
"zd v[124:127],v17 offset:14336\nzc v191,v186,v187\nzm a[192:195],v[88:91],v[32:35],a[192:195]\nzm a[196:199],v[88:91],v[36:39],a[196:199]\nzh v168,16,v176\nzm a[200:203],v[88:91],v[40:43],a[200:203]\nzh v180,16,v188\nzm a[204:207],v[88:91],v[44:47],a[204:207]\n"
"zm a[208:211],v[88:91],v[48:51],a[208:211]\nzo v16,0x8000,v16\nv_and_b32 v169,0xffff0000,v176\nzm a[212:215],v[88:91],v[52:55],a[212:215]\nzo v17,0x8000,v17\nzm a[216:219],v[88:91],v[56:59],a[216:219]\nzo v18,0x8000,v18\nv_and_b32 v181,0xffff0000,v188\n"
"zm a[220:223],v[88:91],v[60:63],a[220:223]\nzo v19,0x8000,v19\nzm a[224:227],v[92:95],v[32:35],a[224:227]\nzo v20,0x8000,v20\nzh v170,16,v177\nzm a[228:231],v[92:95],v[36:39],a[228:231]\nzo v21,0x8000,v21\nzh v182,16,v189\n"
"zm a[232:235],v[92:95],v[40:43],a[232:235]\nzo v22,0x8000,v22\nzm a[236:239],v[92:95],v[44:47],a[236:239]\nzo v23,0x8000,v23\nv_and_b32 v171,0xffff0000,v177\nzm a[240:243],v[92:95],v[48:51],a[240:243]\nzo v24,0x8000,v24\nzm a[244:247],v[92:95],v[52:55],a[244:247]\n"
"zo v25,0x8000,v25\nv_and_b32 v183,0xffff0000,v189\nzm a[248:251],v[92:95],v[56:59],a[248:251]\nzm a[252:255],v[92:95],v[60:63],a[252:255]\nzv vmcnt(3) lgkmcnt(0)\ns_barrier\nzm a[0:3],v[128:131],v[96:99],a[0:3]\nzt v[64:65],v18 offset:0\n"
"s_mov_b32 m0,s65\nzm a[4:7],v[128:131],v[100:103],a[4:7]\nzl v8,s[24:27],s62 offen offset:0 sc1 lds\nzm a[8:11],v[128:131],v[104:107],a[8:11]\nzt v[66:67],v18 offset:1024\nzm a[12:15],v[128:131],v[108:111],a[12:15]\nzm a[16:19],v[128:131],v[112:115],a[16:19]\nzl v9,s[24:27],s62 offen offset:1024 sc1 lds\n"
"zm a[20:23],v[128:131],v[116:119],a[20:23]\nzd v[32:35],v16 offset:0\nzm a[24:27],v[128:131],v[120:123],a[24:27]\nzt v[68:69],v19 offset:0\nzm a[28:31],v[128:131],v[124:127],a[28:31]\nzm a[32:35],v[132:135],v[96:99],a[32:35]\nzl v10,s[24:27],s62 offen offset:2048 sc1 lds\nzh v172,16,v178\n"
"zm a[36:39],v[132:135],v[100:103],a[36:39]\nzt v[70:71],v19 offset:1024\nzh v184,16,v190\nzm a[40:43],v[132:135],v[104:107],a[40:43]\nzd v[36:39],v16 offset:2048\nv_and_b32 v173,0xffff0000,v178\nzm a[44:47],v[132:135],v[108:111],a[44:47]\nzm a[48:51],v[132:135],v[112:115],a[48:51]\n"
"zl v11,s[24:27],s62 offen offset:3072 sc1 lds\nv_and_b32 v185,0xffff0000,v190\nzm a[52:55],v[132:135],v[116:119],a[52:55]\nzt v[72:73],v20 offset:0\nzh v174,16,v179\nzm a[56:59],v[132:135],v[120:123],a[56:59]\nzm a[60:63],v[132:135],v[124:127],a[60:63]\nzt v[74:75],v20 offset:1024\n"
"zp m0,s65,4096\nzh v186,16,v191\nzm a[64:67],v[136:139],v[96:99],a[64:67]\nzl v8,s[24:27],s63 offen offset:0 sc1 lds\nv_and_b32 v175,0xffff0000,v179\nzm a[68:71],v[136:139],v[100:103],a[68:71]\nzd v[40:43],v16 offset:4096\nv_and_b32 v187,0xffff0000,v191\n"
"zm a[72:75],v[136:139],v[104:107],a[72:75]\nzm a[76:79],v[136:139],v[108:111],a[76:79]\nzt v[76:77],v21 offset:0\nzx v168,v168,v194\nzm a[80:83],v[136:139],v[112:115],a[80:83]\nzl v9,s[24:27],s63 offen offset:1024 sc1 lds\nzx v180,v180,v198\nzm a[84:87],v[136:139],v[116:119],a[84:87]\n"
"zt v[78:79],v21 offset:1024\nzm a[88:91],v[136:139],v[120:123],a[88:91]\nzx v169,v169,v194\nzm a[92:95],v[136:139],v[124:127],a[92:95]\nzl v10,s[24:27],s63 offen offset:2048 sc1 lds\nzx v181,v181,v198\nzm a[96:99],v[140:143],v[96:99],a[96:99]\nzd v[44:47],v16 offset:6144\n"
"zm a[100:103],v[140:143],v[100:103],a[100:103]\nzx v170,v170,v194\nzm a[104:107],v[140:143],v[104:107],a[104:107]\nzt v[80:81],v22 offset:0\nzx v182,v182,v198\nzm a[108:111],v[140:143],v[108:111],a[108:111]\nzl v11,s[24:27],s63 offen offset:3072 sc1 lds\nzx v171,v171,v194\n"
"zm a[112:115],v[140:143],v[112:115],a[112:115]\nzt v[82:83],v22 offset:1024\nzm a[116:119],v[140:143],v[116:119],a[116:119]\nzx v183,v183,v198\nzm a[120:123],v[140:143],v[120:123],a[120:123]\nzd v[48:51],v16 offset:8192\ns_mov_b32 m0,s66\nzx v172,v172,v194\n"
"zm a[124:127],v[140:143],v[124:127],a[124:127]\nzl v12,s[28:31],s64 offen offset:0 lds\nzm a[128:131],v[144:147],v[96:99],a[128:131]\nzt v[84:85],v23 offset:0\nzx v184,v184,v198\nzm a[132:135],v[144:147],v[100:103],a[132:135]\nzx v173,v173,v194\nzm a[136:139],v[144:147],v[104:107],a[136:139]\n"
"zt v[86:87],v23 offset:1024\nzx v185,v185,v198\nzm a[140:143],v[144:147],v[108:111],a[140:143]\nzl v13,s[28:31],s64 offen offset:1024 lds\nzm a[144:147],v[144:147],v[112:115],a[144:147]\nzd v[52:55],v16 offset:10240\nzx v174,v174,v194\nzm a[148:151],v[144:147],v[116:119],a[148:151]\n"
"zx v186,v186,v198\nzm a[152:155],v[144:147],v[120:123],a[152:155]\nzt v[88:89],v24 offset:0\nzm a[156:159],v[144:147],v[124:127],a[156:159]\nzl v14,s[28:31],s64 offen offset:2048 lds\nzx v175,v175,v194\nzm a[160:163],v[148:151],v[96:99],a[160:163]\nzx v187,v187,v198\n"
"zm a[164:167],v[148:151],v[100:103],a[164:167]\nzt v[90:91],v24 offset:1024\nzm a[168:171],v[148:151],v[104:107],a[168:171]\nzl v15,s[28:31],s64 offen offset:3072 lds\nzc v176,v168,v169\nzm a[172:175],v[148:151],v[108:111],a[172:175]\nzd v[56:59],v16 offset:12288\nzc v188,v180,v181\n"
"zm a[176:179],v[148:151],v[112:115],a[176:179]\nzc v177,v170,v171\nzm a[180:183],v[148:151],v[116:119],a[180:183]\nzt v[92:93],v25 offset:0\nzp m0,s66,16384\nzm a[184:187],v[148:151],v[120:123],a[184:187]\nzl v12,s[36:39],s64 offen offset:0 lds\nzc v189,v182,v183\n"
"zm a[188:191],v[148:151],v[124:127],a[188:191]\nzt v[94:95],v25 offset:1024\nzc v178,v172,v173\nzm a[192:195],v[152:155],v[96:99],a[192:195]\nzm a[196:199],v[152:155],v[100:103],a[196:199]\nzd v[60:63],v16 offset:14336\nzc v190,v184,v185\nzm a[200:203],v[152:155],v[104:107],a[200:203]\n"
"zl v13,s[36:39],s64 offen offset:1024 lds\nzc v179,v174,v175\nzm a[204:207],v[152:155],v[108:111],a[204:207]\nzc v191,v186,v187\nzm a[208:211],v[152:155],v[112:115],a[208:211]\nzm a[212:215],v[152:155],v[116:119],a[212:215]\nzh v168,16,v176\nzm a[216:219],v[152:155],v[120:123],a[216:219]\n"
"zl v14,s[36:39],s64 offen offset:2048 lds\nzh v180,16,v188\nzm a[220:223],v[152:155],v[124:127],a[220:223]\nzm a[224:227],v[156:159],v[96:99],a[224:227]\nv_and_b32 v169,0xffff0000,v176\nzm a[228:231],v[156:159],v[100:103],a[228:231]\nv_and_b32 v181,0xffff0000,v188\nzm a[232:235],v[156:159],v[104:107],a[232:235]\n"
"zl v15,s[36:39],s64 offen offset:3072 lds\nzm a[236:239],v[156:159],v[108:111],a[236:239]\nzp s93,s64,s74\nzm a[240:243],v[156:159],v[112:115],a[240:243]\nbuffer_load_dword v31,v4,s[24:27],s62 offen offset:128\nzm a[244:247],v[156:159],v[116:119],a[244:247]\nbuffer_load_dword v31,v5,s[28:31],s93 offen\nzm a[248:251],v[156:159],v[120:123],a[248:251]\n"
"global_load_dword v31,v231,s[4:5]\nzp s62,s62,128\nzp s63,s63,128\nzp s64,s64,s74\nzm a[252:255],v[156:159],v[124:127],a[252:255]\ns_xor_b32 s65,s65,0x8000\ns_xor_b32 s66,s66,0x8000\nzv lgkmcnt(0)\n"
"zm a[0:3],v[64:67],v[32:35],a[0:3]\nzh v170,16,v177\nzm a[4:7],v[64:67],v[36:39],a[4:7]\nzt v[128:129],v18 offset:8192\nzh v182,16,v189\nzm a[8:11],v[64:67],v[40:43],a[8:11]\ns_mov_b64 s[4:5],s[6:7]\nzm a[12:15],v[64:67],v[44:47],a[12:15]\n"
"zt v[130:131],v18 offset:9216\nv_and_b32 v171,0xffff0000,v177\nzm a[16:19],v[64:67],v[48:51],a[16:19]\ns_mov_b64 s[6:7],s[8:9]\nzm a[20:23],v[64:67],v[52:55],a[20:23]\nzd v[96:99],v17 offset:0\nv_and_b32 v183,0xffff0000,v189\nzm a[24:27],v[64:67],v[56:59],a[24:27]\n"
"s_mov_b64 s[8:9],s[10:11]\nzm a[28:31],v[64:67],v[60:63],a[28:31]\nzt v[132:133],v19 offset:8192\nzh v172,16,v178\nzm a[32:35],v[68:71],v[32:35],a[32:35]\ns_mov_b64 s[10:11],s[12:13]\nzh v184,16,v190\nzm a[36:39],v[68:71],v[36:39],a[36:39]\n"
"zt v[134:135],v19 offset:9216\nzm a[40:43],v[68:71],v[40:43],a[40:43]\ns_mov_b64 s[12:13],s[14:15]\nv_and_b32 v173,0xffff0000,v178\nzm a[44:47],v[68:71],v[44:47],a[44:47]\nzd v[100:103],v17 offset:2048\nzm a[48:51],v[68:71],v[48:51],a[48:51]\ns_mov_b64 s[14:15],s[16:17]\n"
"v_and_b32 v185,0xffff0000,v190\nzm a[52:55],v[68:71],v[52:55],a[52:55]\nzt v[136:137],v20 offset:8192\nzm a[56:59],v[68:71],v[56:59],a[56:59]\ns_mov_b64 s[16:17],s[18:19]\nzh v174,16,v179\nzm a[60:63],v[68:71],v[60:63],a[60:63]\nzt v[138:139],v20 offset:9216\n"
"zh v186,16,v191\nzm a[64:67],v[72:75],v[32:35],a[64:67]\nzm a[68:71],v[72:75],v[36:39],a[68:71]\nzd v[104:107],v17 offset:4096\nv_and_b32 v175,0xffff0000,v179\nzm a[72:75],v[72:75],v[40:43],a[72:75]\nzm a[76:79],v[72:75],v[44:47],a[76:79]\nzt v[140:141],v21 offset:8192\n"
"v_and_b32 v187,0xffff0000,v191\nzm a[80:83],v[72:75],v[48:51],a[80:83]\nzm a[84:87],v[72:75],v[52:55],a[84:87]\nzt v[142:143],v21 offset:9216\nzx v168,0x3db8aa3b,v168\nzm a[88:91],v[72:75],v[56:59],a[88:91]\nzx v180,0x3db8aa3b,v180\nzm a[92:95],v[72:75],v[60:63],a[92:95]\n"
"zd v[108:111],v17 offset:6144\nzm a[96:99],v[76:79],v[32:35],a[96:99]\nzx v169,0x3db8aa3b,v169\nzm a[100:103],v[76:79],v[36:39],a[100:103]\nzt v[144:145],v22 offset:8192\nzm a[104:107],v[76:79],v[40:43],a[104:107]\nzx v181,0x3db8aa3b,v181\nzm a[108:111],v[76:79],v[44:47],a[108:111]\n"
"zt v[146:147],v22 offset:9216\nzm a[112:115],v[76:79],v[48:51],a[112:115]\nzx v170,0x3db8aa3b,v170\nzm a[116:119],v[76:79],v[52:55],a[116:119]\nzd v[112:115],v17 offset:8192\nzx v182,0x3db8aa3b,v182\nzm a[120:123],v[76:79],v[56:59],a[120:123]\nzm a[124:127],v[76:79],v[60:63],a[124:127]\n"
"zt v[148:149],v23 offset:8192\nzx v171,0x3db8aa3b,v171\nzm a[128:131],v[80:83],v[32:35],a[128:131]\nzm a[132:135],v[80:83],v[36:39],a[132:135]\nzt v[150:151],v23 offset:9216\nzx v183,0x3db8aa3b,v183\nzm a[136:139],v[80:83],v[40:43],a[136:139]\nzm a[140:143],v[80:83],v[44:47],a[140:143]\n"
"zd v[116:119],v17 offset:10240\nzx v172,0x3db8aa3b,v172\nzm a[144:147],v[80:83],v[48:51],a[144:147]\nzx v184,0x3db8aa3b,v184\nzm a[148:151],v[80:83],v[52:55],a[148:151]\nzt v[152:153],v24 offset:8192\nzm a[152:155],v[80:83],v[56:59],a[152:155]\nzx v173,0x3db8aa3b,v173\n"
"zm a[156:159],v[80:83],v[60:63],a[156:159]\nzt v[154:155],v24 offset:9216\nzm a[160:163],v[84:87],v[32:35],a[160:163]\nzx v185,0x3db8aa3b,v185\nzm a[164:167],v[84:87],v[36:39],a[164:167]\nzd v[120:123],v17 offset:12288\nzm a[168:171],v[84:87],v[40:43],a[168:171]\nzx v174,0x3db8aa3b,v174\n"
"zm a[172:175],v[84:87],v[44:47],a[172:175]\nzt v[156:157],v25 offset:8192\nzx v186,0x3db8aa3b,v186\nzm a[176:179],v[84:87],v[48:51],a[176:179]\nzm a[180:183],v[84:87],v[52:55],a[180:183]\nzt v[158:159],v25 offset:9216\nzx v175,0x3db8aa3b,v175\nzm a[184:187],v[84:87],v[56:59],a[184:187]\n"
"zm a[188:191],v[84:87],v[60:63],a[188:191]\nzd v[124:127],v17 offset:14336\nzx v187,0x3db8aa3b,v187\nzm a[192:195],v[88:91],v[32:35],a[192:195]\nzm a[196:199],v[88:91],v[36:39],a[196:199]\nzc v160,v168,v169\nzm a[200:203],v[88:91],v[40:43],a[200:203]\nzc v164,v180,v181\n"
"zm a[204:207],v[88:91],v[44:47],a[204:207]\nzm a[208:211],v[88:91],v[48:51],a[208:211]\nzo v16,0x8000,v16\nzc v161,v170,v171\nzm a[212:215],v[88:91],v[52:55],a[212:215]\nzo v17,0x8000,v17\nzm a[216:219],v[88:91],v[56:59],a[216:219]\nzo v18,0x8000,v18\n"
"zc v165,v182,v183\nzm a[220:223],v[88:91],v[60:63],a[220:223]\nzo v19,0x8000,v19\nzm a[224:227],v[92:95],v[32:35],a[224:227]\nzo v20,0x8000,v20\nzc v162,v172,v173\nzm a[228:231],v[92:95],v[36:39],a[228:231]\nzo v21,0x8000,v21\n"
"zc v166,v184,v185\nzm a[232:235],v[92:95],v[40:43],a[232:235]\nzo v22,0x8000,v22\nzm a[236:239],v[92:95],v[44:47],a[236:239]\nzo v23,0x8000,v23\nzc v163,v174,v175\nzm a[240:243],v[92:95],v[48:51],a[240:243]\nzo v24,0x8000,v24\n"
"zm a[244:247],v[92:95],v[52:55],a[244:247]\nzo v25,0x8000,v25\nzc v167,v186,v187\nzm a[248:251],v[92:95],v[56:59],a[248:251]\nzm a[252:255],v[92:95],v[60:63],a[252:255]\nzv vmcnt(3) lgkmcnt(0)\ns_barrier\nzm a[0:3],v[128:131],v[96:99],a[0:3]\n"
"zs v[176:179],v201,s[88:91],0 offen offset:0 sc1\nzm a[4:7],v[128:131],v[100:103],a[4:7]\nzt v[64:65],v18 offset:0\nzm a[8:11],v[128:131],v[104:107],a[8:11]\nzs v[188:191],v201,s[88:91],0 offen offset:896 sc1\nzm a[12:15],v[128:131],v[108:111],a[12:15]\nzt v[66:67],v18 offset:1024\nzm a[16:19],v[128:131],v[112:115],a[16:19]\n"
"zs v[160:163],v201,s[88:91],s92 offen offset:0 sc1\nzm a[20:23],v[128:131],v[116:119],a[20:23]\nzm a[24:27],v[128:131],v[120:123],a[24:27]\nzs v[164:167],v201,s[88:91],s92 offen offset:896 sc1\nzm a[28:31],v[128:131],v[124:127],a[28:31]\nzd v[32:35],v16 offset:0\ns_mov_b32 m0,s65\nzm a[32:35],v[132:135],v[96:99],a[32:35]\n"
"zl v8,s[24:27],s62 offen offset:0 sc1 lds\nzm a[36:39],v[132:135],v[100:103],a[36:39]\nzt v[68:69],v19 offset:0\nzm a[40:43],v[132:135],v[104:107],a[40:43]\nzm a[44:47],v[132:135],v[108:111],a[44:47]\nzl v9,s[24:27],s62 offen offset:1024 sc1 lds\nzm a[48:51],v[132:135],v[112:115],a[48:51]\nzt v[70:71],v19 offset:1024\n"
"zm a[52:55],v[132:135],v[116:119],a[52:55]\nzd v[36:39],v16 offset:2048\nzm a[56:59],v[132:135],v[120:123],a[56:59]\nzl v10,s[24:27],s62 offen offset:2048 sc1 lds\nzm a[60:63],v[132:135],v[124:127],a[60:63]\nzm a[64:67],v[136:139],v[96:99],a[64:67]\nzt v[72:73],v20 offset:0\nzm a[68:71],v[136:139],v[100:103],a[68:71]\n"
"zt v[74:75],v20 offset:1024\nzm a[72:75],v[136:139],v[104:107],a[72:75]\nzl v11,s[24:27],s62 offen offset:3072 sc1 lds\nzm a[76:79],v[136:139],v[108:111],a[76:79]\nzm a[80:83],v[136:139],v[112:115],a[80:83]\nzd v[40:43],v16 offset:4096\nzp m0,s65,4096\nzm a[84:87],v[136:139],v[116:119],a[84:87]\n"
"zl v8,s[24:27],s63 offen offset:0 sc1 lds\nzm a[88:91],v[136:139],v[120:123],a[88:91]\nzt v[76:77],v21 offset:0\nzm a[92:95],v[136:139],v[124:127],a[92:95]\nzm a[96:99],v[140:143],v[96:99],a[96:99]\nzl v9,s[24:27],s63 offen offset:1024 sc1 lds\nzm a[100:103],v[140:143],v[100:103],a[100:103]\nzt v[78:79],v21 offset:1024\n"
"zm a[104:107],v[140:143],v[104:107],a[104:107]\nzd v[44:47],v16 offset:6144\nzm a[108:111],v[140:143],v[108:111],a[108:111]\nzm a[112:115],v[140:143],v[112:115],a[112:115]\nzl v10,s[24:27],s63 offen offset:2048 sc1 lds\nzm a[116:119],v[140:143],v[116:119],a[116:119]\nzt v[80:81],v22 offset:0\nzm a[120:123],v[140:143],v[120:123],a[120:123]\n"
"zm a[124:127],v[140:143],v[124:127],a[124:127]\nzl v11,s[24:27],s63 offen offset:3072 sc1 lds\nzm a[128:131],v[144:147],v[96:99],a[128:131]\nzt v[82:83],v22 offset:1024\nzm a[132:135],v[144:147],v[100:103],a[132:135]\nzd v[48:51],v16 offset:8192\ns_mov_b32 m0,s66\nzm a[136:139],v[144:147],v[104:107],a[136:139]\n"
"zl v12,s[28:31],s64 offen offset:0 lds\nzm a[140:143],v[144:147],v[108:111],a[140:143]\nzm a[144:147],v[144:147],v[112:115],a[144:147]\nzt v[84:85],v23 offset:0\nzm a[148:151],v[144:147],v[116:119],a[148:151]\nzt v[86:87],v23 offset:1024\nzm a[152:155],v[144:147],v[120:123],a[152:155]\nzl v13,s[28:31],s64 offen offset:1024 lds\n"
"zm a[156:159],v[144:147],v[124:127],a[156:159]\nzm a[160:163],v[148:151],v[96:99],a[160:163]\nzd v[52:55],v16 offset:10240\nzm a[164:167],v[148:151],v[100:103],a[164:167]\nzl v14,s[28:31],s64 offen offset:2048 lds\nzm a[168:171],v[148:151],v[104:107],a[168:171]\nzt v[88:89],v24 offset:0\nzm a[172:175],v[148:151],v[108:111],a[172:175]\n"
"zm a[176:179],v[148:151],v[112:115],a[176:179]\nzl v15,s[28:31],s64 offen offset:3072 lds\nzm a[180:183],v[148:151],v[116:119],a[180:183]\nzt v[90:91],v24 offset:1024\nzm a[184:187],v[148:151],v[120:123],a[184:187]\nzd v[56:59],v16 offset:12288\nzm a[188:191],v[148:151],v[124:127],a[188:191]\nzp m0,s66,16384\n"
"zm a[192:195],v[152:155],v[96:99],a[192:195]\nzl v12,s[36:39],s64 offen offset:0 lds\nzm a[196:199],v[152:155],v[100:103],a[196:199]\nzt v[92:93],v25 offset:0\nzm a[200:203],v[152:155],v[104:107],a[200:203]\nzt v[94:95],v25 offset:1024\nzm a[204:207],v[152:155],v[108:111],a[204:207]\nzl v13,s[36:39],s64 offen offset:1024 lds\n"
"zm a[208:211],v[152:155],v[112:115],a[208:211]\nzm a[212:215],v[152:155],v[116:119],a[212:215]\nzd v[60:63],v16 offset:14336\nzm a[216:219],v[152:155],v[120:123],a[216:219]\nzl v14,s[36:39],s64 offen offset:2048 lds\nzm a[220:223],v[152:155],v[124:127],a[220:223]\nzm a[224:227],v[156:159],v[96:99],a[224:227]\nzm a[228:231],v[156:159],v[100:103],a[228:231]\n"
"zm a[232:235],v[156:159],v[104:107],a[232:235]\nzl v15,s[36:39],s64 offen offset:3072 lds\nzm a[236:239],v[156:159],v[108:111],a[236:239]\nzp s93,s64,s74\nzm a[240:243],v[156:159],v[112:115],a[240:243]\nbuffer_load_dword v31,v4,s[24:27],s62 offen offset:128\nzm a[244:247],v[156:159],v[116:119],a[244:247]\nbuffer_load_dword v31,v5,s[28:31],s93 offen\n"
"zm a[248:251],v[156:159],v[120:123],a[248:251]\nglobal_load_dword v31,v231,s[4:5]\nzp s62,s62,128\nzp s63,s63,128\nzp s64,s64,s74\nzm a[252:255],v[156:159],v[124:127],a[252:255]\ns_xor_b32 s65,s65,0x8000\ns_xor_b32 s66,s66,0x8000\n"
"zv lgkmcnt(0)\ns_mov_b32 s67,2\ns_mov_b32 s75,0\n.p2align 6\nL_P_kloopA:\nzm a[0:3],v[64:67],v[32:35],a[0:3]\nzm a[4:7],v[64:67],v[36:39],a[4:7]\nzt v[128:129],v18 offset:8192\n"
"zm a[8:11],v[64:67],v[40:43],a[8:11]\ns_mov_b64 s[4:5],s[6:7]\nzm a[12:15],v[64:67],v[44:47],a[12:15]\nzt v[130:131],v18 offset:9216\nzm a[16:19],v[64:67],v[48:51],a[16:19]\ns_mov_b64 s[6:7],s[8:9]\nzm a[20:23],v[64:67],v[52:55],a[20:23]\nzd v[96:99],v17 offset:0\n"
"zm a[24:27],v[64:67],v[56:59],a[24:27]\ns_mov_b64 s[8:9],s[10:11]\nzm a[28:31],v[64:67],v[60:63],a[28:31]\nzt v[132:133],v19 offset:8192\nzm a[32:35],v[68:71],v[32:35],a[32:35]\ns_mov_b64 s[10:11],s[12:13]\nzm a[36:39],v[68:71],v[36:39],a[36:39]\nzt v[134:135],v19 offset:9216\n"
"zm a[40:43],v[68:71],v[40:43],a[40:43]\ns_mov_b64 s[12:13],s[14:15]\nzm a[44:47],v[68:71],v[44:47],a[44:47]\nzd v[100:103],v17 offset:2048\nzm a[48:51],v[68:71],v[48:51],a[48:51]\ns_mov_b64 s[14:15],s[16:17]\nzm a[52:55],v[68:71],v[52:55],a[52:55]\nzt v[136:137],v20 offset:8192\n"
"zm a[56:59],v[68:71],v[56:59],a[56:59]\ns_mov_b64 s[16:17],s[18:19]\nzm a[60:63],v[68:71],v[60:63],a[60:63]\nzt v[138:139],v20 offset:9216\nzm a[64:67],v[72:75],v[32:35],a[64:67]\nzm a[68:71],v[72:75],v[36:39],a[68:71]\nzd v[104:107],v17 offset:4096\nzm a[72:75],v[72:75],v[40:43],a[72:75]\n"
"zm a[76:79],v[72:75],v[44:47],a[76:79]\nzt v[140:141],v21 offset:8192\nzm a[80:83],v[72:75],v[48:51],a[80:83]\nzm a[84:87],v[72:75],v[52:55],a[84:87]\nzt v[142:143],v21 offset:9216\nzm a[88:91],v[72:75],v[56:59],a[88:91]\nzm a[92:95],v[72:75],v[60:63],a[92:95]\nzd v[108:111],v17 offset:6144\n"
"zm a[96:99],v[76:79],v[32:35],a[96:99]\nzm a[100:103],v[76:79],v[36:39],a[100:103]\nzt v[144:145],v22 offset:8192\nzm a[104:107],v[76:79],v[40:43],a[104:107]\nzm a[108:111],v[76:79],v[44:47],a[108:111]\nzt v[146:147],v22 offset:9216\nzm a[112:115],v[76:79],v[48:51],a[112:115]\nzm a[116:119],v[76:79],v[52:55],a[116:119]\n"
"zd v[112:115],v17 offset:8192\nzm a[120:123],v[76:79],v[56:59],a[120:123]\nzm a[124:127],v[76:79],v[60:63],a[124:127]\nzt v[148:149],v23 offset:8192\nzm a[128:131],v[80:83],v[32:35],a[128:131]\nzm a[132:135],v[80:83],v[36:39],a[132:135]\nzt v[150:151],v23 offset:9216\nzm a[136:139],v[80:83],v[40:43],a[136:139]\n"
"zm a[140:143],v[80:83],v[44:47],a[140:143]\nzd v[116:119],v17 offset:10240\nzm a[144:147],v[80:83],v[48:51],a[144:147]\nzm a[148:151],v[80:83],v[52:55],a[148:151]\nzt v[152:153],v24 offset:8192\nzm a[152:155],v[80:83],v[56:59],a[152:155]\nzm a[156:159],v[80:83],v[60:63],a[156:159]\nzt v[154:155],v24 offset:9216\n"
"zm a[160:163],v[84:87],v[32:35],a[160:163]\nzm a[164:167],v[84:87],v[36:39],a[164:167]\nzd v[120:123],v17 offset:12288\nzm a[168:171],v[84:87],v[40:43],a[168:171]\nzm a[172:175],v[84:87],v[44:47],a[172:175]\nzt v[156:157],v25 offset:8192\nzm a[176:179],v[84:87],v[48:51],a[176:179]\nzm a[180:183],v[84:87],v[52:55],a[180:183]\n"
"zt v[158:159],v25 offset:9216\nzm a[184:187],v[84:87],v[56:59],a[184:187]\nzm a[188:191],v[84:87],v[60:63],a[188:191]\nzd v[124:127],v17 offset:14336\nzm a[192:195],v[88:91],v[32:35],a[192:195]\nzm a[196:199],v[88:91],v[36:39],a[196:199]\nzm a[200:203],v[88:91],v[40:43],a[200:203]\nzm a[204:207],v[88:91],v[44:47],a[204:207]\n"
"zm a[208:211],v[88:91],v[48:51],a[208:211]\nzo v16,0x8000,v16\nzm a[212:215],v[88:91],v[52:55],a[212:215]\nzo v17,0x8000,v17\nzm a[216:219],v[88:91],v[56:59],a[216:219]\nzo v18,0x8000,v18\nzm a[220:223],v[88:91],v[60:63],a[220:223]\nzo v19,0x8000,v19\n"
"zm a[224:227],v[92:95],v[32:35],a[224:227]\nzo v20,0x8000,v20\nzm a[228:231],v[92:95],v[36:39],a[228:231]\nzo v21,0x8000,v21\nzm a[232:235],v[92:95],v[40:43],a[232:235]\nzo v22,0x8000,v22\nzm a[236:239],v[92:95],v[44:47],a[236:239]\nzo v23,0x8000,v23\n"
"zm a[240:243],v[92:95],v[48:51],a[240:243]\nzo v24,0x8000,v24\nzm a[244:247],v[92:95],v[52:55],a[244:247]\nzo v25,0x8000,v25\nzm a[248:251],v[92:95],v[56:59],a[248:251]\nzm a[252:255],v[92:95],v[60:63],a[252:255]\nzv vmcnt(3) lgkmcnt(0)\ns_barrier\n"
"zm a[0:3],v[128:131],v[96:99],a[0:3]\nzt v[64:65],v18 offset:0\ns_mov_b32 m0,s65\nzm a[4:7],v[128:131],v[100:103],a[4:7]\nzl v8,s[24:27],s62 offen offset:0 sc1 lds\nzm a[8:11],v[128:131],v[104:107],a[8:11]\nzt v[66:67],v18 offset:1024\nzm a[12:15],v[128:131],v[108:111],a[12:15]\n"
"zm a[16:19],v[128:131],v[112:115],a[16:19]\nzl v9,s[24:27],s62 offen offset:1024 sc1 lds\nzm a[20:23],v[128:131],v[116:119],a[20:23]\nzd v[32:35],v16 offset:0\nzm a[24:27],v[128:131],v[120:123],a[24:27]\nzt v[68:69],v19 offset:0\nzm a[28:31],v[128:131],v[124:127],a[28:31]\nzm a[32:35],v[132:135],v[96:99],a[32:35]\n"
"zl v10,s[24:27],s62 offen offset:2048 sc1 lds\nzm a[36:39],v[132:135],v[100:103],a[36:39]\nzt v[70:71],v19 offset:1024\nzm a[40:43],v[132:135],v[104:107],a[40:43]\nzd v[36:39],v16 offset:2048\nzm a[44:47],v[132:135],v[108:111],a[44:47]\nzm a[48:51],v[132:135],v[112:115],a[48:51]\nzl v11,s[24:27],s62 offen offset:3072 sc1 lds\n"
"zm a[52:55],v[132:135],v[116:119],a[52:55]\nzt v[72:73],v20 offset:0\nzm a[56:59],v[132:135],v[120:123],a[56:59]\nzm a[60:63],v[132:135],v[124:127],a[60:63]\nzt v[74:75],v20 offset:1024\nzp m0,s65,4096\nzm a[64:67],v[136:139],v[96:99],a[64:67]\nzl v8,s[24:27],s63 offen offset:0 sc1 lds\n"
"zm a[68:71],v[136:139],v[100:103],a[68:71]\nzd v[40:43],v16 offset:4096\nzm a[72:75],v[136:139],v[104:107],a[72:75]\nzm a[76:79],v[136:139],v[108:111],a[76:79]\nzt v[76:77],v21 offset:0\nzm a[80:83],v[136:139],v[112:115],a[80:83]\nzl v9,s[24:27],s63 offen offset:1024 sc1 lds\nzm a[84:87],v[136:139],v[116:119],a[84:87]\n"
"zt v[78:79],v21 offset:1024\nzm a[88:91],v[136:139],v[120:123],a[88:91]\nzm a[92:95],v[136:139],v[124:127],a[92:95]\nzl v10,s[24:27],s63 offen offset:2048 sc1 lds\nzm a[96:99],v[140:143],v[96:99],a[96:99]\nzd v[44:47],v16 offset:6144\nzm a[100:103],v[140:143],v[100:103],a[100:103]\nzm a[104:107],v[140:143],v[104:107],a[104:107]\n"
"zt v[80:81],v22 offset:0\nzm a[108:111],v[140:143],v[108:111],a[108:111]\nzl v11,s[24:27],s63 offen offset:3072 sc1 lds\nzm a[112:115],v[140:143],v[112:115],a[112:115]\nzt v[82:83],v22 offset:1024\nzm a[116:119],v[140:143],v[116:119],a[116:119]\nzm a[120:123],v[140:143],v[120:123],a[120:123]\nzd v[48:51],v16 offset:8192\n"
"s_mov_b32 m0,s66\nzm a[124:127],v[140:143],v[124:127],a[124:127]\nzl v12,s[28:31],s64 offen offset:0 lds\nzm a[128:131],v[144:147],v[96:99],a[128:131]\nzt v[84:85],v23 offset:0\nzm a[132:135],v[144:147],v[100:103],a[132:135]\nzm a[136:139],v[144:147],v[104:107],a[136:139]\nzt v[86:87],v23 offset:1024\n"
"zm a[140:143],v[144:147],v[108:111],a[140:143]\nzl v13,s[28:31],s64 offen offset:1024 lds\nzm a[144:147],v[144:147],v[112:115],a[144:147]\nzd v[52:55],v16 offset:10240\nzm a[148:151],v[144:147],v[116:119],a[148:151]\nzm a[152:155],v[144:147],v[120:123],a[152:155]\nzt v[88:89],v24 offset:0\nzm a[156:159],v[144:147],v[124:127],a[156:159]\n"
"zl v14,s[28:31],s64 offen offset:2048 lds\nzm a[160:163],v[148:151],v[96:99],a[160:163]\nzm a[164:167],v[148:151],v[100:103],a[164:167]\nzt v[90:91],v24 offset:1024\nzm a[168:171],v[148:151],v[104:107],a[168:171]\nzl v15,s[28:31],s64 offen offset:3072 lds\nzm a[172:175],v[148:151],v[108:111],a[172:175]\nzd v[56:59],v16 offset:12288\n"
"zm a[176:179],v[148:151],v[112:115],a[176:179]\nzm a[180:183],v[148:151],v[116:119],a[180:183]\nzt v[92:93],v25 offset:0\nzp m0,s66,16384\nzm a[184:187],v[148:151],v[120:123],a[184:187]\nzl v12,s[36:39],s64 offen offset:0 lds\nzm a[188:191],v[148:151],v[124:127],a[188:191]\nzt v[94:95],v25 offset:1024\n"
"zm a[192:195],v[152:155],v[96:99],a[192:195]\nzm a[196:199],v[152:155],v[100:103],a[196:199]\nzd v[60:63],v16 offset:14336\nzm a[200:203],v[152:155],v[104:107],a[200:203]\nzl v13,s[36:39],s64 offen offset:1024 lds\nzm a[204:207],v[152:155],v[108:111],a[204:207]\nzm a[208:211],v[152:155],v[112:115],a[208:211]\nzm a[212:215],v[152:155],v[116:119],a[212:215]\n"
"zm a[216:219],v[152:155],v[120:123],a[216:219]\nzl v14,s[36:39],s64 offen offset:2048 lds\nzm a[220:223],v[152:155],v[124:127],a[220:223]\nzm a[224:227],v[156:159],v[96:99],a[224:227]\nzm a[228:231],v[156:159],v[100:103],a[228:231]\nzm a[232:235],v[156:159],v[104:107],a[232:235]\nzl v15,s[36:39],s64 offen offset:3072 lds\nzm a[236:239],v[156:159],v[108:111],a[236:239]\n"
"zp s93,s64,s74\nzm a[240:243],v[156:159],v[112:115],a[240:243]\nbuffer_load_dword v31,v4,s[24:27],s62 offen offset:128\nzm a[244:247],v[156:159],v[116:119],a[244:247]\nbuffer_load_dword v31,v5,s[28:31],s93 offen\nzm a[248:251],v[156:159],v[120:123],a[248:251]\nglobal_load_dword v31,v231,s[4:5]\nzp s62,s62,128\n"
"zp s63,s63,128\nzp s64,s64,s74\nzm a[252:255],v[156:159],v[124:127],a[252:255]\ns_xor_b32 s65,s65,0x8000\ns_xor_b32 s66,s66,0x8000\ns_sub_u32 s67,s67,1\ns_cmp_lg_u32 s67,0\nzv lgkmcnt(0)\n"
"s_cbranch_scc1 L_P_kloopA\ns_cmp_eq_u32 s75,0\ns_cbranch_scc0 L_P_kre0\ns_lshl_b32 s82,s22,2\nzp s82,s82,1024\ns_cmp_eq_u32 s23,0\ns_cbranch_scc0 L_nf_lnk\nv_mov_b32 v26,s82\n"
"v_mov_b32 v27,s34\nglobal_store_dword v26,v27,s[94:95] sc1\nL_nf_lnk:\ns_mov_b32 s75,1\ns_mov_b32 s67,2\ns_branch L_P_kloopA\nL_P_kre0:\ns_mov_b32 s67,4\n"
".p2align 6\nL_P_kloop:\nzm a[0:3],v[64:67],v[32:35],a[0:3]\nzm a[4:7],v[64:67],v[36:39],a[4:7]\nzt v[128:129],v18 offset:8192\nzm a[8:11],v[64:67],v[40:43],a[8:11]\nzm a[12:15],v[64:67],v[44:47],a[12:15]\nzt v[130:131],v18 offset:9216\n"
"zm a[16:19],v[64:67],v[48:51],a[16:19]\nzm a[20:23],v[64:67],v[52:55],a[20:23]\nzd v[96:99],v17 offset:0\nzm a[24:27],v[64:67],v[56:59],a[24:27]\nzm a[28:31],v[64:67],v[60:63],a[28:31]\nzt v[132:133],v19 offset:8192\nzm a[32:35],v[68:71],v[32:35],a[32:35]\nzm a[36:39],v[68:71],v[36:39],a[36:39]\n"
"zt v[134:135],v19 offset:9216\nzm a[40:43],v[68:71],v[40:43],a[40:43]\nzm a[44:47],v[68:71],v[44:47],a[44:47]\nzd v[100:103],v17 offset:2048\nzm a[48:51],v[68:71],v[48:51],a[48:51]\nzm a[52:55],v[68:71],v[52:55],a[52:55]\nzt v[136:137],v20 offset:8192\nzm a[56:59],v[68:71],v[56:59],a[56:59]\n"
"zm a[60:63],v[68:71],v[60:63],a[60:63]\nzt v[138:139],v20 offset:9216\nzm a[64:67],v[72:75],v[32:35],a[64:67]\nzm a[68:71],v[72:75],v[36:39],a[68:71]\nzd v[104:107],v17 offset:4096\nzm a[72:75],v[72:75],v[40:43],a[72:75]\nzm a[76:79],v[72:75],v[44:47],a[76:79]\nzt v[140:141],v21 offset:8192\n"
"zm a[80:83],v[72:75],v[48:51],a[80:83]\nzm a[84:87],v[72:75],v[52:55],a[84:87]\nzt v[142:143],v21 offset:9216\nzm a[88:91],v[72:75],v[56:59],a[88:91]\nzm a[92:95],v[72:75],v[60:63],a[92:95]\nzd v[108:111],v17 offset:6144\nzm a[96:99],v[76:79],v[32:35],a[96:99]\nzm a[100:103],v[76:79],v[36:39],a[100:103]\n"
"zt v[144:145],v22 offset:8192\nzm a[104:107],v[76:79],v[40:43],a[104:107]\nzm a[108:111],v[76:79],v[44:47],a[108:111]\nzt v[146:147],v22 offset:9216\nzm a[112:115],v[76:79],v[48:51],a[112:115]\nzm a[116:119],v[76:79],v[52:55],a[116:119]\nzd v[112:115],v17 offset:8192\nzm a[120:123],v[76:79],v[56:59],a[120:123]\n"
"zm a[124:127],v[76:79],v[60:63],a[124:127]\nzt v[148:149],v23 offset:8192\nzm a[128:131],v[80:83],v[32:35],a[128:131]\nzm a[132:135],v[80:83],v[36:39],a[132:135]\nzt v[150:151],v23 offset:9216\nzm a[136:139],v[80:83],v[40:43],a[136:139]\nzm a[140:143],v[80:83],v[44:47],a[140:143]\nzd v[116:119],v17 offset:10240\n"
"zm a[144:147],v[80:83],v[48:51],a[144:147]\nzm a[148:151],v[80:83],v[52:55],a[148:151]\nzt v[152:153],v24 offset:8192\nzm a[152:155],v[80:83],v[56:59],a[152:155]\nzm a[156:159],v[80:83],v[60:63],a[156:159]\nzt v[154:155],v24 offset:9216\nzm a[160:163],v[84:87],v[32:35],a[160:163]\nzm a[164:167],v[84:87],v[36:39],a[164:167]\n"
"zd v[120:123],v17 offset:12288\nzm a[168:171],v[84:87],v[40:43],a[168:171]\nzm a[172:175],v[84:87],v[44:47],a[172:175]\nzt v[156:157],v25 offset:8192\nzm a[176:179],v[84:87],v[48:51],a[176:179]\nzm a[180:183],v[84:87],v[52:55],a[180:183]\nzt v[158:159],v25 offset:9216\nzm a[184:187],v[84:87],v[56:59],a[184:187]\n"
"zm a[188:191],v[84:87],v[60:63],a[188:191]\nzd v[124:127],v17 offset:14336\nzm a[192:195],v[88:91],v[32:35],a[192:195]\nzm a[196:199],v[88:91],v[36:39],a[196:199]\nzm a[200:203],v[88:91],v[40:43],a[200:203]\nzm a[204:207],v[88:91],v[44:47],a[204:207]\nzm a[208:211],v[88:91],v[48:51],a[208:211]\nzo v16,0x8000,v16\n"
"zm a[212:215],v[88:91],v[52:55],a[212:215]\nzo v17,0x8000,v17\nzm a[216:219],v[88:91],v[56:59],a[216:219]\nzo v18,0x8000,v18\nzm a[220:223],v[88:91],v[60:63],a[220:223]\nzo v19,0x8000,v19\nzm a[224:227],v[92:95],v[32:35],a[224:227]\nzo v20,0x8000,v20\n"
"zm a[228:231],v[92:95],v[36:39],a[228:231]\nzo v21,0x8000,v21\nzm a[232:235],v[92:95],v[40:43],a[232:235]\nzo v22,0x8000,v22\nzm a[236:239],v[92:95],v[44:47],a[236:239]\nzo v23,0x8000,v23\nzm a[240:243],v[92:95],v[48:51],a[240:243]\nzo v24,0x8000,v24\n"
"zm a[244:247],v[92:95],v[52:55],a[244:247]\nzo v25,0x8000,v25\nzm a[248:251],v[92:95],v[56:59],a[248:251]\nzm a[252:255],v[92:95],v[60:63],a[252:255]\nzv vmcnt(2) lgkmcnt(0)\ns_barrier\nzm a[0:3],v[128:131],v[96:99],a[0:3]\nzt v[64:65],v18 offset:0\n"
"s_mov_b32 m0,s65\nzm a[4:7],v[128:131],v[100:103],a[4:7]\nzl v8,s[24:27],s62 offen offset:0 sc1 lds\nzm a[8:11],v[128:131],v[104:107],a[8:11]\nzt v[66:67],v18 offset:1024\nzm a[12:15],v[128:131],v[108:111],a[12:15]\nzm a[16:19],v[128:131],v[112:115],a[16:19]\nzl v9,s[24:27],s62 offen offset:1024 sc1 lds\n"
"zm a[20:23],v[128:131],v[116:119],a[20:23]\nzd v[32:35],v16 offset:0\nzm a[24:27],v[128:131],v[120:123],a[24:27]\nzt v[68:69],v19 offset:0\nzm a[28:31],v[128:131],v[124:127],a[28:31]\nzm a[32:35],v[132:135],v[96:99],a[32:35]\nzl v10,s[24:27],s62 offen offset:2048 sc1 lds\nzm a[36:39],v[132:135],v[100:103],a[36:39]\n"
"zt v[70:71],v19 offset:1024\nzm a[40:43],v[132:135],v[104:107],a[40:43]\nzd v[36:39],v16 offset:2048\nzm a[44:47],v[132:135],v[108:111],a[44:47]\nzm a[48:51],v[132:135],v[112:115],a[48:51]\nzl v11,s[24:27],s62 offen offset:3072 sc1 lds\nzm a[52:55],v[132:135],v[116:119],a[52:55]\nzt v[72:73],v20 offset:0\n"
"zm a[56:59],v[132:135],v[120:123],a[56:59]\nzm a[60:63],v[132:135],v[124:127],a[60:63]\nzt v[74:75],v20 offset:1024\nzp m0,s65,4096\nzm a[64:67],v[136:139],v[96:99],a[64:67]\nzl v8,s[24:27],s63 offen offset:0 sc1 lds\nzm a[68:71],v[136:139],v[100:103],a[68:71]\nzd v[40:43],v16 offset:4096\n"
"zm a[72:75],v[136:139],v[104:107],a[72:75]\nzm a[76:79],v[136:139],v[108:111],a[76:79]\nzt v[76:77],v21 offset:0\nzm a[80:83],v[136:139],v[112:115],a[80:83]\nzl v9,s[24:27],s63 offen offset:1024 sc1 lds\nzm a[84:87],v[136:139],v[116:119],a[84:87]\nzt v[78:79],v21 offset:1024\nzm a[88:91],v[136:139],v[120:123],a[88:91]\n"
"zm a[92:95],v[136:139],v[124:127],a[92:95]\nzl v10,s[24:27],s63 offen offset:2048 sc1 lds\nzm a[96:99],v[140:143],v[96:99],a[96:99]\nzd v[44:47],v16 offset:6144\nzm a[100:103],v[140:143],v[100:103],a[100:103]\nzm a[104:107],v[140:143],v[104:107],a[104:107]\nzt v[80:81],v22 offset:0\nzm a[108:111],v[140:143],v[108:111],a[108:111]\n"
"zl v11,s[24:27],s63 offen offset:3072 sc1 lds\nzm a[112:115],v[140:143],v[112:115],a[112:115]\nzt v[82:83],v22 offset:1024\nzm a[116:119],v[140:143],v[116:119],a[116:119]\nzm a[120:123],v[140:143],v[120:123],a[120:123]\nzd v[48:51],v16 offset:8192\ns_mov_b32 m0,s66\nzm a[124:127],v[140:143],v[124:127],a[124:127]\n"
"zl v12,s[28:31],s64 offen offset:0 lds\nzm a[128:131],v[144:147],v[96:99],a[128:131]\nzt v[84:85],v23 offset:0\nzm a[132:135],v[144:147],v[100:103],a[132:135]\nzm a[136:139],v[144:147],v[104:107],a[136:139]\nzt v[86:87],v23 offset:1024\nzm a[140:143],v[144:147],v[108:111],a[140:143]\nzl v13,s[28:31],s64 offen offset:1024 lds\n"
"zm a[144:147],v[144:147],v[112:115],a[144:147]\nzd v[52:55],v16 offset:10240\nzm a[148:151],v[144:147],v[116:119],a[148:151]\nzm a[152:155],v[144:147],v[120:123],a[152:155]\nzt v[88:89],v24 offset:0\nzm a[156:159],v[144:147],v[124:127],a[156:159]\nzl v14,s[28:31],s64 offen offset:2048 lds\nzm a[160:163],v[148:151],v[96:99],a[160:163]\n"
"zm a[164:167],v[148:151],v[100:103],a[164:167]\nzt v[90:91],v24 offset:1024\nzm a[168:171],v[148:151],v[104:107],a[168:171]\nzl v15,s[28:31],s64 offen offset:3072 lds\nzm a[172:175],v[148:151],v[108:111],a[172:175]\nzd v[56:59],v16 offset:12288\nzm a[176:179],v[148:151],v[112:115],a[176:179]\nzm a[180:183],v[148:151],v[116:119],a[180:183]\n"
"zt v[92:93],v25 offset:0\nzp m0,s66,16384\nzm a[184:187],v[148:151],v[120:123],a[184:187]\nzl v12,s[36:39],s64 offen offset:0 lds\nzm a[188:191],v[148:151],v[124:127],a[188:191]\nzt v[94:95],v25 offset:1024\nzm a[192:195],v[152:155],v[96:99],a[192:195]\nzm a[196:199],v[152:155],v[100:103],a[196:199]\n"
"zd v[60:63],v16 offset:14336\nzm a[200:203],v[152:155],v[104:107],a[200:203]\nzl v13,s[36:39],s64 offen offset:1024 lds\nzm a[204:207],v[152:155],v[108:111],a[204:207]\nzm a[208:211],v[152:155],v[112:115],a[208:211]\nzm a[212:215],v[152:155],v[116:119],a[212:215]\nzm a[216:219],v[152:155],v[120:123],a[216:219]\nzl v14,s[36:39],s64 offen offset:2048 lds\n"
"zm a[220:223],v[152:155],v[124:127],a[220:223]\nzm a[224:227],v[156:159],v[96:99],a[224:227]\nzm a[228:231],v[156:159],v[100:103],a[228:231]\nzm a[232:235],v[156:159],v[104:107],a[232:235]\nzl v15,s[36:39],s64 offen offset:3072 lds\nzm a[236:239],v[156:159],v[108:111],a[236:239]\nzp s93,s64,s74\nzm a[240:243],v[156:159],v[112:115],a[240:243]\n"
"buffer_load_dword v31,v4,s[24:27],s62 offen offset:128\nzm a[244:247],v[156:159],v[116:119],a[244:247]\nbuffer_load_dword v31,v5,s[28:31],s93 offen\nzm a[248:251],v[156:159],v[120:123],a[248:251]\nzp s62,s62,128\nzp s63,s63,128\nzp s64,s64,s74\nzm a[252:255],v[156:159],v[124:127],a[252:255]\n"
"s_xor_b32 s65,s65,0x8000\ns_xor_b32 s66,s66,0x8000\ns_sub_u32 s67,s67,1\ns_cmp_lg_u32 s67,0\nzv lgkmcnt(0)\ns_cbranch_scc1 L_P_kloop\ns_cmp_eq_u32 s75,1\ns_cbranch_scc0 L_P_kre2\n"
"buffer_load_dword v224,v225,s[84:87],0 offen sc1\ns_mov_b32 s75,2\ns_mov_b32 s67,2\ns_branch L_P_kloop\nL_P_kre2:\ns_cmp_eq_u32 s75,2\ns_cbranch_scc0 L_P_klast\ns_mov_b32 s75,3\n"
"s_mov_b32 s67,1\nv_mov_b32 v4,2147483632\nv_mov_b32 v5,2147483632\ns_cmp_eq_u32 s71,2\ns_cbranch_scc1 L_P_knone\nv_cmp_ne_u32 vcc,s34,v224\ns_nop 2\ns_cmp_eq_u32 vcc_lo,0\n"
"s_cbranch_scc1 L_P_klnok\ns_mov_b32 s76,0\nL_P_klnpoll:\nbuffer_load_dword v224,v225,s[84:87],0 offen sc1\nzv vmcnt(0)\nv_cmp_ne_u32 vcc,s34,v224\ns_nop 1\ns_cmp_eq_u32 vcc_lo,0\n"
"s_cbranch_scc1 L_P_klnok\nzp s76,s76,1\ns_cmp_lt_u32 s76,300000\ns_cbranch_scc0 L_P_klnok\ns_sleep 1\ns_branch L_P_klnpoll\nL_P_klnok:\nv_mov_b32 v8,v208\n"
"v_mov_b32 v9,v209\nv_mov_b32 v10,v210\nv_mov_b32 v11,v211\nv_mov_b32 v12,v212\nv_mov_b32 v13,v213\nv_mov_b32 v14,v214\nv_mov_b32 v15,v215\ns_mov_b64 s[24:25],s[44:45]\n"
"s_mov_b64 s[26:27],s[46:47]\ns_mov_b64 s[28:29],s[48:49]\ns_mov_b64 s[30:31],s[50:51]\ns_mov_b64 s[36:37],s[52:53]\ns_mov_b64 s[38:39],s[54:55]\ns_mov_b32 s62,0\ns_mov_b32 s63,28672\ns_mov_b32 s64,0\n"
"s_cmp_eq_u32 s71,1\ns_mov_b32 s74,524288\ns_cselect_b32 s74,229376,s74\ns_mov_b32 s67,1\ns_branch L_P_kloop\nL_P_knone:\ns_mov_b32 s26,0\ns_mov_b32 s30,0\n"
"s_mov_b32 s38,0\ns_branch L_P_kloop\nL_P_klast:\nzm a[0:3],v[64:67],v[32:35],a[0:3]\nzm a[4:7],v[64:67],v[36:39],a[4:7]\nzt v[128:129],v18 offset:8192\nzm a[8:11],v[64:67],v[40:43],a[8:11]\nzm a[12:15],v[64:67],v[44:47],a[12:15]\n"
"zt v[130:131],v18 offset:9216\nzm a[16:19],v[64:67],v[48:51],a[16:19]\nzm a[20:23],v[64:67],v[52:55],a[20:23]\nzd v[96:99],v17 offset:0\nzm a[24:27],v[64:67],v[56:59],a[24:27]\nzm a[28:31],v[64:67],v[60:63],a[28:31]\nzt v[132:133],v19 offset:8192\nzm a[32:35],v[68:71],v[32:35],a[32:35]\n"
"zm a[36:39],v[68:71],v[36:39],a[36:39]\nzt v[134:135],v19 offset:9216\nzm a[40:43],v[68:71],v[40:43],a[40:43]\nzm a[44:47],v[68:71],v[44:47],a[44:47]\nzd v[100:103],v17 offset:2048\nzm a[48:51],v[68:71],v[48:51],a[48:51]\nzm a[52:55],v[68:71],v[52:55],a[52:55]\nzt v[136:137],v20 offset:8192\n"
"zm a[56:59],v[68:71],v[56:59],a[56:59]\nzm a[60:63],v[68:71],v[60:63],a[60:63]\nzt v[138:139],v20 offset:9216\nzm a[64:67],v[72:75],v[32:35],a[64:67]\nzm a[68:71],v[72:75],v[36:39],a[68:71]\nzd v[104:107],v17 offset:4096\nzm a[72:75],v[72:75],v[40:43],a[72:75]\nzm a[76:79],v[72:75],v[44:47],a[76:79]\n"
"zt v[140:141],v21 offset:8192\nzm a[80:83],v[72:75],v[48:51],a[80:83]\nzm a[84:87],v[72:75],v[52:55],a[84:87]\nzt v[142:143],v21 offset:9216\nzm a[88:91],v[72:75],v[56:59],a[88:91]\nzm a[92:95],v[72:75],v[60:63],a[92:95]\nzd v[108:111],v17 offset:6144\nzm a[96:99],v[76:79],v[32:35],a[96:99]\n"
"zm a[100:103],v[76:79],v[36:39],a[100:103]\nzt v[144:145],v22 offset:8192\nzm a[104:107],v[76:79],v[40:43],a[104:107]\nzm a[108:111],v[76:79],v[44:47],a[108:111]\nzt v[146:147],v22 offset:9216\nzm a[112:115],v[76:79],v[48:51],a[112:115]\nzm a[116:119],v[76:79],v[52:55],a[116:119]\nzd v[112:115],v17 offset:8192\n"
"zm a[120:123],v[76:79],v[56:59],a[120:123]\nzm a[124:127],v[76:79],v[60:63],a[124:127]\nzt v[148:149],v23 offset:8192\nzm a[128:131],v[80:83],v[32:35],a[128:131]\nzm a[132:135],v[80:83],v[36:39],a[132:135]\nzt v[150:151],v23 offset:9216\nzm a[136:139],v[80:83],v[40:43],a[136:139]\nzm a[140:143],v[80:83],v[44:47],a[140:143]\n"
"zd v[116:119],v17 offset:10240\nzm a[144:147],v[80:83],v[48:51],a[144:147]\nzm a[148:151],v[80:83],v[52:55],a[148:151]\nzt v[152:153],v24 offset:8192\nzm a[152:155],v[80:83],v[56:59],a[152:155]\nzm a[156:159],v[80:83],v[60:63],a[156:159]\nzt v[154:155],v24 offset:9216\nzm a[160:163],v[84:87],v[32:35],a[160:163]\n"
"zm a[164:167],v[84:87],v[36:39],a[164:167]\nzd v[120:123],v17 offset:12288\nzm a[168:171],v[84:87],v[40:43],a[168:171]\nzm a[172:175],v[84:87],v[44:47],a[172:175]\nzt v[156:157],v25 offset:8192\nzm a[176:179],v[84:87],v[48:51],a[176:179]\nzm a[180:183],v[84:87],v[52:55],a[180:183]\nzt v[158:159],v25 offset:9216\n"
"zm a[184:187],v[84:87],v[56:59],a[184:187]\nzm a[188:191],v[84:87],v[60:63],a[188:191]\nzd v[124:127],v17 offset:14336\nzm a[192:195],v[88:91],v[32:35],a[192:195]\nzm a[196:199],v[88:91],v[36:39],a[196:199]\nzm a[200:203],v[88:91],v[40:43],a[200:203]\nzm a[204:207],v[88:91],v[44:47],a[204:207]\nzm a[208:211],v[88:91],v[48:51],a[208:211]\n"
"zo v16,0x8000,v16\nzm a[212:215],v[88:91],v[52:55],a[212:215]\nzo v17,0x8000,v17\nzm a[216:219],v[88:91],v[56:59],a[216:219]\nv_mov_b32 v18,v216\nzm a[220:223],v[88:91],v[60:63],a[220:223]\nv_mov_b32 v19,v217\nzm a[224:227],v[92:95],v[32:35],a[224:227]\n"
"v_mov_b32 v20,v218\nzm a[228:231],v[92:95],v[36:39],a[228:231]\nv_mov_b32 v21,v219\nzm a[232:235],v[92:95],v[40:43],a[232:235]\nv_mov_b32 v22,v220\nzm a[236:239],v[92:95],v[44:47],a[236:239]\nv_mov_b32 v23,v221\nzm a[240:243],v[92:95],v[48:51],a[240:243]\n"
"v_mov_b32 v24,v222\nzm a[244:247],v[92:95],v[52:55],a[244:247]\nv_mov_b32 v25,v223\nzm a[248:251],v[92:95],v[56:59],a[248:251]\nzm a[252:255],v[92:95],v[60:63],a[252:255]\nzv vmcnt(2) lgkmcnt(0)\ns_barrier\nzm a[0:3],v[128:131],v[96:99],a[0:3]\n"
"zt v[64:65],v18 offset:0\ns_mov_b32 m0,s65\nzm a[4:7],v[128:131],v[100:103],a[4:7]\nzl v8,s[24:27],s62 offen offset:0 sc1 lds\nzm a[8:11],v[128:131],v[104:107],a[8:11]\nzt v[66:67],v18 offset:1024\nzm a[12:15],v[128:131],v[108:111],a[12:15]\nzm a[16:19],v[128:131],v[112:115],a[16:19]\n"
"zl v9,s[24:27],s62 offen offset:1024 sc1 lds\nzm a[20:23],v[128:131],v[116:119],a[20:23]\nzd v[32:35],v16 offset:0\nzm a[24:27],v[128:131],v[120:123],a[24:27]\nzt v[68:69],v19 offset:0\nzm a[28:31],v[128:131],v[124:127],a[28:31]\nzm a[32:35],v[132:135],v[96:99],a[32:35]\nzl v10,s[24:27],s62 offen offset:2048 sc1 lds\n"
"zm a[36:39],v[132:135],v[100:103],a[36:39]\nzt v[70:71],v19 offset:1024\nzm a[40:43],v[132:135],v[104:107],a[40:43]\nzd v[36:39],v16 offset:2048\nzm a[44:47],v[132:135],v[108:111],a[44:47]\nzm a[48:51],v[132:135],v[112:115],a[48:51]\nzl v11,s[24:27],s62 offen offset:3072 sc1 lds\nzm a[52:55],v[132:135],v[116:119],a[52:55]\n"
"zt v[72:73],v20 offset:0\nzm a[56:59],v[132:135],v[120:123],a[56:59]\nzm a[60:63],v[132:135],v[124:127],a[60:63]\nzt v[74:75],v20 offset:1024\nzp m0,s65,4096\nzm a[64:67],v[136:139],v[96:99],a[64:67]\nzl v8,s[24:27],s63 offen offset:0 sc1 lds\nzm a[68:71],v[136:139],v[100:103],a[68:71]\n"
"zd v[40:43],v16 offset:4096\nzm a[72:75],v[136:139],v[104:107],a[72:75]\nzm a[76:79],v[136:139],v[108:111],a[76:79]\nzt v[76:77],v21 offset:0\nzm a[80:83],v[136:139],v[112:115],a[80:83]\nzl v9,s[24:27],s63 offen offset:1024 sc1 lds\nzm a[84:87],v[136:139],v[116:119],a[84:87]\nzt v[78:79],v21 offset:1024\n"
"zm a[88:91],v[136:139],v[120:123],a[88:91]\nzm a[92:95],v[136:139],v[124:127],a[92:95]\nzl v10,s[24:27],s63 offen offset:2048 sc1 lds\nzm a[96:99],v[140:143],v[96:99],a[96:99]\nzd v[44:47],v16 offset:6144\nzm a[100:103],v[140:143],v[100:103],a[100:103]\nzm a[104:107],v[140:143],v[104:107],a[104:107]\nzt v[80:81],v22 offset:0\n"
"zm a[108:111],v[140:143],v[108:111],a[108:111]\nzl v11,s[24:27],s63 offen offset:3072 sc1 lds\nzm a[112:115],v[140:143],v[112:115],a[112:115]\nzt v[82:83],v22 offset:1024\nzm a[116:119],v[140:143],v[116:119],a[116:119]\nzm a[120:123],v[140:143],v[120:123],a[120:123]\nzd v[48:51],v16 offset:8192\ns_mov_b32 m0,s66\n"
"zm a[124:127],v[140:143],v[124:127],a[124:127]\nzl v12,s[28:31],s64 offen offset:0 lds\nzm a[128:131],v[144:147],v[96:99],a[128:131]\nzt v[84:85],v23 offset:0\nzm a[132:135],v[144:147],v[100:103],a[132:135]\nzm a[136:139],v[144:147],v[104:107],a[136:139]\nzt v[86:87],v23 offset:1024\nzm a[140:143],v[144:147],v[108:111],a[140:143]\n"
"zl v13,s[28:31],s64 offen offset:1024 lds\nzm a[144:147],v[144:147],v[112:115],a[144:147]\nzd v[52:55],v16 offset:10240\nzm a[148:151],v[144:147],v[116:119],a[148:151]\nzm a[152:155],v[144:147],v[120:123],a[152:155]\nzt v[88:89],v24 offset:0\nzm a[156:159],v[144:147],v[124:127],a[156:159]\nzl v14,s[28:31],s64 offen offset:2048 lds\n"
"zm a[160:163],v[148:151],v[96:99],a[160:163]\nzm a[164:167],v[148:151],v[100:103],a[164:167]\nzt v[90:91],v24 offset:1024\nzm a[168:171],v[148:151],v[104:107],a[168:171]\nzl v15,s[28:31],s64 offen offset:3072 lds\nzm a[172:175],v[148:151],v[108:111],a[172:175]\nzd v[56:59],v16 offset:12288\nzm a[176:179],v[148:151],v[112:115],a[176:179]\n"
"zm a[180:183],v[148:151],v[116:119],a[180:183]\nzt v[92:93],v25 offset:0\nzp m0,s66,16384\nzm a[184:187],v[148:151],v[120:123],a[184:187]\nzl v12,s[36:39],s64 offen offset:0 lds\nzm a[188:191],v[148:151],v[124:127],a[188:191]\nzt v[94:95],v25 offset:1024\nzm a[192:195],v[152:155],v[96:99],a[192:195]\n"
"zm a[196:199],v[152:155],v[100:103],a[196:199]\nzd v[60:63],v16 offset:14336\nzm a[200:203],v[152:155],v[104:107],a[200:203]\nzl v13,s[36:39],s64 offen offset:1024 lds\nzm a[204:207],v[152:155],v[108:111],a[204:207]\nzm a[208:211],v[152:155],v[112:115],a[208:211]\nzm a[212:215],v[152:155],v[116:119],a[212:215]\nzm a[216:219],v[152:155],v[120:123],a[216:219]\n"
"zl v14,s[36:39],s64 offen offset:2048 lds\nzm a[220:223],v[152:155],v[124:127],a[220:223]\nzm a[224:227],v[156:159],v[96:99],a[224:227]\nzm a[228:231],v[156:159],v[100:103],a[228:231]\nzm a[232:235],v[156:159],v[104:107],a[232:235]\nzl v15,s[36:39],s64 offen offset:3072 lds\nzm a[236:239],v[156:159],v[108:111],a[236:239]\nzm a[240:243],v[156:159],v[112:115],a[240:243]\n"
"zm a[244:247],v[156:159],v[116:119],a[244:247]\nzm a[248:251],v[156:159],v[120:123],a[248:251]\nzp s62,s62,128\nzp s63,s63,128\nzp s64,s64,s74\nzm a[252:255],v[156:159],v[124:127],a[252:255]\ns_xor_b32 s65,s65,0x8000\ns_xor_b32 s66,s66,0x8000\n"
"zv lgkmcnt(0)\ns_nop 7\nglobal_load_dword v31,v[228:229],off\nzh v29,4,v6\ns_lshl_b32 s76,s70,4\ns_lshl_b32 s77,s60,3\nzp s76,s76,s77\ns_lshl_b32 s76,s76,3\n"
"s_lshl_b32 s77,s61,2\nzp s76,s76,s77\ns_lshl_b32 s80,s76,10\ns_mov_b32 s81,8192\ns_mov_b32 s82,8192\nzr v96,a0\nzr v97,a1\nzr v98,a2\n"
"zr v99,a3\nzr v100,a32\nzr v101,a33\nzr v102,a34\nzr v103,a35\nzc v104,v96,v97\nzc v105,v98,v99\nzc v106,v100,v101\n"
"zc v107,v102,v103\nzs v[104:107],v29,s[40:43],s80 offen offset:0 sc1\nzr v108,a64\nzr v109,a65\nzr v110,a66\nzr v111,a67\nzr v112,a96\nzr v113,a97\n"
"zr v114,a98\nzr v115,a99\nzc v116,v108,v109\nzc v117,v110,v111\nzc v118,v112,v113\nzc v119,v114,v115\nzs v[116:119],v29,s[40:43],s80 offen offset:1024 sc1\nzr v120,a128\n"
"zr v121,a129\nzr v122,a130\nzr v123,a131\nzr v124,a160\nzr v125,a161\nzr v126,a162\nzr v127,a163\nzc v128,v120,v121\n"
"zc v129,v122,v123\nzc v130,v124,v125\nzc v131,v126,v127\nzs v[128:131],v29,s[40:43],s80 offen offset:2048 sc1\nzr v132,a192\nzr v133,a193\nzr v134,a194\nzr v135,a195\n"
"zr v136,a224\nzr v137,a225\nzr v138,a226\nzr v139,a227\nzc v140,v132,v133\nzc v141,v134,v135\nzc v142,v136,v137\nzc v143,v138,v139\n"
"zs v[140:143],v29,s[40:43],s80 offen offset:3072 sc1\nzp s80,s80,s81\nzr v96,a4\nzr v97,a5\nzr v98,a6\nzr v99,a7\nzr v100,a36\nzr v101,a37\n"
"zr v102,a38\nzr v103,a39\nzc v104,v96,v97\nzc v105,v98,v99\nzc v106,v100,v101\nzc v107,v102,v103\nzs v[104:107],v29,s[40:43],s80 offen offset:0 sc1\nzr v108,a68\n"
"zr v109,a69\nzr v110,a70\nzr v111,a71\nzr v112,a100\nzr v113,a101\nzr v114,a102\nzr v115,a103\nzc v116,v108,v109\n"
"zc v117,v110,v111\nzc v118,v112,v113\nzc v119,v114,v115\nzs v[116:119],v29,s[40:43],s80 offen offset:1024 sc1\nzr v120,a132\nzr v121,a133\nzr v122,a134\nzr v123,a135\n"
"zr v124,a164\nzr v125,a165\nzr v126,a166\nzr v127,a167\nzc v128,v120,v121\nzc v129,v122,v123\nzc v130,v124,v125\nzc v131,v126,v127\n"
"zs v[128:131],v29,s[40:43],s80 offen offset:2048 sc1\nzr v132,a196\nzr v133,a197\nzr v134,a198\nzr v135,a199\nzr v136,a228\nzr v137,a229\nzr v138,a230\n"
"zr v139,a231\nzc v140,v132,v133\nzc v141,v134,v135\nzc v142,v136,v137\nzc v143,v138,v139\nzs v[140:143],v29,s[40:43],s80 offen offset:3072 sc1\nzp s80,s80,s82\nzr v96,a8\n"
"zr v97,a9\nzr v98,a10\nzr v99,a11\nzr v100,a40\nzr v101,a41\nzr v102,a42\nzr v103,a43\nzc v160,v96,v97\n"
"zc v161,v98,v99\nzc v162,v100,v101\nzc v163,v102,v103\nzr v108,a72\nzr v109,a73\nzr v110,a74\nzr v111,a75\nzr v112,a104\n"
"zr v113,a105\nzr v114,a106\nzr v115,a107\nzc v164,v108,v109\nzc v165,v110,v111\nzc v166,v112,v113\nzc v167,v114,v115\nzr v120,a136\n"
"zr v121,a137\nzr v122,a138\nzr v123,a139\nzr v124,a168\nzr v125,a169\nzr v126,a170\nzr v127,a171\nzc v168,v120,v121\n"
"zc v169,v122,v123\nzc v170,v124,v125\nzc v171,v126,v127\nzr v132,a200\nzr v133,a201\nzr v134,a202\nzr v135,a203\nzr v136,a232\n"
"zr v137,a233\nzr v138,a234\nzr v139,a235\nzc v172,v132,v133\nzc v173,v134,v135\nzc v174,v136,v137\nzc v175,v138,v139\nzr v96,a12\n"
"zr v97,a13\nzr v98,a14\nzr v99,a15\nzr v100,a44\nzr v101,a45\nzr v102,a46\nzr v103,a47\nzc v176,v96,v97\n"
"zc v177,v98,v99\nzc v178,v100,v101\nzc v179,v102,v103\nzr v108,a76\nzr v109,a77\nzr v110,a78\nzr v111,a79\nzr v112,a108\n"
"zr v113,a109\nzr v114,a110\nzr v115,a111\nzc v180,v108,v109\nzc v181,v110,v111\nzc v182,v112,v113\nzc v183,v114,v115\nzr v120,a140\n"
"zr v121,a141\nzr v122,a142\nzr v123,a143\nzr v124,a172\nzr v125,a173\nzr v126,a174\nzr v127,a175\nzc v184,v120,v121\n"
"zc v185,v122,v123\nzc v186,v124,v125\nzc v187,v126,v127\nzr v132,a204\nzr v133,a205\nzr v134,a206\nzr v135,a207\nzr v136,a236\n"
"zr v137,a237\nzr v138,a238\nzr v139,a239\nzc v188,v132,v133\nzc v189,v134,v135\nzc v190,v136,v137\nzc v191,v138,v139\nzr v96,a16\n"
"zr v97,a17\nzr v98,a18\nzr v99,a19\nzr v100,a48\nzr v101,a49\nzr v102,a50\nzr v103,a51\nzc v192,v96,v97\n"
"zc v193,v98,v99\nzc v194,v100,v101\nzc v195,v102,v103\nzr v108,a80\nzr v109,a81\nzr v110,a82\nzr v111,a83\nzr v112,a112\n"
"zr v113,a113\nzr v114,a114\nzr v115,a115\nzc v196,v108,v109\nzc v197,v110,v111\nzc v198,v112,v113\nzc v199,v114,v115\nzr v120,a144\n"
"zr v121,a145\nzr v122,a146\nzr v123,a147\nzr v124,a176\nzr v125,a177\nzr v126,a178\nzr v127,a179\nzc v200,v120,v121\n"
"zc v201,v122,v123\nzc v202,v124,v125\nzc v203,v126,v127\nzr v132,a208\nzr v133,a209\nzr v134,a210\nzr v135,a211\nzr v136,a240\n"
"zr v137,a241\nzr v138,a242\nzr v139,a243\nzc v204,v132,v133\nzc v205,v134,v135\nzc v206,v136,v137\nzc v207,v138,v139\nzr v96,a20\n"
"zr v97,a21\nzr v98,a22\nzr v99,a23\nzr v100,a52\nzr v101,a53\nzr v102,a54\nzr v103,a55\nzc v208,v96,v97\n"
"zc v209,v98,v99\nzc v210,v100,v101\nzc v211,v102,v103\nzr v108,a84\nzr v109,a85\nzr v110,a86\nzr v111,a87\nzr v112,a116\n"
"zr v113,a117\nzr v114,a118\nzr v115,a119\nzc v212,v108,v109\nzc v213,v110,v111\nzc v214,v112,v113\nzc v215,v114,v115\nzr v120,a148\n"
"zr v121,a149\nzr v122,a150\nzr v123,a151\nzr v124,a180\nzr v125,a181\nzr v126,a182\nzr v127,a183\nzc v216,v120,v121\n"
"zc v217,v122,v123\nzc v218,v124,v125\nzc v219,v126,v127\nzr v132,a212\nzr v133,a213\nzr v134,a214\nzr v135,a215\nzr v136,a244\n"
"zr v137,a245\nzr v138,a246\nzr v139,a247\nzc v220,v132,v133\nzc v221,v134,v135\nzc v222,v136,v137\nzc v223,v138,v139\nzr v96,a24\n"
"zr v97,a25\nzr v98,a26\nzr v99,a27\nzr v100,a56\nzr v101,a57\nzr v102,a58\nzr v103,a59\nzc v224,v96,v97\n"
"zc v225,v98,v99\nzc v226,v100,v101\nzc v227,v102,v103\nzr v108,a88\nzr v109,a89\nzr v110,a90\nzr v111,a91\nzr v112,a120\n"
"zr v113,a121\nzr v114,a122\nzr v115,a123\nzc v228,v108,v109\nzc v229,v110,v111\nzc v230,v112,v113\nzc v231,v114,v115\nzr v120,a152\n"
"zr v121,a153\nzr v122,a154\nzr v123,a155\nzr v124,a184\nzr v125,a185\nzr v126,a186\nzr v127,a187\nzc v232,v120,v121\n"
"zc v233,v122,v123\nzc v234,v124,v125\nzc v235,v126,v127\nzr v132,a216\nzr v133,a217\nzr v134,a218\nzr v135,a219\nzr v136,a248\n"
"zr v137,a249\nzr v138,a250\nzr v139,a251\nzc v236,v132,v133\nzc v237,v134,v135\nzc v238,v136,v137\nzc v239,v138,v139\nzr v96,a28\n"
"zr v97,a29\nzr v98,a30\nzr v99,a31\nzr v100,a60\nzr v101,a61\nzr v102,a62\nzr v103,a63\nzc v240,v96,v97\n"
"zc v241,v98,v99\nzc v242,v100,v101\nzc v243,v102,v103\nzr v108,a92\nzr v109,a93\nzr v110,a94\nzr v111,a95\nzr v112,a124\n"
"zr v113,a125\nzr v114,a126\nzr v115,a127\nzc v244,v108,v109\nzc v245,v110,v111\nzc v246,v112,v113\nzc v247,v114,v115\nzr v120,a156\n"
"zr v121,a157\nzr v122,a158\nzr v123,a159\nzr v124,a188\nzr v125,a189\nzr v126,a190\nzr v127,a191\nzc v248,v120,v121\n"
"zc v249,v122,v123\nzc v250,v124,v125\nzc v251,v126,v127\nzr v132,a220\nzr v133,a221\nzr v134,a222\nzr v135,a223\nzr v136,a252\n"
"zr v137,a253\nzr v138,a254\nzr v139,a255\nzc v252,v132,v133\nzc v253,v134,v135\nzc v254,v136,v137\nzc v255,v138,v139\ns_lshl_b32 s79,s69,7\n"
"zp s79,s79,4096\ns_lshl_b32 s78,s70,2\nzp s79,s79,s78\ns_cmp_eq_u32 s71,2\ns_cbranch_scc1 L_P_noT2\nzm a[0:3],v[64:67],v[32:35],0\nzs v[160:163],v29,s[40:43],s80 offen offset:0 sc1\nzm a[4:7],v[64:67],v[36:39],0\n"
"zt v[128:129],v18 offset:8192\nzm a[8:11],v[64:67],v[40:43],0\nzm a[12:15],v[64:67],v[44:47],0\nzt v[130:131],v18 offset:9216\nzm a[16:19],v[64:67],v[48:51],0\nzm a[20:23],v[64:67],v[52:55],0\nzd v[96:99],v17 offset:0\nzm a[24:27],v[64:67],v[56:59],0\n"
"zm a[28:31],v[64:67],v[60:63],0\nzt v[132:133],v19 offset:8192\nzm a[32:35],v[68:71],v[32:35],0\nzm a[36:39],v[68:71],v[36:39],0\nzt v[134:135],v19 offset:9216\nzm a[40:43],v[68:71],v[40:43],0\nzm a[44:47],v[68:71],v[44:47],0\nzd v[100:103],v17 offset:2048\n"
"zm a[48:51],v[68:71],v[48:51],0\nzs v[164:167],v29,s[40:43],s80 offen offset:1024 sc1\nzm a[52:55],v[68:71],v[52:55],0\nzt v[136:137],v20 offset:8192\nzm a[56:59],v[68:71],v[56:59],0\nzm a[60:63],v[68:71],v[60:63],0\nzt v[138:139],v20 offset:9216\nzm a[64:67],v[72:75],v[32:35],0\n"
"zm a[68:71],v[72:75],v[36:39],0\nzd v[104:107],v17 offset:4096\nzm a[72:75],v[72:75],v[40:43],0\nzm a[76:79],v[72:75],v[44:47],0\nzt v[140:141],v21 offset:8192\nzm a[80:83],v[72:75],v[48:51],0\nzm a[84:87],v[72:75],v[52:55],0\nzt v[142:143],v21 offset:9216\n"
"zm a[88:91],v[72:75],v[56:59],0\nzm a[92:95],v[72:75],v[60:63],0\nzd v[108:111],v17 offset:6144\nzm a[96:99],v[76:79],v[32:35],0\nzs v[168:171],v29,s[40:43],s80 offen offset:2048 sc1\nzm a[100:103],v[76:79],v[36:39],0\nzt v[144:145],v22 offset:8192\nzm a[104:107],v[76:79],v[40:43],0\n"
"zm a[108:111],v[76:79],v[44:47],0\nzt v[146:147],v22 offset:9216\nzm a[112:115],v[76:79],v[48:51],0\nzm a[116:119],v[76:79],v[52:55],0\nzd v[112:115],v17 offset:8192\nzm a[120:123],v[76:79],v[56:59],0\nzm a[124:127],v[76:79],v[60:63],0\nzt v[148:149],v23 offset:8192\n"
"zm a[128:131],v[80:83],v[32:35],0\nzm a[132:135],v[80:83],v[36:39],0\nzt v[150:151],v23 offset:9216\nzm a[136:139],v[80:83],v[40:43],0\nzm a[140:143],v[80:83],v[44:47],0\nzd v[116:119],v17 offset:10240\nzm a[144:147],v[80:83],v[48:51],0\nzs v[172:175],v29,s[40:43],s80 offen offset:3072 sc1\n"
"zm a[148:151],v[80:83],v[52:55],0\nzt v[152:153],v24 offset:8192\nzm a[152:155],v[80:83],v[56:59],0\nzm a[156:159],v[80:83],v[60:63],0\nzt v[154:155],v24 offset:9216\nzm a[160:163],v[84:87],v[32:35],0\nzm a[164:167],v[84:87],v[36:39],0\nzd v[120:123],v17 offset:12288\n"
"zm a[168:171],v[84:87],v[40:43],0\nzm a[172:175],v[84:87],v[44:47],0\nzt v[156:157],v25 offset:8192\nzm a[176:179],v[84:87],v[48:51],0\nzm a[180:183],v[84:87],v[52:55],0\nzt v[158:159],v25 offset:9216\nzm a[184:187],v[84:87],v[56:59],0\nzm a[188:191],v[84:87],v[60:63],0\n"
"zd v[124:127],v17 offset:14336\nzm a[192:195],v[88:91],v[32:35],0\nzp s80,s80,s81\nzm a[196:199],v[88:91],v[36:39],0\nzm a[200:203],v[88:91],v[40:43],0\nzm a[204:207],v[88:91],v[44:47],0\nzm a[208:211],v[88:91],v[48:51],0\nzo v16,0x8000,v16\n"
"zm a[212:215],v[88:91],v[52:55],0\nzo v17,0x8000,v17\nzm a[216:219],v[88:91],v[56:59],0\nzo v18,0x8000,v18\nzm a[220:223],v[88:91],v[60:63],0\nzo v19,0x8000,v19\nzm a[224:227],v[92:95],v[32:35],0\nzo v20,0x8000,v20\n"
"zm a[228:231],v[92:95],v[36:39],0\nzo v21,0x8000,v21\nzm a[232:235],v[92:95],v[40:43],0\nzo v22,0x8000,v22\nzm a[236:239],v[92:95],v[44:47],0\nzo v23,0x8000,v23\nzm a[240:243],v[92:95],v[48:51],0\nzo v24,0x8000,v24\n"
"zm a[244:247],v[92:95],v[52:55],0\nzo v25,0x8000,v25\nzm a[248:251],v[92:95],v[56:59],0\nzm a[252:255],v[92:95],v[60:63],0\nzv vmcnt(13) lgkmcnt(0)\ns_barrier\nzm a[0:3],v[128:131],v[96:99],a[0:3]\nzt v[64:65],v18 offset:0\n"
"s_mov_b32 m0,s65\nzm a[4:7],v[128:131],v[100:103],a[4:7]\nzl v8,s[24:27],s62 offen offset:0 sc1 lds\nzm a[8:11],v[128:131],v[104:107],a[8:11]\nzt v[66:67],v18 offset:1024\nzm a[12:15],v[128:131],v[108:111],a[12:15]\nzm a[16:19],v[128:131],v[112:115],a[16:19]\nzl v9,s[24:27],s62 offen offset:1024 sc1 lds\n"
"zm a[20:23],v[128:131],v[116:119],a[20:23]\nzd v[32:35],v16 offset:0\nzm a[24:27],v[128:131],v[120:123],a[24:27]\nzt v[68:69],v19 offset:0\nzm a[28:31],v[128:131],v[124:127],a[28:31]\nzm a[32:35],v[132:135],v[96:99],a[32:35]\nzl v10,s[24:27],s62 offen offset:2048 sc1 lds\nzm a[36:39],v[132:135],v[100:103],a[36:39]\n"
"zt v[70:71],v19 offset:1024\nzm a[40:43],v[132:135],v[104:107],a[40:43]\nzd v[36:39],v16 offset:2048\nzm a[44:47],v[132:135],v[108:111],a[44:47]\nzm a[48:51],v[132:135],v[112:115],a[48:51]\nzl v11,s[24:27],s62 offen offset:3072 sc1 lds\nzm a[52:55],v[132:135],v[116:119],a[52:55]\nzt v[72:73],v20 offset:0\n"
"zm a[56:59],v[132:135],v[120:123],a[56:59]\nzm a[60:63],v[132:135],v[124:127],a[60:63]\nzt v[74:75],v20 offset:1024\nzp m0,s65,4096\nzm a[64:67],v[136:139],v[96:99],a[64:67]\nzl v8,s[24:27],s63 offen offset:0 sc1 lds\nzm a[68:71],v[136:139],v[100:103],a[68:71]\nzd v[40:43],v16 offset:4096\n"
"zm a[72:75],v[136:139],v[104:107],a[72:75]\nzm a[76:79],v[136:139],v[108:111],a[76:79]\nzt v[76:77],v21 offset:0\nzm a[80:83],v[136:139],v[112:115],a[80:83]\nzl v9,s[24:27],s63 offen offset:1024 sc1 lds\nzm a[84:87],v[136:139],v[116:119],a[84:87]\nzt v[78:79],v21 offset:1024\nzm a[88:91],v[136:139],v[120:123],a[88:91]\n"
"zm a[92:95],v[136:139],v[124:127],a[92:95]\nzl v10,s[24:27],s63 offen offset:2048 sc1 lds\nzm a[96:99],v[140:143],v[96:99],a[96:99]\nzd v[44:47],v16 offset:6144\nzm a[100:103],v[140:143],v[100:103],a[100:103]\nzm a[104:107],v[140:143],v[104:107],a[104:107]\nzt v[80:81],v22 offset:0\nzm a[108:111],v[140:143],v[108:111],a[108:111]\n"
"zl v11,s[24:27],s63 offen offset:3072 sc1 lds\nzm a[112:115],v[140:143],v[112:115],a[112:115]\nzt v[82:83],v22 offset:1024\nzm a[116:119],v[140:143],v[116:119],a[116:119]\nzm a[120:123],v[140:143],v[120:123],a[120:123]\nzd v[48:51],v16 offset:8192\ns_mov_b32 m0,s66\nzm a[124:127],v[140:143],v[124:127],a[124:127]\n"
"zl v12,s[28:31],s64 offen offset:0 lds\nzm a[128:131],v[144:147],v[96:99],a[128:131]\nzt v[84:85],v23 offset:0\nzm a[132:135],v[144:147],v[100:103],a[132:135]\nzm a[136:139],v[144:147],v[104:107],a[136:139]\nzt v[86:87],v23 offset:1024\nzm a[140:143],v[144:147],v[108:111],a[140:143]\nzl v13,s[28:31],s64 offen offset:1024 lds\n"
"zm a[144:147],v[144:147],v[112:115],a[144:147]\nzd v[52:55],v16 offset:10240\nzm a[148:151],v[144:147],v[116:119],a[148:151]\nzm a[152:155],v[144:147],v[120:123],a[152:155]\nzt v[88:89],v24 offset:0\nzm a[156:159],v[144:147],v[124:127],a[156:159]\nzl v14,s[28:31],s64 offen offset:2048 lds\nzm a[160:163],v[148:151],v[96:99],a[160:163]\n"
"zm a[164:167],v[148:151],v[100:103],a[164:167]\nzt v[90:91],v24 offset:1024\nzm a[168:171],v[148:151],v[104:107],a[168:171]\nzl v15,s[28:31],s64 offen offset:3072 lds\nzm a[172:175],v[148:151],v[108:111],a[172:175]\nzd v[56:59],v16 offset:12288\nzm a[176:179],v[148:151],v[112:115],a[176:179]\nzm a[180:183],v[148:151],v[116:119],a[180:183]\n"
"zt v[92:93],v25 offset:0\nzp m0,s66,16384\nzm a[184:187],v[148:151],v[120:123],a[184:187]\nzl v12,s[36:39],s64 offen offset:0 lds\nzm a[188:191],v[148:151],v[124:127],a[188:191]\nzt v[94:95],v25 offset:1024\nzm a[192:195],v[152:155],v[96:99],a[192:195]\nzm a[196:199],v[152:155],v[100:103],a[196:199]\n"
"zd v[60:63],v16 offset:14336\nzm a[200:203],v[152:155],v[104:107],a[200:203]\nzl v13,s[36:39],s64 offen offset:1024 lds\nzm a[204:207],v[152:155],v[108:111],a[204:207]\nzm a[208:211],v[152:155],v[112:115],a[208:211]\nzm a[212:215],v[152:155],v[116:119],a[212:215]\nzm a[216:219],v[152:155],v[120:123],a[216:219]\nzl v14,s[36:39],s64 offen offset:2048 lds\n"
"zm a[220:223],v[152:155],v[124:127],a[220:223]\nzm a[224:227],v[156:159],v[96:99],a[224:227]\nzm a[228:231],v[156:159],v[100:103],a[228:231]\nzm a[232:235],v[156:159],v[104:107],a[232:235]\nzl v15,s[36:39],s64 offen offset:3072 lds\nzm a[236:239],v[156:159],v[108:111],a[236:239]\nzm a[240:243],v[156:159],v[112:115],a[240:243]\nzm a[244:247],v[156:159],v[116:119],a[244:247]\n"
"zm a[248:251],v[156:159],v[120:123],a[248:251]\nzp s62,s62,128\nzp s63,s63,128\nzp s64,s64,s74\nzm a[252:255],v[156:159],v[124:127],a[252:255]\ns_xor_b32 s65,s65,0x8000\ns_xor_b32 s66,s66,0x8000\nzv lgkmcnt(0)\n"
"zm a[0:3],v[64:67],v[32:35],a[0:3]\nzs v[176:179],v29,s[40:43],s80 offen offset:0 sc1\nzm a[4:7],v[64:67],v[36:39],a[4:7]\nzt v[128:129],v18 offset:8192\nzm a[8:11],v[64:67],v[40:43],a[8:11]\nzm a[12:15],v[64:67],v[44:47],a[12:15]\nzt v[130:131],v18 offset:9216\nzm a[16:19],v[64:67],v[48:51],a[16:19]\n"
"zm a[20:23],v[64:67],v[52:55],a[20:23]\nzd v[96:99],v17 offset:0\nzm a[24:27],v[64:67],v[56:59],a[24:27]\nzm a[28:31],v[64:67],v[60:63],a[28:31]\nzt v[132:133],v19 offset:8192\nzm a[32:35],v[68:71],v[32:35],a[32:35]\nzm a[36:39],v[68:71],v[36:39],a[36:39]\nzt v[134:135],v19 offset:9216\n"
"zm a[40:43],v[68:71],v[40:43],a[40:43]\nzm a[44:47],v[68:71],v[44:47],a[44:47]\nzd v[100:103],v17 offset:2048\nzm a[48:51],v[68:71],v[48:51],a[48:51]\nzs v[180:183],v29,s[40:43],s80 offen offset:1024 sc1\nzm a[52:55],v[68:71],v[52:55],a[52:55]\nzt v[136:137],v20 offset:8192\nzm a[56:59],v[68:71],v[56:59],a[56:59]\n"
"zm a[60:63],v[68:71],v[60:63],a[60:63]\nzt v[138:139],v20 offset:9216\nzm a[64:67],v[72:75],v[32:35],a[64:67]\nzm a[68:71],v[72:75],v[36:39],a[68:71]\nzd v[104:107],v17 offset:4096\nzm a[72:75],v[72:75],v[40:43],a[72:75]\nzm a[76:79],v[72:75],v[44:47],a[76:79]\nzt v[140:141],v21 offset:8192\n"
"zm a[80:83],v[72:75],v[48:51],a[80:83]\nzm a[84:87],v[72:75],v[52:55],a[84:87]\nzt v[142:143],v21 offset:9216\nzm a[88:91],v[72:75],v[56:59],a[88:91]\nzm a[92:95],v[72:75],v[60:63],a[92:95]\nzd v[108:111],v17 offset:6144\nzm a[96:99],v[76:79],v[32:35],a[96:99]\nzs v[184:187],v29,s[40:43],s80 offen offset:2048 sc1\n"
"zm a[100:103],v[76:79],v[36:39],a[100:103]\nzt v[144:145],v22 offset:8192\nzm a[104:107],v[76:79],v[40:43],a[104:107]\nzm a[108:111],v[76:79],v[44:47],a[108:111]\nzt v[146:147],v22 offset:9216\nzm a[112:115],v[76:79],v[48:51],a[112:115]\nzm a[116:119],v[76:79],v[52:55],a[116:119]\nzd v[112:115],v17 offset:8192\n"
"zm a[120:123],v[76:79],v[56:59],a[120:123]\nzm a[124:127],v[76:79],v[60:63],a[124:127]\nzt v[148:149],v23 offset:8192\nzm a[128:131],v[80:83],v[32:35],a[128:131]\nzm a[132:135],v[80:83],v[36:39],a[132:135]\nzt v[150:151],v23 offset:9216\nzm a[136:139],v[80:83],v[40:43],a[136:139]\nzm a[140:143],v[80:83],v[44:47],a[140:143]\n"
"zd v[116:119],v17 offset:10240\nzm a[144:147],v[80:83],v[48:51],a[144:147]\nzs v[188:191],v29,s[40:43],s80 offen offset:3072 sc1\nzm a[148:151],v[80:83],v[52:55],a[148:151]\nzt v[152:153],v24 offset:8192\nzm a[152:155],v[80:83],v[56:59],a[152:155]\nzm a[156:159],v[80:83],v[60:63],a[156:159]\nzt v[154:155],v24 offset:9216\n"
"zm a[160:163],v[84:87],v[32:35],a[160:163]\nzm a[164:167],v[84:87],v[36:39],a[164:167]\nzd v[120:123],v17 offset:12288\nzm a[168:171],v[84:87],v[40:43],a[168:171]\nzm a[172:175],v[84:87],v[44:47],a[172:175]\nzt v[156:157],v25 offset:8192\nzm a[176:179],v[84:87],v[48:51],a[176:179]\nzm a[180:183],v[84:87],v[52:55],a[180:183]\n"
"zt v[158:159],v25 offset:9216\nzm a[184:187],v[84:87],v[56:59],a[184:187]\nzm a[188:191],v[84:87],v[60:63],a[188:191]\nzd v[124:127],v17 offset:14336\nzm a[192:195],v[88:91],v[32:35],a[192:195]\nzp s80,s80,s82\nzm a[196:199],v[88:91],v[36:39],a[196:199]\nzm a[200:203],v[88:91],v[40:43],a[200:203]\n"
"zm a[204:207],v[88:91],v[44:47],a[204:207]\nzm a[208:211],v[88:91],v[48:51],a[208:211]\nzo v16,0x8000,v16\nzm a[212:215],v[88:91],v[52:55],a[212:215]\nzo v17,0x8000,v17\nzm a[216:219],v[88:91],v[56:59],a[216:219]\nzo v18,0x8000,v18\nzm a[220:223],v[88:91],v[60:63],a[220:223]\n"
"zo v19,0x8000,v19\nzm a[224:227],v[92:95],v[32:35],a[224:227]\nzo v20,0x8000,v20\nzm a[228:231],v[92:95],v[36:39],a[228:231]\nzo v21,0x8000,v21\nzm a[232:235],v[92:95],v[40:43],a[232:235]\nzo v22,0x8000,v22\nzm a[236:239],v[92:95],v[44:47],a[236:239]\n"
"zo v23,0x8000,v23\nzm a[240:243],v[92:95],v[48:51],a[240:243]\nzo v24,0x8000,v24\nzm a[244:247],v[92:95],v[52:55],a[244:247]\nzo v25,0x8000,v25\nzm a[248:251],v[92:95],v[56:59],a[248:251]\nzm a[252:255],v[92:95],v[60:63],a[252:255]\nzv vmcnt(4) lgkmcnt(0)\n"
"s_barrier\nzm a[0:3],v[128:131],v[96:99],a[0:3]\nzt v[64:65],v18 offset:0\ns_mov_b32 m0,s65\nzm a[4:7],v[128:131],v[100:103],a[4:7]\nzl v8,s[24:27],s62 offen offset:0 sc1 lds\nzm a[8:11],v[128:131],v[104:107],a[8:11]\nzt v[66:67],v18 offset:1024\n"
"zm a[12:15],v[128:131],v[108:111],a[12:15]\nzm a[16:19],v[128:131],v[112:115],a[16:19]\nzl v9,s[24:27],s62 offen offset:1024 sc1 lds\nzm a[20:23],v[128:131],v[116:119],a[20:23]\nzd v[32:35],v16 offset:0\nzm a[24:27],v[128:131],v[120:123],a[24:27]\nzt v[68:69],v19 offset:0\nzm a[28:31],v[128:131],v[124:127],a[28:31]\n"
"zm a[32:35],v[132:135],v[96:99],a[32:35]\nzl v10,s[24:27],s62 offen offset:2048 sc1 lds\nzm a[36:39],v[132:135],v[100:103],a[36:39]\nzt v[70:71],v19 offset:1024\nzm a[40:43],v[132:135],v[104:107],a[40:43]\nzd v[36:39],v16 offset:2048\nzm a[44:47],v[132:135],v[108:111],a[44:47]\nzm a[48:51],v[132:135],v[112:115],a[48:51]\n"
"zl v11,s[24:27],s62 offen offset:3072 sc1 lds\nzm a[52:55],v[132:135],v[116:119],a[52:55]\nzt v[72:73],v20 offset:0\nzm a[56:59],v[132:135],v[120:123],a[56:59]\nzm a[60:63],v[132:135],v[124:127],a[60:63]\nzt v[74:75],v20 offset:1024\nzp m0,s65,4096\nzm a[64:67],v[136:139],v[96:99],a[64:67]\n"
"zl v8,s[24:27],s63 offen offset:0 sc1 lds\nzm a[68:71],v[136:139],v[100:103],a[68:71]\nzd v[40:43],v16 offset:4096\nzm a[72:75],v[136:139],v[104:107],a[72:75]\nzm a[76:79],v[136:139],v[108:111],a[76:79]\nzt v[76:77],v21 offset:0\nzm a[80:83],v[136:139],v[112:115],a[80:83]\nzl v9,s[24:27],s63 offen offset:1024 sc1 lds\n"
"zm a[84:87],v[136:139],v[116:119],a[84:87]\nzt v[78:79],v21 offset:1024\nzm a[88:91],v[136:139],v[120:123],a[88:91]\nzm a[92:95],v[136:139],v[124:127],a[92:95]\nzl v10,s[24:27],s63 offen offset:2048 sc1 lds\nzm a[96:99],v[140:143],v[96:99],a[96:99]\nzd v[44:47],v16 offset:6144\nzm a[100:103],v[140:143],v[100:103],a[100:103]\n"
"zm a[104:107],v[140:143],v[104:107],a[104:107]\nzt v[80:81],v22 offset:0\nzm a[108:111],v[140:143],v[108:111],a[108:111]\nzl v11,s[24:27],s63 offen offset:3072 sc1 lds\nzm a[112:115],v[140:143],v[112:115],a[112:115]\nzt v[82:83],v22 offset:1024\nzm a[116:119],v[140:143],v[116:119],a[116:119]\nzm a[120:123],v[140:143],v[120:123],a[120:123]\n"
"zd v[48:51],v16 offset:8192\ns_mov_b32 m0,s66\nzm a[124:127],v[140:143],v[124:127],a[124:127]\nzl v12,s[28:31],s64 offen offset:0 lds\nzm a[128:131],v[144:147],v[96:99],a[128:131]\nzt v[84:85],v23 offset:0\nzm a[132:135],v[144:147],v[100:103],a[132:135]\nzm a[136:139],v[144:147],v[104:107],a[136:139]\n"
"zt v[86:87],v23 offset:1024\nzm a[140:143],v[144:147],v[108:111],a[140:143]\nzl v13,s[28:31],s64 offen offset:1024 lds\nzm a[144:147],v[144:147],v[112:115],a[144:147]\nzd v[52:55],v16 offset:10240\nzm a[148:151],v[144:147],v[116:119],a[148:151]\nzm a[152:155],v[144:147],v[120:123],a[152:155]\nzt v[88:89],v24 offset:0\n"
"zm a[156:159],v[144:147],v[124:127],a[156:159]\nzl v14,s[28:31],s64 offen offset:2048 lds\nzm a[160:163],v[148:151],v[96:99],a[160:163]\nzm a[164:167],v[148:151],v[100:103],a[164:167]\nzt v[90:91],v24 offset:1024\nzm a[168:171],v[148:151],v[104:107],a[168:171]\nzl v15,s[28:31],s64 offen offset:3072 lds\nzm a[172:175],v[148:151],v[108:111],a[172:175]\n"
"zd v[56:59],v16 offset:12288\nzm a[176:179],v[148:151],v[112:115],a[176:179]\nzm a[180:183],v[148:151],v[116:119],a[180:183]\nzt v[92:93],v25 offset:0\nzp m0,s66,16384\nzm a[184:187],v[148:151],v[120:123],a[184:187]\nzl v12,s[36:39],s64 offen offset:0 lds\nzm a[188:191],v[148:151],v[124:127],a[188:191]\n"
"zt v[94:95],v25 offset:1024\nzm a[192:195],v[152:155],v[96:99],a[192:195]\nzm a[196:199],v[152:155],v[100:103],a[196:199]\nzd v[60:63],v16 offset:14336\nzm a[200:203],v[152:155],v[104:107],a[200:203]\nzl v13,s[36:39],s64 offen offset:1024 lds\nzm a[204:207],v[152:155],v[108:111],a[204:207]\nzm a[208:211],v[152:155],v[112:115],a[208:211]\n"
"zm a[212:215],v[152:155],v[116:119],a[212:215]\nzm a[216:219],v[152:155],v[120:123],a[216:219]\nzl v14,s[36:39],s64 offen offset:2048 lds\nzm a[220:223],v[152:155],v[124:127],a[220:223]\nzm a[224:227],v[156:159],v[96:99],a[224:227]\nzm a[228:231],v[156:159],v[100:103],a[228:231]\nzm a[232:235],v[156:159],v[104:107],a[232:235]\nzl v15,s[36:39],s64 offen offset:3072 lds\n"
"zm a[236:239],v[156:159],v[108:111],a[236:239]\nzm a[240:243],v[156:159],v[112:115],a[240:243]\nzm a[244:247],v[156:159],v[116:119],a[244:247]\nzm a[248:251],v[156:159],v[120:123],a[248:251]\nzp s62,s62,128\nzp s63,s63,128\nzp s64,s64,s74\nzm a[252:255],v[156:159],v[124:127],a[252:255]\n"
"s_xor_b32 s65,s65,0x8000\ns_xor_b32 s66,s66,0x8000\nzv lgkmcnt(0)\nzm a[0:3],v[64:67],v[32:35],a[0:3]\nzs v[192:195],v29,s[40:43],s80 offen offset:0 sc1\nzm a[4:7],v[64:67],v[36:39],a[4:7]\nzt v[128:129],v18 offset:8192\nzm a[8:11],v[64:67],v[40:43],a[8:11]\n"
"zm a[12:15],v[64:67],v[44:47],a[12:15]\nzt v[130:131],v18 offset:9216\nzm a[16:19],v[64:67],v[48:51],a[16:19]\nzm a[20:23],v[64:67],v[52:55],a[20:23]\nzd v[96:99],v17 offset:0\nzm a[24:27],v[64:67],v[56:59],a[24:27]\nzm a[28:31],v[64:67],v[60:63],a[28:31]\nzt v[132:133],v19 offset:8192\n"
"zm a[32:35],v[68:71],v[32:35],a[32:35]\nzm a[36:39],v[68:71],v[36:39],a[36:39]\nzt v[134:135],v19 offset:9216\nzm a[40:43],v[68:71],v[40:43],a[40:43]\nzm a[44:47],v[68:71],v[44:47],a[44:47]\nzd v[100:103],v17 offset:2048\nzm a[48:51],v[68:71],v[48:51],a[48:51]\nzs v[196:199],v29,s[40:43],s80 offen offset:1024 sc1\n"
"zm a[52:55],v[68:71],v[52:55],a[52:55]\nzt v[136:137],v20 offset:8192\nzm a[56:59],v[68:71],v[56:59],a[56:59]\nzm a[60:63],v[68:71],v[60:63],a[60:63]\nzt v[138:139],v20 offset:9216\nzm a[64:67],v[72:75],v[32:35],a[64:67]\nzm a[68:71],v[72:75],v[36:39],a[68:71]\nzd v[104:107],v17 offset:4096\n"
"zm a[72:75],v[72:75],v[40:43],a[72:75]\nzm a[76:79],v[72:75],v[44:47],a[76:79]\nzt v[140:141],v21 offset:8192\nzm a[80:83],v[72:75],v[48:51],a[80:83]\nzm a[84:87],v[72:75],v[52:55],a[84:87]\nzt v[142:143],v21 offset:9216\nzm a[88:91],v[72:75],v[56:59],a[88:91]\nzm a[92:95],v[72:75],v[60:63],a[92:95]\n"
"zd v[108:111],v17 offset:6144\nzm a[96:99],v[76:79],v[32:35],a[96:99]\nzs v[200:203],v29,s[40:43],s80 offen offset:2048 sc1\nzm a[100:103],v[76:79],v[36:39],a[100:103]\nzt v[144:145],v22 offset:8192\nzm a[104:107],v[76:79],v[40:43],a[104:107]\nzm a[108:111],v[76:79],v[44:47],a[108:111]\nzt v[146:147],v22 offset:9216\n"
"zm a[112:115],v[76:79],v[48:51],a[112:115]\nzm a[116:119],v[76:79],v[52:55],a[116:119]\nzd v[112:115],v17 offset:8192\nzm a[120:123],v[76:79],v[56:59],a[120:123]\nzm a[124:127],v[76:79],v[60:63],a[124:127]\nzt v[148:149],v23 offset:8192\nzm a[128:131],v[80:83],v[32:35],a[128:131]\nzm a[132:135],v[80:83],v[36:39],a[132:135]\n"
"zt v[150:151],v23 offset:9216\nzm a[136:139],v[80:83],v[40:43],a[136:139]\nzm a[140:143],v[80:83],v[44:47],a[140:143]\nzd v[116:119],v17 offset:10240\nzm a[144:147],v[80:83],v[48:51],a[144:147]\nzs v[204:207],v29,s[40:43],s80 offen offset:3072 sc1\nzm a[148:151],v[80:83],v[52:55],a[148:151]\nzt v[152:153],v24 offset:8192\n"
"zm a[152:155],v[80:83],v[56:59],a[152:155]\nzm a[156:159],v[80:83],v[60:63],a[156:159]\nzt v[154:155],v24 offset:9216\nzm a[160:163],v[84:87],v[32:35],a[160:163]\nzm a[164:167],v[84:87],v[36:39],a[164:167]\nzd v[120:123],v17 offset:12288\nzm a[168:171],v[84:87],v[40:43],a[168:171]\nzm a[172:175],v[84:87],v[44:47],a[172:175]\n"
"zt v[156:157],v25 offset:8192\nzm a[176:179],v[84:87],v[48:51],a[176:179]\nzm a[180:183],v[84:87],v[52:55],a[180:183]\nzt v[158:159],v25 offset:9216\nzm a[184:187],v[84:87],v[56:59],a[184:187]\nzm a[188:191],v[84:87],v[60:63],a[188:191]\nzd v[124:127],v17 offset:14336\nzm a[192:195],v[88:91],v[32:35],a[192:195]\n"
"zp s80,s80,s81\nzm a[196:199],v[88:91],v[36:39],a[196:199]\nzm a[200:203],v[88:91],v[40:43],a[200:203]\nzm a[204:207],v[88:91],v[44:47],a[204:207]\nzm a[208:211],v[88:91],v[48:51],a[208:211]\nzo v16,0x8000,v16\nzm a[212:215],v[88:91],v[52:55],a[212:215]\nzo v17,0x8000,v17\n"
"zm a[216:219],v[88:91],v[56:59],a[216:219]\nzo v18,0x8000,v18\nzm a[220:223],v[88:91],v[60:63],a[220:223]\nzo v19,0x8000,v19\nzm a[224:227],v[92:95],v[32:35],a[224:227]\nzo v20,0x8000,v20\nzm a[228:231],v[92:95],v[36:39],a[228:231]\nzo v21,0x8000,v21\n"
"zm a[232:235],v[92:95],v[40:43],a[232:235]\nzo v22,0x8000,v22\nzm a[236:239],v[92:95],v[44:47],a[236:239]\nzo v23,0x8000,v23\nzm a[240:243],v[92:95],v[48:51],a[240:243]\nzo v24,0x8000,v24\nzm a[244:247],v[92:95],v[52:55],a[244:247]\nzo v25,0x8000,v25\n"
"zm a[248:251],v[92:95],v[56:59],a[248:251]\nzm a[252:255],v[92:95],v[60:63],a[252:255]\nzv vmcnt(4) lgkmcnt(0)\ns_barrier\nzm a[0:3],v[128:131],v[96:99],a[0:3]\nzt v[64:65],v18 offset:0\ns_mov_b32 m0,s65\nzm a[4:7],v[128:131],v[100:103],a[4:7]\n"
"zl v8,s[24:27],s62 offen offset:0 sc1 lds\nzm a[8:11],v[128:131],v[104:107],a[8:11]\nzt v[66:67],v18 offset:1024\nzm a[12:15],v[128:131],v[108:111],a[12:15]\nzm a[16:19],v[128:131],v[112:115],a[16:19]\nzl v9,s[24:27],s62 offen offset:1024 sc1 lds\nzm a[20:23],v[128:131],v[116:119],a[20:23]\nzd v[32:35],v16 offset:0\n"
"zm a[24:27],v[128:131],v[120:123],a[24:27]\nzt v[68:69],v19 offset:0\nzm a[28:31],v[128:131],v[124:127],a[28:31]\nzm a[32:35],v[132:135],v[96:99],a[32:35]\nzl v10,s[24:27],s62 offen offset:2048 sc1 lds\nzm a[36:39],v[132:135],v[100:103],a[36:39]\nzt v[70:71],v19 offset:1024\nzm a[40:43],v[132:135],v[104:107],a[40:43]\n"
"zd v[36:39],v16 offset:2048\nzm a[44:47],v[132:135],v[108:111],a[44:47]\nzm a[48:51],v[132:135],v[112:115],a[48:51]\nzl v11,s[24:27],s62 offen offset:3072 sc1 lds\nzm a[52:55],v[132:135],v[116:119],a[52:55]\nzt v[72:73],v20 offset:0\nzm a[56:59],v[132:135],v[120:123],a[56:59]\nzm a[60:63],v[132:135],v[124:127],a[60:63]\n"
"zt v[74:75],v20 offset:1024\nzp m0,s65,4096\nzm a[64:67],v[136:139],v[96:99],a[64:67]\nzl v8,s[24:27],s63 offen offset:0 sc1 lds\nzm a[68:71],v[136:139],v[100:103],a[68:71]\nzd v[40:43],v16 offset:4096\nzm a[72:75],v[136:139],v[104:107],a[72:75]\nzm a[76:79],v[136:139],v[108:111],a[76:79]\n"
"zt v[76:77],v21 offset:0\nzm a[80:83],v[136:139],v[112:115],a[80:83]\nzl v9,s[24:27],s63 offen offset:1024 sc1 lds\nzm a[84:87],v[136:139],v[116:119],a[84:87]\nzt v[78:79],v21 offset:1024\nzm a[88:91],v[136:139],v[120:123],a[88:91]\nzm a[92:95],v[136:139],v[124:127],a[92:95]\nzl v10,s[24:27],s63 offen offset:2048 sc1 lds\n"
"zm a[96:99],v[140:143],v[96:99],a[96:99]\nzd v[44:47],v16 offset:6144\nzm a[100:103],v[140:143],v[100:103],a[100:103]\nzm a[104:107],v[140:143],v[104:107],a[104:107]\nzt v[80:81],v22 offset:0\nzm a[108:111],v[140:143],v[108:111],a[108:111]\nzl v11,s[24:27],s63 offen offset:3072 sc1 lds\nzm a[112:115],v[140:143],v[112:115],a[112:115]\n"
"zt v[82:83],v22 offset:1024\nzm a[116:119],v[140:143],v[116:119],a[116:119]\nzm a[120:123],v[140:143],v[120:123],a[120:123]\nzd v[48:51],v16 offset:8192\ns_mov_b32 m0,s66\nzm a[124:127],v[140:143],v[124:127],a[124:127]\nzl v12,s[28:31],s64 offen offset:0 lds\nzm a[128:131],v[144:147],v[96:99],a[128:131]\n"
"zt v[84:85],v23 offset:0\nzm a[132:135],v[144:147],v[100:103],a[132:135]\nzm a[136:139],v[144:147],v[104:107],a[136:139]\nzt v[86:87],v23 offset:1024\nzm a[140:143],v[144:147],v[108:111],a[140:143]\nzl v13,s[28:31],s64 offen offset:1024 lds\nzm a[144:147],v[144:147],v[112:115],a[144:147]\nzd v[52:55],v16 offset:10240\n"
"zm a[148:151],v[144:147],v[116:119],a[148:151]\nzm a[152:155],v[144:147],v[120:123],a[152:155]\nzt v[88:89],v24 offset:0\nzm a[156:159],v[144:147],v[124:127],a[156:159]\nzl v14,s[28:31],s64 offen offset:2048 lds\nzm a[160:163],v[148:151],v[96:99],a[160:163]\nzm a[164:167],v[148:151],v[100:103],a[164:167]\nzt v[90:91],v24 offset:1024\n"
"zm a[168:171],v[148:151],v[104:107],a[168:171]\nzl v15,s[28:31],s64 offen offset:3072 lds\nzm a[172:175],v[148:151],v[108:111],a[172:175]\nzd v[56:59],v16 offset:12288\nzm a[176:179],v[148:151],v[112:115],a[176:179]\nzm a[180:183],v[148:151],v[116:119],a[180:183]\nzt v[92:93],v25 offset:0\nzp m0,s66,16384\n"
"zm a[184:187],v[148:151],v[120:123],a[184:187]\nzl v12,s[36:39],s64 offen offset:0 lds\nzm a[188:191],v[148:151],v[124:127],a[188:191]\nzt v[94:95],v25 offset:1024\nzm a[192:195],v[152:155],v[96:99],a[192:195]\nzm a[196:199],v[152:155],v[100:103],a[196:199]\nzd v[60:63],v16 offset:14336\nzm a[200:203],v[152:155],v[104:107],a[200:203]\n"
"zl v13,s[36:39],s64 offen offset:1024 lds\nzm a[204:207],v[152:155],v[108:111],a[204:207]\nzm a[208:211],v[152:155],v[112:115],a[208:211]\nzm a[212:215],v[152:155],v[116:119],a[212:215]\nzm a[216:219],v[152:155],v[120:123],a[216:219]\nzl v14,s[36:39],s64 offen offset:2048 lds\nzm a[220:223],v[152:155],v[124:127],a[220:223]\nzm a[224:227],v[156:159],v[96:99],a[224:227]\n"
"zm a[228:231],v[156:159],v[100:103],a[228:231]\nzm a[232:235],v[156:159],v[104:107],a[232:235]\nzl v15,s[36:39],s64 offen offset:3072 lds\nzm a[236:239],v[156:159],v[108:111],a[236:239]\nzm a[240:243],v[156:159],v[112:115],a[240:243]\nzm a[244:247],v[156:159],v[116:119],a[244:247]\nzm a[248:251],v[156:159],v[120:123],a[248:251]\nzp s62,s62,128\n"
"zp s63,s63,128\nzp s64,s64,s74\nzm a[252:255],v[156:159],v[124:127],a[252:255]\ns_xor_b32 s65,s65,0x8000\ns_xor_b32 s66,s66,0x8000\nzv lgkmcnt(0)\nzm a[0:3],v[64:67],v[32:35],a[0:3]\nzs v[208:211],v29,s[40:43],s80 offen offset:0 sc1\n"
"zm a[4:7],v[64:67],v[36:39],a[4:7]\nzt v[128:129],v18 offset:8192\nzm a[8:11],v[64:67],v[40:43],a[8:11]\nzm a[12:15],v[64:67],v[44:47],a[12:15]\nzt v[130:131],v18 offset:9216\nzm a[16:19],v[64:67],v[48:51],a[16:19]\nzm a[20:23],v[64:67],v[52:55],a[20:23]\nzd v[96:99],v17 offset:0\n"
"zm a[24:27],v[64:67],v[56:59],a[24:27]\nzm a[28:31],v[64:67],v[60:63],a[28:31]\nzt v[132:133],v19 offset:8192\nzm a[32:35],v[68:71],v[32:35],a[32:35]\nzm a[36:39],v[68:71],v[36:39],a[36:39]\nzt v[134:135],v19 offset:9216\nzm a[40:43],v[68:71],v[40:43],a[40:43]\nzm a[44:47],v[68:71],v[44:47],a[44:47]\n"
"zd v[100:103],v17 offset:2048\nzm a[48:51],v[68:71],v[48:51],a[48:51]\nzs v[212:215],v29,s[40:43],s80 offen offset:1024 sc1\nzm a[52:55],v[68:71],v[52:55],a[52:55]\nzt v[136:137],v20 offset:8192\nzm a[56:59],v[68:71],v[56:59],a[56:59]\nzm a[60:63],v[68:71],v[60:63],a[60:63]\nzt v[138:139],v20 offset:9216\n"
"zm a[64:67],v[72:75],v[32:35],a[64:67]\nzm a[68:71],v[72:75],v[36:39],a[68:71]\nzd v[104:107],v17 offset:4096\nzm a[72:75],v[72:75],v[40:43],a[72:75]\nzm a[76:79],v[72:75],v[44:47],a[76:79]\nzt v[140:141],v21 offset:8192\nzm a[80:83],v[72:75],v[48:51],a[80:83]\nzm a[84:87],v[72:75],v[52:55],a[84:87]\n"
"zt v[142:143],v21 offset:9216\nzm a[88:91],v[72:75],v[56:59],a[88:91]\nzm a[92:95],v[72:75],v[60:63],a[92:95]\nzd v[108:111],v17 offset:6144\nzm a[96:99],v[76:79],v[32:35],a[96:99]\nzs v[216:219],v29,s[40:43],s80 offen offset:2048 sc1\nzm a[100:103],v[76:79],v[36:39],a[100:103]\nzt v[144:145],v22 offset:8192\n"
"zm a[104:107],v[76:79],v[40:43],a[104:107]\nzm a[108:111],v[76:79],v[44:47],a[108:111]\nzt v[146:147],v22 offset:9216\nzm a[112:115],v[76:79],v[48:51],a[112:115]\nzm a[116:119],v[76:79],v[52:55],a[116:119]\nzd v[112:115],v17 offset:8192\nzm a[120:123],v[76:79],v[56:59],a[120:123]\nzm a[124:127],v[76:79],v[60:63],a[124:127]\n"
"zt v[148:149],v23 offset:8192\nzm a[128:131],v[80:83],v[32:35],a[128:131]\nzm a[132:135],v[80:83],v[36:39],a[132:135]\nzt v[150:151],v23 offset:9216\nzm a[136:139],v[80:83],v[40:43],a[136:139]\nzm a[140:143],v[80:83],v[44:47],a[140:143]\nzd v[116:119],v17 offset:10240\nzm a[144:147],v[80:83],v[48:51],a[144:147]\n"
"zs v[220:223],v29,s[40:43],s80 offen offset:3072 sc1\nzm a[148:151],v[80:83],v[52:55],a[148:151]\nzt v[152:153],v24 offset:8192\nzm a[152:155],v[80:83],v[56:59],a[152:155]\nzm a[156:159],v[80:83],v[60:63],a[156:159]\nzt v[154:155],v24 offset:9216\nzm a[160:163],v[84:87],v[32:35],a[160:163]\nzm a[164:167],v[84:87],v[36:39],a[164:167]\n"
"zd v[120:123],v17 offset:12288\nzm a[168:171],v[84:87],v[40:43],a[168:171]\nzm a[172:175],v[84:87],v[44:47],a[172:175]\nzt v[156:157],v25 offset:8192\nzm a[176:179],v[84:87],v[48:51],a[176:179]\nzm a[180:183],v[84:87],v[52:55],a[180:183]\nzt v[158:159],v25 offset:9216\nzm a[184:187],v[84:87],v[56:59],a[184:187]\n"
"zm a[188:191],v[84:87],v[60:63],a[188:191]\nzd v[124:127],v17 offset:14336\nzm a[192:195],v[88:91],v[32:35],a[192:195]\nzp s80,s80,s82\nzm a[196:199],v[88:91],v[36:39],a[196:199]\nzm a[200:203],v[88:91],v[40:43],a[200:203]\nzm a[204:207],v[88:91],v[44:47],a[204:207]\nzm a[208:211],v[88:91],v[48:51],a[208:211]\n"
"zo v16,0x8000,v16\nzm a[212:215],v[88:91],v[52:55],a[212:215]\nzo v17,0x8000,v17\nzm a[216:219],v[88:91],v[56:59],a[216:219]\nzo v18,0x8000,v18\nzm a[220:223],v[88:91],v[60:63],a[220:223]\nzo v19,0x8000,v19\nzm a[224:227],v[92:95],v[32:35],a[224:227]\n"
"zo v20,0x8000,v20\nzm a[228:231],v[92:95],v[36:39],a[228:231]\nzo v21,0x8000,v21\nzm a[232:235],v[92:95],v[40:43],a[232:235]\nzo v22,0x8000,v22\nzm a[236:239],v[92:95],v[44:47],a[236:239]\nzo v23,0x8000,v23\nzm a[240:243],v[92:95],v[48:51],a[240:243]\n"
"zo v24,0x8000,v24\nzm a[244:247],v[92:95],v[52:55],a[244:247]\nzo v25,0x8000,v25\nzm a[248:251],v[92:95],v[56:59],a[248:251]\nzm a[252:255],v[92:95],v[60:63],a[252:255]\nzv vmcnt(4) lgkmcnt(0)\ns_barrier\nzm a[0:3],v[128:131],v[96:99],a[0:3]\n"
"zt v[64:65],v18 offset:0\ns_mov_b32 m0,s65\nzm a[4:7],v[128:131],v[100:103],a[4:7]\nzl v8,s[24:27],s62 offen offset:0 sc1 lds\nzm a[8:11],v[128:131],v[104:107],a[8:11]\nzt v[66:67],v18 offset:1024\nzm a[12:15],v[128:131],v[108:111],a[12:15]\nzm a[16:19],v[128:131],v[112:115],a[16:19]\n"
"zl v9,s[24:27],s62 offen offset:1024 sc1 lds\nzm a[20:23],v[128:131],v[116:119],a[20:23]\nzd v[32:35],v16 offset:0\nzm a[24:27],v[128:131],v[120:123],a[24:27]\nzt v[68:69],v19 offset:0\nzm a[28:31],v[128:131],v[124:127],a[28:31]\nzm a[32:35],v[132:135],v[96:99],a[32:35]\nzl v10,s[24:27],s62 offen offset:2048 sc1 lds\n"
"zm a[36:39],v[132:135],v[100:103],a[36:39]\nzt v[70:71],v19 offset:1024\nzm a[40:43],v[132:135],v[104:107],a[40:43]\nzd v[36:39],v16 offset:2048\nzm a[44:47],v[132:135],v[108:111],a[44:47]\nzm a[48:51],v[132:135],v[112:115],a[48:51]\nzl v11,s[24:27],s62 offen offset:3072 sc1 lds\nzm a[52:55],v[132:135],v[116:119],a[52:55]\n"
"zt v[72:73],v20 offset:0\nzm a[56:59],v[132:135],v[120:123],a[56:59]\nzm a[60:63],v[132:135],v[124:127],a[60:63]\nzt v[74:75],v20 offset:1024\nzp m0,s65,4096\nzm a[64:67],v[136:139],v[96:99],a[64:67]\nzl v8,s[24:27],s63 offen offset:0 sc1 lds\nzm a[68:71],v[136:139],v[100:103],a[68:71]\n"
"zd v[40:43],v16 offset:4096\nzm a[72:75],v[136:139],v[104:107],a[72:75]\nzm a[76:79],v[136:139],v[108:111],a[76:79]\nzt v[76:77],v21 offset:0\nzm a[80:83],v[136:139],v[112:115],a[80:83]\nzl v9,s[24:27],s63 offen offset:1024 sc1 lds\nzm a[84:87],v[136:139],v[116:119],a[84:87]\nzt v[78:79],v21 offset:1024\n"
"zm a[88:91],v[136:139],v[120:123],a[88:91]\nzm a[92:95],v[136:139],v[124:127],a[92:95]\nzl v10,s[24:27],s63 offen offset:2048 sc1 lds\nzm a[96:99],v[140:143],v[96:99],a[96:99]\nzd v[44:47],v16 offset:6144\nzm a[100:103],v[140:143],v[100:103],a[100:103]\nzm a[104:107],v[140:143],v[104:107],a[104:107]\nzt v[80:81],v22 offset:0\n"
"zm a[108:111],v[140:143],v[108:111],a[108:111]\nzl v11,s[24:27],s63 offen offset:3072 sc1 lds\nzm a[112:115],v[140:143],v[112:115],a[112:115]\nzt v[82:83],v22 offset:1024\nzm a[116:119],v[140:143],v[116:119],a[116:119]\nzm a[120:123],v[140:143],v[120:123],a[120:123]\nzd v[48:51],v16 offset:8192\ns_mov_b32 m0,s66\n"
"zm a[124:127],v[140:143],v[124:127],a[124:127]\nzl v12,s[28:31],s64 offen offset:0 lds\nzm a[128:131],v[144:147],v[96:99],a[128:131]\nzt v[84:85],v23 offset:0\nzm a[132:135],v[144:147],v[100:103],a[132:135]\nzm a[136:139],v[144:147],v[104:107],a[136:139]\nzt v[86:87],v23 offset:1024\nzm a[140:143],v[144:147],v[108:111],a[140:143]\n"
"zl v13,s[28:31],s64 offen offset:1024 lds\nzm a[144:147],v[144:147],v[112:115],a[144:147]\nzd v[52:55],v16 offset:10240\nzm a[148:151],v[144:147],v[116:119],a[148:151]\nzm a[152:155],v[144:147],v[120:123],a[152:155]\nzt v[88:89],v24 offset:0\nzm a[156:159],v[144:147],v[124:127],a[156:159]\nzl v14,s[28:31],s64 offen offset:2048 lds\n"
"zm a[160:163],v[148:151],v[96:99],a[160:163]\nzm a[164:167],v[148:151],v[100:103],a[164:167]\nzt v[90:91],v24 offset:1024\nzm a[168:171],v[148:151],v[104:107],a[168:171]\nzl v15,s[28:31],s64 offen offset:3072 lds\nzm a[172:175],v[148:151],v[108:111],a[172:175]\nzd v[56:59],v16 offset:12288\nzm a[176:179],v[148:151],v[112:115],a[176:179]\n"
"zm a[180:183],v[148:151],v[116:119],a[180:183]\nzt v[92:93],v25 offset:0\nzp m0,s66,16384\nzm a[184:187],v[148:151],v[120:123],a[184:187]\nzl v12,s[36:39],s64 offen offset:0 lds\nzm a[188:191],v[148:151],v[124:127],a[188:191]\nzt v[94:95],v25 offset:1024\nzm a[192:195],v[152:155],v[96:99],a[192:195]\n"
"zm a[196:199],v[152:155],v[100:103],a[196:199]\nzd v[60:63],v16 offset:14336\nzm a[200:203],v[152:155],v[104:107],a[200:203]\nzl v13,s[36:39],s64 offen offset:1024 lds\nzm a[204:207],v[152:155],v[108:111],a[204:207]\nzm a[208:211],v[152:155],v[112:115],a[208:211]\nzm a[212:215],v[152:155],v[116:119],a[212:215]\nzm a[216:219],v[152:155],v[120:123],a[216:219]\n"
"zl v14,s[36:39],s64 offen offset:2048 lds\nzm a[220:223],v[152:155],v[124:127],a[220:223]\nzm a[224:227],v[156:159],v[96:99],a[224:227]\nzm a[228:231],v[156:159],v[100:103],a[228:231]\nzm a[232:235],v[156:159],v[104:107],a[232:235]\nzl v15,s[36:39],s64 offen offset:3072 lds\nzm a[236:239],v[156:159],v[108:111],a[236:239]\nzm a[240:243],v[156:159],v[112:115],a[240:243]\n"
"zm a[244:247],v[156:159],v[116:119],a[244:247]\nzm a[248:251],v[156:159],v[120:123],a[248:251]\nzp s62,s62,128\nzp s63,s63,128\nzp s64,s64,s74\nzm a[252:255],v[156:159],v[124:127],a[252:255]\ns_xor_b32 s65,s65,0x8000\ns_xor_b32 s66,s66,0x8000\n"
"zv lgkmcnt(0)\nzm a[0:3],v[64:67],v[32:35],a[0:3]\nzs v[224:227],v29,s[40:43],s80 offen offset:0 sc1\nzm a[4:7],v[64:67],v[36:39],a[4:7]\nzt v[128:129],v18 offset:8192\nzm a[8:11],v[64:67],v[40:43],a[8:11]\nzm a[12:15],v[64:67],v[44:47],a[12:15]\nzt v[130:131],v18 offset:9216\n"
"zm a[16:19],v[64:67],v[48:51],a[16:19]\nzm a[20:23],v[64:67],v[52:55],a[20:23]\nzd v[96:99],v17 offset:0\nzm a[24:27],v[64:67],v[56:59],a[24:27]\nzm a[28:31],v[64:67],v[60:63],a[28:31]\nzt v[132:133],v19 offset:8192\nzm a[32:35],v[68:71],v[32:35],a[32:35]\nzm a[36:39],v[68:71],v[36:39],a[36:39]\n"
"zt v[134:135],v19 offset:9216\nzm a[40:43],v[68:71],v[40:43],a[40:43]\nzm a[44:47],v[68:71],v[44:47],a[44:47]\nzd v[100:103],v17 offset:2048\nzm a[48:51],v[68:71],v[48:51],a[48:51]\nzs v[228:231],v29,s[40:43],s80 offen offset:1024 sc1\nzm a[52:55],v[68:71],v[52:55],a[52:55]\nzt v[136:137],v20 offset:8192\n"
"zm a[56:59],v[68:71],v[56:59],a[56:59]\nzm a[60:63],v[68:71],v[60:63],a[60:63]\nzt v[138:139],v20 offset:9216\nzm a[64:67],v[72:75],v[32:35],a[64:67]\nzm a[68:71],v[72:75],v[36:39],a[68:71]\nzd v[104:107],v17 offset:4096\nzm a[72:75],v[72:75],v[40:43],a[72:75]\nzm a[76:79],v[72:75],v[44:47],a[76:79]\n"
"zt v[140:141],v21 offset:8192\nzm a[80:83],v[72:75],v[48:51],a[80:83]\nzm a[84:87],v[72:75],v[52:55],a[84:87]\nzt v[142:143],v21 offset:9216\nzm a[88:91],v[72:75],v[56:59],a[88:91]\nzm a[92:95],v[72:75],v[60:63],a[92:95]\nzd v[108:111],v17 offset:6144\nzm a[96:99],v[76:79],v[32:35],a[96:99]\n"
"zs v[232:235],v29,s[40:43],s80 offen offset:2048 sc1\nzm a[100:103],v[76:79],v[36:39],a[100:103]\nzt v[144:145],v22 offset:8192\nzm a[104:107],v[76:79],v[40:43],a[104:107]\nzm a[108:111],v[76:79],v[44:47],a[108:111]\nzt v[146:147],v22 offset:9216\nzm a[112:115],v[76:79],v[48:51],a[112:115]\nzm a[116:119],v[76:79],v[52:55],a[116:119]\n"
"zd v[112:115],v17 offset:8192\nzm a[120:123],v[76:79],v[56:59],a[120:123]\nzm a[124:127],v[76:79],v[60:63],a[124:127]\nzt v[148:149],v23 offset:8192\nzm a[128:131],v[80:83],v[32:35],a[128:131]\nzm a[132:135],v[80:83],v[36:39],a[132:135]\nzt v[150:151],v23 offset:9216\nzm a[136:139],v[80:83],v[40:43],a[136:139]\n"
"zm a[140:143],v[80:83],v[44:47],a[140:143]\nzd v[116:119],v17 offset:10240\nzm a[144:147],v[80:83],v[48:51],a[144:147]\nzs v[236:239],v29,s[40:43],s80 offen offset:3072 sc1\nzm a[148:151],v[80:83],v[52:55],a[148:151]\nzt v[152:153],v24 offset:8192\nzm a[152:155],v[80:83],v[56:59],a[152:155]\nzm a[156:159],v[80:83],v[60:63],a[156:159]\n"
"zt v[154:155],v24 offset:9216\nzm a[160:163],v[84:87],v[32:35],a[160:163]\nzm a[164:167],v[84:87],v[36:39],a[164:167]\nzd v[120:123],v17 offset:12288\nzm a[168:171],v[84:87],v[40:43],a[168:171]\nzm a[172:175],v[84:87],v[44:47],a[172:175]\nzt v[156:157],v25 offset:8192\nzm a[176:179],v[84:87],v[48:51],a[176:179]\n"
"zm a[180:183],v[84:87],v[52:55],a[180:183]\nzt v[158:159],v25 offset:9216\nzm a[184:187],v[84:87],v[56:59],a[184:187]\nzm a[188:191],v[84:87],v[60:63],a[188:191]\nzd v[124:127],v17 offset:14336\nzm a[192:195],v[88:91],v[32:35],a[192:195]\nzp s80,s80,s81\nzm a[196:199],v[88:91],v[36:39],a[196:199]\n"
"zm a[200:203],v[88:91],v[40:43],a[200:203]\nzm a[204:207],v[88:91],v[44:47],a[204:207]\nzm a[208:211],v[88:91],v[48:51],a[208:211]\nzo v16,0x8000,v16\nzm a[212:215],v[88:91],v[52:55],a[212:215]\nzo v17,0x8000,v17\nzm a[216:219],v[88:91],v[56:59],a[216:219]\nzo v18,0x8000,v18\n"
"zm a[220:223],v[88:91],v[60:63],a[220:223]\nzo v19,0x8000,v19\nzm a[224:227],v[92:95],v[32:35],a[224:227]\nzo v20,0x8000,v20\nzm a[228:231],v[92:95],v[36:39],a[228:231]\nzo v21,0x8000,v21\nzm a[232:235],v[92:95],v[40:43],a[232:235]\nzo v22,0x8000,v22\n"
"zm a[236:239],v[92:95],v[44:47],a[236:239]\nzo v23,0x8000,v23\nzm a[240:243],v[92:95],v[48:51],a[240:243]\nzo v24,0x8000,v24\nzm a[244:247],v[92:95],v[52:55],a[244:247]\nzo v25,0x8000,v25\nzm a[248:251],v[92:95],v[56:59],a[248:251]\nzm a[252:255],v[92:95],v[60:63],a[252:255]\n"
"zv vmcnt(4) lgkmcnt(0)\ns_barrier\nzm a[0:3],v[128:131],v[96:99],a[0:3]\nzt v[64:65],v18 offset:0\ns_mov_b32 m0,s65\nzm a[4:7],v[128:131],v[100:103],a[4:7]\nzl v8,s[24:27],s62 offen offset:0 sc1 lds\nzm a[8:11],v[128:131],v[104:107],a[8:11]\n"
"zt v[66:67],v18 offset:1024\nzm a[12:15],v[128:131],v[108:111],a[12:15]\nzm a[16:19],v[128:131],v[112:115],a[16:19]\nzl v9,s[24:27],s62 offen offset:1024 sc1 lds\nzm a[20:23],v[128:131],v[116:119],a[20:23]\nzd v[32:35],v16 offset:0\nzm a[24:27],v[128:131],v[120:123],a[24:27]\nzt v[68:69],v19 offset:0\n"
"zm a[28:31],v[128:131],v[124:127],a[28:31]\nzm a[32:35],v[132:135],v[96:99],a[32:35]\nzl v10,s[24:27],s62 offen offset:2048 sc1 lds\nzm a[36:39],v[132:135],v[100:103],a[36:39]\nzt v[70:71],v19 offset:1024\nzm a[40:43],v[132:135],v[104:107],a[40:43]\nzd v[36:39],v16 offset:2048\nzm a[44:47],v[132:135],v[108:111],a[44:47]\n"
"zm a[48:51],v[132:135],v[112:115],a[48:51]\nzl v11,s[24:27],s62 offen offset:3072 sc1 lds\nzm a[52:55],v[132:135],v[116:119],a[52:55]\nzt v[72:73],v20 offset:0\nzm a[56:59],v[132:135],v[120:123],a[56:59]\nzm a[60:63],v[132:135],v[124:127],a[60:63]\nzt v[74:75],v20 offset:1024\nzp m0,s65,4096\n"
"zm a[64:67],v[136:139],v[96:99],a[64:67]\nzl v8,s[24:27],s63 offen offset:0 sc1 lds\nzm a[68:71],v[136:139],v[100:103],a[68:71]\nzd v[40:43],v16 offset:4096\nzm a[72:75],v[136:139],v[104:107],a[72:75]\nzm a[76:79],v[136:139],v[108:111],a[76:79]\nzt v[76:77],v21 offset:0\nzm a[80:83],v[136:139],v[112:115],a[80:83]\n"
"zl v9,s[24:27],s63 offen offset:1024 sc1 lds\nzm a[84:87],v[136:139],v[116:119],a[84:87]\nzt v[78:79],v21 offset:1024\nzm a[88:91],v[136:139],v[120:123],a[88:91]\nzm a[92:95],v[136:139],v[124:127],a[92:95]\nzl v10,s[24:27],s63 offen offset:2048 sc1 lds\nzm a[96:99],v[140:143],v[96:99],a[96:99]\nzd v[44:47],v16 offset:6144\n"
"zm a[100:103],v[140:143],v[100:103],a[100:103]\nzm a[104:107],v[140:143],v[104:107],a[104:107]\nzt v[80:81],v22 offset:0\nzm a[108:111],v[140:143],v[108:111],a[108:111]\nzl v11,s[24:27],s63 offen offset:3072 sc1 lds\nzm a[112:115],v[140:143],v[112:115],a[112:115]\nzt v[82:83],v22 offset:1024\nzm a[116:119],v[140:143],v[116:119],a[116:119]\n"
"zm a[120:123],v[140:143],v[120:123],a[120:123]\nzd v[48:51],v16 offset:8192\ns_mov_b32 m0,s66\nzm a[124:127],v[140:143],v[124:127],a[124:127]\nzl v12,s[28:31],s64 offen offset:0 lds\nzm a[128:131],v[144:147],v[96:99],a[128:131]\nzt v[84:85],v23 offset:0\nzm a[132:135],v[144:147],v[100:103],a[132:135]\n"
"zm a[136:139],v[144:147],v[104:107],a[136:139]\nzt v[86:87],v23 offset:1024\nzm a[140:143],v[144:147],v[108:111],a[140:143]\nzl v13,s[28:31],s64 offen offset:1024 lds\nzm a[144:147],v[144:147],v[112:115],a[144:147]\nzd v[52:55],v16 offset:10240\nzm a[148:151],v[144:147],v[116:119],a[148:151]\nzm a[152:155],v[144:147],v[120:123],a[152:155]\n"
"zt v[88:89],v24 offset:0\nzm a[156:159],v[144:147],v[124:127],a[156:159]\nzl v14,s[28:31],s64 offen offset:2048 lds\nzm a[160:163],v[148:151],v[96:99],a[160:163]\nzm a[164:167],v[148:151],v[100:103],a[164:167]\nzt v[90:91],v24 offset:1024\nzm a[168:171],v[148:151],v[104:107],a[168:171]\nzl v15,s[28:31],s64 offen offset:3072 lds\n"
"zm a[172:175],v[148:151],v[108:111],a[172:175]\nzd v[56:59],v16 offset:12288\nzm a[176:179],v[148:151],v[112:115],a[176:179]\nzm a[180:183],v[148:151],v[116:119],a[180:183]\nzt v[92:93],v25 offset:0\nzp m0,s66,16384\nzm a[184:187],v[148:151],v[120:123],a[184:187]\nzl v12,s[36:39],s64 offen offset:0 lds\n"
"zm a[188:191],v[148:151],v[124:127],a[188:191]\nzt v[94:95],v25 offset:1024\nzm a[192:195],v[152:155],v[96:99],a[192:195]\nzm a[196:199],v[152:155],v[100:103],a[196:199]\nzd v[60:63],v16 offset:14336\nzm a[200:203],v[152:155],v[104:107],a[200:203]\nzl v13,s[36:39],s64 offen offset:1024 lds\nzm a[204:207],v[152:155],v[108:111],a[204:207]\n"
"zm a[208:211],v[152:155],v[112:115],a[208:211]\nzm a[212:215],v[152:155],v[116:119],a[212:215]\nzm a[216:219],v[152:155],v[120:123],a[216:219]\nzl v14,s[36:39],s64 offen offset:2048 lds\nzm a[220:223],v[152:155],v[124:127],a[220:223]\nzm a[224:227],v[156:159],v[96:99],a[224:227]\nzm a[228:231],v[156:159],v[100:103],a[228:231]\nzm a[232:235],v[156:159],v[104:107],a[232:235]\n"
"zl v15,s[36:39],s64 offen offset:3072 lds\nzm a[236:239],v[156:159],v[108:111],a[236:239]\nzm a[240:243],v[156:159],v[112:115],a[240:243]\nzm a[244:247],v[156:159],v[116:119],a[244:247]\nzm a[248:251],v[156:159],v[120:123],a[248:251]\nzp s62,s62,128\nzp s63,s63,128\nzp s64,s64,s74\n"
"zm a[252:255],v[156:159],v[124:127],a[252:255]\ns_xor_b32 s65,s65,0x8000\ns_xor_b32 s66,s66,0x8000\nzv lgkmcnt(0)\nzm a[0:3],v[64:67],v[32:35],a[0:3]\nzs v[240:243],v29,s[40:43],s80 offen offset:0 sc1\nzm a[4:7],v[64:67],v[36:39],a[4:7]\nzt v[128:129],v18 offset:8192\n"
"zm a[8:11],v[64:67],v[40:43],a[8:11]\nzm a[12:15],v[64:67],v[44:47],a[12:15]\nzt v[130:131],v18 offset:9216\nzm a[16:19],v[64:67],v[48:51],a[16:19]\nzm a[20:23],v[64:67],v[52:55],a[20:23]\nzd v[96:99],v17 offset:0\nzm a[24:27],v[64:67],v[56:59],a[24:27]\nzm a[28:31],v[64:67],v[60:63],a[28:31]\n"
"zt v[132:133],v19 offset:8192\nzm a[32:35],v[68:71],v[32:35],a[32:35]\nzm a[36:39],v[68:71],v[36:39],a[36:39]\nzt v[134:135],v19 offset:9216\nzm a[40:43],v[68:71],v[40:43],a[40:43]\nzm a[44:47],v[68:71],v[44:47],a[44:47]\nzd v[100:103],v17 offset:2048\nzm a[48:51],v[68:71],v[48:51],a[48:51]\n"
"zs v[244:247],v29,s[40:43],s80 offen offset:1024 sc1\nzm a[52:55],v[68:71],v[52:55],a[52:55]\nzt v[136:137],v20 offset:8192\nzm a[56:59],v[68:71],v[56:59],a[56:59]\nzm a[60:63],v[68:71],v[60:63],a[60:63]\nzt v[138:139],v20 offset:9216\nzm a[64:67],v[72:75],v[32:35],a[64:67]\nzm a[68:71],v[72:75],v[36:39],a[68:71]\n"
"zd v[104:107],v17 offset:4096\nzm a[72:75],v[72:75],v[40:43],a[72:75]\nzm a[76:79],v[72:75],v[44:47],a[76:79]\nzt v[140:141],v21 offset:8192\nzm a[80:83],v[72:75],v[48:51],a[80:83]\nzm a[84:87],v[72:75],v[52:55],a[84:87]\nzt v[142:143],v21 offset:9216\nzm a[88:91],v[72:75],v[56:59],a[88:91]\n"
"zm a[92:95],v[72:75],v[60:63],a[92:95]\nzd v[108:111],v17 offset:6144\nzm a[96:99],v[76:79],v[32:35],a[96:99]\nzs v[248:251],v29,s[40:43],s80 offen offset:2048 sc1\nzm a[100:103],v[76:79],v[36:39],a[100:103]\nzt v[144:145],v22 offset:8192\nzm a[104:107],v[76:79],v[40:43],a[104:107]\nzm a[108:111],v[76:79],v[44:47],a[108:111]\n"
"zt v[146:147],v22 offset:9216\nzm a[112:115],v[76:79],v[48:51],a[112:115]\nzm a[116:119],v[76:79],v[52:55],a[116:119]\nzd v[112:115],v17 offset:8192\nzm a[120:123],v[76:79],v[56:59],a[120:123]\nzm a[124:127],v[76:79],v[60:63],a[124:127]\nzt v[148:149],v23 offset:8192\nzm a[128:131],v[80:83],v[32:35],a[128:131]\n"
"zm a[132:135],v[80:83],v[36:39],a[132:135]\nzt v[150:151],v23 offset:9216\nzm a[136:139],v[80:83],v[40:43],a[136:139]\nzm a[140:143],v[80:83],v[44:47],a[140:143]\nzd v[116:119],v17 offset:10240\nzm a[144:147],v[80:83],v[48:51],a[144:147]\nzs v[252:255],v29,s[40:43],s80 offen offset:3072 sc1\nzm a[148:151],v[80:83],v[52:55],a[148:151]\n"
"zt v[152:153],v24 offset:8192\nzm a[152:155],v[80:83],v[56:59],a[152:155]\nzm a[156:159],v[80:83],v[60:63],a[156:159]\nzt v[154:155],v24 offset:9216\nzm a[160:163],v[84:87],v[32:35],a[160:163]\nzm a[164:167],v[84:87],v[36:39],a[164:167]\nzd v[120:123],v17 offset:12288\nzm a[168:171],v[84:87],v[40:43],a[168:171]\n"
"zm a[172:175],v[84:87],v[44:47],a[172:175]\nzt v[156:157],v25 offset:8192\nzm a[176:179],v[84:87],v[48:51],a[176:179]\nzm a[180:183],v[84:87],v[52:55],a[180:183]\nzt v[158:159],v25 offset:9216\nzm a[184:187],v[84:87],v[56:59],a[184:187]\nzm a[188:191],v[84:87],v[60:63],a[188:191]\nzd v[124:127],v17 offset:14336\n"
"zm a[192:195],v[88:91],v[32:35],a[192:195]\nzp s80,s80,s82\nzm a[196:199],v[88:91],v[36:39],a[196:199]\nzm a[200:203],v[88:91],v[40:43],a[200:203]\nzm a[204:207],v[88:91],v[44:47],a[204:207]\nzm a[208:211],v[88:91],v[48:51],a[208:211]\nzo v16,0x8000,v16\nzm a[212:215],v[88:91],v[52:55],a[212:215]\n"
"zo v17,0x8000,v17\nzm a[216:219],v[88:91],v[56:59],a[216:219]\nzo v18,0x8000,v18\nzm a[220:223],v[88:91],v[60:63],a[220:223]\nzo v19,0x8000,v19\nzm a[224:227],v[92:95],v[32:35],a[224:227]\nzo v20,0x8000,v20\nzm a[228:231],v[92:95],v[36:39],a[228:231]\n"
"zo v21,0x8000,v21\nzm a[232:235],v[92:95],v[40:43],a[232:235]\nzo v22,0x8000,v22\nzm a[236:239],v[92:95],v[44:47],a[236:239]\nzo v23,0x8000,v23\nzm a[240:243],v[92:95],v[48:51],a[240:243]\nzo v24,0x8000,v24\nzm a[244:247],v[92:95],v[52:55],a[244:247]\n"
"zo v25,0x8000,v25\nzm a[248:251],v[92:95],v[56:59],a[248:251]\nzm a[252:255],v[92:95],v[60:63],a[252:255]\nzv vmcnt(4) lgkmcnt(0)\ns_barrier\nzm a[0:3],v[128:131],v[96:99],a[0:3]\nzm a[4:7],v[128:131],v[100:103],a[4:7]\nzt v[64:65],v18 offset:0\n"
"zm a[8:11],v[128:131],v[104:107],a[8:11]\nzm a[12:15],v[128:131],v[108:111],a[12:15]\nzt v[66:67],v18 offset:1024\nzm a[16:19],v[128:131],v[112:115],a[16:19]\nzm a[20:23],v[128:131],v[116:119],a[20:23]\nzd v[32:35],v16 offset:0\nzm a[24:27],v[128:131],v[120:123],a[24:27]\nzm a[28:31],v[128:131],v[124:127],a[28:31]\n"
"zt v[68:69],v19 offset:0\nzm a[32:35],v[132:135],v[96:99],a[32:35]\nzm a[36:39],v[132:135],v[100:103],a[36:39]\nzt v[70:71],v19 offset:1024\nzm a[40:43],v[132:135],v[104:107],a[40:43]\nzm a[44:47],v[132:135],v[108:111],a[44:47]\nzd v[36:39],v16 offset:2048\nzm a[48:51],v[132:135],v[112:115],a[48:51]\n"
"zm a[52:55],v[132:135],v[116:119],a[52:55]\nzt v[72:73],v20 offset:0\nzm a[56:59],v[132:135],v[120:123],a[56:59]\nzm a[60:63],v[132:135],v[124:127],a[60:63]\nzt v[74:75],v20 offset:1024\nzm a[64:67],v[136:139],v[96:99],a[64:67]\nzm a[68:71],v[136:139],v[100:103],a[68:71]\nzd v[40:43],v16 offset:4096\n"
"zm a[72:75],v[136:139],v[104:107],a[72:75]\nzm a[76:79],v[136:139],v[108:111],a[76:79]\nzt v[76:77],v21 offset:0\nzm a[80:83],v[136:139],v[112:115],a[80:83]\nzm a[84:87],v[136:139],v[116:119],a[84:87]\nzt v[78:79],v21 offset:1024\nzm a[88:91],v[136:139],v[120:123],a[88:91]\nzm a[92:95],v[136:139],v[124:127],a[92:95]\n"
"zd v[44:47],v16 offset:6144\nzm a[96:99],v[140:143],v[96:99],a[96:99]\nzm a[100:103],v[140:143],v[100:103],a[100:103]\nzt v[80:81],v22 offset:0\nzm a[104:107],v[140:143],v[104:107],a[104:107]\nzm a[108:111],v[140:143],v[108:111],a[108:111]\nzt v[82:83],v22 offset:1024\nzm a[112:115],v[140:143],v[112:115],a[112:115]\n"
"zm a[116:119],v[140:143],v[116:119],a[116:119]\nzd v[48:51],v16 offset:8192\nzm a[120:123],v[140:143],v[120:123],a[120:123]\nzm a[124:127],v[140:143],v[124:127],a[124:127]\nzt v[84:85],v23 offset:0\nzm a[128:131],v[144:147],v[96:99],a[128:131]\nzm a[132:135],v[144:147],v[100:103],a[132:135]\nzt v[86:87],v23 offset:1024\n"
"zm a[136:139],v[144:147],v[104:107],a[136:139]\nzm a[140:143],v[144:147],v[108:111],a[140:143]\nzd v[52:55],v16 offset:10240\nzm a[144:147],v[144:147],v[112:115],a[144:147]\nzm a[148:151],v[144:147],v[116:119],a[148:151]\nzt v[88:89],v24 offset:0\nzm a[152:155],v[144:147],v[120:123],a[152:155]\nzm a[156:159],v[144:147],v[124:127],a[156:159]\n"
"zt v[90:91],v24 offset:1024\nzm a[160:163],v[148:151],v[96:99],a[160:163]\nzm a[164:167],v[148:151],v[100:103],a[164:167]\nzd v[56:59],v16 offset:12288\nzm a[168:171],v[148:151],v[104:107],a[168:171]\nzm a[172:175],v[148:151],v[108:111],a[172:175]\nzt v[92:93],v25 offset:0\nzm a[176:179],v[148:151],v[112:115],a[176:179]\n"
"zm a[180:183],v[148:151],v[116:119],a[180:183]\nzt v[94:95],v25 offset:1024\nzm a[184:187],v[148:151],v[120:123],a[184:187]\nzm a[188:191],v[148:151],v[124:127],a[188:191]\nzd v[60:63],v16 offset:14336\nzm a[192:195],v[152:155],v[96:99],a[192:195]\nzm a[196:199],v[152:155],v[100:103],a[196:199]\nzm a[200:203],v[152:155],v[104:107],a[200:203]\n"
"zm a[204:207],v[152:155],v[108:111],a[204:207]\nzm a[208:211],v[152:155],v[112:115],a[208:211]\nzm a[212:215],v[152:155],v[116:119],a[212:215]\nzm a[216:219],v[152:155],v[120:123],a[216:219]\nzm a[220:223],v[152:155],v[124:127],a[220:223]\nzm a[224:227],v[156:159],v[96:99],a[224:227]\nzm a[228:231],v[156:159],v[100:103],a[228:231]\nzm a[232:235],v[156:159],v[104:107],a[232:235]\n"
"zm a[236:239],v[156:159],v[108:111],a[236:239]\nzm a[240:243],v[156:159],v[112:115],a[240:243]\nzm a[244:247],v[156:159],v[116:119],a[244:247]\nzm a[248:251],v[156:159],v[120:123],a[248:251]\nzp s62,s62,128\nzp s63,s63,128\nzp s64,s64,s74\nzm a[252:255],v[156:159],v[124:127],a[252:255]\n"
"s_xor_b32 s65,s65,0x8000\ns_xor_b32 s66,s66,0x8000\nzv lgkmcnt(0)\nzm a[0:3],v[64:67],v[32:35],a[0:3]\nzm a[4:7],v[64:67],v[36:39],a[4:7]\nzt v[128:129],v18 offset:8192\nzm a[8:11],v[64:67],v[40:43],a[8:11]\nzm a[12:15],v[64:67],v[44:47],a[12:15]\n"
"zt v[130:131],v18 offset:9216\nzm a[16:19],v[64:67],v[48:51],a[16:19]\nzm a[20:23],v[64:67],v[52:55],a[20:23]\nzd v[96:99],v17 offset:0\nzm a[24:27],v[64:67],v[56:59],a[24:27]\nzm a[28:31],v[64:67],v[60:63],a[28:31]\nzt v[132:133],v19 offset:8192\nzm a[32:35],v[68:71],v[32:35],a[32:35]\n"
"zm a[36:39],v[68:71],v[36:39],a[36:39]\nzt v[134:135],v19 offset:9216\nzm a[40:43],v[68:71],v[40:43],a[40:43]\nzm a[44:47],v[68:71],v[44:47],a[44:47]\nzd v[100:103],v17 offset:2048\nzm a[48:51],v[68:71],v[48:51],a[48:51]\nzm a[52:55],v[68:71],v[52:55],a[52:55]\nzt v[136:137],v20 offset:8192\n"
"zm a[56:59],v[68:71],v[56:59],a[56:59]\nzm a[60:63],v[68:71],v[60:63],a[60:63]\nzt v[138:139],v20 offset:9216\nzm a[64:67],v[72:75],v[32:35],a[64:67]\nzm a[68:71],v[72:75],v[36:39],a[68:71]\nzd v[104:107],v17 offset:4096\nzm a[72:75],v[72:75],v[40:43],a[72:75]\nzm a[76:79],v[72:75],v[44:47],a[76:79]\n"
"zt v[140:141],v21 offset:8192\nzm a[80:83],v[72:75],v[48:51],a[80:83]\nzm a[84:87],v[72:75],v[52:55],a[84:87]\nzt v[142:143],v21 offset:9216\nzm a[88:91],v[72:75],v[56:59],a[88:91]\nzm a[92:95],v[72:75],v[60:63],a[92:95]\nzd v[108:111],v17 offset:6144\nzm a[96:99],v[76:79],v[32:35],a[96:99]\n"
"zm a[100:103],v[76:79],v[36:39],a[100:103]\nzt v[144:145],v22 offset:8192\nzm a[104:107],v[76:79],v[40:43],a[104:107]\nzm a[108:111],v[76:79],v[44:47],a[108:111]\nzt v[146:147],v22 offset:9216\nzm a[112:115],v[76:79],v[48:51],a[112:115]\nzm a[116:119],v[76:79],v[52:55],a[116:119]\nzd v[112:115],v17 offset:8192\n"
"zm a[120:123],v[76:79],v[56:59],a[120:123]\nzm a[124:127],v[76:79],v[60:63],a[124:127]\nzt v[148:149],v23 offset:8192\nzm a[128:131],v[80:83],v[32:35],a[128:131]\nzm a[132:135],v[80:83],v[36:39],a[132:135]\nzt v[150:151],v23 offset:9216\nzm a[136:139],v[80:83],v[40:43],a[136:139]\nzm a[140:143],v[80:83],v[44:47],a[140:143]\n"
"zd v[116:119],v17 offset:10240\nzm a[144:147],v[80:83],v[48:51],a[144:147]\nzm a[148:151],v[80:83],v[52:55],a[148:151]\nzt v[152:153],v24 offset:8192\nzm a[152:155],v[80:83],v[56:59],a[152:155]\nzm a[156:159],v[80:83],v[60:63],a[156:159]\nzt v[154:155],v24 offset:9216\nzm a[160:163],v[84:87],v[32:35],a[160:163]\n"
"zm a[164:167],v[84:87],v[36:39],a[164:167]\nzd v[120:123],v17 offset:12288\nzm a[168:171],v[84:87],v[40:43],a[168:171]\nzm a[172:175],v[84:87],v[44:47],a[172:175]\nzt v[156:157],v25 offset:8192\nzm a[176:179],v[84:87],v[48:51],a[176:179]\nzm a[180:183],v[84:87],v[52:55],a[180:183]\nzt v[158:159],v25 offset:9216\n"
"zm a[184:187],v[84:87],v[56:59],a[184:187]\nzm a[188:191],v[84:87],v[60:63],a[188:191]\nzd v[124:127],v17 offset:14336\nzm a[192:195],v[88:91],v[32:35],a[192:195]\nzm a[196:199],v[88:91],v[36:39],a[196:199]\nzm a[200:203],v[88:91],v[40:43],a[200:203]\nzm a[204:207],v[88:91],v[44:47],a[204:207]\nzm a[208:211],v[88:91],v[48:51],a[208:211]\n"
"zo v16,0x8000,v16\nzm a[212:215],v[88:91],v[52:55],a[212:215]\nzo v17,0x8000,v17\nzm a[216:219],v[88:91],v[56:59],a[216:219]\nzo v18,0x8000,v18\nzm a[220:223],v[88:91],v[60:63],a[220:223]\nzo v19,0x8000,v19\nzm a[224:227],v[92:95],v[32:35],a[224:227]\n"
"zo v20,0x8000,v20\nzm a[228:231],v[92:95],v[36:39],a[228:231]\nzo v21,0x8000,v21\nzm a[232:235],v[92:95],v[40:43],a[232:235]\nzo v22,0x8000,v22\nzm a[236:239],v[92:95],v[44:47],a[236:239]\nzo v23,0x8000,v23\nzm a[240:243],v[92:95],v[48:51],a[240:243]\n"
"zo v24,0x8000,v24\nzm a[244:247],v[92:95],v[52:55],a[244:247]\nzo v25,0x8000,v25\nzm a[248:251],v[92:95],v[56:59],a[248:251]\nzm a[252:255],v[92:95],v[60:63],a[252:255]\nzv vmcnt(0) lgkmcnt(0)\ns_barrier\ns_cmp_eq_u32 s23,0\n"
"s_cbranch_scc0 L_nf_kvk\nv_mov_b32 v26,s79\nv_mov_b32 v27,s34\nglobal_store_dword v26,v27,s[94:95] sc1\nL_nf_kvk:\nzm a[0:3],v[128:131],v[96:99],a[0:3]\nzm a[4:7],v[128:131],v[100:103],a[4:7]\nzt v[64:65],v18 offset:0\n"
"zm a[8:11],v[128:131],v[104:107],a[8:11]\nzm a[12:15],v[128:131],v[108:111],a[12:15]\nzt v[66:67],v18 offset:1024\nzm a[16:19],v[128:131],v[112:115],a[16:19]\nzm a[20:23],v[128:131],v[116:119],a[20:23]\nzd v[32:35],v16 offset:0\nzm a[24:27],v[128:131],v[120:123],a[24:27]\nzm a[28:31],v[128:131],v[124:127],a[28:31]\n"
"zt v[68:69],v19 offset:0\nzm a[32:35],v[132:135],v[96:99],a[32:35]\nzm a[36:39],v[132:135],v[100:103],a[36:39]\nzt v[70:71],v19 offset:1024\nzm a[40:43],v[132:135],v[104:107],a[40:43]\nzm a[44:47],v[132:135],v[108:111],a[44:47]\nzd v[36:39],v16 offset:2048\nzm a[48:51],v[132:135],v[112:115],a[48:51]\n"
"zm a[52:55],v[132:135],v[116:119],a[52:55]\nzt v[72:73],v20 offset:0\nzm a[56:59],v[132:135],v[120:123],a[56:59]\nzm a[60:63],v[132:135],v[124:127],a[60:63]\nzt v[74:75],v20 offset:1024\nzm a[64:67],v[136:139],v[96:99],a[64:67]\nzm a[68:71],v[136:139],v[100:103],a[68:71]\nzd v[40:43],v16 offset:4096\n"
"zm a[72:75],v[136:139],v[104:107],a[72:75]\nzm a[76:79],v[136:139],v[108:111],a[76:79]\nzt v[76:77],v21 offset:0\nzm a[80:83],v[136:139],v[112:115],a[80:83]\nzm a[84:87],v[136:139],v[116:119],a[84:87]\nzt v[78:79],v21 offset:1024\nzm a[88:91],v[136:139],v[120:123],a[88:91]\nzm a[92:95],v[136:139],v[124:127],a[92:95]\n"
"zd v[44:47],v16 offset:6144\nzm a[96:99],v[140:143],v[96:99],a[96:99]\nzm a[100:103],v[140:143],v[100:103],a[100:103]\nzt v[80:81],v22 offset:0\nzm a[104:107],v[140:143],v[104:107],a[104:107]\nzm a[108:111],v[140:143],v[108:111],a[108:111]\nzt v[82:83],v22 offset:1024\nzm a[112:115],v[140:143],v[112:115],a[112:115]\n"
"zm a[116:119],v[140:143],v[116:119],a[116:119]\nzd v[48:51],v16 offset:8192\nzm a[120:123],v[140:143],v[120:123],a[120:123]\nzm a[124:127],v[140:143],v[124:127],a[124:127]\nzt v[84:85],v23 offset:0\nzm a[128:131],v[144:147],v[96:99],a[128:131]\nzm a[132:135],v[144:147],v[100:103],a[132:135]\nzt v[86:87],v23 offset:1024\n"
"zm a[136:139],v[144:147],v[104:107],a[136:139]\nzm a[140:143],v[144:147],v[108:111],a[140:143]\nzd v[52:55],v16 offset:10240\nzm a[144:147],v[144:147],v[112:115],a[144:147]\nzm a[148:151],v[144:147],v[116:119],a[148:151]\nzt v[88:89],v24 offset:0\nzm a[152:155],v[144:147],v[120:123],a[152:155]\nzm a[156:159],v[144:147],v[124:127],a[156:159]\n"
"zt v[90:91],v24 offset:1024\nzm a[160:163],v[148:151],v[96:99],a[160:163]\nzm a[164:167],v[148:151],v[100:103],a[164:167]\nzd v[56:59],v16 offset:12288\nzm a[168:171],v[148:151],v[104:107],a[168:171]\nzm a[172:175],v[148:151],v[108:111],a[172:175]\nzt v[92:93],v25 offset:0\nzm a[176:179],v[148:151],v[112:115],a[176:179]\n"
"zm a[180:183],v[148:151],v[116:119],a[180:183]\nzt v[94:95],v25 offset:1024\nzm a[184:187],v[148:151],v[120:123],a[184:187]\nzm a[188:191],v[148:151],v[124:127],a[188:191]\nzd v[60:63],v16 offset:14336\nzm a[192:195],v[152:155],v[96:99],a[192:195]\nzm a[196:199],v[152:155],v[100:103],a[196:199]\nzm a[200:203],v[152:155],v[104:107],a[200:203]\n"
"zm a[204:207],v[152:155],v[108:111],a[204:207]\nzm a[208:211],v[152:155],v[112:115],a[208:211]\nzm a[212:215],v[152:155],v[116:119],a[212:215]\nzm a[216:219],v[152:155],v[120:123],a[216:219]\nzm a[220:223],v[152:155],v[124:127],a[220:223]\nzm a[224:227],v[156:159],v[96:99],a[224:227]\nzm a[228:231],v[156:159],v[100:103],a[228:231]\nzm a[232:235],v[156:159],v[104:107],a[232:235]\n"
"zm a[236:239],v[156:159],v[108:111],a[236:239]\nzm a[240:243],v[156:159],v[112:115],a[240:243]\nzm a[244:247],v[156:159],v[116:119],a[244:247]\nzm a[248:251],v[156:159],v[120:123],a[248:251]\nzp s62,s62,128\nzp s63,s63,128\nzp s64,s64,s74\nzm a[252:255],v[156:159],v[124:127],a[252:255]\n"
"s_xor_b32 s65,s65,0x8000\ns_xor_b32 s66,s66,0x8000\nzv lgkmcnt(0)\ns_branch L_P_t2e\nL_P_v0:\nzm a[0:3],v[32:35],v[64:67],0\nzh v168,16,v160\nzm a[4:7],v[36:39],v[64:67],0\n"
"zt v[128:129],v18 offset:8192\nzh v180,16,v164\nzm a[8:11],v[40:43],v[64:67],0\nzm a[12:15],v[44:47],v[64:67],0\nzt v[130:131],v18 offset:9216\nv_and_b32 v169,0xffff0000,v160\nzm a[16:19],v[48:51],v[64:67],0\nzm a[20:23],v[52:55],v[64:67],0\n"
"zd v[96:99],v17 offset:0\nv_and_b32 v181,0xffff0000,v164\nzm a[24:27],v[56:59],v[64:67],0\nzm a[28:31],v[60:63],v[64:67],0\nzt v[132:133],v19 offset:8192\nzh v170,16,v161\nzm a[32:35],v[32:35],v[68:71],0\nzm a[36:39],v[36:39],v[68:71],0\n"
"zt v[134:135],v19 offset:9216\nzh v182,16,v165\nzm a[40:43],v[40:43],v[68:71],0\nv_and_b32 v171,0xffff0000,v161\nzm a[44:47],v[44:47],v[68:71],0\nzd v[100:103],v17 offset:2048\nzm a[48:51],v[48:51],v[68:71],0\nv_and_b32 v183,0xffff0000,v165\n"
"zm a[52:55],v[52:55],v[68:71],0\nzt v[136:137],v20 offset:8192\nzm a[56:59],v[56:59],v[68:71],0\nzh v172,16,v162\nzm a[60:63],v[60:63],v[68:71],0\nzt v[138:139],v20 offset:9216\nzm a[64:67],v[32:35],v[72:75],0\nzh v184,16,v166\n"
"zm a[68:71],v[36:39],v[72:75],0\nzd v[104:107],v17 offset:4096\nzm a[72:75],v[40:43],v[72:75],0\nv_and_b32 v173,0xffff0000,v162\nzm a[76:79],v[44:47],v[72:75],0\nzt v[140:141],v21 offset:8192\nv_and_b32 v185,0xffff0000,v166\nzm a[80:83],v[48:51],v[72:75],0\n"
"zm a[84:87],v[52:55],v[72:75],0\nzt v[142:143],v21 offset:9216\nzh v174,16,v163\nzm a[88:91],v[56:59],v[72:75],0\nzm a[92:95],v[60:63],v[72:75],0\nzd v[108:111],v17 offset:6144\nzh v186,16,v167\nzm a[96:99],v[32:35],v[76:79],0\n"
"zm a[100:103],v[36:39],v[76:79],0\nzt v[144:145],v22 offset:8192\nv_and_b32 v175,0xffff0000,v163\nzm a[104:107],v[40:43],v[76:79],0\nzm a[108:111],v[44:47],v[76:79],0\nzt v[146:147],v22 offset:9216\nv_and_b32 v187,0xffff0000,v167\nzm a[112:115],v[48:51],v[76:79],0\n"
"za v192,v168,v169\nzm a[116:119],v[52:55],v[76:79],0\nzd v[112:115],v17 offset:8192\nzm a[120:123],v[56:59],v[76:79],0\nza v196,v180,v181\nzm a[124:127],v[60:63],v[76:79],0\nzt v[148:149],v23 offset:8192\nzm a[128:131],v[32:35],v[80:83],0\n"
"za v192,v192,v170\nzm a[132:135],v[36:39],v[80:83],0\nzt v[150:151],v23 offset:9216\nzm a[136:139],v[40:43],v[80:83],0\nza v196,v196,v182\nzm a[140:143],v[44:47],v[80:83],0\nzd v[116:119],v17 offset:10240\nzm a[144:147],v[48:51],v[80:83],0\n"
"za v192,v192,v171\nzm a[148:151],v[52:55],v[80:83],0\nzt v[152:153],v24 offset:8192\nza v196,v196,v183\nzm a[152:155],v[56:59],v[80:83],0\nzm a[156:159],v[60:63],v[80:83],0\nzt v[154:155],v24 offset:9216\nza v192,v192,v172\n"
"zm a[160:163],v[32:35],v[84:87],0\nzm a[164:167],v[36:39],v[84:87],0\nzd v[120:123],v17 offset:12288\nza v196,v196,v184\nzm a[168:171],v[40:43],v[84:87],0\nzm a[172:175],v[44:47],v[84:87],0\nzt v[156:157],v25 offset:8192\nza v192,v192,v173\n"
"zm a[176:179],v[48:51],v[84:87],0\nzm a[180:183],v[52:55],v[84:87],0\nzt v[158:159],v25 offset:9216\nza v196,v196,v185\nzm a[184:187],v[56:59],v[84:87],0\nza v192,v192,v174\nzm a[188:191],v[60:63],v[84:87],0\nzd v[124:127],v17 offset:14336\n"
"zm a[192:195],v[32:35],v[88:91],0\nza v196,v196,v186\nzm a[196:199],v[36:39],v[88:91],0\nzm a[200:203],v[40:43],v[88:91],0\nza v192,v192,v175\nzm a[204:207],v[44:47],v[88:91],0\nzm a[208:211],v[48:51],v[88:91],0\nzo v16,0x8000,v16\n"
"za v196,v196,v187\nzm a[212:215],v[52:55],v[88:91],0\nzo v17,0x8000,v17\nzm a[216:219],v[56:59],v[88:91],0\nzo v18,0x8000,v18\ns_nop 1\nzm a[220:223],v[60:63],v[88:91],0\nzo v19,0x8000,v19\n"
"v_add_f32_dpp v192,v192,v192 row_ror:8 row_mask:0xf bank_mask:0xf\nzm a[224:227],v[32:35],v[92:95],0\nzo v20,0x8000,v20\nzm a[228:231],v[36:39],v[92:95],0\nzo v21,0x8000,v21\nv_add_f32_dpp v196,v196,v196 row_ror:8 row_mask:0xf bank_mask:0xf\nzm a[232:235],v[40:43],v[92:95],0\nzo v22,0x8000,v22\n"
"zm a[236:239],v[44:47],v[92:95],0\nzo v23,0x8000,v23\ns_nop 1\nzm a[240:243],v[48:51],v[92:95],0\nzo v24,0x8000,v24\nzm a[244:247],v[52:55],v[92:95],0\nzo v25,0x8000,v25\nv_add_f32_dpp v192,v192,v192 row_ror:4 row_mask:0xf bank_mask:0xf\n"
"zm a[248:251],v[56:59],v[92:95],0\nzm a[252:255],v[60:63],v[92:95],0\nzv vmcnt(3) lgkmcnt(0)\ns_barrier\nzm a[0:3],v[96:99],v[128:131],a[0:3]\nzt v[64:65],v18 offset:0\ns_mov_b32 m0,s65\nzm a[4:7],v[100:103],v[128:131],a[4:7]\n"
"zl v8,s[24:27],s62 offen offset:0 sc1 lds\nzm a[8:11],v[104:107],v[128:131],a[8:11]\nzt v[66:67],v18 offset:1024\nzm a[12:15],v[108:111],v[128:131],a[12:15]\nzm a[16:19],v[112:115],v[128:131],a[16:19]\nzl v9,s[24:27],s62 offen offset:1024 sc1 lds\nzm a[20:23],v[116:119],v[128:131],a[20:23]\nzd v[32:35],v16 offset:0\n"
"zm a[24:27],v[120:123],v[128:131],a[24:27]\nzt v[68:69],v19 offset:0\nzm a[28:31],v[124:127],v[128:131],a[28:31]\nzm a[32:35],v[96:99],v[132:135],a[32:35]\nzl v10,s[24:27],s62 offen offset:2048 sc1 lds\nv_add_f32_dpp v196,v196,v196 row_ror:4 row_mask:0xf bank_mask:0xf\nzm a[36:39],v[100:103],v[132:135],a[36:39]\nzt v[70:71],v19 offset:1024\n"
"s_nop 1\nzm a[40:43],v[104:107],v[132:135],a[40:43]\nzd v[36:39],v16 offset:2048\nv_add_f32_dpp v192,v192,v192 row_ror:2 row_mask:0xf bank_mask:0xf\nzm a[44:47],v[108:111],v[132:135],a[44:47]\nzm a[48:51],v[112:115],v[132:135],a[48:51]\nzl v11,s[24:27],s62 offen offset:3072 sc1 lds\nv_add_f32_dpp v196,v196,v196 row_ror:2 row_mask:0xf bank_mask:0xf\n"
"zm a[52:55],v[116:119],v[132:135],a[52:55]\nzt v[72:73],v20 offset:0\ns_nop 1\nzm a[56:59],v[120:123],v[132:135],a[56:59]\nzm a[60:63],v[124:127],v[132:135],a[60:63]\nzt v[74:75],v20 offset:1024\nzp m0,s65,4096\nv_add_f32_dpp v192,v192,v192 row_ror:1 row_mask:0xf bank_mask:0xf\n"
"zm a[64:67],v[96:99],v[136:139],a[64:67]\nzl v8,s[24:27],s63 offen offset:0 sc1 lds\nv_add_f32_dpp v196,v196,v196 row_ror:1 row_mask:0xf bank_mask:0xf\nzm a[68:71],v[100:103],v[136:139],a[68:71]\nzd v[40:43],v16 offset:4096\nv_mov_b32 v193,v192\nzm a[72:75],v[104:107],v[136:139],a[72:75]\nzm a[76:79],v[108:111],v[136:139],a[76:79]\n"
"zt v[76:77],v21 offset:0\nv_mov_b32 v197,v196\nzm a[80:83],v[112:115],v[136:139],a[80:83]\nzl v9,s[24:27],s63 offen offset:1024 sc1 lds\ns_nop 1\nzm a[84:87],v[116:119],v[136:139],a[84:87]\nzt v[78:79],v21 offset:1024\nzm a[88:91],v[120:123],v[136:139],a[88:91]\n"
"v_permlane16_swap_b32 v192,v193\nzm a[92:95],v[124:127],v[136:139],a[92:95]\nzl v10,s[24:27],s63 offen offset:2048 sc1 lds\nv_permlane16_swap_b32 v196,v197\nzm a[96:99],v[96:99],v[140:143],a[96:99]\nzd v[44:47],v16 offset:6144\nzm a[100:103],v[100:103],v[140:143],a[100:103]\ns_nop 1\n"
"zm a[104:107],v[104:107],v[140:143],a[104:107]\nzt v[80:81],v22 offset:0\nza v192,v192,v193\nzm a[108:111],v[108:111],v[140:143],a[108:111]\nzl v11,s[24:27],s63 offen offset:3072 sc1 lds\nza v196,v196,v197\nzm a[112:115],v[112:115],v[140:143],a[112:115]\nzt v[82:83],v22 offset:1024\n"
"zm a[116:119],v[116:119],v[140:143],a[116:119]\nv_mov_b32 v193,v192\nzm a[120:123],v[120:123],v[140:143],a[120:123]\nzd v[48:51],v16 offset:8192\ns_mov_b32 m0,s66\nv_mov_b32 v197,v196\nzm a[124:127],v[124:127],v[140:143],a[124:127]\nzl v12,s[28:31],s64 offen offset:0 lds\n"
"zm a[128:131],v[96:99],v[144:147],a[128:131]\nzt v[84:85],v23 offset:0\ns_nop 1\nzm a[132:135],v[100:103],v[144:147],a[132:135]\nv_permlane32_swap_b32 v192,v193\nzm a[136:139],v[104:107],v[144:147],a[136:139]\nzt v[86:87],v23 offset:1024\nv_permlane32_swap_b32 v196,v197\n"
"zm a[140:143],v[108:111],v[144:147],a[140:143]\nzl v13,s[28:31],s64 offen offset:1024 lds\nzm a[144:147],v[112:115],v[144:147],a[144:147]\nzd v[52:55],v16 offset:10240\ns_nop 1\nzm a[148:151],v[116:119],v[144:147],a[148:151]\nza v192,v192,v193\nzm a[152:155],v[120:123],v[144:147],a[152:155]\n"
"zt v[88:89],v24 offset:0\nzm a[156:159],v[124:127],v[144:147],a[156:159]\nzl v14,s[28:31],s64 offen offset:2048 lds\nza v196,v196,v197\nzm a[160:163],v[96:99],v[148:151],a[160:163]\nzx v192,0x3b124925,v192\nzm a[164:167],v[100:103],v[148:151],a[164:167]\nzt v[90:91],v24 offset:1024\n"
"zm a[168:171],v[104:107],v[148:151],a[168:171]\nzl v15,s[28:31],s64 offen offset:3072 lds\nzx v196,0x3b124925,v196\nzm a[172:175],v[108:111],v[148:151],a[172:175]\nzd v[56:59],v16 offset:12288\nzu v195,v168,v192\nzm a[176:179],v[112:115],v[148:151],a[176:179]\nzu v199,v180,v196\n"
"zm a[180:183],v[116:119],v[148:151],a[180:183]\nzt v[92:93],v25 offset:0\nzp m0,s66,16384\nzm a[184:187],v[120:123],v[148:151],a[184:187]\nzl v12,s[36:39],s64 offen offset:0 lds\nzx v194,v195,v195\nzm a[188:191],v[124:127],v[148:151],a[188:191]\nzt v[94:95],v25 offset:1024\n"
"zx v198,v199,v199\nzm a[192:195],v[96:99],v[152:155],a[192:195]\nzm a[196:199],v[100:103],v[152:155],a[196:199]\nzd v[60:63],v16 offset:14336\nzu v195,v169,v192\nzm a[200:203],v[104:107],v[152:155],a[200:203]\nzl v13,s[36:39],s64 offen offset:1024 lds\nzu v199,v181,v196\n"
"zm a[204:207],v[108:111],v[152:155],a[204:207]\nzf v194,v195,v195\nzm a[208:211],v[112:115],v[152:155],a[208:211]\nzm a[212:215],v[116:119],v[152:155],a[212:215]\nzf v198,v199,v199\nzm a[216:219],v[120:123],v[152:155],a[216:219]\nzl v14,s[36:39],s64 offen offset:2048 lds\nzu v195,v170,v192\n"
"zm a[220:223],v[124:127],v[152:155],a[220:223]\nzm a[224:227],v[96:99],v[156:159],a[224:227]\nzu v199,v182,v196\nzm a[228:231],v[100:103],v[156:159],a[228:231]\nzf v194,v195,v195\nzm a[232:235],v[104:107],v[156:159],a[232:235]\nzl v15,s[36:39],s64 offen offset:3072 lds\nzm a[236:239],v[108:111],v[156:159],a[236:239]\n"
"zp s93,s64,s74\nzm a[240:243],v[112:115],v[156:159],a[240:243]\nbuffer_load_dword v31,v4,s[24:27],s62 offen offset:128\nzm a[244:247],v[116:119],v[156:159],a[244:247]\nbuffer_load_dword v31,v5,s[28:31],s93 offen\nzm a[248:251],v[120:123],v[156:159],a[248:251]\nglobal_load_dword v31,v231,s[4:5]\nzp s62,s62,128\n"
"zp s63,s63,128\nzp s64,s64,s74\nzm a[252:255],v[124:127],v[156:159],a[252:255]\ns_xor_b32 s65,s65,0x8000\ns_xor_b32 s66,s66,0x8000\nzv lgkmcnt(0)\nzm a[0:3],v[32:35],v[64:67],a[0:3]\nzf v198,v199,v199\n"
"zm a[4:7],v[36:39],v[64:67],a[4:7]\nzt v[128:129],v18 offset:8192\nzu v195,v171,v192\nzm a[8:11],v[40:43],v[64:67],a[8:11]\ns_mov_b64 s[4:5],s[6:7]\nzm a[12:15],v[44:47],v[64:67],a[12:15]\nzt v[130:131],v18 offset:9216\nzu v199,v183,v196\n"
"zm a[16:19],v[48:51],v[64:67],a[16:19]\ns_mov_b64 s[6:7],s[8:9]\nzm a[20:23],v[52:55],v[64:67],a[20:23]\nzd v[96:99],v17 offset:0\nzf v194,v195,v195\nzm a[24:27],v[56:59],v[64:67],a[24:27]\ns_mov_b64 s[8:9],s[10:11]\nzm a[28:31],v[60:63],v[64:67],a[28:31]\n"
"zt v[132:133],v19 offset:8192\nzf v198,v199,v199\nzm a[32:35],v[32:35],v[68:71],a[32:35]\ns_mov_b64 s[10:11],s[12:13]\nzu v195,v172,v192\nzm a[36:39],v[36:39],v[68:71],a[36:39]\nzt v[134:135],v19 offset:9216\nzm a[40:43],v[40:43],v[68:71],a[40:43]\n"
"s_mov_b64 s[12:13],s[14:15]\nzu v199,v184,v196\nzm a[44:47],v[44:47],v[68:71],a[44:47]\nzd v[100:103],v17 offset:2048\nzm a[48:51],v[48:51],v[68:71],a[48:51]\ns_mov_b64 s[14:15],s[16:17]\nzf v194,v195,v195\nzm a[52:55],v[52:55],v[68:71],a[52:55]\n"
"zt v[136:137],v20 offset:8192\nzm a[56:59],v[56:59],v[68:71],a[56:59]\ns_mov_b64 s[16:17],s[18:19]\nzf v198,v199,v199\nzm a[60:63],v[60:63],v[68:71],a[60:63]\nzt v[138:139],v20 offset:9216\nzu v195,v173,v192\nzm a[64:67],v[32:35],v[72:75],a[64:67]\n"
"zm a[68:71],v[36:39],v[72:75],a[68:71]\nzd v[104:107],v17 offset:4096\nzu v199,v185,v196\nzm a[72:75],v[40:43],v[72:75],a[72:75]\nzm a[76:79],v[44:47],v[72:75],a[76:79]\nzt v[140:141],v21 offset:8192\nzf v194,v195,v195\nzm a[80:83],v[48:51],v[72:75],a[80:83]\n"
"zm a[84:87],v[52:55],v[72:75],a[84:87]\nzt v[142:143],v21 offset:9216\nzf v198,v199,v199\nzm a[88:91],v[56:59],v[72:75],a[88:91]\nzu v195,v174,v192\nzm a[92:95],v[60:63],v[72:75],a[92:95]\nzd v[108:111],v17 offset:6144\nzm a[96:99],v[32:35],v[76:79],a[96:99]\n"
"zu v199,v186,v196\nzm a[100:103],v[36:39],v[76:79],a[100:103]\nzt v[144:145],v22 offset:8192\nzm a[104:107],v[40:43],v[76:79],a[104:107]\nzf v194,v195,v195\nzm a[108:111],v[44:47],v[76:79],a[108:111]\nzt v[146:147],v22 offset:9216\nzm a[112:115],v[48:51],v[76:79],a[112:115]\n"
"zf v198,v199,v199\nzm a[116:119],v[52:55],v[76:79],a[116:119]\nzd v[112:115],v17 offset:8192\nzu v195,v175,v192\nzm a[120:123],v[56:59],v[76:79],a[120:123]\nzm a[124:127],v[60:63],v[76:79],a[124:127]\nzt v[148:149],v23 offset:8192\nzu v199,v187,v196\n"
"zm a[128:131],v[32:35],v[80:83],a[128:131]\nzm a[132:135],v[36:39],v[80:83],a[132:135]\nzt v[150:151],v23 offset:9216\nzf v194,v195,v195\nzm a[136:139],v[40:43],v[80:83],a[136:139]\nzm a[140:143],v[44:47],v[80:83],a[140:143]\nzd v[116:119],v17 offset:10240\nzf v198,v199,v199\n"
"zm a[144:147],v[48:51],v[80:83],a[144:147]\nzx v194,v194,v200\nzm a[148:151],v[52:55],v[80:83],a[148:151]\nzt v[152:153],v24 offset:8192\nzm a[152:155],v[56:59],v[80:83],a[152:155]\nzx v198,v198,v200\nzm a[156:159],v[60:63],v[80:83],a[156:159]\nzt v[154:155],v24 offset:9216\n"
"zm a[160:163],v[32:35],v[84:87],a[160:163]\ns_nop 1\nzm a[164:167],v[36:39],v[84:87],a[164:167]\nzd v[120:123],v17 offset:12288\nzm a[168:171],v[40:43],v[84:87],a[168:171]\nv_add_f32_dpp v194,v194,v194 row_ror:8 row_mask:0xf bank_mask:0xf\nzm a[172:175],v[44:47],v[84:87],a[172:175]\nzt v[156:157],v25 offset:8192\n"
"v_add_f32_dpp v198,v198,v198 row_ror:8 row_mask:0xf bank_mask:0xf\nzm a[176:179],v[48:51],v[84:87],a[176:179]\nzm a[180:183],v[52:55],v[84:87],a[180:183]\nzt v[158:159],v25 offset:9216\ns_nop 1\nzm a[184:187],v[56:59],v[84:87],a[184:187]\nzm a[188:191],v[60:63],v[84:87],a[188:191]\nzd v[124:127],v17 offset:14336\n"
"v_add_f32_dpp v194,v194,v194 row_ror:4 row_mask:0xf bank_mask:0xf\nzm a[192:195],v[32:35],v[88:91],a[192:195]\nzm a[196:199],v[36:39],v[88:91],a[196:199]\nv_add_f32_dpp v198,v198,v198 row_ror:4 row_mask:0xf bank_mask:0xf\nzm a[200:203],v[40:43],v[88:91],a[200:203]\ns_nop 1\nzm a[204:207],v[44:47],v[88:91],a[204:207]\nzm a[208:211],v[48:51],v[88:91],a[208:211]\n"
"zo v16,0x8000,v16\nv_add_f32_dpp v194,v194,v194 row_ror:2 row_mask:0xf bank_mask:0xf\nzm a[212:215],v[52:55],v[88:91],a[212:215]\nzo v17,0x8000,v17\nzm a[216:219],v[56:59],v[88:91],a[216:219]\nzo v18,0x8000,v18\nv_add_f32_dpp v198,v198,v198 row_ror:2 row_mask:0xf bank_mask:0xf\nzm a[220:223],v[60:63],v[88:91],a[220:223]\n"
"zo v19,0x8000,v19\nzm a[224:227],v[32:35],v[92:95],a[224:227]\nzo v20,0x8000,v20\ns_nop 1\nzm a[228:231],v[36:39],v[92:95],a[228:231]\nzo v21,0x8000,v21\nv_add_f32_dpp v194,v194,v194 row_ror:1 row_mask:0xf bank_mask:0xf\nzm a[232:235],v[40:43],v[92:95],a[232:235]\n"
"zo v22,0x8000,v22\nzm a[236:239],v[44:47],v[92:95],a[236:239]\nzo v23,0x8000,v23\nv_add_f32_dpp v198,v198,v198 row_ror:1 row_mask:0xf bank_mask:0xf\nzm a[240:243],v[48:51],v[92:95],a[240:243]\nzo v24,0x8000,v24\nzm a[244:247],v[52:55],v[92:95],a[244:247]\nzo v25,0x8000,v25\n"
"v_mov_b32 v193,v194\nzm a[248:251],v[56:59],v[92:95],a[248:251]\nzm a[252:255],v[60:63],v[92:95],a[252:255]\nzv vmcnt(3) lgkmcnt(0)\ns_barrier\nzm a[0:3],v[96:99],v[128:131],a[0:3]\nzt v[64:65],v18 offset:0\ns_mov_b32 m0,s65\n"
"zm a[4:7],v[100:103],v[128:131],a[4:7]\nzl v8,s[24:27],s62 offen offset:0 sc1 lds\nzm a[8:11],v[104:107],v[128:131],a[8:11]\nzt v[66:67],v18 offset:1024\nzm a[12:15],v[108:111],v[128:131],a[12:15]\nzm a[16:19],v[112:115],v[128:131],a[16:19]\nzl v9,s[24:27],s62 offen offset:1024 sc1 lds\nzm a[20:23],v[116:119],v[128:131],a[20:23]\n"
"zd v[32:35],v16 offset:0\nzm a[24:27],v[120:123],v[128:131],a[24:27]\nzt v[68:69],v19 offset:0\nzm a[28:31],v[124:127],v[128:131],a[28:31]\nzm a[32:35],v[96:99],v[132:135],a[32:35]\nzl v10,s[24:27],s62 offen offset:2048 sc1 lds\nv_mov_b32 v197,v198\nzm a[36:39],v[100:103],v[132:135],a[36:39]\n"
"zt v[70:71],v19 offset:1024\ns_nop 1\nzm a[40:43],v[104:107],v[132:135],a[40:43]\nzd v[36:39],v16 offset:2048\nv_permlane16_swap_b32 v194,v193\nzm a[44:47],v[108:111],v[132:135],a[44:47]\nzm a[48:51],v[112:115],v[132:135],a[48:51]\nzl v11,s[24:27],s62 offen offset:3072 sc1 lds\n"
"v_permlane16_swap_b32 v198,v197\nzm a[52:55],v[116:119],v[132:135],a[52:55]\nzt v[72:73],v20 offset:0\ns_nop 1\nzm a[56:59],v[120:123],v[132:135],a[56:59]\nzm a[60:63],v[124:127],v[132:135],a[60:63]\nzt v[74:75],v20 offset:1024\nzp m0,s65,4096\n"
"za v194,v194,v193\nzm a[64:67],v[96:99],v[136:139],a[64:67]\nzl v8,s[24:27],s63 offen offset:0 sc1 lds\nza v198,v198,v197\nzm a[68:71],v[100:103],v[136:139],a[68:71]\nzd v[40:43],v16 offset:4096\nv_mov_b32 v193,v194\nzm a[72:75],v[104:107],v[136:139],a[72:75]\n"
"zm a[76:79],v[108:111],v[136:139],a[76:79]\nzt v[76:77],v21 offset:0\nv_mov_b32 v197,v198\nzm a[80:83],v[112:115],v[136:139],a[80:83]\nzl v9,s[24:27],s63 offen offset:1024 sc1 lds\ns_nop 1\nzm a[84:87],v[116:119],v[136:139],a[84:87]\nzt v[78:79],v21 offset:1024\n"
"zm a[88:91],v[120:123],v[136:139],a[88:91]\nv_permlane32_swap_b32 v194,v193\nzm a[92:95],v[124:127],v[136:139],a[92:95]\nzl v10,s[24:27],s63 offen offset:2048 sc1 lds\nv_permlane32_swap_b32 v198,v197\nzm a[96:99],v[96:99],v[140:143],a[96:99]\nzd v[44:47],v16 offset:6144\nzm a[100:103],v[100:103],v[140:143],a[100:103]\n"
"s_nop 1\nzm a[104:107],v[104:107],v[140:143],a[104:107]\nzt v[80:81],v22 offset:0\nza v194,v194,v193\nzm a[108:111],v[108:111],v[140:143],a[108:111]\nzl v11,s[24:27],s63 offen offset:3072 sc1 lds\nza v198,v198,v197\nzm a[112:115],v[112:115],v[140:143],a[112:115]\n"
"zt v[82:83],v22 offset:1024\nzm a[116:119],v[116:119],v[140:143],a[116:119]\nzx v194,0x3b124925,v194\nzm a[120:123],v[120:123],v[140:143],a[120:123]\nzd v[48:51],v16 offset:8192\ns_mov_b32 m0,s66\nzx v198,0x3b124925,v198\nzm a[124:127],v[124:127],v[140:143],a[124:127]\n"
"zl v12,s[28:31],s64 offen offset:0 lds\nzm a[128:131],v[96:99],v[144:147],a[128:131]\nzt v[84:85],v23 offset:0\nzc v192,v192,v192\nzm a[132:135],v[100:103],v[144:147],a[132:135]\nzc v196,v196,v196\nzm a[136:139],v[104:107],v[144:147],a[136:139]\nzt v[86:87],v23 offset:1024\n"
"zh v192,16,v192\nzm a[140:143],v[108:111],v[144:147],a[140:143]\nzl v13,s[28:31],s64 offen offset:1024 lds\nzm a[144:147],v[112:115],v[144:147],a[144:147]\nzd v[52:55],v16 offset:10240\nzh v196,16,v196\nzm a[148:151],v[116:119],v[144:147],a[148:151]\nzc v194,v194,v194\n"
"zm a[152:155],v[120:123],v[144:147],a[152:155]\nzt v[88:89],v24 offset:0\nzm a[156:159],v[124:127],v[144:147],a[156:159]\nzl v14,s[28:31],s64 offen offset:2048 lds\nzc v198,v198,v198\nzm a[160:163],v[96:99],v[148:151],a[160:163]\nzh v194,16,v194\nzm a[164:167],v[100:103],v[148:151],a[164:167]\n"
"zt v[90:91],v24 offset:1024\nzm a[168:171],v[104:107],v[148:151],a[168:171]\nzl v15,s[28:31],s64 offen offset:3072 lds\nzh v198,16,v198\nzm a[172:175],v[108:111],v[148:151],a[172:175]\nzd v[56:59],v16 offset:12288\nza v194,0x3727c5ac,v194\nzm a[176:179],v[112:115],v[148:151],a[176:179]\n"
"za v198,0x3727c5ac,v198\nzm a[180:183],v[116:119],v[148:151],a[180:183]\nzt v[92:93],v25 offset:0\nzp m0,s66,16384\nzm a[184:187],v[120:123],v[148:151],a[184:187]\nzl v12,s[36:39],s64 offen offset:0 lds\nzc v194,v194,v194\nzm a[188:191],v[124:127],v[148:151],a[188:191]\n"
"zt v[94:95],v25 offset:1024\nzc v198,v198,v198\nzm a[192:195],v[96:99],v[152:155],a[192:195]\nzm a[196:199],v[100:103],v[152:155],a[196:199]\nzd v[60:63],v16 offset:14336\nzh v194,16,v194\nzm a[200:203],v[104:107],v[152:155],a[200:203]\nzl v13,s[36:39],s64 offen offset:1024 lds\n"
"zh v198,16,v198\nzm a[204:207],v[108:111],v[152:155],a[204:207]\nv_sqrt_f32 v194,v194\nzm a[208:211],v[112:115],v[152:155],a[208:211]\nzm a[212:215],v[116:119],v[152:155],a[212:215]\nv_sqrt_f32 v198,v198\nzm a[216:219],v[120:123],v[152:155],a[216:219]\nzl v14,s[36:39],s64 offen offset:2048 lds\n"
"s_nop 0\nzm a[220:223],v[124:127],v[152:155],a[220:223]\nzm a[224:227],v[96:99],v[156:159],a[224:227]\nzc v194,v194,v194\nzm a[228:231],v[100:103],v[156:159],a[228:231]\nzc v198,v198,v198\nzm a[232:235],v[104:107],v[156:159],a[232:235]\nzl v15,s[36:39],s64 offen offset:3072 lds\n"
"zm a[236:239],v[108:111],v[156:159],a[236:239]\nzp s93,s64,s74\nzm a[240:243],v[112:115],v[156:159],a[240:243]\nbuffer_load_dword v31,v4,s[24:27],s62 offen offset:128\nzm a[244:247],v[116:119],v[156:159],a[244:247]\nbuffer_load_dword v31,v5,s[28:31],s93 offen\nzm a[248:251],v[120:123],v[156:159],a[248:251]\nglobal_load_dword v31,v231,s[4:5]\n"
"zp s62,s62,128\nzp s63,s63,128\nzp s64,s64,s74\nzm a[252:255],v[124:127],v[156:159],a[252:255]\ns_xor_b32 s65,s65,0x8000\ns_xor_b32 s66,s66,0x8000\nzv lgkmcnt(0)\nzm a[0:3],v[32:35],v[64:67],a[0:3]\n"
"zh v194,16,v194\nzm a[4:7],v[36:39],v[64:67],a[4:7]\nzt v[128:129],v18 offset:8192\nzh v198,16,v198\nzm a[8:11],v[40:43],v[64:67],a[8:11]\ns_mov_b64 s[4:5],s[6:7]\nzm a[12:15],v[44:47],v[64:67],a[12:15]\nzt v[130:131],v18 offset:9216\n"
"zq v194,v194\nzm a[16:19],v[48:51],v[64:67],a[16:19]\ns_mov_b64 s[6:7],s[8:9]\nzm a[20:23],v[52:55],v[64:67],a[20:23]\nzd v[96:99],v17 offset:0\nzq v198,v198\nzm a[24:27],v[56:59],v[64:67],a[24:27]\ns_mov_b64 s[8:9],s[10:11]\n"
"zm a[28:31],v[60:63],v[64:67],a[28:31]\nzt v[132:133],v19 offset:8192\nzu v168,v168,v192\nzm a[32:35],v[32:35],v[68:71],a[32:35]\ns_mov_b64 s[10:11],s[12:13]\nzu v180,v180,v196\nzm a[36:39],v[36:39],v[68:71],a[36:39]\nzt v[134:135],v19 offset:9216\n"
"zm a[40:43],v[40:43],v[68:71],a[40:43]\ns_mov_b64 s[12:13],s[14:15]\nzu v169,v169,v192\nzm a[44:47],v[44:47],v[68:71],a[44:47]\nzd v[100:103],v17 offset:2048\nzm a[48:51],v[48:51],v[68:71],a[48:51]\ns_mov_b64 s[14:15],s[16:17]\nzu v181,v181,v196\n"
"zm a[52:55],v[52:55],v[68:71],a[52:55]\nzt v[136:137],v20 offset:8192\nzm a[56:59],v[56:59],v[68:71],a[56:59]\ns_mov_b64 s[16:17],s[18:19]\nzu v170,v170,v192\nzm a[60:63],v[60:63],v[68:71],a[60:63]\nzt v[138:139],v20 offset:9216\nzu v182,v182,v196\n"
"zm a[64:67],v[32:35],v[72:75],a[64:67]\nzm a[68:71],v[36:39],v[72:75],a[68:71]\nzd v[104:107],v17 offset:4096\nzu v171,v171,v192\nzm a[72:75],v[40:43],v[72:75],a[72:75]\nzm a[76:79],v[44:47],v[72:75],a[76:79]\nzt v[140:141],v21 offset:8192\nzu v183,v183,v196\n"
"zm a[80:83],v[48:51],v[72:75],a[80:83]\nzm a[84:87],v[52:55],v[72:75],a[84:87]\nzt v[142:143],v21 offset:9216\nzu v172,v172,v192\nzm a[88:91],v[56:59],v[72:75],a[88:91]\nzu v184,v184,v196\nzm a[92:95],v[60:63],v[72:75],a[92:95]\nzd v[108:111],v17 offset:6144\n"
"zm a[96:99],v[32:35],v[76:79],a[96:99]\nzu v173,v173,v192\nzm a[100:103],v[36:39],v[76:79],a[100:103]\nzt v[144:145],v22 offset:8192\nzm a[104:107],v[40:43],v[76:79],a[104:107]\nzu v185,v185,v196\nzm a[108:111],v[44:47],v[76:79],a[108:111]\nzt v[146:147],v22 offset:9216\n"
"zm a[112:115],v[48:51],v[76:79],a[112:115]\nzu v174,v174,v192\nzm a[116:119],v[52:55],v[76:79],a[116:119]\nzd v[112:115],v17 offset:8192\nzu v186,v186,v196\nzm a[120:123],v[56:59],v[76:79],a[120:123]\nzm a[124:127],v[60:63],v[76:79],a[124:127]\nzt v[148:149],v23 offset:8192\n"
"zu v175,v175,v192\nzm a[128:131],v[32:35],v[80:83],a[128:131]\nzm a[132:135],v[36:39],v[80:83],a[132:135]\nzt v[150:151],v23 offset:9216\nzu v187,v187,v196\nzm a[136:139],v[40:43],v[80:83],a[136:139]\nzm a[140:143],v[44:47],v[80:83],a[140:143]\nzd v[116:119],v17 offset:10240\n"
"zc v176,v168,v169\nzm a[144:147],v[48:51],v[80:83],a[144:147]\nzc v188,v180,v181\nzm a[148:151],v[52:55],v[80:83],a[148:151]\nzt v[152:153],v24 offset:8192\nzm a[152:155],v[56:59],v[80:83],a[152:155]\nzc v177,v170,v171\nzm a[156:159],v[60:63],v[80:83],a[156:159]\n"
"zt v[154:155],v24 offset:9216\nzm a[160:163],v[32:35],v[84:87],a[160:163]\nzc v189,v182,v183\nzm a[164:167],v[36:39],v[84:87],a[164:167]\nzd v[120:123],v17 offset:12288\nzm a[168:171],v[40:43],v[84:87],a[168:171]\nzc v178,v172,v173\nzm a[172:175],v[44:47],v[84:87],a[172:175]\n"
"zt v[156:157],v25 offset:8192\nzc v190,v184,v185\nzm a[176:179],v[48:51],v[84:87],a[176:179]\nzm a[180:183],v[52:55],v[84:87],a[180:183]\nzt v[158:159],v25 offset:9216\nzc v179,v174,v175\nzm a[184:187],v[56:59],v[84:87],a[184:187]\nzm a[188:191],v[60:63],v[84:87],a[188:191]\n"
"zd v[124:127],v17 offset:14336\nzc v191,v186,v187\nzm a[192:195],v[32:35],v[88:91],a[192:195]\nzm a[196:199],v[36:39],v[88:91],a[196:199]\nzh v168,16,v176\nzm a[200:203],v[40:43],v[88:91],a[200:203]\nzh v180,16,v188\nzm a[204:207],v[44:47],v[88:91],a[204:207]\n"
"zm a[208:211],v[48:51],v[88:91],a[208:211]\nzo v16,0x8000,v16\nv_and_b32 v169,0xffff0000,v176\nzm a[212:215],v[52:55],v[88:91],a[212:215]\nzo v17,0x8000,v17\nzm a[216:219],v[56:59],v[88:91],a[216:219]\nzo v18,0x8000,v18\nv_and_b32 v181,0xffff0000,v188\n"
"zm a[220:223],v[60:63],v[88:91],a[220:223]\nzo v19,0x8000,v19\nzm a[224:227],v[32:35],v[92:95],a[224:227]\nzo v20,0x8000,v20\nzh v170,16,v177\nzm a[228:231],v[36:39],v[92:95],a[228:231]\nzo v21,0x8000,v21\nzh v182,16,v189\n"
"zm a[232:235],v[40:43],v[92:95],a[232:235]\nzo v22,0x8000,v22\nzm a[236:239],v[44:47],v[92:95],a[236:239]\nzo v23,0x8000,v23\nv_and_b32 v171,0xffff0000,v177\nzm a[240:243],v[48:51],v[92:95],a[240:243]\nzo v24,0x8000,v24\nzm a[244:247],v[52:55],v[92:95],a[244:247]\n"
"zo v25,0x8000,v25\nv_and_b32 v183,0xffff0000,v189\nzm a[248:251],v[56:59],v[92:95],a[248:251]\nzm a[252:255],v[60:63],v[92:95],a[252:255]\nzv vmcnt(3) lgkmcnt(0)\ns_barrier\nzm a[0:3],v[96:99],v[128:131],a[0:3]\nzt v[64:65],v18 offset:0\n"
"s_mov_b32 m0,s65\nzm a[4:7],v[100:103],v[128:131],a[4:7]\nzl v8,s[24:27],s62 offen offset:0 sc1 lds\nzm a[8:11],v[104:107],v[128:131],a[8:11]\nzt v[66:67],v18 offset:1024\nzm a[12:15],v[108:111],v[128:131],a[12:15]\nzm a[16:19],v[112:115],v[128:131],a[16:19]\nzl v9,s[24:27],s62 offen offset:1024 sc1 lds\n"
"zm a[20:23],v[116:119],v[128:131],a[20:23]\nzd v[32:35],v16 offset:0\nzm a[24:27],v[120:123],v[128:131],a[24:27]\nzt v[68:69],v19 offset:0\nzm a[28:31],v[124:127],v[128:131],a[28:31]\nzm a[32:35],v[96:99],v[132:135],a[32:35]\nzl v10,s[24:27],s62 offen offset:2048 sc1 lds\nzh v172,16,v178\n"
"zm a[36:39],v[100:103],v[132:135],a[36:39]\nzt v[70:71],v19 offset:1024\nzh v184,16,v190\nzm a[40:43],v[104:107],v[132:135],a[40:43]\nzd v[36:39],v16 offset:2048\nv_and_b32 v173,0xffff0000,v178\nzm a[44:47],v[108:111],v[132:135],a[44:47]\nzm a[48:51],v[112:115],v[132:135],a[48:51]\n"
"zl v11,s[24:27],s62 offen offset:3072 sc1 lds\nv_and_b32 v185,0xffff0000,v190\nzm a[52:55],v[116:119],v[132:135],a[52:55]\nzt v[72:73],v20 offset:0\nzh v174,16,v179\nzm a[56:59],v[120:123],v[132:135],a[56:59]\nzm a[60:63],v[124:127],v[132:135],a[60:63]\nzt v[74:75],v20 offset:1024\n"
"zp m0,s65,4096\nzh v186,16,v191\nzm a[64:67],v[96:99],v[136:139],a[64:67]\nzl v8,s[24:27],s63 offen offset:0 sc1 lds\nv_and_b32 v175,0xffff0000,v179\nzm a[68:71],v[100:103],v[136:139],a[68:71]\nzd v[40:43],v16 offset:4096\nv_and_b32 v187,0xffff0000,v191\n"
"zm a[72:75],v[104:107],v[136:139],a[72:75]\nzm a[76:79],v[108:111],v[136:139],a[76:79]\nzt v[76:77],v21 offset:0\nzx v168,v168,v194\nzm a[80:83],v[112:115],v[136:139],a[80:83]\nzl v9,s[24:27],s63 offen offset:1024 sc1 lds\nzx v180,v180,v198\nzm a[84:87],v[116:119],v[136:139],a[84:87]\n"
"zt v[78:79],v21 offset:1024\nzm a[88:91],v[120:123],v[136:139],a[88:91]\nzx v169,v169,v194\nzm a[92:95],v[124:127],v[136:139],a[92:95]\nzl v10,s[24:27],s63 offen offset:2048 sc1 lds\nzx v181,v181,v198\nzm a[96:99],v[96:99],v[140:143],a[96:99]\nzd v[44:47],v16 offset:6144\n"
"zm a[100:103],v[100:103],v[140:143],a[100:103]\nzx v170,v170,v194\nzm a[104:107],v[104:107],v[140:143],a[104:107]\nzt v[80:81],v22 offset:0\nzx v182,v182,v198\nzm a[108:111],v[108:111],v[140:143],a[108:111]\nzl v11,s[24:27],s63 offen offset:3072 sc1 lds\nzx v171,v171,v194\n"
"zm a[112:115],v[112:115],v[140:143],a[112:115]\nzt v[82:83],v22 offset:1024\nzm a[116:119],v[116:119],v[140:143],a[116:119]\nzx v183,v183,v198\nzm a[120:123],v[120:123],v[140:143],a[120:123]\nzd v[48:51],v16 offset:8192\ns_mov_b32 m0,s66\nzx v172,v172,v194\n"
"zm a[124:127],v[124:127],v[140:143],a[124:127]\nzl v12,s[28:31],s64 offen offset:0 lds\nzm a[128:131],v[96:99],v[144:147],a[128:131]\nzt v[84:85],v23 offset:0\nzx v184,v184,v198\nzm a[132:135],v[100:103],v[144:147],a[132:135]\nzx v173,v173,v194\nzm a[136:139],v[104:107],v[144:147],a[136:139]\n"
"zt v[86:87],v23 offset:1024\nzx v185,v185,v198\nzm a[140:143],v[108:111],v[144:147],a[140:143]\nzl v13,s[28:31],s64 offen offset:1024 lds\nzm a[144:147],v[112:115],v[144:147],a[144:147]\nzd v[52:55],v16 offset:10240\nzx v174,v174,v194\nzm a[148:151],v[116:119],v[144:147],a[148:151]\n"
"zx v186,v186,v198\nzm a[152:155],v[120:123],v[144:147],a[152:155]\nzt v[88:89],v24 offset:0\nzm a[156:159],v[124:127],v[144:147],a[156:159]\nzl v14,s[28:31],s64 offen offset:2048 lds\nzx v175,v175,v194\nzm a[160:163],v[96:99],v[148:151],a[160:163]\nzx v187,v187,v198\n"
"zm a[164:167],v[100:103],v[148:151],a[164:167]\nzt v[90:91],v24 offset:1024\nzm a[168:171],v[104:107],v[148:151],a[168:171]\nzl v15,s[28:31],s64 offen offset:3072 lds\nzc v176,v168,v169\nzm a[172:175],v[108:111],v[148:151],a[172:175]\nzd v[56:59],v16 offset:12288\nzc v188,v180,v181\n"
"zm a[176:179],v[112:115],v[148:151],a[176:179]\nzc v177,v170,v171\nzm a[180:183],v[116:119],v[148:151],a[180:183]\nzt v[92:93],v25 offset:0\nzp m0,s66,16384\nzm a[184:187],v[120:123],v[148:151],a[184:187]\nzl v12,s[36:39],s64 offen offset:0 lds\nzc v189,v182,v183\n"
"zm a[188:191],v[124:127],v[148:151],a[188:191]\nzt v[94:95],v25 offset:1024\nzc v178,v172,v173\nzm a[192:195],v[96:99],v[152:155],a[192:195]\nzm a[196:199],v[100:103],v[152:155],a[196:199]\nzd v[60:63],v16 offset:14336\nzc v190,v184,v185\nzm a[200:203],v[104:107],v[152:155],a[200:203]\n"
"zl v13,s[36:39],s64 offen offset:1024 lds\nzc v179,v174,v175\nzm a[204:207],v[108:111],v[152:155],a[204:207]\nzc v191,v186,v187\nzm a[208:211],v[112:115],v[152:155],a[208:211]\nzm a[212:215],v[116:119],v[152:155],a[212:215]\nzh v168,16,v176\nzm a[216:219],v[120:123],v[152:155],a[216:219]\n"
"zl v14,s[36:39],s64 offen offset:2048 lds\nzh v180,16,v188\nzm a[220:223],v[124:127],v[152:155],a[220:223]\nzm a[224:227],v[96:99],v[156:159],a[224:227]\nv_and_b32 v169,0xffff0000,v176\nzm a[228:231],v[100:103],v[156:159],a[228:231]\nv_and_b32 v181,0xffff0000,v188\nzm a[232:235],v[104:107],v[156:159],a[232:235]\n"
"zl v15,s[36:39],s64 offen offset:3072 lds\nzm a[236:239],v[108:111],v[156:159],a[236:239]\nzp s93,s64,s74\nzm a[240:243],v[112:115],v[156:159],a[240:243]\nbuffer_load_dword v31,v4,s[24:27],s62 offen offset:128\nzm a[244:247],v[116:119],v[156:159],a[244:247]\nbuffer_load_dword v31,v5,s[28:31],s93 offen\nzm a[248:251],v[120:123],v[156:159],a[248:251]\n"
"global_load_dword v31,v231,s[4:5]\nzp s62,s62,128\nzp s63,s63,128\nzp s64,s64,s74\nzm a[252:255],v[124:127],v[156:159],a[252:255]\ns_xor_b32 s65,s65,0x8000\ns_xor_b32 s66,s66,0x8000\nzv lgkmcnt(0)\n"
"zm a[0:3],v[32:35],v[64:67],a[0:3]\nzh v170,16,v177\nzm a[4:7],v[36:39],v[64:67],a[4:7]\nzt v[128:129],v18 offset:8192\nzh v182,16,v189\nzm a[8:11],v[40:43],v[64:67],a[8:11]\ns_mov_b64 s[4:5],s[6:7]\nzm a[12:15],v[44:47],v[64:67],a[12:15]\n"
"zt v[130:131],v18 offset:9216\nv_and_b32 v171,0xffff0000,v177\nzm a[16:19],v[48:51],v[64:67],a[16:19]\ns_mov_b64 s[6:7],s[8:9]\nzm a[20:23],v[52:55],v[64:67],a[20:23]\nzd v[96:99],v17 offset:0\nv_and_b32 v183,0xffff0000,v189\nzm a[24:27],v[56:59],v[64:67],a[24:27]\n"
"s_mov_b64 s[8:9],s[10:11]\nzm a[28:31],v[60:63],v[64:67],a[28:31]\nzt v[132:133],v19 offset:8192\nzh v172,16,v178\nzm a[32:35],v[32:35],v[68:71],a[32:35]\ns_mov_b64 s[10:11],s[12:13]\nzh v184,16,v190\nzm a[36:39],v[36:39],v[68:71],a[36:39]\n"
"zt v[134:135],v19 offset:9216\nzm a[40:43],v[40:43],v[68:71],a[40:43]\ns_mov_b64 s[12:13],s[14:15]\nv_and_b32 v173,0xffff0000,v178\nzm a[44:47],v[44:47],v[68:71],a[44:47]\nzd v[100:103],v17 offset:2048\nzm a[48:51],v[48:51],v[68:71],a[48:51]\ns_mov_b64 s[14:15],s[16:17]\n"
"v_and_b32 v185,0xffff0000,v190\nzm a[52:55],v[52:55],v[68:71],a[52:55]\nzt v[136:137],v20 offset:8192\nzm a[56:59],v[56:59],v[68:71],a[56:59]\ns_mov_b64 s[16:17],s[18:19]\nzh v174,16,v179\nzm a[60:63],v[60:63],v[68:71],a[60:63]\nzt v[138:139],v20 offset:9216\n"
"zh v186,16,v191\nzm a[64:67],v[32:35],v[72:75],a[64:67]\nzm a[68:71],v[36:39],v[72:75],a[68:71]\nzd v[104:107],v17 offset:4096\nv_and_b32 v175,0xffff0000,v179\nzm a[72:75],v[40:43],v[72:75],a[72:75]\nzm a[76:79],v[44:47],v[72:75],a[76:79]\nzt v[140:141],v21 offset:8192\n"
"v_and_b32 v187,0xffff0000,v191\nzm a[80:83],v[48:51],v[72:75],a[80:83]\nzm a[84:87],v[52:55],v[72:75],a[84:87]\nzt v[142:143],v21 offset:9216\nzx v168,0x3db8aa3b,v168\nzm a[88:91],v[56:59],v[72:75],a[88:91]\nzx v180,0x3db8aa3b,v180\nzm a[92:95],v[60:63],v[72:75],a[92:95]\n"
"zd v[108:111],v17 offset:6144\nzm a[96:99],v[32:35],v[76:79],a[96:99]\nzx v169,0x3db8aa3b,v169\nzm a[100:103],v[36:39],v[76:79],a[100:103]\nzt v[144:145],v22 offset:8192\nzm a[104:107],v[40:43],v[76:79],a[104:107]\nzx v181,0x3db8aa3b,v181\nzm a[108:111],v[44:47],v[76:79],a[108:111]\n"
"zt v[146:147],v22 offset:9216\nzm a[112:115],v[48:51],v[76:79],a[112:115]\nzx v170,0x3db8aa3b,v170\nzm a[116:119],v[52:55],v[76:79],a[116:119]\nzd v[112:115],v17 offset:8192\nzx v182,0x3db8aa3b,v182\nzm a[120:123],v[56:59],v[76:79],a[120:123]\nzm a[124:127],v[60:63],v[76:79],a[124:127]\n"
"zt v[148:149],v23 offset:8192\nzx v171,0x3db8aa3b,v171\nzm a[128:131],v[32:35],v[80:83],a[128:131]\nzm a[132:135],v[36:39],v[80:83],a[132:135]\nzt v[150:151],v23 offset:9216\nzx v183,0x3db8aa3b,v183\nzm a[136:139],v[40:43],v[80:83],a[136:139]\nzm a[140:143],v[44:47],v[80:83],a[140:143]\n"
"zd v[116:119],v17 offset:10240\nzx v172,0x3db8aa3b,v172\nzm a[144:147],v[48:51],v[80:83],a[144:147]\nzx v184,0x3db8aa3b,v184\nzm a[148:151],v[52:55],v[80:83],a[148:151]\nzt v[152:153],v24 offset:8192\nzm a[152:155],v[56:59],v[80:83],a[152:155]\nzx v173,0x3db8aa3b,v173\n"
"zm a[156:159],v[60:63],v[80:83],a[156:159]\nzt v[154:155],v24 offset:9216\nzm a[160:163],v[32:35],v[84:87],a[160:163]\nzx v185,0x3db8aa3b,v185\nzm a[164:167],v[36:39],v[84:87],a[164:167]\nzd v[120:123],v17 offset:12288\nzm a[168:171],v[40:43],v[84:87],a[168:171]\nzx v174,0x3db8aa3b,v174\n"
"zm a[172:175],v[44:47],v[84:87],a[172:175]\nzt v[156:157],v25 offset:8192\nzx v186,0x3db8aa3b,v186\nzm a[176:179],v[48:51],v[84:87],a[176:179]\nzm a[180:183],v[52:55],v[84:87],a[180:183]\nzt v[158:159],v25 offset:9216\nzx v175,0x3db8aa3b,v175\nzm a[184:187],v[56:59],v[84:87],a[184:187]\n"
"zm a[188:191],v[60:63],v[84:87],a[188:191]\nzd v[124:127],v17 offset:14336\nzx v187,0x3db8aa3b,v187\nzm a[192:195],v[32:35],v[88:91],a[192:195]\nzm a[196:199],v[36:39],v[88:91],a[196:199]\nzc v160,v168,v169\nzm a[200:203],v[40:43],v[88:91],a[200:203]\nzc v164,v180,v181\n"
"zm a[204:207],v[44:47],v[88:91],a[204:207]\nzm a[208:211],v[48:51],v[88:91],a[208:211]\nzo v16,0x8000,v16\nzc v161,v170,v171\nzm a[212:215],v[52:55],v[88:91],a[212:215]\nzo v17,0x8000,v17\nzm a[216:219],v[56:59],v[88:91],a[216:219]\nzo v18,0x8000,v18\n"
"zc v165,v182,v183\nzm a[220:223],v[60:63],v[88:91],a[220:223]\nzo v19,0x8000,v19\nzm a[224:227],v[32:35],v[92:95],a[224:227]\nzo v20,0x8000,v20\nzc v162,v172,v173\nzm a[228:231],v[36:39],v[92:95],a[228:231]\nzo v21,0x8000,v21\n"
"zc v166,v184,v185\nzm a[232:235],v[40:43],v[92:95],a[232:235]\nzo v22,0x8000,v22\nzm a[236:239],v[44:47],v[92:95],a[236:239]\nzo v23,0x8000,v23\nzc v163,v174,v175\nzm a[240:243],v[48:51],v[92:95],a[240:243]\nzo v24,0x8000,v24\n"
"zm a[244:247],v[52:55],v[92:95],a[244:247]\nzo v25,0x8000,v25\nzc v167,v186,v187\nzm a[248:251],v[56:59],v[92:95],a[248:251]\nzm a[252:255],v[60:63],v[92:95],a[252:255]\nzv vmcnt(3) lgkmcnt(0)\ns_barrier\nzm a[0:3],v[96:99],v[128:131],a[0:3]\n"
"zs v[176:179],v201,s[88:91],0 offen offset:0 sc1\nzm a[4:7],v[100:103],v[128:131],a[4:7]\nzt v[64:65],v18 offset:0\nzm a[8:11],v[104:107],v[128:131],a[8:11]\nzs v[188:191],v201,s[88:91],0 offen offset:896 sc1\nzm a[12:15],v[108:111],v[128:131],a[12:15]\nzt v[66:67],v18 offset:1024\nzm a[16:19],v[112:115],v[128:131],a[16:19]\n"
"zs v[160:163],v201,s[88:91],s92 offen offset:0 sc1\nzm a[20:23],v[116:119],v[128:131],a[20:23]\nzm a[24:27],v[120:123],v[128:131],a[24:27]\nzs v[164:167],v201,s[88:91],s92 offen offset:896 sc1\nzm a[28:31],v[124:127],v[128:131],a[28:31]\nzd v[32:35],v16 offset:0\ns_mov_b32 m0,s65\nzm a[32:35],v[96:99],v[132:135],a[32:35]\n"
"zl v8,s[24:27],s62 offen offset:0 sc1 lds\nzm a[36:39],v[100:103],v[132:135],a[36:39]\nzt v[68:69],v19 offset:0\nzm a[40:43],v[104:107],v[132:135],a[40:43]\nzm a[44:47],v[108:111],v[132:135],a[44:47]\nzl v9,s[24:27],s62 offen offset:1024 sc1 lds\nzm a[48:51],v[112:115],v[132:135],a[48:51]\nzt v[70:71],v19 offset:1024\n"
"zm a[52:55],v[116:119],v[132:135],a[52:55]\nzd v[36:39],v16 offset:2048\nzm a[56:59],v[120:123],v[132:135],a[56:59]\nzl v10,s[24:27],s62 offen offset:2048 sc1 lds\nzm a[60:63],v[124:127],v[132:135],a[60:63]\nzm a[64:67],v[96:99],v[136:139],a[64:67]\nzt v[72:73],v20 offset:0\nzm a[68:71],v[100:103],v[136:139],a[68:71]\n"
"zt v[74:75],v20 offset:1024\nzm a[72:75],v[104:107],v[136:139],a[72:75]\nzl v11,s[24:27],s62 offen offset:3072 sc1 lds\nzm a[76:79],v[108:111],v[136:139],a[76:79]\nzm a[80:83],v[112:115],v[136:139],a[80:83]\nzd v[40:43],v16 offset:4096\nzp m0,s65,4096\nzm a[84:87],v[116:119],v[136:139],a[84:87]\n"
"zl v8,s[24:27],s63 offen offset:0 sc1 lds\nzm a[88:91],v[120:123],v[136:139],a[88:91]\nzt v[76:77],v21 offset:0\nzm a[92:95],v[124:127],v[136:139],a[92:95]\nzm a[96:99],v[96:99],v[140:143],a[96:99]\nzl v9,s[24:27],s63 offen offset:1024 sc1 lds\nzm a[100:103],v[100:103],v[140:143],a[100:103]\nzt v[78:79],v21 offset:1024\n"
"zm a[104:107],v[104:107],v[140:143],a[104:107]\nzd v[44:47],v16 offset:6144\nzm a[108:111],v[108:111],v[140:143],a[108:111]\nzm a[112:115],v[112:115],v[140:143],a[112:115]\nzl v10,s[24:27],s63 offen offset:2048 sc1 lds\nzm a[116:119],v[116:119],v[140:143],a[116:119]\nzt v[80:81],v22 offset:0\nzm a[120:123],v[120:123],v[140:143],a[120:123]\n"
"zm a[124:127],v[124:127],v[140:143],a[124:127]\nzl v11,s[24:27],s63 offen offset:3072 sc1 lds\nzm a[128:131],v[96:99],v[144:147],a[128:131]\nzt v[82:83],v22 offset:1024\nzm a[132:135],v[100:103],v[144:147],a[132:135]\nzd v[48:51],v16 offset:8192\ns_mov_b32 m0,s66\nzm a[136:139],v[104:107],v[144:147],a[136:139]\n"
"zl v12,s[28:31],s64 offen offset:0 lds\nzm a[140:143],v[108:111],v[144:147],a[140:143]\nzm a[144:147],v[112:115],v[144:147],a[144:147]\nzt v[84:85],v23 offset:0\nzm a[148:151],v[116:119],v[144:147],a[148:151]\nzt v[86:87],v23 offset:1024\nzm a[152:155],v[120:123],v[144:147],a[152:155]\nzl v13,s[28:31],s64 offen offset:1024 lds\n"
"zm a[156:159],v[124:127],v[144:147],a[156:159]\nzm a[160:163],v[96:99],v[148:151],a[160:163]\nzd v[52:55],v16 offset:10240\nzm a[164:167],v[100:103],v[148:151],a[164:167]\nzl v14,s[28:31],s64 offen offset:2048 lds\nzm a[168:171],v[104:107],v[148:151],a[168:171]\nzt v[88:89],v24 offset:0\nzm a[172:175],v[108:111],v[148:151],a[172:175]\n"
"zm a[176:179],v[112:115],v[148:151],a[176:179]\nzl v15,s[28:31],s64 offen offset:3072 lds\nzm a[180:183],v[116:119],v[148:151],a[180:183]\nzt v[90:91],v24 offset:1024\nzm a[184:187],v[120:123],v[148:151],a[184:187]\nzd v[56:59],v16 offset:12288\nzm a[188:191],v[124:127],v[148:151],a[188:191]\nzp m0,s66,16384\n"
"zm a[192:195],v[96:99],v[152:155],a[192:195]\nzl v12,s[36:39],s64 offen offset:0 lds\nzm a[196:199],v[100:103],v[152:155],a[196:199]\nzt v[92:93],v25 offset:0\nzm a[200:203],v[104:107],v[152:155],a[200:203]\nzt v[94:95],v25 offset:1024\nzm a[204:207],v[108:111],v[152:155],a[204:207]\nzl v13,s[36:39],s64 offen offset:1024 lds\n"
"zm a[208:211],v[112:115],v[152:155],a[208:211]\nzm a[212:215],v[116:119],v[152:155],a[212:215]\nzd v[60:63],v16 offset:14336\nzm a[216:219],v[120:123],v[152:155],a[216:219]\nzl v14,s[36:39],s64 offen offset:2048 lds\nzm a[220:223],v[124:127],v[152:155],a[220:223]\nzm a[224:227],v[96:99],v[156:159],a[224:227]\nzm a[228:231],v[100:103],v[156:159],a[228:231]\n"
"zm a[232:235],v[104:107],v[156:159],a[232:235]\nzl v15,s[36:39],s64 offen offset:3072 lds\nzm a[236:239],v[108:111],v[156:159],a[236:239]\nzp s93,s64,s74\nzm a[240:243],v[112:115],v[156:159],a[240:243]\nbuffer_load_dword v31,v4,s[24:27],s62 offen offset:128\nzm a[244:247],v[116:119],v[156:159],a[244:247]\nbuffer_load_dword v31,v5,s[28:31],s93 offen\n"
"zm a[248:251],v[120:123],v[156:159],a[248:251]\nglobal_load_dword v31,v231,s[4:5]\nzp s62,s62,128\nzp s63,s63,128\nzp s64,s64,s74\nzm a[252:255],v[124:127],v[156:159],a[252:255]\ns_xor_b32 s65,s65,0x8000\ns_xor_b32 s66,s66,0x8000\n"
"zv lgkmcnt(0)\ns_mov_b32 s67,2\ns_mov_b32 s75,0\n.p2align 6\nL_P_vloopA:\nzm a[0:3],v[32:35],v[64:67],a[0:3]\nzm a[4:7],v[36:39],v[64:67],a[4:7]\nzt v[128:129],v18 offset:8192\n"
"zm a[8:11],v[40:43],v[64:67],a[8:11]\ns_mov_b64 s[4:5],s[6:7]\nzm a[12:15],v[44:47],v[64:67],a[12:15]\nzt v[130:131],v18 offset:9216\nzm a[16:19],v[48:51],v[64:67],a[16:19]\ns_mov_b64 s[6:7],s[8:9]\nzm a[20:23],v[52:55],v[64:67],a[20:23]\nzd v[96:99],v17 offset:0\n"
"zm a[24:27],v[56:59],v[64:67],a[24:27]\ns_mov_b64 s[8:9],s[10:11]\nzm a[28:31],v[60:63],v[64:67],a[28:31]\nzt v[132:133],v19 offset:8192\nzm a[32:35],v[32:35],v[68:71],a[32:35]\ns_mov_b64 s[10:11],s[12:13]\nzm a[36:39],v[36:39],v[68:71],a[36:39]\nzt v[134:135],v19 offset:9216\n"
"zm a[40:43],v[40:43],v[68:71],a[40:43]\ns_mov_b64 s[12:13],s[14:15]\nzm a[44:47],v[44:47],v[68:71],a[44:47]\nzd v[100:103],v17 offset:2048\nzm a[48:51],v[48:51],v[68:71],a[48:51]\ns_mov_b64 s[14:15],s[16:17]\nzm a[52:55],v[52:55],v[68:71],a[52:55]\nzt v[136:137],v20 offset:8192\n"
"zm a[56:59],v[56:59],v[68:71],a[56:59]\ns_mov_b64 s[16:17],s[18:19]\nzm a[60:63],v[60:63],v[68:71],a[60:63]\nzt v[138:139],v20 offset:9216\nzm a[64:67],v[32:35],v[72:75],a[64:67]\nzm a[68:71],v[36:39],v[72:75],a[68:71]\nzd v[104:107],v17 offset:4096\nzm a[72:75],v[40:43],v[72:75],a[72:75]\n"
"zm a[76:79],v[44:47],v[72:75],a[76:79]\nzt v[140:141],v21 offset:8192\nzm a[80:83],v[48:51],v[72:75],a[80:83]\nzm a[84:87],v[52:55],v[72:75],a[84:87]\nzt v[142:143],v21 offset:9216\nzm a[88:91],v[56:59],v[72:75],a[88:91]\nzm a[92:95],v[60:63],v[72:75],a[92:95]\nzd v[108:111],v17 offset:6144\n"
"zm a[96:99],v[32:35],v[76:79],a[96:99]\nzm a[100:103],v[36:39],v[76:79],a[100:103]\nzt v[144:145],v22 offset:8192\nzm a[104:107],v[40:43],v[76:79],a[104:107]\nzm a[108:111],v[44:47],v[76:79],a[108:111]\nzt v[146:147],v22 offset:9216\nzm a[112:115],v[48:51],v[76:79],a[112:115]\nzm a[116:119],v[52:55],v[76:79],a[116:119]\n"
"zd v[112:115],v17 offset:8192\nzm a[120:123],v[56:59],v[76:79],a[120:123]\nzm a[124:127],v[60:63],v[76:79],a[124:127]\nzt v[148:149],v23 offset:8192\nzm a[128:131],v[32:35],v[80:83],a[128:131]\nzm a[132:135],v[36:39],v[80:83],a[132:135]\nzt v[150:151],v23 offset:9216\nzm a[136:139],v[40:43],v[80:83],a[136:139]\n"
"zm a[140:143],v[44:47],v[80:83],a[140:143]\nzd v[116:119],v17 offset:10240\nzm a[144:147],v[48:51],v[80:83],a[144:147]\nzm a[148:151],v[52:55],v[80:83],a[148:151]\nzt v[152:153],v24 offset:8192\nzm a[152:155],v[56:59],v[80:83],a[152:155]\nzm a[156:159],v[60:63],v[80:83],a[156:159]\nzt v[154:155],v24 offset:9216\n"
"zm a[160:163],v[32:35],v[84:87],a[160:163]\nzm a[164:167],v[36:39],v[84:87],a[164:167]\nzd v[120:123],v17 offset:12288\nzm a[168:171],v[40:43],v[84:87],a[168:171]\nzm a[172:175],v[44:47],v[84:87],a[172:175]\nzt v[156:157],v25 offset:8192\nzm a[176:179],v[48:51],v[84:87],a[176:179]\nzm a[180:183],v[52:55],v[84:87],a[180:183]\n"
"zt v[158:159],v25 offset:9216\nzm a[184:187],v[56:59],v[84:87],a[184:187]\nzm a[188:191],v[60:63],v[84:87],a[188:191]\nzd v[124:127],v17 offset:14336\nzm a[192:195],v[32:35],v[88:91],a[192:195]\nzm a[196:199],v[36:39],v[88:91],a[196:199]\nzm a[200:203],v[40:43],v[88:91],a[200:203]\nzm a[204:207],v[44:47],v[88:91],a[204:207]\n"
"zm a[208:211],v[48:51],v[88:91],a[208:211]\nzo v16,0x8000,v16\nzm a[212:215],v[52:55],v[88:91],a[212:215]\nzo v17,0x8000,v17\nzm a[216:219],v[56:59],v[88:91],a[216:219]\nzo v18,0x8000,v18\nzm a[220:223],v[60:63],v[88:91],a[220:223]\nzo v19,0x8000,v19\n"
"zm a[224:227],v[32:35],v[92:95],a[224:227]\nzo v20,0x8000,v20\nzm a[228:231],v[36:39],v[92:95],a[228:231]\nzo v21,0x8000,v21\nzm a[232:235],v[40:43],v[92:95],a[232:235]\nzo v22,0x8000,v22\nzm a[236:239],v[44:47],v[92:95],a[236:239]\nzo v23,0x8000,v23\n"
"zm a[240:243],v[48:51],v[92:95],a[240:243]\nzo v24,0x8000,v24\nzm a[244:247],v[52:55],v[92:95],a[244:247]\nzo v25,0x8000,v25\nzm a[248:251],v[56:59],v[92:95],a[248:251]\nzm a[252:255],v[60:63],v[92:95],a[252:255]\nzv vmcnt(3) lgkmcnt(0)\ns_barrier\n"
"zm a[0:3],v[96:99],v[128:131],a[0:3]\nzt v[64:65],v18 offset:0\ns_mov_b32 m0,s65\nzm a[4:7],v[100:103],v[128:131],a[4:7]\nzl v8,s[24:27],s62 offen offset:0 sc1 lds\nzm a[8:11],v[104:107],v[128:131],a[8:11]\nzt v[66:67],v18 offset:1024\nzm a[12:15],v[108:111],v[128:131],a[12:15]\n"
"zm a[16:19],v[112:115],v[128:131],a[16:19]\nzl v9,s[24:27],s62 offen offset:1024 sc1 lds\nzm a[20:23],v[116:119],v[128:131],a[20:23]\nzd v[32:35],v16 offset:0\nzm a[24:27],v[120:123],v[128:131],a[24:27]\nzt v[68:69],v19 offset:0\nzm a[28:31],v[124:127],v[128:131],a[28:31]\nzm a[32:35],v[96:99],v[132:135],a[32:35]\n"
"zl v10,s[24:27],s62 offen offset:2048 sc1 lds\nzm a[36:39],v[100:103],v[132:135],a[36:39]\nzt v[70:71],v19 offset:1024\nzm a[40:43],v[104:107],v[132:135],a[40:43]\nzd v[36:39],v16 offset:2048\nzm a[44:47],v[108:111],v[132:135],a[44:47]\nzm a[48:51],v[112:115],v[132:135],a[48:51]\nzl v11,s[24:27],s62 offen offset:3072 sc1 lds\n"
"zm a[52:55],v[116:119],v[132:135],a[52:55]\nzt v[72:73],v20 offset:0\nzm a[56:59],v[120:123],v[132:135],a[56:59]\nzm a[60:63],v[124:127],v[132:135],a[60:63]\nzt v[74:75],v20 offset:1024\nzp m0,s65,4096\nzm a[64:67],v[96:99],v[136:139],a[64:67]\nzl v8,s[24:27],s63 offen offset:0 sc1 lds\n"
"zm a[68:71],v[100:103],v[136:139],a[68:71]\nzd v[40:43],v16 offset:4096\nzm a[72:75],v[104:107],v[136:139],a[72:75]\nzm a[76:79],v[108:111],v[136:139],a[76:79]\nzt v[76:77],v21 offset:0\nzm a[80:83],v[112:115],v[136:139],a[80:83]\nzl v9,s[24:27],s63 offen offset:1024 sc1 lds\nzm a[84:87],v[116:119],v[136:139],a[84:87]\n"
"zt v[78:79],v21 offset:1024\nzm a[88:91],v[120:123],v[136:139],a[88:91]\nzm a[92:95],v[124:127],v[136:139],a[92:95]\nzl v10,s[24:27],s63 offen offset:2048 sc1 lds\nzm a[96:99],v[96:99],v[140:143],a[96:99]\nzd v[44:47],v16 offset:6144\nzm a[100:103],v[100:103],v[140:143],a[100:103]\nzm a[104:107],v[104:107],v[140:143],a[104:107]\n"
"zt v[80:81],v22 offset:0\nzm a[108:111],v[108:111],v[140:143],a[108:111]\nzl v11,s[24:27],s63 offen offset:3072 sc1 lds\nzm a[112:115],v[112:115],v[140:143],a[112:115]\nzt v[82:83],v22 offset:1024\nzm a[116:119],v[116:119],v[140:143],a[116:119]\nzm a[120:123],v[120:123],v[140:143],a[120:123]\nzd v[48:51],v16 offset:8192\n"
"s_mov_b32 m0,s66\nzm a[124:127],v[124:127],v[140:143],a[124:127]\nzl v12,s[28:31],s64 offen offset:0 lds\nzm a[128:131],v[96:99],v[144:147],a[128:131]\nzt v[84:85],v23 offset:0\nzm a[132:135],v[100:103],v[144:147],a[132:135]\nzm a[136:139],v[104:107],v[144:147],a[136:139]\nzt v[86:87],v23 offset:1024\n"
"zm a[140:143],v[108:111],v[144:147],a[140:143]\nzl v13,s[28:31],s64 offen offset:1024 lds\nzm a[144:147],v[112:115],v[144:147],a[144:147]\nzd v[52:55],v16 offset:10240\nzm a[148:151],v[116:119],v[144:147],a[148:151]\nzm a[152:155],v[120:123],v[144:147],a[152:155]\nzt v[88:89],v24 offset:0\nzm a[156:159],v[124:127],v[144:147],a[156:159]\n"
"zl v14,s[28:31],s64 offen offset:2048 lds\nzm a[160:163],v[96:99],v[148:151],a[160:163]\nzm a[164:167],v[100:103],v[148:151],a[164:167]\nzt v[90:91],v24 offset:1024\nzm a[168:171],v[104:107],v[148:151],a[168:171]\nzl v15,s[28:31],s64 offen offset:3072 lds\nzm a[172:175],v[108:111],v[148:151],a[172:175]\nzd v[56:59],v16 offset:12288\n"
"zm a[176:179],v[112:115],v[148:151],a[176:179]\nzm a[180:183],v[116:119],v[148:151],a[180:183]\nzt v[92:93],v25 offset:0\nzp m0,s66,16384\nzm a[184:187],v[120:123],v[148:151],a[184:187]\nzl v12,s[36:39],s64 offen offset:0 lds\nzm a[188:191],v[124:127],v[148:151],a[188:191]\nzt v[94:95],v25 offset:1024\n"
"zm a[192:195],v[96:99],v[152:155],a[192:195]\nzm a[196:199],v[100:103],v[152:155],a[196:199]\nzd v[60:63],v16 offset:14336\nzm a[200:203],v[104:107],v[152:155],a[200:203]\nzl v13,s[36:39],s64 offen offset:1024 lds\nzm a[204:207],v[108:111],v[152:155],a[204:207]\nzm a[208:211],v[112:115],v[152:155],a[208:211]\nzm a[212:215],v[116:119],v[152:155],a[212:215]\n"
"zm a[216:219],v[120:123],v[152:155],a[216:219]\nzl v14,s[36:39],s64 offen offset:2048 lds\nzm a[220:223],v[124:127],v[152:155],a[220:223]\nzm a[224:227],v[96:99],v[156:159],a[224:227]\nzm a[228:231],v[100:103],v[156:159],a[228:231]\nzm a[232:235],v[104:107],v[156:159],a[232:235]\nzl v15,s[36:39],s64 offen offset:3072 lds\nzm a[236:239],v[108:111],v[156:159],a[236:239]\n"
"zp s93,s64,s74\nzm a[240:243],v[112:115],v[156:159],a[240:243]\nbuffer_load_dword v31,v4,s[24:27],s62 offen offset:128\nzm a[244:247],v[116:119],v[156:159],a[244:247]\nbuffer_load_dword v31,v5,s[28:31],s93 offen\nzm a[248:251],v[120:123],v[156:159],a[248:251]\nglobal_load_dword v31,v231,s[4:5]\nzp s62,s62,128\n"
"zp s63,s63,128\nzp s64,s64,s74\nzm a[252:255],v[124:127],v[156:159],a[252:255]\ns_xor_b32 s65,s65,0x8000\ns_xor_b32 s66,s66,0x8000\ns_sub_u32 s67,s67,1\ns_cmp_lg_u32 s67,0\nzv lgkmcnt(0)\n"
"s_cbranch_scc1 L_P_vloopA\ns_cmp_eq_u32 s75,0\ns_cbranch_scc0 L_P_vre0\ns_lshl_b32 s82,s22,2\nzp s82,s82,1024\ns_cmp_eq_u32 s23,0\ns_cbranch_scc0 L_nf_lnv\nv_mov_b32 v26,s82\n"
"v_mov_b32 v27,s34\nglobal_store_dword v26,v27,s[94:95] sc1\nL_nf_lnv:\ns_mov_b32 s75,1\ns_mov_b32 s67,2\ns_branch L_P_vloopA\nL_P_vre0:\ns_mov_b32 s67,4\n"
".p2align 6\nL_P_vloop:\nzm a[0:3],v[32:35],v[64:67],a[0:3]\nzm a[4:7],v[36:39],v[64:67],a[4:7]\nzt v[128:129],v18 offset:8192\nzm a[8:11],v[40:43],v[64:67],a[8:11]\nzm a[12:15],v[44:47],v[64:67],a[12:15]\nzt v[130:131],v18 offset:9216\n"
"zm a[16:19],v[48:51],v[64:67],a[16:19]\nzm a[20:23],v[52:55],v[64:67],a[20:23]\nzd v[96:99],v17 offset:0\nzm a[24:27],v[56:59],v[64:67],a[24:27]\nzm a[28:31],v[60:63],v[64:67],a[28:31]\nzt v[132:133],v19 offset:8192\nzm a[32:35],v[32:35],v[68:71],a[32:35]\nzm a[36:39],v[36:39],v[68:71],a[36:39]\n"
"zt v[134:135],v19 offset:9216\nzm a[40:43],v[40:43],v[68:71],a[40:43]\nzm a[44:47],v[44:47],v[68:71],a[44:47]\nzd v[100:103],v17 offset:2048\nzm a[48:51],v[48:51],v[68:71],a[48:51]\nzm a[52:55],v[52:55],v[68:71],a[52:55]\nzt v[136:137],v20 offset:8192\nzm a[56:59],v[56:59],v[68:71],a[56:59]\n"
"zm a[60:63],v[60:63],v[68:71],a[60:63]\nzt v[138:139],v20 offset:9216\nzm a[64:67],v[32:35],v[72:75],a[64:67]\nzm a[68:71],v[36:39],v[72:75],a[68:71]\nzd v[104:107],v17 offset:4096\nzm a[72:75],v[40:43],v[72:75],a[72:75]\nzm a[76:79],v[44:47],v[72:75],a[76:79]\nzt v[140:141],v21 offset:8192\n"
"zm a[80:83],v[48:51],v[72:75],a[80:83]\nzm a[84:87],v[52:55],v[72:75],a[84:87]\nzt v[142:143],v21 offset:9216\nzm a[88:91],v[56:59],v[72:75],a[88:91]\nzm a[92:95],v[60:63],v[72:75],a[92:95]\nzd v[108:111],v17 offset:6144\nzm a[96:99],v[32:35],v[76:79],a[96:99]\nzm a[100:103],v[36:39],v[76:79],a[100:103]\n"
"zt v[144:145],v22 offset:8192\nzm a[104:107],v[40:43],v[76:79],a[104:107]\nzm a[108:111],v[44:47],v[76:79],a[108:111]\nzt v[146:147],v22 offset:9216\nzm a[112:115],v[48:51],v[76:79],a[112:115]\nzm a[116:119],v[52:55],v[76:79],a[116:119]\nzd v[112:115],v17 offset:8192\nzm a[120:123],v[56:59],v[76:79],a[120:123]\n"
"zm a[124:127],v[60:63],v[76:79],a[124:127]\nzt v[148:149],v23 offset:8192\nzm a[128:131],v[32:35],v[80:83],a[128:131]\nzm a[132:135],v[36:39],v[80:83],a[132:135]\nzt v[150:151],v23 offset:9216\nzm a[136:139],v[40:43],v[80:83],a[136:139]\nzm a[140:143],v[44:47],v[80:83],a[140:143]\nzd v[116:119],v17 offset:10240\n"
"zm a[144:147],v[48:51],v[80:83],a[144:147]\nzm a[148:151],v[52:55],v[80:83],a[148:151]\nzt v[152:153],v24 offset:8192\nzm a[152:155],v[56:59],v[80:83],a[152:155]\nzm a[156:159],v[60:63],v[80:83],a[156:159]\nzt v[154:155],v24 offset:9216\nzm a[160:163],v[32:35],v[84:87],a[160:163]\nzm a[164:167],v[36:39],v[84:87],a[164:167]\n"
"zd v[120:123],v17 offset:12288\nzm a[168:171],v[40:43],v[84:87],a[168:171]\nzm a[172:175],v[44:47],v[84:87],a[172:175]\nzt v[156:157],v25 offset:8192\nzm a[176:179],v[48:51],v[84:87],a[176:179]\nzm a[180:183],v[52:55],v[84:87],a[180:183]\nzt v[158:159],v25 offset:9216\nzm a[184:187],v[56:59],v[84:87],a[184:187]\n"
"zm a[188:191],v[60:63],v[84:87],a[188:191]\nzd v[124:127],v17 offset:14336\nzm a[192:195],v[32:35],v[88:91],a[192:195]\nzm a[196:199],v[36:39],v[88:91],a[196:199]\nzm a[200:203],v[40:43],v[88:91],a[200:203]\nzm a[204:207],v[44:47],v[88:91],a[204:207]\nzm a[208:211],v[48:51],v[88:91],a[208:211]\nzo v16,0x8000,v16\n"
"zm a[212:215],v[52:55],v[88:91],a[212:215]\nzo v17,0x8000,v17\nzm a[216:219],v[56:59],v[88:91],a[216:219]\nzo v18,0x8000,v18\nzm a[220:223],v[60:63],v[88:91],a[220:223]\nzo v19,0x8000,v19\nzm a[224:227],v[32:35],v[92:95],a[224:227]\nzo v20,0x8000,v20\n"
"zm a[228:231],v[36:39],v[92:95],a[228:231]\nzo v21,0x8000,v21\nzm a[232:235],v[40:43],v[92:95],a[232:235]\nzo v22,0x8000,v22\nzm a[236:239],v[44:47],v[92:95],a[236:239]\nzo v23,0x8000,v23\nzm a[240:243],v[48:51],v[92:95],a[240:243]\nzo v24,0x8000,v24\n"
"zm a[244:247],v[52:55],v[92:95],a[244:247]\nzo v25,0x8000,v25\nzm a[248:251],v[56:59],v[92:95],a[248:251]\nzm a[252:255],v[60:63],v[92:95],a[252:255]\nzv vmcnt(2) lgkmcnt(0)\ns_barrier\nzm a[0:3],v[96:99],v[128:131],a[0:3]\nzt v[64:65],v18 offset:0\n"
"s_mov_b32 m0,s65\nzm a[4:7],v[100:103],v[128:131],a[4:7]\nzl v8,s[24:27],s62 offen offset:0 sc1 lds\nzm a[8:11],v[104:107],v[128:131],a[8:11]\nzt v[66:67],v18 offset:1024\nzm a[12:15],v[108:111],v[128:131],a[12:15]\nzm a[16:19],v[112:115],v[128:131],a[16:19]\nzl v9,s[24:27],s62 offen offset:1024 sc1 lds\n"
"zm a[20:23],v[116:119],v[128:131],a[20:23]\nzd v[32:35],v16 offset:0\nzm a[24:27],v[120:123],v[128:131],a[24:27]\nzt v[68:69],v19 offset:0\nzm a[28:31],v[124:127],v[128:131],a[28:31]\nzm a[32:35],v[96:99],v[132:135],a[32:35]\nzl v10,s[24:27],s62 offen offset:2048 sc1 lds\nzm a[36:39],v[100:103],v[132:135],a[36:39]\n"
"zt v[70:71],v19 offset:1024\nzm a[40:43],v[104:107],v[132:135],a[40:43]\nzd v[36:39],v16 offset:2048\nzm a[44:47],v[108:111],v[132:135],a[44:47]\nzm a[48:51],v[112:115],v[132:135],a[48:51]\nzl v11,s[24:27],s62 offen offset:3072 sc1 lds\nzm a[52:55],v[116:119],v[132:135],a[52:55]\nzt v[72:73],v20 offset:0\n"
"zm a[56:59],v[120:123],v[132:135],a[56:59]\nzm a[60:63],v[124:127],v[132:135],a[60:63]\nzt v[74:75],v20 offset:1024\nzp m0,s65,4096\nzm a[64:67],v[96:99],v[136:139],a[64:67]\nzl v8,s[24:27],s63 offen offset:0 sc1 lds\nzm a[68:71],v[100:103],v[136:139],a[68:71]\nzd v[40:43],v16 offset:4096\n"
"zm a[72:75],v[104:107],v[136:139],a[72:75]\nzm a[76:79],v[108:111],v[136:139],a[76:79]\nzt v[76:77],v21 offset:0\nzm a[80:83],v[112:115],v[136:139],a[80:83]\nzl v9,s[24:27],s63 offen offset:1024 sc1 lds\nzm a[84:87],v[116:119],v[136:139],a[84:87]\nzt v[78:79],v21 offset:1024\nzm a[88:91],v[120:123],v[136:139],a[88:91]\n"
"zm a[92:95],v[124:127],v[136:139],a[92:95]\nzl v10,s[24:27],s63 offen offset:2048 sc1 lds\nzm a[96:99],v[96:99],v[140:143],a[96:99]\nzd v[44:47],v16 offset:6144\nzm a[100:103],v[100:103],v[140:143],a[100:103]\nzm a[104:107],v[104:107],v[140:143],a[104:107]\nzt v[80:81],v22 offset:0\nzm a[108:111],v[108:111],v[140:143],a[108:111]\n"
"zl v11,s[24:27],s63 offen offset:3072 sc1 lds\nzm a[112:115],v[112:115],v[140:143],a[112:115]\nzt v[82:83],v22 offset:1024\nzm a[116:119],v[116:119],v[140:143],a[116:119]\nzm a[120:123],v[120:123],v[140:143],a[120:123]\nzd v[48:51],v16 offset:8192\ns_mov_b32 m0,s66\nzm a[124:127],v[124:127],v[140:143],a[124:127]\n"
"zl v12,s[28:31],s64 offen offset:0 lds\nzm a[128:131],v[96:99],v[144:147],a[128:131]\nzt v[84:85],v23 offset:0\nzm a[132:135],v[100:103],v[144:147],a[132:135]\nzm a[136:139],v[104:107],v[144:147],a[136:139]\nzt v[86:87],v23 offset:1024\nzm a[140:143],v[108:111],v[144:147],a[140:143]\nzl v13,s[28:31],s64 offen offset:1024 lds\n"
"zm a[144:147],v[112:115],v[144:147],a[144:147]\nzd v[52:55],v16 offset:10240\nzm a[148:151],v[116:119],v[144:147],a[148:151]\nzm a[152:155],v[120:123],v[144:147],a[152:155]\nzt v[88:89],v24 offset:0\nzm a[156:159],v[124:127],v[144:147],a[156:159]\nzl v14,s[28:31],s64 offen offset:2048 lds\nzm a[160:163],v[96:99],v[148:151],a[160:163]\n"
"zm a[164:167],v[100:103],v[148:151],a[164:167]\nzt v[90:91],v24 offset:1024\nzm a[168:171],v[104:107],v[148:151],a[168:171]\nzl v15,s[28:31],s64 offen offset:3072 lds\nzm a[172:175],v[108:111],v[148:151],a[172:175]\nzd v[56:59],v16 offset:12288\nzm a[176:179],v[112:115],v[148:151],a[176:179]\nzm a[180:183],v[116:119],v[148:151],a[180:183]\n"
"zt v[92:93],v25 offset:0\nzp m0,s66,16384\nzm a[184:187],v[120:123],v[148:151],a[184:187]\nzl v12,s[36:39],s64 offen offset:0 lds\nzm a[188:191],v[124:127],v[148:151],a[188:191]\nzt v[94:95],v25 offset:1024\nzm a[192:195],v[96:99],v[152:155],a[192:195]\nzm a[196:199],v[100:103],v[152:155],a[196:199]\n"
"zd v[60:63],v16 offset:14336\nzm a[200:203],v[104:107],v[152:155],a[200:203]\nzl v13,s[36:39],s64 offen offset:1024 lds\nzm a[204:207],v[108:111],v[152:155],a[204:207]\nzm a[208:211],v[112:115],v[152:155],a[208:211]\nzm a[212:215],v[116:119],v[152:155],a[212:215]\nzm a[216:219],v[120:123],v[152:155],a[216:219]\nzl v14,s[36:39],s64 offen offset:2048 lds\n"
"zm a[220:223],v[124:127],v[152:155],a[220:223]\nzm a[224:227],v[96:99],v[156:159],a[224:227]\nzm a[228:231],v[100:103],v[156:159],a[228:231]\nzm a[232:235],v[104:107],v[156:159],a[232:235]\nzl v15,s[36:39],s64 offen offset:3072 lds\nzm a[236:239],v[108:111],v[156:159],a[236:239]\nzp s93,s64,s74\nzm a[240:243],v[112:115],v[156:159],a[240:243]\n"
"buffer_load_dword v31,v4,s[24:27],s62 offen offset:128\nzm a[244:247],v[116:119],v[156:159],a[244:247]\nbuffer_load_dword v31,v5,s[28:31],s93 offen\nzm a[248:251],v[120:123],v[156:159],a[248:251]\nzp s62,s62,128\nzp s63,s63,128\nzp s64,s64,s74\nzm a[252:255],v[124:127],v[156:159],a[252:255]\n"
"s_xor_b32 s65,s65,0x8000\ns_xor_b32 s66,s66,0x8000\ns_sub_u32 s67,s67,1\ns_cmp_lg_u32 s67,0\nzv lgkmcnt(0)\ns_cbranch_scc1 L_P_vloop\ns_cmp_eq_u32 s75,1\ns_cbranch_scc0 L_P_vre2\n"
"buffer_load_dword v224,v225,s[84:87],0 offen sc1\ns_mov_b32 s75,2\ns_mov_b32 s67,2\ns_branch L_P_vloop\nL_P_vre2:\ns_cmp_eq_u32 s75,2\ns_cbranch_scc0 L_P_vlast\ns_mov_b32 s75,3\n"
"s_mov_b32 s67,1\nv_mov_b32 v4,2147483632\nv_mov_b32 v5,2147483632\ns_cmp_eq_u32 s71,2\ns_cbranch_scc1 L_P_vnone\nv_cmp_ne_u32 vcc,s34,v224\ns_nop 2\ns_cmp_eq_u32 vcc_lo,0\n"
"s_cbranch_scc1 L_P_vlnok\ns_mov_b32 s76,0\nL_P_vlnpoll:\nbuffer_load_dword v224,v225,s[84:87],0 offen sc1\nzv vmcnt(0)\nv_cmp_ne_u32 vcc,s34,v224\ns_nop 1\ns_cmp_eq_u32 vcc_lo,0\n"
"s_cbranch_scc1 L_P_vlnok\nzp s76,s76,1\ns_cmp_lt_u32 s76,300000\ns_cbranch_scc0 L_P_vlnok\ns_sleep 1\ns_branch L_P_vlnpoll\nL_P_vlnok:\nv_mov_b32 v8,v208\n"
"v_mov_b32 v9,v209\nv_mov_b32 v10,v210\nv_mov_b32 v11,v211\nv_mov_b32 v12,v212\nv_mov_b32 v13,v213\nv_mov_b32 v14,v214\nv_mov_b32 v15,v215\ns_mov_b64 s[24:25],s[44:45]\n"
"s_mov_b64 s[26:27],s[46:47]\ns_mov_b64 s[28:29],s[48:49]\ns_mov_b64 s[30:31],s[50:51]\ns_mov_b64 s[36:37],s[52:53]\ns_mov_b64 s[38:39],s[54:55]\ns_mov_b32 s62,0\ns_mov_b32 s63,28672\ns_mov_b32 s64,0\n"
"s_cmp_eq_u32 s71,1\ns_mov_b32 s74,524288\ns_cselect_b32 s74,229376,s74\ns_mov_b32 s67,1\ns_branch L_P_vloop\nL_P_vnone:\ns_mov_b32 s26,0\ns_mov_b32 s30,0\n"
"s_mov_b32 s38,0\ns_branch L_P_vloop\nL_P_vlast:\nzm a[0:3],v[32:35],v[64:67],a[0:3]\nzm a[4:7],v[36:39],v[64:67],a[4:7]\nzt v[128:129],v18 offset:8192\nzm a[8:11],v[40:43],v[64:67],a[8:11]\nzm a[12:15],v[44:47],v[64:67],a[12:15]\n"
"zt v[130:131],v18 offset:9216\nzm a[16:19],v[48:51],v[64:67],a[16:19]\nzm a[20:23],v[52:55],v[64:67],a[20:23]\nzd v[96:99],v17 offset:0\nzm a[24:27],v[56:59],v[64:67],a[24:27]\nzm a[28:31],v[60:63],v[64:67],a[28:31]\nzt v[132:133],v19 offset:8192\nzm a[32:35],v[32:35],v[68:71],a[32:35]\n"
"zm a[36:39],v[36:39],v[68:71],a[36:39]\nzt v[134:135],v19 offset:9216\nzm a[40:43],v[40:43],v[68:71],a[40:43]\nzm a[44:47],v[44:47],v[68:71],a[44:47]\nzd v[100:103],v17 offset:2048\nzm a[48:51],v[48:51],v[68:71],a[48:51]\nzm a[52:55],v[52:55],v[68:71],a[52:55]\nzt v[136:137],v20 offset:8192\n"
"zm a[56:59],v[56:59],v[68:71],a[56:59]\nzm a[60:63],v[60:63],v[68:71],a[60:63]\nzt v[138:139],v20 offset:9216\nzm a[64:67],v[32:35],v[72:75],a[64:67]\nzm a[68:71],v[36:39],v[72:75],a[68:71]\nzd v[104:107],v17 offset:4096\nzm a[72:75],v[40:43],v[72:75],a[72:75]\nzm a[76:79],v[44:47],v[72:75],a[76:79]\n"
"zt v[140:141],v21 offset:8192\nzm a[80:83],v[48:51],v[72:75],a[80:83]\nzm a[84:87],v[52:55],v[72:75],a[84:87]\nzt v[142:143],v21 offset:9216\nzm a[88:91],v[56:59],v[72:75],a[88:91]\nzm a[92:95],v[60:63],v[72:75],a[92:95]\nzd v[108:111],v17 offset:6144\nzm a[96:99],v[32:35],v[76:79],a[96:99]\n"
"zm a[100:103],v[36:39],v[76:79],a[100:103]\nzt v[144:145],v22 offset:8192\nzm a[104:107],v[40:43],v[76:79],a[104:107]\nzm a[108:111],v[44:47],v[76:79],a[108:111]\nzt v[146:147],v22 offset:9216\nzm a[112:115],v[48:51],v[76:79],a[112:115]\nzm a[116:119],v[52:55],v[76:79],a[116:119]\nzd v[112:115],v17 offset:8192\n"
"zm a[120:123],v[56:59],v[76:79],a[120:123]\nzm a[124:127],v[60:63],v[76:79],a[124:127]\nzt v[148:149],v23 offset:8192\nzm a[128:131],v[32:35],v[80:83],a[128:131]\nzm a[132:135],v[36:39],v[80:83],a[132:135]\nzt v[150:151],v23 offset:9216\nzm a[136:139],v[40:43],v[80:83],a[136:139]\nzm a[140:143],v[44:47],v[80:83],a[140:143]\n"
"zd v[116:119],v17 offset:10240\nzm a[144:147],v[48:51],v[80:83],a[144:147]\nzm a[148:151],v[52:55],v[80:83],a[148:151]\nzt v[152:153],v24 offset:8192\nzm a[152:155],v[56:59],v[80:83],a[152:155]\nzm a[156:159],v[60:63],v[80:83],a[156:159]\nzt v[154:155],v24 offset:9216\nzm a[160:163],v[32:35],v[84:87],a[160:163]\n"
"zm a[164:167],v[36:39],v[84:87],a[164:167]\nzd v[120:123],v17 offset:12288\nzm a[168:171],v[40:43],v[84:87],a[168:171]\nzm a[172:175],v[44:47],v[84:87],a[172:175]\nzt v[156:157],v25 offset:8192\nzm a[176:179],v[48:51],v[84:87],a[176:179]\nzm a[180:183],v[52:55],v[84:87],a[180:183]\nzt v[158:159],v25 offset:9216\n"
"zm a[184:187],v[56:59],v[84:87],a[184:187]\nzm a[188:191],v[60:63],v[84:87],a[188:191]\nzd v[124:127],v17 offset:14336\nzm a[192:195],v[32:35],v[88:91],a[192:195]\nzm a[196:199],v[36:39],v[88:91],a[196:199]\nzm a[200:203],v[40:43],v[88:91],a[200:203]\nzm a[204:207],v[44:47],v[88:91],a[204:207]\nzm a[208:211],v[48:51],v[88:91],a[208:211]\n"
"zo v16,0x8000,v16\nzm a[212:215],v[52:55],v[88:91],a[212:215]\nzo v17,0x8000,v17\nzm a[216:219],v[56:59],v[88:91],a[216:219]\nv_mov_b32 v18,v216\nzm a[220:223],v[60:63],v[88:91],a[220:223]\nv_mov_b32 v19,v217\nzm a[224:227],v[32:35],v[92:95],a[224:227]\n"
"v_mov_b32 v20,v218\nzm a[228:231],v[36:39],v[92:95],a[228:231]\nv_mov_b32 v21,v219\nzm a[232:235],v[40:43],v[92:95],a[232:235]\nv_mov_b32 v22,v220\nzm a[236:239],v[44:47],v[92:95],a[236:239]\nv_mov_b32 v23,v221\nzm a[240:243],v[48:51],v[92:95],a[240:243]\n"
"v_mov_b32 v24,v222\nzm a[244:247],v[52:55],v[92:95],a[244:247]\nv_mov_b32 v25,v223\nzm a[248:251],v[56:59],v[92:95],a[248:251]\nzm a[252:255],v[60:63],v[92:95],a[252:255]\nzv vmcnt(2) lgkmcnt(0)\ns_barrier\nzm a[0:3],v[96:99],v[128:131],a[0:3]\n"
"zt v[64:65],v18 offset:0\ns_mov_b32 m0,s65\nzm a[4:7],v[100:103],v[128:131],a[4:7]\nzl v8,s[24:27],s62 offen offset:0 sc1 lds\nzm a[8:11],v[104:107],v[128:131],a[8:11]\nzt v[66:67],v18 offset:1024\nzm a[12:15],v[108:111],v[128:131],a[12:15]\nzm a[16:19],v[112:115],v[128:131],a[16:19]\n"
"zl v9,s[24:27],s62 offen offset:1024 sc1 lds\nzm a[20:23],v[116:119],v[128:131],a[20:23]\nzd v[32:35],v16 offset:0\nzm a[24:27],v[120:123],v[128:131],a[24:27]\nzt v[68:69],v19 offset:0\nzm a[28:31],v[124:127],v[128:131],a[28:31]\nzm a[32:35],v[96:99],v[132:135],a[32:35]\nzl v10,s[24:27],s62 offen offset:2048 sc1 lds\n"
"zm a[36:39],v[100:103],v[132:135],a[36:39]\nzt v[70:71],v19 offset:1024\nzm a[40:43],v[104:107],v[132:135],a[40:43]\nzd v[36:39],v16 offset:2048\nzm a[44:47],v[108:111],v[132:135],a[44:47]\nzm a[48:51],v[112:115],v[132:135],a[48:51]\nzl v11,s[24:27],s62 offen offset:3072 sc1 lds\nzm a[52:55],v[116:119],v[132:135],a[52:55]\n"
"zt v[72:73],v20 offset:0\nzm a[56:59],v[120:123],v[132:135],a[56:59]\nzm a[60:63],v[124:127],v[132:135],a[60:63]\nzt v[74:75],v20 offset:1024\nzp m0,s65,4096\nzm a[64:67],v[96:99],v[136:139],a[64:67]\nzl v8,s[24:27],s63 offen offset:0 sc1 lds\nzm a[68:71],v[100:103],v[136:139],a[68:71]\n"
"zd v[40:43],v16 offset:4096\nzm a[72:75],v[104:107],v[136:139],a[72:75]\nzm a[76:79],v[108:111],v[136:139],a[76:79]\nzt v[76:77],v21 offset:0\nzm a[80:83],v[112:115],v[136:139],a[80:83]\nzl v9,s[24:27],s63 offen offset:1024 sc1 lds\nzm a[84:87],v[116:119],v[136:139],a[84:87]\nzt v[78:79],v21 offset:1024\n"
"zm a[88:91],v[120:123],v[136:139],a[88:91]\nzm a[92:95],v[124:127],v[136:139],a[92:95]\nzl v10,s[24:27],s63 offen offset:2048 sc1 lds\nzm a[96:99],v[96:99],v[140:143],a[96:99]\nzd v[44:47],v16 offset:6144\nzm a[100:103],v[100:103],v[140:143],a[100:103]\nzm a[104:107],v[104:107],v[140:143],a[104:107]\nzt v[80:81],v22 offset:0\n"
"zm a[108:111],v[108:111],v[140:143],a[108:111]\nzl v11,s[24:27],s63 offen offset:3072 sc1 lds\nzm a[112:115],v[112:115],v[140:143],a[112:115]\nzt v[82:83],v22 offset:1024\nzm a[116:119],v[116:119],v[140:143],a[116:119]\nzm a[120:123],v[120:123],v[140:143],a[120:123]\nzd v[48:51],v16 offset:8192\ns_mov_b32 m0,s66\n"
"zm a[124:127],v[124:127],v[140:143],a[124:127]\nzl v12,s[28:31],s64 offen offset:0 lds\nzm a[128:131],v[96:99],v[144:147],a[128:131]\nzt v[84:85],v23 offset:0\nzm a[132:135],v[100:103],v[144:147],a[132:135]\nzm a[136:139],v[104:107],v[144:147],a[136:139]\nzt v[86:87],v23 offset:1024\nzm a[140:143],v[108:111],v[144:147],a[140:143]\n"
"zl v13,s[28:31],s64 offen offset:1024 lds\nzm a[144:147],v[112:115],v[144:147],a[144:147]\nzd v[52:55],v16 offset:10240\nzm a[148:151],v[116:119],v[144:147],a[148:151]\nzm a[152:155],v[120:123],v[144:147],a[152:155]\nzt v[88:89],v24 offset:0\nzm a[156:159],v[124:127],v[144:147],a[156:159]\nzl v14,s[28:31],s64 offen offset:2048 lds\n"
"zm a[160:163],v[96:99],v[148:151],a[160:163]\nzm a[164:167],v[100:103],v[148:151],a[164:167]\nzt v[90:91],v24 offset:1024\nzm a[168:171],v[104:107],v[148:151],a[168:171]\nzl v15,s[28:31],s64 offen offset:3072 lds\nzm a[172:175],v[108:111],v[148:151],a[172:175]\nzd v[56:59],v16 offset:12288\nzm a[176:179],v[112:115],v[148:151],a[176:179]\n"
"zm a[180:183],v[116:119],v[148:151],a[180:183]\nzt v[92:93],v25 offset:0\nzp m0,s66,16384\nzm a[184:187],v[120:123],v[148:151],a[184:187]\nzl v12,s[36:39],s64 offen offset:0 lds\nzm a[188:191],v[124:127],v[148:151],a[188:191]\nzt v[94:95],v25 offset:1024\nzm a[192:195],v[96:99],v[152:155],a[192:195]\n"
"zm a[196:199],v[100:103],v[152:155],a[196:199]\nzd v[60:63],v16 offset:14336\nzm a[200:203],v[104:107],v[152:155],a[200:203]\nzl v13,s[36:39],s64 offen offset:1024 lds\nzm a[204:207],v[108:111],v[152:155],a[204:207]\nzm a[208:211],v[112:115],v[152:155],a[208:211]\nzm a[212:215],v[116:119],v[152:155],a[212:215]\nzm a[216:219],v[120:123],v[152:155],a[216:219]\n"
"zl v14,s[36:39],s64 offen offset:2048 lds\nzm a[220:223],v[124:127],v[152:155],a[220:223]\nzm a[224:227],v[96:99],v[156:159],a[224:227]\nzm a[228:231],v[100:103],v[156:159],a[228:231]\nzm a[232:235],v[104:107],v[156:159],a[232:235]\nzl v15,s[36:39],s64 offen offset:3072 lds\nzm a[236:239],v[108:111],v[156:159],a[236:239]\nzm a[240:243],v[112:115],v[156:159],a[240:243]\n"
"zm a[244:247],v[116:119],v[156:159],a[244:247]\nzm a[248:251],v[120:123],v[156:159],a[248:251]\nzp s62,s62,128\nzp s63,s63,128\nzp s64,s64,s74\nzm a[252:255],v[124:127],v[156:159],a[252:255]\ns_xor_b32 s65,s65,0x8000\ns_xor_b32 s66,s66,0x8000\n"
"zv lgkmcnt(0)\ns_nop 7\nglobal_load_dword v31,v[228:229],off\nzh v29,4,v6\ns_lshl_b32 s76,s70,3\ns_lshl_b32 s77,s60,2\nzp s76,s76,s77\ns_lshl_b32 s76,s76,4\n"
"s_lshl_b32 s77,s61,3\nzp s76,s76,s77\ns_lshl_b32 s80,s76,10\ns_mov_b32 s81,4096\ns_mov_b32 s82,12288\nzr v96,a0\nzr v97,a1\nzr v98,a2\n"
"zr v99,a3\nzr v100,a4\nzr v101,a5\nzr v102,a6\nzr v103,a7\nzc v104,v96,v97\nzc v105,v98,v99\nzc v106,v100,v101\n"
"zc v107,v102,v103\nzs v[104:107],v29,s[40:43],s80 offen offset:0 sc1\nzr v108,a32\nzr v109,a33\nzr v110,a34\nzr v111,a35\nzr v112,a36\nzr v113,a37\n"
"zr v114,a38\nzr v115,a39\nzc v116,v108,v109\nzc v117,v110,v111\nzc v118,v112,v113\nzc v119,v114,v115\nzs v[116:119],v29,s[40:43],s80 offen offset:1024 sc1\nzr v120,a64\n"
"zr v121,a65\nzr v122,a66\nzr v123,a67\nzr v124,a68\nzr v125,a69\nzr v126,a70\nzr v127,a71\nzc v128,v120,v121\n"
"zc v129,v122,v123\nzc v130,v124,v125\nzc v131,v126,v127\nzs v[128:131],v29,s[40:43],s80 offen offset:2048 sc1\nzr v132,a96\nzr v133,a97\nzr v134,a98\nzr v135,a99\n"
"zr v136,a100\nzr v137,a101\nzr v138,a102\nzr v139,a103\nzc v140,v132,v133\nzc v141,v134,v135\nzc v142,v136,v137\nzc v143,v138,v139\n"
"zs v[140:143],v29,s[40:43],s80 offen offset:3072 sc1\nzp s80,s80,s81\nzr v96,a128\nzr v97,a129\nzr v98,a130\nzr v99,a131\nzr v100,a132\nzr v101,a133\n"
"zr v102,a134\nzr v103,a135\nzc v104,v96,v97\nzc v105,v98,v99\nzc v106,v100,v101\nzc v107,v102,v103\nzs v[104:107],v29,s[40:43],s80 offen offset:0 sc1\nzr v108,a160\n"
"zr v109,a161\nzr v110,a162\nzr v111,a163\nzr v112,a164\nzr v113,a165\nzr v114,a166\nzr v115,a167\nzc v116,v108,v109\n"
"zc v117,v110,v111\nzc v118,v112,v113\nzc v119,v114,v115\nzs v[116:119],v29,s[40:43],s80 offen offset:1024 sc1\nzr v120,a192\nzr v121,a193\nzr v122,a194\nzr v123,a195\n"
"zr v124,a196\nzr v125,a197\nzr v126,a198\nzr v127,a199\nzc v128,v120,v121\nzc v129,v122,v123\nzc v130,v124,v125\nzc v131,v126,v127\n"
"zs v[128:131],v29,s[40:43],s80 offen offset:2048 sc1\nzr v132,a224\nzr v133,a225\nzr v134,a226\nzr v135,a227\nzr v136,a228\nzr v137,a229\nzr v138,a230\n"
"zr v139,a231\nzc v140,v132,v133\nzc v141,v134,v135\nzc v142,v136,v137\nzc v143,v138,v139\nzs v[140:143],v29,s[40:43],s80 offen offset:3072 sc1\nzp s80,s80,s82\nzr v96,a8\n"
"zr v97,a9\nzr v98,a10\nzr v99,a11\nzr v100,a12\nzr v101,a13\nzr v102,a14\nzr v103,a15\nzc v160,v96,v97\n"
"zc v161,v98,v99\nzc v162,v100,v101\nzc v163,v102,v103\nzr v108,a40\nzr v109,a41\nzr v110,a42\nzr v111,a43\nzr v112,a44\n"
"zr v113,a45\nzr v114,a46\nzr v115,a47\nzc v164,v108,v109\nzc v165,v110,v111\nzc v166,v112,v113\nzc v167,v114,v115\nzr v120,a72\n"
"zr v121,a73\nzr v122,a74\nzr v123,a75\nzr v124,a76\nzr v125,a77\nzr v126,a78\nzr v127,a79\nzc v168,v120,v121\n"
"zc v169,v122,v123\nzc v170,v124,v125\nzc v171,v126,v127\nzr v132,a104\nzr v133,a105\nzr v134,a106\nzr v135,a107\nzr v136,a108\n"
"zr v137,a109\nzr v138,a110\nzr v139,a111\nzc v172,v132,v133\nzc v173,v134,v135\nzc v174,v136,v137\nzc v175,v138,v139\nzr v96,a136\n"
"zr v97,a137\nzr v98,a138\nzr v99,a139\nzr v100,a140\nzr v101,a141\nzr v102,a142\nzr v103,a143\nzc v176,v96,v97\n"
"zc v177,v98,v99\nzc v178,v100,v101\nzc v179,v102,v103\nzr v108,a168\nzr v109,a169\nzr v110,a170\nzr v111,a171\nzr v112,a172\n"
"zr v113,a173\nzr v114,a174\nzr v115,a175\nzc v180,v108,v109\nzc v181,v110,v111\nzc v182,v112,v113\nzc v183,v114,v115\nzr v120,a200\n"
"zr v121,a201\nzr v122,a202\nzr v123,a203\nzr v124,a204\nzr v125,a205\nzr v126,a206\nzr v127,a207\nzc v184,v120,v121\n"
"zc v185,v122,v123\nzc v186,v124,v125\nzc v187,v126,v127\nzr v132,a232\nzr v133,a233\nzr v134,a234\nzr v135,a235\nzr v136,a236\n"
"zr v137,a237\nzr v138,a238\nzr v139,a239\nzc v188,v132,v133\nzc v189,v134,v135\nzc v190,v136,v137\nzc v191,v138,v139\nzr v96,a16\n"
"zr v97,a17\nzr v98,a18\nzr v99,a19\nzr v100,a20\nzr v101,a21\nzr v102,a22\nzr v103,a23\nzc v192,v96,v97\n"
"zc v193,v98,v99\nzc v194,v100,v101\nzc v195,v102,v103\nzr v108,a48\nzr v109,a49\nzr v110,a50\nzr v111,a51\nzr v112,a52\n"
"zr v113,a53\nzr v114,a54\nzr v115,a55\nzc v196,v108,v109\nzc v197,v110,v111\nzc v198,v112,v113\nzc v199,v114,v115\nzr v120,a80\n"
"zr v121,a81\nzr v122,a82\nzr v123,a83\nzr v124,a84\nzr v125,a85\nzr v126,a86\nzr v127,a87\nzc v200,v120,v121\n"
"zc v201,v122,v123\nzc v202,v124,v125\nzc v203,v126,v127\nzr v132,a112\nzr v133,a113\nzr v134,a114\nzr v135,a115\nzr v136,a116\n"
"zr v137,a117\nzr v138,a118\nzr v139,a119\nzc v204,v132,v133\nzc v205,v134,v135\nzc v206,v136,v137\nzc v207,v138,v139\nzr v96,a144\n"
"zr v97,a145\nzr v98,a146\nzr v99,a147\nzr v100,a148\nzr v101,a149\nzr v102,a150\nzr v103,a151\nzc v208,v96,v97\n"
"zc v209,v98,v99\nzc v210,v100,v101\nzc v211,v102,v103\nzr v108,a176\nzr v109,a177\nzr v110,a178\nzr v111,a179\nzr v112,a180\n"
"zr v113,a181\nzr v114,a182\nzr v115,a183\nzc v212,v108,v109\nzc v213,v110,v111\nzc v214,v112,v113\nzc v215,v114,v115\nzr v120,a208\n"
"zr v121,a209\nzr v122,a210\nzr v123,a211\nzr v124,a212\nzr v125,a213\nzr v126,a214\nzr v127,a215\nzc v216,v120,v121\n"
"zc v217,v122,v123\nzc v218,v124,v125\nzc v219,v126,v127\nzr v132,a240\nzr v133,a241\nzr v134,a242\nzr v135,a243\nzr v136,a244\n"
"zr v137,a245\nzr v138,a246\nzr v139,a247\nzc v220,v132,v133\nzc v221,v134,v135\nzc v222,v136,v137\nzc v223,v138,v139\nzr v96,a24\n"
"zr v97,a25\nzr v98,a26\nzr v99,a27\nzr v100,a28\nzr v101,a29\nzr v102,a30\nzr v103,a31\nzc v224,v96,v97\n"
"zc v225,v98,v99\nzc v226,v100,v101\nzc v227,v102,v103\nzr v108,a56\nzr v109,a57\nzr v110,a58\nzr v111,a59\nzr v112,a60\n"
"zr v113,a61\nzr v114,a62\nzr v115,a63\nzc v228,v108,v109\nzc v229,v110,v111\nzc v230,v112,v113\nzc v231,v114,v115\nzr v120,a88\n"
"zr v121,a89\nzr v122,a90\nzr v123,a91\nzr v124,a92\nzr v125,a93\nzr v126,a94\nzr v127,a95\nzc v232,v120,v121\n"
"zc v233,v122,v123\nzc v234,v124,v125\nzc v235,v126,v127\nzr v132,a120\nzr v133,a121\nzr v134,a122\nzr v135,a123\nzr v136,a124\n"
"zr v137,a125\nzr v138,a126\nzr v139,a127\nzc v236,v132,v133\nzc v237,v134,v135\nzc v238,v136,v137\nzc v239,v138,v139\nzr v96,a152\n"
"zr v97,a153\nzr v98,a154\nzr v99,a155\nzr v100,a156\nzr v101,a157\nzr v102,a158\nzr v103,a159\nzc v240,v96,v97\n"
"zc v241,v98,v99\nzc v242,v100,v101\nzc v243,v102,v103\nzr v108,a184\nzr v109,a185\nzr v110,a186\nzr v111,a187\nzr v112,a188\n"
"zr v113,a189\nzr v114,a190\nzr v115,a191\nzc v244,v108,v109\nzc v245,v110,v111\nzc v246,v112,v113\nzc v247,v114,v115\nzr v120,a216\n"
"zr v121,a217\nzr v122,a218\nzr v123,a219\nzr v124,a220\nzr v125,a221\nzr v126,a222\nzr v127,a223\nzc v248,v120,v121\n"
"zc v249,v122,v123\nzc v250,v124,v125\nzc v251,v126,v127\nzr v132,a248\nzr v133,a249\nzr v134,a250\nzr v135,a251\nzr v136,a252\n"
"zr v137,a253\nzr v138,a254\nzr v139,a255\nzc v252,v132,v133\nzc v253,v134,v135\nzc v254,v136,v137\nzc v255,v138,v139\ns_lshl_b32 s79,s69,7\n"
"zp s79,s79,4128\ns_lshl_b32 s78,s70,2\nzp s79,s79,s78\ns_cmp_eq_u32 s71,2\ns_cbranch_scc1 L_P_noT2\nzm a[0:3],v[64:67],v[32:35],0\nzm a[4:7],v[64:67],v[36:39],0\nzt v[128:129],v18 offset:8192\n"
"zm a[8:11],v[64:67],v[40:43],0\nzm a[12:15],v[64:67],v[44:47],0\nzt v[130:131],v18 offset:9216\nzm a[16:19],v[64:67],v[48:51],0\nzm a[20:23],v[64:67],v[52:55],0\nzd v[96:99],v17 offset:0\nzm a[24:27],v[64:67],v[56:59],0\nzm a[28:31],v[64:67],v[60:63],0\n"
"zt v[132:133],v19 offset:8192\nzs v[164:167],v29,s[40:43],s80 offen offset:1024 sc1\nzm a[32:35],v[68:71],v[32:35],0\nzm a[36:39],v[68:71],v[36:39],0\nzt v[134:135],v19 offset:9216\nzm a[40:43],v[68:71],v[40:43],0\nzm a[44:47],v[68:71],v[44:47],0\nzd v[100:103],v17 offset:2048\n"
"zm a[48:51],v[68:71],v[48:51],0\nzm a[52:55],v[68:71],v[52:55],0\nzt v[136:137],v20 offset:8192\nzm a[56:59],v[68:71],v[56:59],0\nzm a[60:63],v[68:71],v[60:63],0\nzt v[138:139],v20 offset:9216\nzm a[64:67],v[72:75],v[32:35],0\nzm a[68:71],v[72:75],v[36:39],0\n"
"zd v[104:107],v17 offset:4096\nzm a[72:75],v[72:75],v[40:43],0\nzs v[160:163],v29,s[40:43],s80 offen offset:0 sc1\nzm a[76:79],v[72:75],v[44:47],0\nzt v[140:141],v21 offset:8192\nzm a[80:83],v[72:75],v[48:51],0\nzm a[84:87],v[72:75],v[52:55],0\nzt v[142:143],v21 offset:9216\n"
"zm a[88:91],v[72:75],v[56:59],0\nzm a[92:95],v[72:75],v[60:63],0\nzd v[108:111],v17 offset:6144\nzm a[96:99],v[76:79],v[32:35],0\nzm a[100:103],v[76:79],v[36:39],0\nzt v[144:145],v22 offset:8192\nzm a[104:107],v[76:79],v[40:43],0\nzm a[108:111],v[76:79],v[44:47],0\n"
"zt v[146:147],v22 offset:9216\nzm a[112:115],v[76:79],v[48:51],0\nzm a[116:119],v[76:79],v[52:55],0\nzd v[112:115],v17 offset:8192\nzs v[172:175],v29,s[40:43],s80 offen offset:3072 sc1\nzm a[120:123],v[76:79],v[56:59],0\nzm a[124:127],v[76:79],v[60:63],0\nzt v[148:149],v23 offset:8192\n"
"zm a[128:131],v[80:83],v[32:35],0\nzm a[132:135],v[80:83],v[36:39],0\nzt v[150:151],v23 offset:9216\nzm a[136:139],v[80:83],v[40:43],0\nzm a[140:143],v[80:83],v[44:47],0\nzd v[116:119],v17 offset:10240\nzm a[144:147],v[80:83],v[48:51],0\nzm a[148:151],v[80:83],v[52:55],0\n"
"zt v[152:153],v24 offset:8192\nzm a[152:155],v[80:83],v[56:59],0\nzm a[156:159],v[80:83],v[60:63],0\nzt v[154:155],v24 offset:9216\nzm a[160:163],v[84:87],v[32:35],0\nzs v[168:171],v29,s[40:43],s80 offen offset:2048 sc1\nzm a[164:167],v[84:87],v[36:39],0\nzd v[120:123],v17 offset:12288\n"
"zm a[168:171],v[84:87],v[40:43],0\nzm a[172:175],v[84:87],v[44:47],0\nzt v[156:157],v25 offset:8192\nzm a[176:179],v[84:87],v[48:51],0\nzm a[180:183],v[84:87],v[52:55],0\nzt v[158:159],v25 offset:9216\nzm a[184:187],v[84:87],v[56:59],0\nzm a[188:191],v[84:87],v[60:63],0\n"
"zd v[124:127],v17 offset:14336\nzm a[192:195],v[88:91],v[32:35],0\nzm a[196:199],v[88:91],v[36:39],0\nzm a[200:203],v[88:91],v[40:43],0\nzm a[204:207],v[88:91],v[44:47],0\nzp s80,s80,s81\nzm a[208:211],v[88:91],v[48:51],0\nzo v16,0x8000,v16\n"
"zm a[212:215],v[88:91],v[52:55],0\nzo v17,0x8000,v17\nzm a[216:219],v[88:91],v[56:59],0\nzo v18,0x8000,v18\nzm a[220:223],v[88:91],v[60:63],0\nzo v19,0x8000,v19\nzm a[224:227],v[92:95],v[32:35],0\nzo v20,0x8000,v20\n"
"zm a[228:231],v[92:95],v[36:39],0\nzo v21,0x8000,v21\nzm a[232:235],v[92:95],v[40:43],0\nzo v22,0x8000,v22\nzm a[236:239],v[92:95],v[44:47],0\nzo v23,0x8000,v23\nzm a[240:243],v[92:95],v[48:51],0\nzo v24,0x8000,v24\n"
"zm a[244:247],v[92:95],v[52:55],0\nzo v25,0x8000,v25\nzm a[248:251],v[92:95],v[56:59],0\nzm a[252:255],v[92:95],v[60:63],0\nzv vmcnt(13) lgkmcnt(0)\ns_barrier\nzm a[0:3],v[128:131],v[96:99],a[0:3]\nzt v[64:65],v18 offset:0\n"
"s_mov_b32 m0,s65\nzm a[4:7],v[128:131],v[100:103],a[4:7]\nzl v8,s[24:27],s62 offen offset:0 sc1 lds\nzm a[8:11],v[128:131],v[104:107],a[8:11]\nzt v[66:67],v18 offset:1024\nzm a[12:15],v[128:131],v[108:111],a[12:15]\nzm a[16:19],v[128:131],v[112:115],a[16:19]\nzl v9,s[24:27],s62 offen offset:1024 sc1 lds\n"
"zm a[20:23],v[128:131],v[116:119],a[20:23]\nzd v[32:35],v16 offset:0\nzm a[24:27],v[128:131],v[120:123],a[24:27]\nzt v[68:69],v19 offset:0\nzm a[28:31],v[128:131],v[124:127],a[28:31]\nzm a[32:35],v[132:135],v[96:99],a[32:35]\nzl v10,s[24:27],s62 offen offset:2048 sc1 lds\nzm a[36:39],v[132:135],v[100:103],a[36:39]\n"
"zt v[70:71],v19 offset:1024\nzm a[40:43],v[132:135],v[104:107],a[40:43]\nzd v[36:39],v16 offset:2048\nzm a[44:47],v[132:135],v[108:111],a[44:47]\nzm a[48:51],v[132:135],v[112:115],a[48:51]\nzl v11,s[24:27],s62 offen offset:3072 sc1 lds\nzm a[52:55],v[132:135],v[116:119],a[52:55]\nzt v[72:73],v20 offset:0\n"
"zm a[56:59],v[132:135],v[120:123],a[56:59]\nzm a[60:63],v[132:135],v[124:127],a[60:63]\nzt v[74:75],v20 offset:1024\nzp m0,s65,4096\nzm a[64:67],v[136:139],v[96:99],a[64:67]\nzl v8,s[24:27],s63 offen offset:0 sc1 lds\nzm a[68:71],v[136:139],v[100:103],a[68:71]\nzd v[40:43],v16 offset:4096\n"
"zm a[72:75],v[136:139],v[104:107],a[72:75]\nzm a[76:79],v[136:139],v[108:111],a[76:79]\nzt v[76:77],v21 offset:0\nzm a[80:83],v[136:139],v[112:115],a[80:83]\nzl v9,s[24:27],s63 offen offset:1024 sc1 lds\nzm a[84:87],v[136:139],v[116:119],a[84:87]\nzt v[78:79],v21 offset:1024\nzm a[88:91],v[136:139],v[120:123],a[88:91]\n"
"zm a[92:95],v[136:139],v[124:127],a[92:95]\nzl v10,s[24:27],s63 offen offset:2048 sc1 lds\nzm a[96:99],v[140:143],v[96:99],a[96:99]\nzd v[44:47],v16 offset:6144\nzm a[100:103],v[140:143],v[100:103],a[100:103]\nzm a[104:107],v[140:143],v[104:107],a[104:107]\nzt v[80:81],v22 offset:0\nzm a[108:111],v[140:143],v[108:111],a[108:111]\n"
"zl v11,s[24:27],s63 offen offset:3072 sc1 lds\nzm a[112:115],v[140:143],v[112:115],a[112:115]\nzt v[82:83],v22 offset:1024\nzm a[116:119],v[140:143],v[116:119],a[116:119]\nzm a[120:123],v[140:143],v[120:123],a[120:123]\nzd v[48:51],v16 offset:8192\ns_mov_b32 m0,s66\nzm a[124:127],v[140:143],v[124:127],a[124:127]\n"
"zl v12,s[28:31],s64 offen offset:0 lds\nzm a[128:131],v[144:147],v[96:99],a[128:131]\nzt v[84:85],v23 offset:0\nzm a[132:135],v[144:147],v[100:103],a[132:135]\nzm a[136:139],v[144:147],v[104:107],a[136:139]\nzt v[86:87],v23 offset:1024\nzm a[140:143],v[144:147],v[108:111],a[140:143]\nzl v13,s[28:31],s64 offen offset:1024 lds\n"
"zm a[144:147],v[144:147],v[112:115],a[144:147]\nzd v[52:55],v16 offset:10240\nzm a[148:151],v[144:147],v[116:119],a[148:151]\nzm a[152:155],v[144:147],v[120:123],a[152:155]\nzt v[88:89],v24 offset:0\nzm a[156:159],v[144:147],v[124:127],a[156:159]\nzl v14,s[28:31],s64 offen offset:2048 lds\nzm a[160:163],v[148:151],v[96:99],a[160:163]\n"
"zm a[164:167],v[148:151],v[100:103],a[164:167]\nzt v[90:91],v24 offset:1024\nzm a[168:171],v[148:151],v[104:107],a[168:171]\nzl v15,s[28:31],s64 offen offset:3072 lds\nzm a[172:175],v[148:151],v[108:111],a[172:175]\nzd v[56:59],v16 offset:12288\nzm a[176:179],v[148:151],v[112:115],a[176:179]\nzm a[180:183],v[148:151],v[116:119],a[180:183]\n"
"zt v[92:93],v25 offset:0\nzp m0,s66,16384\nzm a[184:187],v[148:151],v[120:123],a[184:187]\nzl v12,s[36:39],s64 offen offset:0 lds\nzm a[188:191],v[148:151],v[124:127],a[188:191]\nzt v[94:95],v25 offset:1024\nzm a[192:195],v[152:155],v[96:99],a[192:195]\nzm a[196:199],v[152:155],v[100:103],a[196:199]\n"
"zd v[60:63],v16 offset:14336\nzm a[200:203],v[152:155],v[104:107],a[200:203]\nzl v13,s[36:39],s64 offen offset:1024 lds\nzm a[204:207],v[152:155],v[108:111],a[204:207]\nzm a[208:211],v[152:155],v[112:115],a[208:211]\nzm a[212:215],v[152:155],v[116:119],a[212:215]\nzm a[216:219],v[152:155],v[120:123],a[216:219]\nzl v14,s[36:39],s64 offen offset:2048 lds\n"
"zm a[220:223],v[152:155],v[124:127],a[220:223]\nzm a[224:227],v[156:159],v[96:99],a[224:227]\nzm a[228:231],v[156:159],v[100:103],a[228:231]\nzm a[232:235],v[156:159],v[104:107],a[232:235]\nzl v15,s[36:39],s64 offen offset:3072 lds\nzm a[236:239],v[156:159],v[108:111],a[236:239]\nzm a[240:243],v[156:159],v[112:115],a[240:243]\nzm a[244:247],v[156:159],v[116:119],a[244:247]\n"
"zm a[248:251],v[156:159],v[120:123],a[248:251]\nzp s62,s62,128\nzp s63,s63,128\nzp s64,s64,s74\nzm a[252:255],v[156:159],v[124:127],a[252:255]\ns_xor_b32 s65,s65,0x8000\ns_xor_b32 s66,s66,0x8000\nzv lgkmcnt(0)\n"
"zm a[0:3],v[64:67],v[32:35],a[0:3]\nzm a[4:7],v[64:67],v[36:39],a[4:7]\nzt v[128:129],v18 offset:8192\nzm a[8:11],v[64:67],v[40:43],a[8:11]\nzm a[12:15],v[64:67],v[44:47],a[12:15]\nzt v[130:131],v18 offset:9216\nzm a[16:19],v[64:67],v[48:51],a[16:19]\nzm a[20:23],v[64:67],v[52:55],a[20:23]\n"
"zd v[96:99],v17 offset:0\nzm a[24:27],v[64:67],v[56:59],a[24:27]\nzm a[28:31],v[64:67],v[60:63],a[28:31]\nzt v[132:133],v19 offset:8192\nzs v[180:183],v29,s[40:43],s80 offen offset:1024 sc1\nzm a[32:35],v[68:71],v[32:35],a[32:35]\nzm a[36:39],v[68:71],v[36:39],a[36:39]\nzt v[134:135],v19 offset:9216\n"
"zm a[40:43],v[68:71],v[40:43],a[40:43]\nzm a[44:47],v[68:71],v[44:47],a[44:47]\nzd v[100:103],v17 offset:2048\nzm a[48:51],v[68:71],v[48:51],a[48:51]\nzm a[52:55],v[68:71],v[52:55],a[52:55]\nzt v[136:137],v20 offset:8192\nzm a[56:59],v[68:71],v[56:59],a[56:59]\nzm a[60:63],v[68:71],v[60:63],a[60:63]\n"
"zt v[138:139],v20 offset:9216\nzm a[64:67],v[72:75],v[32:35],a[64:67]\nzm a[68:71],v[72:75],v[36:39],a[68:71]\nzd v[104:107],v17 offset:4096\nzm a[72:75],v[72:75],v[40:43],a[72:75]\nzs v[176:179],v29,s[40:43],s80 offen offset:0 sc1\nzm a[76:79],v[72:75],v[44:47],a[76:79]\nzt v[140:141],v21 offset:8192\n"
"zm a[80:83],v[72:75],v[48:51],a[80:83]\nzm a[84:87],v[72:75],v[52:55],a[84:87]\nzt v[142:143],v21 offset:9216\nzm a[88:91],v[72:75],v[56:59],a[88:91]\nzm a[92:95],v[72:75],v[60:63],a[92:95]\nzd v[108:111],v17 offset:6144\nzm a[96:99],v[76:79],v[32:35],a[96:99]\nzm a[100:103],v[76:79],v[36:39],a[100:103]\n"
"zt v[144:145],v22 offset:8192\nzm a[104:107],v[76:79],v[40:43],a[104:107]\nzm a[108:111],v[76:79],v[44:47],a[108:111]\nzt v[146:147],v22 offset:9216\nzm a[112:115],v[76:79],v[48:51],a[112:115]\nzm a[116:119],v[76:79],v[52:55],a[116:119]\nzd v[112:115],v17 offset:8192\nzs v[188:191],v29,s[40:43],s80 offen offset:3072 sc1\n"
"zm a[120:123],v[76:79],v[56:59],a[120:123]\nzm a[124:127],v[76:79],v[60:63],a[124:127]\nzt v[148:149],v23 offset:8192\nzm a[128:131],v[80:83],v[32:35],a[128:131]\nzm a[132:135],v[80:83],v[36:39],a[132:135]\nzt v[150:151],v23 offset:9216\nzm a[136:139],v[80:83],v[40:43],a[136:139]\nzm a[140:143],v[80:83],v[44:47],a[140:143]\n"
"zd v[116:119],v17 offset:10240\nzm a[144:147],v[80:83],v[48:51],a[144:147]\nzm a[148:151],v[80:83],v[52:55],a[148:151]\nzt v[152:153],v24 offset:8192\nzm a[152:155],v[80:83],v[56:59],a[152:155]\nzm a[156:159],v[80:83],v[60:63],a[156:159]\nzt v[154:155],v24 offset:9216\nzm a[160:163],v[84:87],v[32:35],a[160:163]\n"
"zs v[184:187],v29,s[40:43],s80 offen offset:2048 sc1\nzm a[164:167],v[84:87],v[36:39],a[164:167]\nzd v[120:123],v17 offset:12288\nzm a[168:171],v[84:87],v[40:43],a[168:171]\nzm a[172:175],v[84:87],v[44:47],a[172:175]\nzt v[156:157],v25 offset:8192\nzm a[176:179],v[84:87],v[48:51],a[176:179]\nzm a[180:183],v[84:87],v[52:55],a[180:183]\n"
"zt v[158:159],v25 offset:9216\nzm a[184:187],v[84:87],v[56:59],a[184:187]\nzm a[188:191],v[84:87],v[60:63],a[188:191]\nzd v[124:127],v17 offset:14336\nzm a[192:195],v[88:91],v[32:35],a[192:195]\nzm a[196:199],v[88:91],v[36:39],a[196:199]\nzm a[200:203],v[88:91],v[40:43],a[200:203]\nzm a[204:207],v[88:91],v[44:47],a[204:207]\n"
"zp s80,s80,s82\nzm a[208:211],v[88:91],v[48:51],a[208:211]\nzo v16,0x8000,v16\nzm a[212:215],v[88:91],v[52:55],a[212:215]\nzo v17,0x8000,v17\nzm a[216:219],v[88:91],v[56:59],a[216:219]\nzo v18,0x8000,v18\nzm a[220:223],v[88:91],v[60:63],a[220:223]\n"
"zo v19,0x8000,v19\nzm a[224:227],v[92:95],v[32:35],a[224:227]\nzo v20,0x8000,v20\nzm a[228:231],v[92:95],v[36:39],a[228:231]\nzo v21,0x8000,v21\nzm a[232:235],v[92:95],v[40:43],a[232:235]\nzo v22,0x8000,v22\nzm a[236:239],v[92:95],v[44:47],a[236:239]\n"
"zo v23,0x8000,v23\nzm a[240:243],v[92:95],v[48:51],a[240:243]\nzo v24,0x8000,v24\nzm a[244:247],v[92:95],v[52:55],a[244:247]\nzo v25,0x8000,v25\nzm a[248:251],v[92:95],v[56:59],a[248:251]\nzm a[252:255],v[92:95],v[60:63],a[252:255]\nzv vmcnt(4) lgkmcnt(0)\n"
"s_barrier\nzm a[0:3],v[128:131],v[96:99],a[0:3]\nzt v[64:65],v18 offset:0\ns_mov_b32 m0,s65\nzm a[4:7],v[128:131],v[100:103],a[4:7]\nzl v8,s[24:27],s62 offen offset:0 sc1 lds\nzm a[8:11],v[128:131],v[104:107],a[8:11]\nzt v[66:67],v18 offset:1024\n"
"zm a[12:15],v[128:131],v[108:111],a[12:15]\nzm a[16:19],v[128:131],v[112:115],a[16:19]\nzl v9,s[24:27],s62 offen offset:1024 sc1 lds\nzm a[20:23],v[128:131],v[116:119],a[20:23]\nzd v[32:35],v16 offset:0\nzm a[24:27],v[128:131],v[120:123],a[24:27]\nzt v[68:69],v19 offset:0\nzm a[28:31],v[128:131],v[124:127],a[28:31]\n"
"zm a[32:35],v[132:135],v[96:99],a[32:35]\nzl v10,s[24:27],s62 offen offset:2048 sc1 lds\nzm a[36:39],v[132:135],v[100:103],a[36:39]\nzt v[70:71],v19 offset:1024\nzm a[40:43],v[132:135],v[104:107],a[40:43]\nzd v[36:39],v16 offset:2048\nzm a[44:47],v[132:135],v[108:111],a[44:47]\nzm a[48:51],v[132:135],v[112:115],a[48:51]\n"
"zl v11,s[24:27],s62 offen offset:3072 sc1 lds\nzm a[52:55],v[132:135],v[116:119],a[52:55]\nzt v[72:73],v20 offset:0\nzm a[56:59],v[132:135],v[120:123],a[56:59]\nzm a[60:63],v[132:135],v[124:127],a[60:63]\nzt v[74:75],v20 offset:1024\nzp m0,s65,4096\nzm a[64:67],v[136:139],v[96:99],a[64:67]\n"
"zl v8,s[24:27],s63 offen offset:0 sc1 lds\nzm a[68:71],v[136:139],v[100:103],a[68:71]\nzd v[40:43],v16 offset:4096\nzm a[72:75],v[136:139],v[104:107],a[72:75]\nzm a[76:79],v[136:139],v[108:111],a[76:79]\nzt v[76:77],v21 offset:0\nzm a[80:83],v[136:139],v[112:115],a[80:83]\nzl v9,s[24:27],s63 offen offset:1024 sc1 lds\n"
"zm a[84:87],v[136:139],v[116:119],a[84:87]\nzt v[78:79],v21 offset:1024\nzm a[88:91],v[136:139],v[120:123],a[88:91]\nzm a[92:95],v[136:139],v[124:127],a[92:95]\nzl v10,s[24:27],s63 offen offset:2048 sc1 lds\nzm a[96:99],v[140:143],v[96:99],a[96:99]\nzd v[44:47],v16 offset:6144\nzm a[100:103],v[140:143],v[100:103],a[100:103]\n"
"zm a[104:107],v[140:143],v[104:107],a[104:107]\nzt v[80:81],v22 offset:0\nzm a[108:111],v[140:143],v[108:111],a[108:111]\nzl v11,s[24:27],s63 offen offset:3072 sc1 lds\nzm a[112:115],v[140:143],v[112:115],a[112:115]\nzt v[82:83],v22 offset:1024\nzm a[116:119],v[140:143],v[116:119],a[116:119]\nzm a[120:123],v[140:143],v[120:123],a[120:123]\n"
"zd v[48:51],v16 offset:8192\ns_mov_b32 m0,s66\nzm a[124:127],v[140:143],v[124:127],a[124:127]\nzl v12,s[28:31],s64 offen offset:0 lds\nzm a[128:131],v[144:147],v[96:99],a[128:131]\nzt v[84:85],v23 offset:0\nzm a[132:135],v[144:147],v[100:103],a[132:135]\nzm a[136:139],v[144:147],v[104:107],a[136:139]\n"
"zt v[86:87],v23 offset:1024\nzm a[140:143],v[144:147],v[108:111],a[140:143]\nzl v13,s[28:31],s64 offen offset:1024 lds\nzm a[144:147],v[144:147],v[112:115],a[144:147]\nzd v[52:55],v16 offset:10240\nzm a[148:151],v[144:147],v[116:119],a[148:151]\nzm a[152:155],v[144:147],v[120:123],a[152:155]\nzt v[88:89],v24 offset:0\n"
"zm a[156:159],v[144:147],v[124:127],a[156:159]\nzl v14,s[28:31],s64 offen offset:2048 lds\nzm a[160:163],v[148:151],v[96:99],a[160:163]\nzm a[164:167],v[148:151],v[100:103],a[164:167]\nzt v[90:91],v24 offset:1024\nzm a[168:171],v[148:151],v[104:107],a[168:171]\nzl v15,s[28:31],s64 offen offset:3072 lds\nzm a[172:175],v[148:151],v[108:111],a[172:175]\n"
"zd v[56:59],v16 offset:12288\nzm a[176:179],v[148:151],v[112:115],a[176:179]\nzm a[180:183],v[148:151],v[116:119],a[180:183]\nzt v[92:93],v25 offset:0\nzp m0,s66,16384\nzm a[184:187],v[148:151],v[120:123],a[184:187]\nzl v12,s[36:39],s64 offen offset:0 lds\nzm a[188:191],v[148:151],v[124:127],a[188:191]\n"
"zt v[94:95],v25 offset:1024\nzm a[192:195],v[152:155],v[96:99],a[192:195]\nzm a[196:199],v[152:155],v[100:103],a[196:199]\nzd v[60:63],v16 offset:14336\nzm a[200:203],v[152:155],v[104:107],a[200:203]\nzl v13,s[36:39],s64 offen offset:1024 lds\nzm a[204:207],v[152:155],v[108:111],a[204:207]\nzm a[208:211],v[152:155],v[112:115],a[208:211]\n"
"zm a[212:215],v[152:155],v[116:119],a[212:215]\nzm a[216:219],v[152:155],v[120:123],a[216:219]\nzl v14,s[36:39],s64 offen offset:2048 lds\nzm a[220:223],v[152:155],v[124:127],a[220:223]\nzm a[224:227],v[156:159],v[96:99],a[224:227]\nzm a[228:231],v[156:159],v[100:103],a[228:231]\nzm a[232:235],v[156:159],v[104:107],a[232:235]\nzl v15,s[36:39],s64 offen offset:3072 lds\n"
"zm a[236:239],v[156:159],v[108:111],a[236:239]\nzm a[240:243],v[156:159],v[112:115],a[240:243]\nzm a[244:247],v[156:159],v[116:119],a[244:247]\nzm a[248:251],v[156:159],v[120:123],a[248:251]\nzp s62,s62,128\nzp s63,s63,128\nzp s64,s64,s74\nzm a[252:255],v[156:159],v[124:127],a[252:255]\n"
"s_xor_b32 s65,s65,0x8000\ns_xor_b32 s66,s66,0x8000\nzv lgkmcnt(0)\nzm a[0:3],v[64:67],v[32:35],a[0:3]\nzm a[4:7],v[64:67],v[36:39],a[4:7]\nzt v[128:129],v18 offset:8192\nzm a[8:11],v[64:67],v[40:43],a[8:11]\nzm a[12:15],v[64:67],v[44:47],a[12:15]\n"
"zt v[130:131],v18 offset:9216\nzm a[16:19],v[64:67],v[48:51],a[16:19]\nzm a[20:23],v[64:67],v[52:55],a[20:23]\nzd v[96:99],v17 offset:0\nzm a[24:27],v[64:67],v[56:59],a[24:27]\nzm a[28:31],v[64:67],v[60:63],a[28:31]\nzt v[132:133],v19 offset:8192\nzs v[196:199],v29,s[40:43],s80 offen offset:1024 sc1\n"
"zm a[32:35],v[68:71],v[32:35],a[32:35]\nzm a[36:39],v[68:71],v[36:39],a[36:39]\nzt v[134:135],v19 offset:9216\nzm a[40:43],v[68:71],v[40:43],a[40:43]\nzm a[44:47],v[68:71],v[44:47],a[44:47]\nzd v[100:103],v17 offset:2048\nzm a[48:51],v[68:71],v[48:51],a[48:51]\nzm a[52:55],v[68:71],v[52:55],a[52:55]\n"
"zt v[136:137],v20 offset:8192\nzm a[56:59],v[68:71],v[56:59],a[56:59]\nzm a[60:63],v[68:71],v[60:63],a[60:63]\nzt v[138:139],v20 offset:9216\nzm a[64:67],v[72:75],v[32:35],a[64:67]\nzm a[68:71],v[72:75],v[36:39],a[68:71]\nzd v[104:107],v17 offset:4096\nzm a[72:75],v[72:75],v[40:43],a[72:75]\n"
"zs v[192:195],v29,s[40:43],s80 offen offset:0 sc1\nzm a[76:79],v[72:75],v[44:47],a[76:79]\nzt v[140:141],v21 offset:8192\nzm a[80:83],v[72:75],v[48:51],a[80:83]\nzm a[84:87],v[72:75],v[52:55],a[84:87]\nzt v[142:143],v21 offset:9216\nzm a[88:91],v[72:75],v[56:59],a[88:91]\nzm a[92:95],v[72:75],v[60:63],a[92:95]\n"
"zd v[108:111],v17 offset:6144\nzm a[96:99],v[76:79],v[32:35],a[96:99]\nzm a[100:103],v[76:79],v[36:39],a[100:103]\nzt v[144:145],v22 offset:8192\nzm a[104:107],v[76:79],v[40:43],a[104:107]\nzm a[108:111],v[76:79],v[44:47],a[108:111]\nzt v[146:147],v22 offset:9216\nzm a[112:115],v[76:79],v[48:51],a[112:115]\n"
"zm a[116:119],v[76:79],v[52:55],a[116:119]\nzd v[112:115],v17 offset:8192\nzs v[204:207],v29,s[40:43],s80 offen offset:3072 sc1\nzm a[120:123],v[76:79],v[56:59],a[120:123]\nzm a[124:127],v[76:79],v[60:63],a[124:127]\nzt v[148:149],v23 offset:8192\nzm a[128:131],v[80:83],v[32:35],a[128:131]\nzm a[132:135],v[80:83],v[36:39],a[132:135]\n"
"zt v[150:151],v23 offset:9216\nzm a[136:139],v[80:83],v[40:43],a[136:139]\nzm a[140:143],v[80:83],v[44:47],a[140:143]\nzd v[116:119],v17 offset:10240\nzm a[144:147],v[80:83],v[48:51],a[144:147]\nzm a[148:151],v[80:83],v[52:55],a[148:151]\nzt v[152:153],v24 offset:8192\nzm a[152:155],v[80:83],v[56:59],a[152:155]\n"
"zm a[156:159],v[80:83],v[60:63],a[156:159]\nzt v[154:155],v24 offset:9216\nzm a[160:163],v[84:87],v[32:35],a[160:163]\nzs v[200:203],v29,s[40:43],s80 offen offset:2048 sc1\nzm a[164:167],v[84:87],v[36:39],a[164:167]\nzd v[120:123],v17 offset:12288\nzm a[168:171],v[84:87],v[40:43],a[168:171]\nzm a[172:175],v[84:87],v[44:47],a[172:175]\n"
"zt v[156:157],v25 offset:8192\nzm a[176:179],v[84:87],v[48:51],a[176:179]\nzm a[180:183],v[84:87],v[52:55],a[180:183]\nzt v[158:159],v25 offset:9216\nzm a[184:187],v[84:87],v[56:59],a[184:187]\nzm a[188:191],v[84:87],v[60:63],a[188:191]\nzd v[124:127],v17 offset:14336\nzm a[192:195],v[88:91],v[32:35],a[192:195]\n"
"zm a[196:199],v[88:91],v[36:39],a[196:199]\nzm a[200:203],v[88:91],v[40:43],a[200:203]\nzm a[204:207],v[88:91],v[44:47],a[204:207]\nzp s80,s80,s81\nzm a[208:211],v[88:91],v[48:51],a[208:211]\nzo v16,0x8000,v16\nzm a[212:215],v[88:91],v[52:55],a[212:215]\nzo v17,0x8000,v17\n"
"zm a[216:219],v[88:91],v[56:59],a[216:219]\nzo v18,0x8000,v18\nzm a[220:223],v[88:91],v[60:63],a[220:223]\nzo v19,0x8000,v19\nzm a[224:227],v[92:95],v[32:35],a[224:227]\nzo v20,0x8000,v20\nzm a[228:231],v[92:95],v[36:39],a[228:231]\nzo v21,0x8000,v21\n"
"zm a[232:235],v[92:95],v[40:43],a[232:235]\nzo v22,0x8000,v22\nzm a[236:239],v[92:95],v[44:47],a[236:239]\nzo v23,0x8000,v23\nzm a[240:243],v[92:95],v[48:51],a[240:243]\nzo v24,0x8000,v24\nzm a[244:247],v[92:95],v[52:55],a[244:247]\nzo v25,0x8000,v25\n"
"zm a[248:251],v[92:95],v[56:59],a[248:251]\nzm a[252:255],v[92:95],v[60:63],a[252:255]\nzv vmcnt(4) lgkmcnt(0)\ns_barrier\nzm a[0:3],v[128:131],v[96:99],a[0:3]\nzt v[64:65],v18 offset:0\ns_mov_b32 m0,s65\nzm a[4:7],v[128:131],v[100:103],a[4:7]\n"
"zl v8,s[24:27],s62 offen offset:0 sc1 lds\nzm a[8:11],v[128:131],v[104:107],a[8:11]\nzt v[66:67],v18 offset:1024\nzm a[12:15],v[128:131],v[108:111],a[12:15]\nzm a[16:19],v[128:131],v[112:115],a[16:19]\nzl v9,s[24:27],s62 offen offset:1024 sc1 lds\nzm a[20:23],v[128:131],v[116:119],a[20:23]\nzd v[32:35],v16 offset:0\n"
"zm a[24:27],v[128:131],v[120:123],a[24:27]\nzt v[68:69],v19 offset:0\nzm a[28:31],v[128:131],v[124:127],a[28:31]\nzm a[32:35],v[132:135],v[96:99],a[32:35]\nzl v10,s[24:27],s62 offen offset:2048 sc1 lds\nzm a[36:39],v[132:135],v[100:103],a[36:39]\nzt v[70:71],v19 offset:1024\nzm a[40:43],v[132:135],v[104:107],a[40:43]\n"
"zd v[36:39],v16 offset:2048\nzm a[44:47],v[132:135],v[108:111],a[44:47]\nzm a[48:51],v[132:135],v[112:115],a[48:51]\nzl v11,s[24:27],s62 offen offset:3072 sc1 lds\nzm a[52:55],v[132:135],v[116:119],a[52:55]\nzt v[72:73],v20 offset:0\nzm a[56:59],v[132:135],v[120:123],a[56:59]\nzm a[60:63],v[132:135],v[124:127],a[60:63]\n"
"zt v[74:75],v20 offset:1024\nzp m0,s65,4096\nzm a[64:67],v[136:139],v[96:99],a[64:67]\nzl v8,s[24:27],s63 offen offset:0 sc1 lds\nzm a[68:71],v[136:139],v[100:103],a[68:71]\nzd v[40:43],v16 offset:4096\nzm a[72:75],v[136:139],v[104:107],a[72:75]\nzm a[76:79],v[136:139],v[108:111],a[76:79]\n"
"zt v[76:77],v21 offset:0\nzm a[80:83],v[136:139],v[112:115],a[80:83]\nzl v9,s[24:27],s63 offen offset:1024 sc1 lds\nzm a[84:87],v[136:139],v[116:119],a[84:87]\nzt v[78:79],v21 offset:1024\nzm a[88:91],v[136:139],v[120:123],a[88:91]\nzm a[92:95],v[136:139],v[124:127],a[92:95]\nzl v10,s[24:27],s63 offen offset:2048 sc1 lds\n"
"zm a[96:99],v[140:143],v[96:99],a[96:99]\nzd v[44:47],v16 offset:6144\nzm a[100:103],v[140:143],v[100:103],a[100:103]\nzm a[104:107],v[140:143],v[104:107],a[104:107]\nzt v[80:81],v22 offset:0\nzm a[108:111],v[140:143],v[108:111],a[108:111]\nzl v11,s[24:27],s63 offen offset:3072 sc1 lds\nzm a[112:115],v[140:143],v[112:115],a[112:115]\n"
"zt v[82:83],v22 offset:1024\nzm a[116:119],v[140:143],v[116:119],a[116:119]\nzm a[120:123],v[140:143],v[120:123],a[120:123]\nzd v[48:51],v16 offset:8192\ns_mov_b32 m0,s66\nzm a[124:127],v[140:143],v[124:127],a[124:127]\nzl v12,s[28:31],s64 offen offset:0 lds\nzm a[128:131],v[144:147],v[96:99],a[128:131]\n"
"zt v[84:85],v23 offset:0\nzm a[132:135],v[144:147],v[100:103],a[132:135]\nzm a[136:139],v[144:147],v[104:107],a[136:139]\nzt v[86:87],v23 offset:1024\nzm a[140:143],v[144:147],v[108:111],a[140:143]\nzl v13,s[28:31],s64 offen offset:1024 lds\nzm a[144:147],v[144:147],v[112:115],a[144:147]\nzd v[52:55],v16 offset:10240\n"
"zm a[148:151],v[144:147],v[116:119],a[148:151]\nzm a[152:155],v[144:147],v[120:123],a[152:155]\nzt v[88:89],v24 offset:0\nzm a[156:159],v[144:147],v[124:127],a[156:159]\nzl v14,s[28:31],s64 offen offset:2048 lds\nzm a[160:163],v[148:151],v[96:99],a[160:163]\nzm a[164:167],v[148:151],v[100:103],a[164:167]\nzt v[90:91],v24 offset:1024\n"
"zm a[168:171],v[148:151],v[104:107],a[168:171]\nzl v15,s[28:31],s64 offen offset:3072 lds\nzm a[172:175],v[148:151],v[108:111],a[172:175]\nzd v[56:59],v16 offset:12288\nzm a[176:179],v[148:151],v[112:115],a[176:179]\nzm a[180:183],v[148:151],v[116:119],a[180:183]\nzt v[92:93],v25 offset:0\nzp m0,s66,16384\n"
"zm a[184:187],v[148:151],v[120:123],a[184:187]\nzl v12,s[36:39],s64 offen offset:0 lds\nzm a[188:191],v[148:151],v[124:127],a[188:191]\nzt v[94:95],v25 offset:1024\nzm a[192:195],v[152:155],v[96:99],a[192:195]\nzm a[196:199],v[152:155],v[100:103],a[196:199]\nzd v[60:63],v16 offset:14336\nzm a[200:203],v[152:155],v[104:107],a[200:203]\n"
"zl v13,s[36:39],s64 offen offset:1024 lds\nzm a[204:207],v[152:155],v[108:111],a[204:207]\nzm a[208:211],v[152:155],v[112:115],a[208:211]\nzm a[212:215],v[152:155],v[116:119],a[212:215]\nzm a[216:219],v[152:155],v[120:123],a[216:219]\nzl v14,s[36:39],s64 offen offset:2048 lds\nzm a[220:223],v[152:155],v[124:127],a[220:223]\nzm a[224:227],v[156:159],v[96:99],a[224:227]\n"
"zm a[228:231],v[156:159],v[100:103],a[228:231]\nzm a[232:235],v[156:159],v[104:107],a[232:235]\nzl v15,s[36:39],s64 offen offset:3072 lds\nzm a[236:239],v[156:159],v[108:111],a[236:239]\nzm a[240:243],v[156:159],v[112:115],a[240:243]\nzm a[244:247],v[156:159],v[116:119],a[244:247]\nzm a[248:251],v[156:159],v[120:123],a[248:251]\nzp s62,s62,128\n"
"zp s63,s63,128\nzp s64,s64,s74\nzm a[252:255],v[156:159],v[124:127],a[252:255]\ns_xor_b32 s65,s65,0x8000\ns_xor_b32 s66,s66,0x8000\nzv lgkmcnt(0)\nzm a[0:3],v[64:67],v[32:35],a[0:3]\nzm a[4:7],v[64:67],v[36:39],a[4:7]\n"
"zt v[128:129],v18 offset:8192\nzm a[8:11],v[64:67],v[40:43],a[8:11]\nzm a[12:15],v[64:67],v[44:47],a[12:15]\nzt v[130:131],v18 offset:9216\nzm a[16:19],v[64:67],v[48:51],a[16:19]\nzm a[20:23],v[64:67],v[52:55],a[20:23]\nzd v[96:99],v17 offset:0\nzm a[24:27],v[64:67],v[56:59],a[24:27]\n"
"zm a[28:31],v[64:67],v[60:63],a[28:31]\nzt v[132:133],v19 offset:8192\nzs v[212:215],v29,s[40:43],s80 offen offset:1024 sc1\nzm a[32:35],v[68:71],v[32:35],a[32:35]\nzm a[36:39],v[68:71],v[36:39],a[36:39]\nzt v[134:135],v19 offset:9216\nzm a[40:43],v[68:71],v[40:43],a[40:43]\nzm a[44:47],v[68:71],v[44:47],a[44:47]\n"
"zd v[100:103],v17 offset:2048\nzm a[48:51],v[68:71],v[48:51],a[48:51]\nzm a[52:55],v[68:71],v[52:55],a[52:55]\nzt v[136:137],v20 offset:8192\nzm a[56:59],v[68:71],v[56:59],a[56:59]\nzm a[60:63],v[68:71],v[60:63],a[60:63]\nzt v[138:139],v20 offset:9216\nzm a[64:67],v[72:75],v[32:35],a[64:67]\n"
"zm a[68:71],v[72:75],v[36:39],a[68:71]\nzd v[104:107],v17 offset:4096\nzm a[72:75],v[72:75],v[40:43],a[72:75]\nzs v[208:211],v29,s[40:43],s80 offen offset:0 sc1\nzm a[76:79],v[72:75],v[44:47],a[76:79]\nzt v[140:141],v21 offset:8192\nzm a[80:83],v[72:75],v[48:51],a[80:83]\nzm a[84:87],v[72:75],v[52:55],a[84:87]\n"
"zt v[142:143],v21 offset:9216\nzm a[88:91],v[72:75],v[56:59],a[88:91]\nzm a[92:95],v[72:75],v[60:63],a[92:95]\nzd v[108:111],v17 offset:6144\nzm a[96:99],v[76:79],v[32:35],a[96:99]\nzm a[100:103],v[76:79],v[36:39],a[100:103]\nzt v[144:145],v22 offset:8192\nzm a[104:107],v[76:79],v[40:43],a[104:107]\n"
"zm a[108:111],v[76:79],v[44:47],a[108:111]\nzt v[146:147],v22 offset:9216\nzm a[112:115],v[76:79],v[48:51],a[112:115]\nzm a[116:119],v[76:79],v[52:55],a[116:119]\nzd v[112:115],v17 offset:8192\nzs v[220:223],v29,s[40:43],s80 offen offset:3072 sc1\nzm a[120:123],v[76:79],v[56:59],a[120:123]\nzm a[124:127],v[76:79],v[60:63],a[124:127]\n"
"zt v[148:149],v23 offset:8192\nzm a[128:131],v[80:83],v[32:35],a[128:131]\nzm a[132:135],v[80:83],v[36:39],a[132:135]\nzt v[150:151],v23 offset:9216\nzm a[136:139],v[80:83],v[40:43],a[136:139]\nzm a[140:143],v[80:83],v[44:47],a[140:143]\nzd v[116:119],v17 offset:10240\nzm a[144:147],v[80:83],v[48:51],a[144:147]\n"
"zm a[148:151],v[80:83],v[52:55],a[148:151]\nzt v[152:153],v24 offset:8192\nzm a[152:155],v[80:83],v[56:59],a[152:155]\nzm a[156:159],v[80:83],v[60:63],a[156:159]\nzt v[154:155],v24 offset:9216\nzm a[160:163],v[84:87],v[32:35],a[160:163]\nzs v[216:219],v29,s[40:43],s80 offen offset:2048 sc1\nzm a[164:167],v[84:87],v[36:39],a[164:167]\n"
"zd v[120:123],v17 offset:12288\nzm a[168:171],v[84:87],v[40:43],a[168:171]\nzm a[172:175],v[84:87],v[44:47],a[172:175]\nzt v[156:157],v25 offset:8192\nzm a[176:179],v[84:87],v[48:51],a[176:179]\nzm a[180:183],v[84:87],v[52:55],a[180:183]\nzt v[158:159],v25 offset:9216\nzm a[184:187],v[84:87],v[56:59],a[184:187]\n"
"zm a[188:191],v[84:87],v[60:63],a[188:191]\nzd v[124:127],v17 offset:14336\nzm a[192:195],v[88:91],v[32:35],a[192:195]\nzm a[196:199],v[88:91],v[36:39],a[196:199]\nzm a[200:203],v[88:91],v[40:43],a[200:203]\nzm a[204:207],v[88:91],v[44:47],a[204:207]\nzp s80,s80,s82\nzm a[208:211],v[88:91],v[48:51],a[208:211]\n"
"zo v16,0x8000,v16\nzm a[212:215],v[88:91],v[52:55],a[212:215]\nzo v17,0x8000,v17\nzm a[216:219],v[88:91],v[56:59],a[216:219]\nzo v18,0x8000,v18\nzm a[220:223],v[88:91],v[60:63],a[220:223]\nzo v19,0x8000,v19\nzm a[224:227],v[92:95],v[32:35],a[224:227]\n"
"zo v20,0x8000,v20\nzm a[228:231],v[92:95],v[36:39],a[228:231]\nzo v21,0x8000,v21\nzm a[232:235],v[92:95],v[40:43],a[232:235]\nzo v22,0x8000,v22\nzm a[236:239],v[92:95],v[44:47],a[236:239]\nzo v23,0x8000,v23\nzm a[240:243],v[92:95],v[48:51],a[240:243]\n"
"zo v24,0x8000,v24\nzm a[244:247],v[92:95],v[52:55],a[244:247]\nzo v25,0x8000,v25\nzm a[248:251],v[92:95],v[56:59],a[248:251]\nzm a[252:255],v[92:95],v[60:63],a[252:255]\nzv vmcnt(4) lgkmcnt(0)\ns_barrier\nzm a[0:3],v[128:131],v[96:99],a[0:3]\n"
"zt v[64:65],v18 offset:0\ns_mov_b32 m0,s65\nzm a[4:7],v[128:131],v[100:103],a[4:7]\nzl v8,s[24:27],s62 offen offset:0 sc1 lds\nzm a[8:11],v[128:131],v[104:107],a[8:11]\nzt v[66:67],v18 offset:1024\nzm a[12:15],v[128:131],v[108:111],a[12:15]\nzm a[16:19],v[128:131],v[112:115],a[16:19]\n"
"zl v9,s[24:27],s62 offen offset:1024 sc1 lds\nzm a[20:23],v[128:131],v[116:119],a[20:23]\nzd v[32:35],v16 offset:0\nzm a[24:27],v[128:131],v[120:123],a[24:27]\nzt v[68:69],v19 offset:0\nzm a[28:31],v[128:131],v[124:127],a[28:31]\nzm a[32:35],v[132:135],v[96:99],a[32:35]\nzl v10,s[24:27],s62 offen offset:2048 sc1 lds\n"
"zm a[36:39],v[132:135],v[100:103],a[36:39]\nzt v[70:71],v19 offset:1024\nzm a[40:43],v[132:135],v[104:107],a[40:43]\nzd v[36:39],v16 offset:2048\nzm a[44:47],v[132:135],v[108:111],a[44:47]\nzm a[48:51],v[132:135],v[112:115],a[48:51]\nzl v11,s[24:27],s62 offen offset:3072 sc1 lds\nzm a[52:55],v[132:135],v[116:119],a[52:55]\n"
"zt v[72:73],v20 offset:0\nzm a[56:59],v[132:135],v[120:123],a[56:59]\nzm a[60:63],v[132:135],v[124:127],a[60:63]\nzt v[74:75],v20 offset:1024\nzp m0,s65,4096\nzm a[64:67],v[136:139],v[96:99],a[64:67]\nzl v8,s[24:27],s63 offen offset:0 sc1 lds\nzm a[68:71],v[136:139],v[100:103],a[68:71]\n"
"zd v[40:43],v16 offset:4096\nzm a[72:75],v[136:139],v[104:107],a[72:75]\nzm a[76:79],v[136:139],v[108:111],a[76:79]\nzt v[76:77],v21 offset:0\nzm a[80:83],v[136:139],v[112:115],a[80:83]\nzl v9,s[24:27],s63 offen offset:1024 sc1 lds\nzm a[84:87],v[136:139],v[116:119],a[84:87]\nzt v[78:79],v21 offset:1024\n"
"zm a[88:91],v[136:139],v[120:123],a[88:91]\nzm a[92:95],v[136:139],v[124:127],a[92:95]\nzl v10,s[24:27],s63 offen offset:2048 sc1 lds\nzm a[96:99],v[140:143],v[96:99],a[96:99]\nzd v[44:47],v16 offset:6144\nzm a[100:103],v[140:143],v[100:103],a[100:103]\nzm a[104:107],v[140:143],v[104:107],a[104:107]\nzt v[80:81],v22 offset:0\n"
"zm a[108:111],v[140:143],v[108:111],a[108:111]\nzl v11,s[24:27],s63 offen offset:3072 sc1 lds\nzm a[112:115],v[140:143],v[112:115],a[112:115]\nzt v[82:83],v22 offset:1024\nzm a[116:119],v[140:143],v[116:119],a[116:119]\nzm a[120:123],v[140:143],v[120:123],a[120:123]\nzd v[48:51],v16 offset:8192\ns_mov_b32 m0,s66\n"
"zm a[124:127],v[140:143],v[124:127],a[124:127]\nzl v12,s[28:31],s64 offen offset:0 lds\nzm a[128:131],v[144:147],v[96:99],a[128:131]\nzt v[84:85],v23 offset:0\nzm a[132:135],v[144:147],v[100:103],a[132:135]\nzm a[136:139],v[144:147],v[104:107],a[136:139]\nzt v[86:87],v23 offset:1024\nzm a[140:143],v[144:147],v[108:111],a[140:143]\n"
"zl v13,s[28:31],s64 offen offset:1024 lds\nzm a[144:147],v[144:147],v[112:115],a[144:147]\nzd v[52:55],v16 offset:10240\nzm a[148:151],v[144:147],v[116:119],a[148:151]\nzm a[152:155],v[144:147],v[120:123],a[152:155]\nzt v[88:89],v24 offset:0\nzm a[156:159],v[144:147],v[124:127],a[156:159]\nzl v14,s[28:31],s64 offen offset:2048 lds\n"
"zm a[160:163],v[148:151],v[96:99],a[160:163]\nzm a[164:167],v[148:151],v[100:103],a[164:167]\nzt v[90:91],v24 offset:1024\nzm a[168:171],v[148:151],v[104:107],a[168:171]\nzl v15,s[28:31],s64 offen offset:3072 lds\nzm a[172:175],v[148:151],v[108:111],a[172:175]\nzd v[56:59],v16 offset:12288\nzm a[176:179],v[148:151],v[112:115],a[176:179]\n"
"zm a[180:183],v[148:151],v[116:119],a[180:183]\nzt v[92:93],v25 offset:0\nzp m0,s66,16384\nzm a[184:187],v[148:151],v[120:123],a[184:187]\nzl v12,s[36:39],s64 offen offset:0 lds\nzm a[188:191],v[148:151],v[124:127],a[188:191]\nzt v[94:95],v25 offset:1024\nzm a[192:195],v[152:155],v[96:99],a[192:195]\n"
"zm a[196:199],v[152:155],v[100:103],a[196:199]\nzd v[60:63],v16 offset:14336\nzm a[200:203],v[152:155],v[104:107],a[200:203]\nzl v13,s[36:39],s64 offen offset:1024 lds\nzm a[204:207],v[152:155],v[108:111],a[204:207]\nzm a[208:211],v[152:155],v[112:115],a[208:211]\nzm a[212:215],v[152:155],v[116:119],a[212:215]\nzm a[216:219],v[152:155],v[120:123],a[216:219]\n"
"zl v14,s[36:39],s64 offen offset:2048 lds\nzm a[220:223],v[152:155],v[124:127],a[220:223]\nzm a[224:227],v[156:159],v[96:99],a[224:227]\nzm a[228:231],v[156:159],v[100:103],a[228:231]\nzm a[232:235],v[156:159],v[104:107],a[232:235]\nzl v15,s[36:39],s64 offen offset:3072 lds\nzm a[236:239],v[156:159],v[108:111],a[236:239]\nzm a[240:243],v[156:159],v[112:115],a[240:243]\n"
"zm a[244:247],v[156:159],v[116:119],a[244:247]\nzm a[248:251],v[156:159],v[120:123],a[248:251]\nzp s62,s62,128\nzp s63,s63,128\nzp s64,s64,s74\nzm a[252:255],v[156:159],v[124:127],a[252:255]\ns_xor_b32 s65,s65,0x8000\ns_xor_b32 s66,s66,0x8000\n"
"zv lgkmcnt(0)\nzm a[0:3],v[64:67],v[32:35],a[0:3]\nzm a[4:7],v[64:67],v[36:39],a[4:7]\nzt v[128:129],v18 offset:8192\nzm a[8:11],v[64:67],v[40:43],a[8:11]\nzm a[12:15],v[64:67],v[44:47],a[12:15]\nzt v[130:131],v18 offset:9216\nzm a[16:19],v[64:67],v[48:51],a[16:19]\n"
"zm a[20:23],v[64:67],v[52:55],a[20:23]\nzd v[96:99],v17 offset:0\nzm a[24:27],v[64:67],v[56:59],a[24:27]\nzm a[28:31],v[64:67],v[60:63],a[28:31]\nzt v[132:133],v19 offset:8192\nzs v[228:231],v29,s[40:43],s80 offen offset:1024 sc1\nzm a[32:35],v[68:71],v[32:35],a[32:35]\nzm a[36:39],v[68:71],v[36:39],a[36:39]\n"
"zt v[134:135],v19 offset:9216\nzm a[40:43],v[68:71],v[40:43],a[40:43]\nzm a[44:47],v[68:71],v[44:47],a[44:47]\nzd v[100:103],v17 offset:2048\nzm a[48:51],v[68:71],v[48:51],a[48:51]\nzm a[52:55],v[68:71],v[52:55],a[52:55]\nzt v[136:137],v20 offset:8192\nzm a[56:59],v[68:71],v[56:59],a[56:59]\n"
"zm a[60:63],v[68:71],v[60:63],a[60:63]\nzt v[138:139],v20 offset:9216\nzm a[64:67],v[72:75],v[32:35],a[64:67]\nzm a[68:71],v[72:75],v[36:39],a[68:71]\nzd v[104:107],v17 offset:4096\nzm a[72:75],v[72:75],v[40:43],a[72:75]\nzs v[224:227],v29,s[40:43],s80 offen offset:0 sc1\nzm a[76:79],v[72:75],v[44:47],a[76:79]\n"
"zt v[140:141],v21 offset:8192\nzm a[80:83],v[72:75],v[48:51],a[80:83]\nzm a[84:87],v[72:75],v[52:55],a[84:87]\nzt v[142:143],v21 offset:9216\nzm a[88:91],v[72:75],v[56:59],a[88:91]\nzm a[92:95],v[72:75],v[60:63],a[92:95]\nzd v[108:111],v17 offset:6144\nzm a[96:99],v[76:79],v[32:35],a[96:99]\n"
"zm a[100:103],v[76:79],v[36:39],a[100:103]\nzt v[144:145],v22 offset:8192\nzm a[104:107],v[76:79],v[40:43],a[104:107]\nzm a[108:111],v[76:79],v[44:47],a[108:111]\nzt v[146:147],v22 offset:9216\nzm a[112:115],v[76:79],v[48:51],a[112:115]\nzm a[116:119],v[76:79],v[52:55],a[116:119]\nzd v[112:115],v17 offset:8192\n"
"zs v[236:239],v29,s[40:43],s80 offen offset:3072 sc1\nzm a[120:123],v[76:79],v[56:59],a[120:123]\nzm a[124:127],v[76:79],v[60:63],a[124:127]\nzt v[148:149],v23 offset:8192\nzm a[128:131],v[80:83],v[32:35],a[128:131]\nzm a[132:135],v[80:83],v[36:39],a[132:135]\nzt v[150:151],v23 offset:9216\nzm a[136:139],v[80:83],v[40:43],a[136:139]\n"
"zm a[140:143],v[80:83],v[44:47],a[140:143]\nzd v[116:119],v17 offset:10240\nzm a[144:147],v[80:83],v[48:51],a[144:147]\nzm a[148:151],v[80:83],v[52:55],a[148:151]\nzt v[152:153],v24 offset:8192\nzm a[152:155],v[80:83],v[56:59],a[152:155]\nzm a[156:159],v[80:83],v[60:63],a[156:159]\nzt v[154:155],v24 offset:9216\n"
"zm a[160:163],v[84:87],v[32:35],a[160:163]\nzs v[232:235],v29,s[40:43],s80 offen offset:2048 sc1\nzm a[164:167],v[84:87],v[36:39],a[164:167]\nzd v[120:123],v17 offset:12288\nzm a[168:171],v[84:87],v[40:43],a[168:171]\nzm a[172:175],v[84:87],v[44:47],a[172:175]\nzt v[156:157],v25 offset:8192\nzm a[176:179],v[84:87],v[48:51],a[176:179]\n"
"zm a[180:183],v[84:87],v[52:55],a[180:183]\nzt v[158:159],v25 offset:9216\nzm a[184:187],v[84:87],v[56:59],a[184:187]\nzm a[188:191],v[84:87],v[60:63],a[188:191]\nzd v[124:127],v17 offset:14336\nzm a[192:195],v[88:91],v[32:35],a[192:195]\nzm a[196:199],v[88:91],v[36:39],a[196:199]\nzm a[200:203],v[88:91],v[40:43],a[200:203]\n"
"zm a[204:207],v[88:91],v[44:47],a[204:207]\nzp s80,s80,s81\nzm a[208:211],v[88:91],v[48:51],a[208:211]\nzo v16,0x8000,v16\nzm a[212:215],v[88:91],v[52:55],a[212:215]\nzo v17,0x8000,v17\nzm a[216:219],v[88:91],v[56:59],a[216:219]\nzo v18,0x8000,v18\n"
"zm a[220:223],v[88:91],v[60:63],a[220:223]\nzo v19,0x8000,v19\nzm a[224:227],v[92:95],v[32:35],a[224:227]\nzo v20,0x8000,v20\nzm a[228:231],v[92:95],v[36:39],a[228:231]\nzo v21,0x8000,v21\nzm a[232:235],v[92:95],v[40:43],a[232:235]\nzo v22,0x8000,v22\n"
"zm a[236:239],v[92:95],v[44:47],a[236:239]\nzo v23,0x8000,v23\nzm a[240:243],v[92:95],v[48:51],a[240:243]\nzo v24,0x8000,v24\nzm a[244:247],v[92:95],v[52:55],a[244:247]\nzo v25,0x8000,v25\nzm a[248:251],v[92:95],v[56:59],a[248:251]\nzm a[252:255],v[92:95],v[60:63],a[252:255]\n"
"zv vmcnt(4) lgkmcnt(0)\ns_barrier\nzm a[0:3],v[128:131],v[96:99],a[0:3]\nzt v[64:65],v18 offset:0\ns_mov_b32 m0,s65\nzm a[4:7],v[128:131],v[100:103],a[4:7]\nzl v8,s[24:27],s62 offen offset:0 sc1 lds\nzm a[8:11],v[128:131],v[104:107],a[8:11]\n"
"zt v[66:67],v18 offset:1024\nzm a[12:15],v[128:131],v[108:111],a[12:15]\nzm a[16:19],v[128:131],v[112:115],a[16:19]\nzl v9,s[24:27],s62 offen offset:1024 sc1 lds\nzm a[20:23],v[128:131],v[116:119],a[20:23]\nzd v[32:35],v16 offset:0\nzm a[24:27],v[128:131],v[120:123],a[24:27]\nzt v[68:69],v19 offset:0\n"
"zm a[28:31],v[128:131],v[124:127],a[28:31]\nzm a[32:35],v[132:135],v[96:99],a[32:35]\nzl v10,s[24:27],s62 offen offset:2048 sc1 lds\nzm a[36:39],v[132:135],v[100:103],a[36:39]\nzt v[70:71],v19 offset:1024\nzm a[40:43],v[132:135],v[104:107],a[40:43]\nzd v[36:39],v16 offset:2048\nzm a[44:47],v[132:135],v[108:111],a[44:47]\n"
"zm a[48:51],v[132:135],v[112:115],a[48:51]\nzl v11,s[24:27],s62 offen offset:3072 sc1 lds\nzm a[52:55],v[132:135],v[116:119],a[52:55]\nzt v[72:73],v20 offset:0\nzm a[56:59],v[132:135],v[120:123],a[56:59]\nzm a[60:63],v[132:135],v[124:127],a[60:63]\nzt v[74:75],v20 offset:1024\nzp m0,s65,4096\n"
"zm a[64:67],v[136:139],v[96:99],a[64:67]\nzl v8,s[24:27],s63 offen offset:0 sc1 lds\nzm a[68:71],v[136:139],v[100:103],a[68:71]\nzd v[40:43],v16 offset:4096\nzm a[72:75],v[136:139],v[104:107],a[72:75]\nzm a[76:79],v[136:139],v[108:111],a[76:79]\nzt v[76:77],v21 offset:0\nzm a[80:83],v[136:139],v[112:115],a[80:83]\n"
"zl v9,s[24:27],s63 offen offset:1024 sc1 lds\nzm a[84:87],v[136:139],v[116:119],a[84:87]\nzt v[78:79],v21 offset:1024\nzm a[88:91],v[136:139],v[120:123],a[88:91]\nzm a[92:95],v[136:139],v[124:127],a[92:95]\nzl v10,s[24:27],s63 offen offset:2048 sc1 lds\nzm a[96:99],v[140:143],v[96:99],a[96:99]\nzd v[44:47],v16 offset:6144\n"
"zm a[100:103],v[140:143],v[100:103],a[100:103]\nzm a[104:107],v[140:143],v[104:107],a[104:107]\nzt v[80:81],v22 offset:0\nzm a[108:111],v[140:143],v[108:111],a[108:111]\nzl v11,s[24:27],s63 offen offset:3072 sc1 lds\nzm a[112:115],v[140:143],v[112:115],a[112:115]\nzt v[82:83],v22 offset:1024\nzm a[116:119],v[140:143],v[116:119],a[116:119]\n"
"zm a[120:123],v[140:143],v[120:123],a[120:123]\nzd v[48:51],v16 offset:8192\ns_mov_b32 m0,s66\nzm a[124:127],v[140:143],v[124:127],a[124:127]\nzl v12,s[28:31],s64 offen offset:0 lds\nzm a[128:131],v[144:147],v[96:99],a[128:131]\nzt v[84:85],v23 offset:0\nzm a[132:135],v[144:147],v[100:103],a[132:135]\n"
"zm a[136:139],v[144:147],v[104:107],a[136:139]\nzt v[86:87],v23 offset:1024\nzm a[140:143],v[144:147],v[108:111],a[140:143]\nzl v13,s[28:31],s64 offen offset:1024 lds\nzm a[144:147],v[144:147],v[112:115],a[144:147]\nzd v[52:55],v16 offset:10240\nzm a[148:151],v[144:147],v[116:119],a[148:151]\nzm a[152:155],v[144:147],v[120:123],a[152:155]\n"
"zt v[88:89],v24 offset:0\nzm a[156:159],v[144:147],v[124:127],a[156:159]\nzl v14,s[28:31],s64 offen offset:2048 lds\nzm a[160:163],v[148:151],v[96:99],a[160:163]\nzm a[164:167],v[148:151],v[100:103],a[164:167]\nzt v[90:91],v24 offset:1024\nzm a[168:171],v[148:151],v[104:107],a[168:171]\nzl v15,s[28:31],s64 offen offset:3072 lds\n"
"zm a[172:175],v[148:151],v[108:111],a[172:175]\nzd v[56:59],v16 offset:12288\nzm a[176:179],v[148:151],v[112:115],a[176:179]\nzm a[180:183],v[148:151],v[116:119],a[180:183]\nzt v[92:93],v25 offset:0\nzp m0,s66,16384\nzm a[184:187],v[148:151],v[120:123],a[184:187]\nzl v12,s[36:39],s64 offen offset:0 lds\n"
"zm a[188:191],v[148:151],v[124:127],a[188:191]\nzt v[94:95],v25 offset:1024\nzm a[192:195],v[152:155],v[96:99],a[192:195]\nzm a[196:199],v[152:155],v[100:103],a[196:199]\nzd v[60:63],v16 offset:14336\nzm a[200:203],v[152:155],v[104:107],a[200:203]\nzl v13,s[36:39],s64 offen offset:1024 lds\nzm a[204:207],v[152:155],v[108:111],a[204:207]\n"
"zm a[208:211],v[152:155],v[112:115],a[208:211]\nzm a[212:215],v[152:155],v[116:119],a[212:215]\nzm a[216:219],v[152:155],v[120:123],a[216:219]\nzl v14,s[36:39],s64 offen offset:2048 lds\nzm a[220:223],v[152:155],v[124:127],a[220:223]\nzm a[224:227],v[156:159],v[96:99],a[224:227]\nzm a[228:231],v[156:159],v[100:103],a[228:231]\nzm a[232:235],v[156:159],v[104:107],a[232:235]\n"
"zl v15,s[36:39],s64 offen offset:3072 lds\nzm a[236:239],v[156:159],v[108:111],a[236:239]\nzm a[240:243],v[156:159],v[112:115],a[240:243]\nzm a[244:247],v[156:159],v[116:119],a[244:247]\nzm a[248:251],v[156:159],v[120:123],a[248:251]\nzp s62,s62,128\nzp s63,s63,128\nzp s64,s64,s74\n"
"zm a[252:255],v[156:159],v[124:127],a[252:255]\ns_xor_b32 s65,s65,0x8000\ns_xor_b32 s66,s66,0x8000\nzv lgkmcnt(0)\nzm a[0:3],v[64:67],v[32:35],a[0:3]\nzm a[4:7],v[64:67],v[36:39],a[4:7]\nzt v[128:129],v18 offset:8192\nzm a[8:11],v[64:67],v[40:43],a[8:11]\n"
"zm a[12:15],v[64:67],v[44:47],a[12:15]\nzt v[130:131],v18 offset:9216\nzm a[16:19],v[64:67],v[48:51],a[16:19]\nzm a[20:23],v[64:67],v[52:55],a[20:23]\nzd v[96:99],v17 offset:0\nzm a[24:27],v[64:67],v[56:59],a[24:27]\nzm a[28:31],v[64:67],v[60:63],a[28:31]\nzt v[132:133],v19 offset:8192\n"
"zs v[244:247],v29,s[40:43],s80 offen offset:1024 sc1\nzm a[32:35],v[68:71],v[32:35],a[32:35]\nzm a[36:39],v[68:71],v[36:39],a[36:39]\nzt v[134:135],v19 offset:9216\nzm a[40:43],v[68:71],v[40:43],a[40:43]\nzm a[44:47],v[68:71],v[44:47],a[44:47]\nzd v[100:103],v17 offset:2048\nzm a[48:51],v[68:71],v[48:51],a[48:51]\n"
"zm a[52:55],v[68:71],v[52:55],a[52:55]\nzt v[136:137],v20 offset:8192\nzm a[56:59],v[68:71],v[56:59],a[56:59]\nzm a[60:63],v[68:71],v[60:63],a[60:63]\nzt v[138:139],v20 offset:9216\nzm a[64:67],v[72:75],v[32:35],a[64:67]\nzm a[68:71],v[72:75],v[36:39],a[68:71]\nzd v[104:107],v17 offset:4096\n"
"zm a[72:75],v[72:75],v[40:43],a[72:75]\nzs v[240:243],v29,s[40:43],s80 offen offset:0 sc1\nzm a[76:79],v[72:75],v[44:47],a[76:79]\nzt v[140:141],v21 offset:8192\nzm a[80:83],v[72:75],v[48:51],a[80:83]\nzm a[84:87],v[72:75],v[52:55],a[84:87]\nzt v[142:143],v21 offset:9216\nzm a[88:91],v[72:75],v[56:59],a[88:91]\n"
"zm a[92:95],v[72:75],v[60:63],a[92:95]\nzd v[108:111],v17 offset:6144\nzm a[96:99],v[76:79],v[32:35],a[96:99]\nzm a[100:103],v[76:79],v[36:39],a[100:103]\nzt v[144:145],v22 offset:8192\nzm a[104:107],v[76:79],v[40:43],a[104:107]\nzm a[108:111],v[76:79],v[44:47],a[108:111]\nzt v[146:147],v22 offset:9216\n"
"zm a[112:115],v[76:79],v[48:51],a[112:115]\nzm a[116:119],v[76:79],v[52:55],a[116:119]\nzd v[112:115],v17 offset:8192\nzs v[252:255],v29,s[40:43],s80 offen offset:3072 sc1\nzm a[120:123],v[76:79],v[56:59],a[120:123]\nzm a[124:127],v[76:79],v[60:63],a[124:127]\nzt v[148:149],v23 offset:8192\nzm a[128:131],v[80:83],v[32:35],a[128:131]\n"
"zm a[132:135],v[80:83],v[36:39],a[132:135]\nzt v[150:151],v23 offset:9216\nzm a[136:139],v[80:83],v[40:43],a[136:139]\nzm a[140:143],v[80:83],v[44:47],a[140:143]\nzd v[116:119],v17 offset:10240\nzm a[144:147],v[80:83],v[48:51],a[144:147]\nzm a[148:151],v[80:83],v[52:55],a[148:151]\nzt v[152:153],v24 offset:8192\n"
"zm a[152:155],v[80:83],v[56:59],a[152:155]\nzm a[156:159],v[80:83],v[60:63],a[156:159]\nzt v[154:155],v24 offset:9216\nzm a[160:163],v[84:87],v[32:35],a[160:163]\nzs v[248:251],v29,s[40:43],s80 offen offset:2048 sc1\nzm a[164:167],v[84:87],v[36:39],a[164:167]\nzd v[120:123],v17 offset:12288\nzm a[168:171],v[84:87],v[40:43],a[168:171]\n"
"zm a[172:175],v[84:87],v[44:47],a[172:175]\nzt v[156:157],v25 offset:8192\nzm a[176:179],v[84:87],v[48:51],a[176:179]\nzm a[180:183],v[84:87],v[52:55],a[180:183]\nzt v[158:159],v25 offset:9216\nzm a[184:187],v[84:87],v[56:59],a[184:187]\nzm a[188:191],v[84:87],v[60:63],a[188:191]\nzd v[124:127],v17 offset:14336\n"
"zm a[192:195],v[88:91],v[32:35],a[192:195]\nzm a[196:199],v[88:91],v[36:39],a[196:199]\nzm a[200:203],v[88:91],v[40:43],a[200:203]\nzm a[204:207],v[88:91],v[44:47],a[204:207]\nzp s80,s80,s82\nzm a[208:211],v[88:91],v[48:51],a[208:211]\nzo v16,0x8000,v16\nzm a[212:215],v[88:91],v[52:55],a[212:215]\n"
"zo v17,0x8000,v17\nzm a[216:219],v[88:91],v[56:59],a[216:219]\nzo v18,0x8000,v18\nzm a[220:223],v[88:91],v[60:63],a[220:223]\nzo v19,0x8000,v19\nzm a[224:227],v[92:95],v[32:35],a[224:227]\nzo v20,0x8000,v20\nzm a[228:231],v[92:95],v[36:39],a[228:231]\n"
"zo v21,0x8000,v21\nzm a[232:235],v[92:95],v[40:43],a[232:235]\nzo v22,0x8000,v22\nzm a[236:239],v[92:95],v[44:47],a[236:239]\nzo v23,0x8000,v23\nzm a[240:243],v[92:95],v[48:51],a[240:243]\nzo v24,0x8000,v24\nzm a[244:247],v[92:95],v[52:55],a[244:247]\n"
"zo v25,0x8000,v25\nzm a[248:251],v[92:95],v[56:59],a[248:251]\nzm a[252:255],v[92:95],v[60:63],a[252:255]\nzv vmcnt(4) lgkmcnt(0)\ns_barrier\nzm a[0:3],v[128:131],v[96:99],a[0:3]\nzm a[4:7],v[128:131],v[100:103],a[4:7]\nzt v[64:65],v18 offset:0\n"
"zm a[8:11],v[128:131],v[104:107],a[8:11]\nzm a[12:15],v[128:131],v[108:111],a[12:15]\nzt v[66:67],v18 offset:1024\nzm a[16:19],v[128:131],v[112:115],a[16:19]\nzm a[20:23],v[128:131],v[116:119],a[20:23]\nzd v[32:35],v16 offset:0\nzm a[24:27],v[128:131],v[120:123],a[24:27]\nzm a[28:31],v[128:131],v[124:127],a[28:31]\n"
"zt v[68:69],v19 offset:0\nzm a[32:35],v[132:135],v[96:99],a[32:35]\nzm a[36:39],v[132:135],v[100:103],a[36:39]\nzt v[70:71],v19 offset:1024\nzm a[40:43],v[132:135],v[104:107],a[40:43]\nzm a[44:47],v[132:135],v[108:111],a[44:47]\nzd v[36:39],v16 offset:2048\nzm a[48:51],v[132:135],v[112:115],a[48:51]\n"
"zm a[52:55],v[132:135],v[116:119],a[52:55]\nzt v[72:73],v20 offset:0\nzm a[56:59],v[132:135],v[120:123],a[56:59]\nzm a[60:63],v[132:135],v[124:127],a[60:63]\nzt v[74:75],v20 offset:1024\nzm a[64:67],v[136:139],v[96:99],a[64:67]\nzm a[68:71],v[136:139],v[100:103],a[68:71]\nzd v[40:43],v16 offset:4096\n"
"zm a[72:75],v[136:139],v[104:107],a[72:75]\nzm a[76:79],v[136:139],v[108:111],a[76:79]\nzt v[76:77],v21 offset:0\nzm a[80:83],v[136:139],v[112:115],a[80:83]\nzm a[84:87],v[136:139],v[116:119],a[84:87]\nzt v[78:79],v21 offset:1024\nzm a[88:91],v[136:139],v[120:123],a[88:91]\nzm a[92:95],v[136:139],v[124:127],a[92:95]\n"
"zd v[44:47],v16 offset:6144\nzm a[96:99],v[140:143],v[96:99],a[96:99]\nzm a[100:103],v[140:143],v[100:103],a[100:103]\nzt v[80:81],v22 offset:0\nzm a[104:107],v[140:143],v[104:107],a[104:107]\nzm a[108:111],v[140:143],v[108:111],a[108:111]\nzt v[82:83],v22 offset:1024\nzm a[112:115],v[140:143],v[112:115],a[112:115]\n"
"zm a[116:119],v[140:143],v[116:119],a[116:119]\nzd v[48:51],v16 offset:8192\nzm a[120:123],v[140:143],v[120:123],a[120:123]\nzm a[124:127],v[140:143],v[124:127],a[124:127]\nzt v[84:85],v23 offset:0\nzm a[128:131],v[144:147],v[96:99],a[128:131]\nzm a[132:135],v[144:147],v[100:103],a[132:135]\nzt v[86:87],v23 offset:1024\n"
"zm a[136:139],v[144:147],v[104:107],a[136:139]\nzm a[140:143],v[144:147],v[108:111],a[140:143]\nzd v[52:55],v16 offset:10240\nzm a[144:147],v[144:147],v[112:115],a[144:147]\nzm a[148:151],v[144:147],v[116:119],a[148:151]\nzt v[88:89],v24 offset:0\nzm a[152:155],v[144:147],v[120:123],a[152:155]\nzm a[156:159],v[144:147],v[124:127],a[156:159]\n"
"zt v[90:91],v24 offset:1024\nzm a[160:163],v[148:151],v[96:99],a[160:163]\nzm a[164:167],v[148:151],v[100:103],a[164:167]\nzd v[56:59],v16 offset:12288\nzm a[168:171],v[148:151],v[104:107],a[168:171]\nzm a[172:175],v[148:151],v[108:111],a[172:175]\nzt v[92:93],v25 offset:0\nzm a[176:179],v[148:151],v[112:115],a[176:179]\n"
"zm a[180:183],v[148:151],v[116:119],a[180:183]\nzt v[94:95],v25 offset:1024\nzm a[184:187],v[148:151],v[120:123],a[184:187]\nzm a[188:191],v[148:151],v[124:127],a[188:191]\nzd v[60:63],v16 offset:14336\nzm a[192:195],v[152:155],v[96:99],a[192:195]\nzm a[196:199],v[152:155],v[100:103],a[196:199]\nzm a[200:203],v[152:155],v[104:107],a[200:203]\n"
"zm a[204:207],v[152:155],v[108:111],a[204:207]\nzm a[208:211],v[152:155],v[112:115],a[208:211]\nzm a[212:215],v[152:155],v[116:119],a[212:215]\nzm a[216:219],v[152:155],v[120:123],a[216:219]\nzm a[220:223],v[152:155],v[124:127],a[220:223]\nzm a[224:227],v[156:159],v[96:99],a[224:227]\nzm a[228:231],v[156:159],v[100:103],a[228:231]\nzm a[232:235],v[156:159],v[104:107],a[232:235]\n"
"zm a[236:239],v[156:159],v[108:111],a[236:239]\nzm a[240:243],v[156:159],v[112:115],a[240:243]\nzm a[244:247],v[156:159],v[116:119],a[244:247]\nzm a[248:251],v[156:159],v[120:123],a[248:251]\nzp s62,s62,128\nzp s63,s63,128\nzp s64,s64,s74\nzm a[252:255],v[156:159],v[124:127],a[252:255]\n"
"s_xor_b32 s65,s65,0x8000\ns_xor_b32 s66,s66,0x8000\nzv lgkmcnt(0)\nzm a[0:3],v[64:67],v[32:35],a[0:3]\nzm a[4:7],v[64:67],v[36:39],a[4:7]\nzt v[128:129],v18 offset:8192\nzm a[8:11],v[64:67],v[40:43],a[8:11]\nzm a[12:15],v[64:67],v[44:47],a[12:15]\n"
"zt v[130:131],v18 offset:9216\nzm a[16:19],v[64:67],v[48:51],a[16:19]\nzm a[20:23],v[64:67],v[52:55],a[20:23]\nzd v[96:99],v17 offset:0\nzm a[24:27],v[64:67],v[56:59],a[24:27]\nzm a[28:31],v[64:67],v[60:63],a[28:31]\nzt v[132:133],v19 offset:8192\nzm a[32:35],v[68:71],v[32:35],a[32:35]\n"
"zm a[36:39],v[68:71],v[36:39],a[36:39]\nzt v[134:135],v19 offset:9216\nzm a[40:43],v[68:71],v[40:43],a[40:43]\nzm a[44:47],v[68:71],v[44:47],a[44:47]\nzd v[100:103],v17 offset:2048\nzm a[48:51],v[68:71],v[48:51],a[48:51]\nzm a[52:55],v[68:71],v[52:55],a[52:55]\nzt v[136:137],v20 offset:8192\n"
"zm a[56:59],v[68:71],v[56:59],a[56:59]\nzm a[60:63],v[68:71],v[60:63],a[60:63]\nzt v[138:139],v20 offset:9216\nzm a[64:67],v[72:75],v[32:35],a[64:67]\nzm a[68:71],v[72:75],v[36:39],a[68:71]\nzd v[104:107],v17 offset:4096\nzm a[72:75],v[72:75],v[40:43],a[72:75]\nzm a[76:79],v[72:75],v[44:47],a[76:79]\n"
"zt v[140:141],v21 offset:8192\nzm a[80:83],v[72:75],v[48:51],a[80:83]\nzm a[84:87],v[72:75],v[52:55],a[84:87]\nzt v[142:143],v21 offset:9216\nzm a[88:91],v[72:75],v[56:59],a[88:91]\nzm a[92:95],v[72:75],v[60:63],a[92:95]\nzd v[108:111],v17 offset:6144\nzm a[96:99],v[76:79],v[32:35],a[96:99]\n"
"zm a[100:103],v[76:79],v[36:39],a[100:103]\nzt v[144:145],v22 offset:8192\nzm a[104:107],v[76:79],v[40:43],a[104:107]\nzm a[108:111],v[76:79],v[44:47],a[108:111]\nzt v[146:147],v22 offset:9216\nzm a[112:115],v[76:79],v[48:51],a[112:115]\nzm a[116:119],v[76:79],v[52:55],a[116:119]\nzd v[112:115],v17 offset:8192\n"
"zm a[120:123],v[76:79],v[56:59],a[120:123]\nzm a[124:127],v[76:79],v[60:63],a[124:127]\nzt v[148:149],v23 offset:8192\nzm a[128:131],v[80:83],v[32:35],a[128:131]\nzm a[132:135],v[80:83],v[36:39],a[132:135]\nzt v[150:151],v23 offset:9216\nzm a[136:139],v[80:83],v[40:43],a[136:139]\nzm a[140:143],v[80:83],v[44:47],a[140:143]\n"
"zd v[116:119],v17 offset:10240\nzm a[144:147],v[80:83],v[48:51],a[144:147]\nzm a[148:151],v[80:83],v[52:55],a[148:151]\nzt v[152:153],v24 offset:8192\nzm a[152:155],v[80:83],v[56:59],a[152:155]\nzm a[156:159],v[80:83],v[60:63],a[156:159]\nzt v[154:155],v24 offset:9216\nzm a[160:163],v[84:87],v[32:35],a[160:163]\n"
"zm a[164:167],v[84:87],v[36:39],a[164:167]\nzd v[120:123],v17 offset:12288\nzm a[168:171],v[84:87],v[40:43],a[168:171]\nzm a[172:175],v[84:87],v[44:47],a[172:175]\nzt v[156:157],v25 offset:8192\nzm a[176:179],v[84:87],v[48:51],a[176:179]\nzm a[180:183],v[84:87],v[52:55],a[180:183]\nzt v[158:159],v25 offset:9216\n"
"zm a[184:187],v[84:87],v[56:59],a[184:187]\nzm a[188:191],v[84:87],v[60:63],a[188:191]\nzd v[124:127],v17 offset:14336\nzm a[192:195],v[88:91],v[32:35],a[192:195]\nzm a[196:199],v[88:91],v[36:39],a[196:199]\nzm a[200:203],v[88:91],v[40:43],a[200:203]\nzm a[204:207],v[88:91],v[44:47],a[204:207]\nzm a[208:211],v[88:91],v[48:51],a[208:211]\n"
"zo v16,0x8000,v16\nzm a[212:215],v[88:91],v[52:55],a[212:215]\nzo v17,0x8000,v17\nzm a[216:219],v[88:91],v[56:59],a[216:219]\nzo v18,0x8000,v18\nzm a[220:223],v[88:91],v[60:63],a[220:223]\nzo v19,0x8000,v19\nzm a[224:227],v[92:95],v[32:35],a[224:227]\n"
"zo v20,0x8000,v20\nzm a[228:231],v[92:95],v[36:39],a[228:231]\nzo v21,0x8000,v21\nzm a[232:235],v[92:95],v[40:43],a[232:235]\nzo v22,0x8000,v22\nzm a[236:239],v[92:95],v[44:47],a[236:239]\nzo v23,0x8000,v23\nzm a[240:243],v[92:95],v[48:51],a[240:243]\n"
"zo v24,0x8000,v24\nzm a[244:247],v[92:95],v[52:55],a[244:247]\nzo v25,0x8000,v25\nzm a[248:251],v[92:95],v[56:59],a[248:251]\nzm a[252:255],v[92:95],v[60:63],a[252:255]\nzv vmcnt(0) lgkmcnt(0)\ns_barrier\ns_cmp_eq_u32 s23,0\n"
"s_cbranch_scc0 L_nf_kvv\nv_mov_b32 v26,s79\nv_mov_b32 v27,s34\nglobal_store_dword v26,v27,s[94:95] sc1\nL_nf_kvv:\nzm a[0:3],v[128:131],v[96:99],a[0:3]\nzm a[4:7],v[128:131],v[100:103],a[4:7]\nzt v[64:65],v18 offset:0\n"
"zm a[8:11],v[128:131],v[104:107],a[8:11]\nzm a[12:15],v[128:131],v[108:111],a[12:15]\nzt v[66:67],v18 offset:1024\nzm a[16:19],v[128:131],v[112:115],a[16:19]\nzm a[20:23],v[128:131],v[116:119],a[20:23]\nzd v[32:35],v16 offset:0\nzm a[24:27],v[128:131],v[120:123],a[24:27]\nzm a[28:31],v[128:131],v[124:127],a[28:31]\n"
"zt v[68:69],v19 offset:0\nzm a[32:35],v[132:135],v[96:99],a[32:35]\nzm a[36:39],v[132:135],v[100:103],a[36:39]\nzt v[70:71],v19 offset:1024\nzm a[40:43],v[132:135],v[104:107],a[40:43]\nzm a[44:47],v[132:135],v[108:111],a[44:47]\nzd v[36:39],v16 offset:2048\nzm a[48:51],v[132:135],v[112:115],a[48:51]\n"
"zm a[52:55],v[132:135],v[116:119],a[52:55]\nzt v[72:73],v20 offset:0\nzm a[56:59],v[132:135],v[120:123],a[56:59]\nzm a[60:63],v[132:135],v[124:127],a[60:63]\nzt v[74:75],v20 offset:1024\nzm a[64:67],v[136:139],v[96:99],a[64:67]\nzm a[68:71],v[136:139],v[100:103],a[68:71]\nzd v[40:43],v16 offset:4096\n"
"zm a[72:75],v[136:139],v[104:107],a[72:75]\nzm a[76:79],v[136:139],v[108:111],a[76:79]\nzt v[76:77],v21 offset:0\nzm a[80:83],v[136:139],v[112:115],a[80:83]\nzm a[84:87],v[136:139],v[116:119],a[84:87]\nzt v[78:79],v21 offset:1024\nzm a[88:91],v[136:139],v[120:123],a[88:91]\nzm a[92:95],v[136:139],v[124:127],a[92:95]\n"
"zd v[44:47],v16 offset:6144\nzm a[96:99],v[140:143],v[96:99],a[96:99]\nzm a[100:103],v[140:143],v[100:103],a[100:103]\nzt v[80:81],v22 offset:0\nzm a[104:107],v[140:143],v[104:107],a[104:107]\nzm a[108:111],v[140:143],v[108:111],a[108:111]\nzt v[82:83],v22 offset:1024\nzm a[112:115],v[140:143],v[112:115],a[112:115]\n"
"zm a[116:119],v[140:143],v[116:119],a[116:119]\nzd v[48:51],v16 offset:8192\nzm a[120:123],v[140:143],v[120:123],a[120:123]\nzm a[124:127],v[140:143],v[124:127],a[124:127]\nzt v[84:85],v23 offset:0\nzm a[128:131],v[144:147],v[96:99],a[128:131]\nzm a[132:135],v[144:147],v[100:103],a[132:135]\nzt v[86:87],v23 offset:1024\n"
"zm a[136:139],v[144:147],v[104:107],a[136:139]\nzm a[140:143],v[144:147],v[108:111],a[140:143]\nzd v[52:55],v16 offset:10240\nzm a[144:147],v[144:147],v[112:115],a[144:147]\nzm a[148:151],v[144:147],v[116:119],a[148:151]\nzt v[88:89],v24 offset:0\nzm a[152:155],v[144:147],v[120:123],a[152:155]\nzm a[156:159],v[144:147],v[124:127],a[156:159]\n"
"zt v[90:91],v24 offset:1024\nzm a[160:163],v[148:151],v[96:99],a[160:163]\nzm a[164:167],v[148:151],v[100:103],a[164:167]\nzd v[56:59],v16 offset:12288\nzm a[168:171],v[148:151],v[104:107],a[168:171]\nzm a[172:175],v[148:151],v[108:111],a[172:175]\nzt v[92:93],v25 offset:0\nzm a[176:179],v[148:151],v[112:115],a[176:179]\n"
"zm a[180:183],v[148:151],v[116:119],a[180:183]\nzt v[94:95],v25 offset:1024\nzm a[184:187],v[148:151],v[120:123],a[184:187]\nzm a[188:191],v[148:151],v[124:127],a[188:191]\nzd v[60:63],v16 offset:14336\nzm a[192:195],v[152:155],v[96:99],a[192:195]\nzm a[196:199],v[152:155],v[100:103],a[196:199]\nzm a[200:203],v[152:155],v[104:107],a[200:203]\n"
"zm a[204:207],v[152:155],v[108:111],a[204:207]\nzm a[208:211],v[152:155],v[112:115],a[208:211]\nzm a[212:215],v[152:155],v[116:119],a[212:215]\nzm a[216:219],v[152:155],v[120:123],a[216:219]\nzm a[220:223],v[152:155],v[124:127],a[220:223]\nzm a[224:227],v[156:159],v[96:99],a[224:227]\nzm a[228:231],v[156:159],v[100:103],a[228:231]\nzm a[232:235],v[156:159],v[104:107],a[232:235]\n"
"zm a[236:239],v[156:159],v[108:111],a[236:239]\nzm a[240:243],v[156:159],v[112:115],a[240:243]\nzm a[244:247],v[156:159],v[116:119],a[244:247]\nzm a[248:251],v[156:159],v[120:123],a[248:251]\nzp s62,s62,128\nzp s63,s63,128\nzp s64,s64,s74\nzm a[252:255],v[156:159],v[124:127],a[252:255]\n"
"s_xor_b32 s65,s65,0x8000\ns_xor_b32 s66,s66,0x8000\nzv lgkmcnt(0)\nL_P_t2e:\ns_mov_b32 s66,s71\ns_mov_b32 s67,s72\ns_mov_b32 s68,s73\ns_branch L_P_pre\n"
"L_P_noT2:\nzs v[160:163],v29,s[40:43],s80 offen offset:0 sc1\nzs v[164:167],v29,s[40:43],s80 offen offset:1024 sc1\nzs v[168:171],v29,s[40:43],s80 offen offset:2048 sc1\nzs v[172:175],v29,s[40:43],s80 offen offset:3072 sc1\nzp s80,s80,s81\nzs v[176:179],v29,s[40:43],s80 offen offset:0 sc1\nzs v[180:183],v29,s[40:43],s80 offen offset:1024 sc1\n"
"zs v[184:187],v29,s[40:43],s80 offen offset:2048 sc1\nzs v[188:191],v29,s[40:43],s80 offen offset:3072 sc1\nzp s80,s80,s82\nzs v[192:195],v29,s[40:43],s80 offen offset:0 sc1\nzs v[196:199],v29,s[40:43],s80 offen offset:1024 sc1\nzs v[200:203],v29,s[40:43],s80 offen offset:2048 sc1\nzs v[204:207],v29,s[40:43],s80 offen offset:3072 sc1\nzp s80,s80,s81\n"
"zs v[208:211],v29,s[40:43],s80 offen offset:0 sc1\nzs v[212:215],v29,s[40:43],s80 offen offset:1024 sc1\nzs v[216:219],v29,s[40:43],s80 offen offset:2048 sc1\nzs v[220:223],v29,s[40:43],s80 offen offset:3072 sc1\nzp s80,s80,s82\nzs v[224:227],v29,s[40:43],s80 offen offset:0 sc1\nzs v[228:231],v29,s[40:43],s80 offen offset:1024 sc1\nzs v[232:235],v29,s[40:43],s80 offen offset:2048 sc1\n"
"zs v[236:239],v29,s[40:43],s80 offen offset:3072 sc1\nzp s80,s80,s81\nzs v[240:243],v29,s[40:43],s80 offen offset:0 sc1\nzs v[244:247],v29,s[40:43],s80 offen offset:1024 sc1\nzs v[248:251],v29,s[40:43],s80 offen offset:2048 sc1\nzs v[252:255],v29,s[40:43],s80 offen offset:3072 sc1\nzp s80,s80,s82\ns_mov_b32 s66,2\n"
"zv vmcnt(0)\ns_barrier\ns_cmp_eq_u32 s23,0\ns_cbranch_scc0 L_nf_kvn\nv_mov_b32 v26,s79\nv_mov_b32 v27,s34\nglobal_store_dword v26,v27,s[94:95] sc1\nL_nf_kvn:\n"
"v_mov_b32 v26,0\ns_lshl_b32 s76,s23,2\nzp s76,s76,131072\nv_mov_b32 v27,s76\nds_write_b32 v27,v26\nL_P_pre:\nzv lgkmcnt(0)\ns_barrier\n"
"s_nop 7\ns_cmp_eq_u32 s66,0\ns_cbranch_scc0 L_P_notQ\nzh v5,4,v6\ns_lshl_b32 s89,s23,12\nv_add_u32 v4,s89,v5\ns_and_b32 s89,s22,7\ns_lshr_b32 s90,s22,3\n"
"s_lshr_b32 s91,s90,4\ns_lshl_b32 s89,s89,1\nzp s73,s89,s91\ns_and_b32 s86,s90,1\ns_lshr_b32 s85,s90,1\ns_and_b32 s85,s85,7\ns_lshl_b32 s89,s73,20\nzp s90,s89,8388608\n"
"zp s24,s94,s90\ns_addc_u32 s25,s95,0\ns_and_b32 s25,s25,0xffff\ns_mov_b32 s26,1048576\ns_mov_b32 s27,0x20000\nzp s90,s89,25165824\nzp s28,s94,s90\ns_addc_u32 s29,s95,0\n"
"s_and_b32 s29,s29,0xffff\ns_mov_b32 s30,1048576\ns_mov_b32 s31,0x20000\nzp s90,s89,41943040\nzp s36,s94,s90\ns_addc_u32 s37,s95,0\ns_and_b32 s37,s37,0xffff\ns_mov_b32 s38,1048576\n"
"s_mov_b32 s39,0x20000\ns_mov_b32 s87,0\ns_cmp_eq_u32 s87,0\ns_cbranch_scc0 L_A_seg1aq\ns_sub_u32 s74,15,s85\ns_lshl_b32 s89,s74,2\nzp s89,s89,4\ns_cmp_eq_u32 s86,0\n"
"s_cbranch_scc0 L_A_seg0Baq\ns_mov_b32 s75,0\ns_min_u32 s90,s89,40\ns_cmp_lt_u32 40,s89\ns_cselect_b32 s84,2,0\ns_branch L_A_seggoaq\nL_A_seg0Baq:\ns_mov_b32 s75,40\n"
"s_mov_b32 s90,s89\ns_mov_b32 s84,1\ns_cmp_lt_u32 40,s89\ns_cbranch_scc1 L_A_seggoaq\ns_mov_b32 s87,1\nL_A_seg1aq:\ns_mov_b32 s74,s85\ns_mov_b32 s75,0\n"
"s_lshl_b32 s90,s74,2\nzp s90,s90,4\ns_mov_b32 s84,0\nL_A_seggoaq:\ns_sub_u32 s91,s90,2\ns_lshl_b32 s92,s74,2\ns_sub_i32 s92,s92,2\ns_min_i32 s92,s92,s91\n"
"s_sub_i32 s92,s92,s75\ns_max_i32 s92,s92,0\ns_lshr_b32 s76,s92,1\ns_sub_u32 s91,s91,s75\ns_lshr_b32 s91,s91,1\ns_sub_u32 s77,s91,s76\ns_lshl_b32 s89,s73,7\nzp s89,s89,4096\n"
"zv vmcnt(0)\ns_cmp_eq_u32 s23,0\ns_cbranch_scc0 L_wfb_kvq\nzp s40,s94,s89\ns_addc_u32 s41,s95,0\ns_and_b32 s41,s41,0xffff\ns_mov_b32 s42,64\ns_mov_b32 s43,0x20000\n"
"zh v209,2,v6\ns_mov_b32 s90,0\nL_wf_kvq:\nbuffer_load_dword v208,v209,s[40:43],0 offen sc1\nzv vmcnt(0)\nv_cmp_ne_u32 vcc,s34,v208\ns_nop 2\ns_and_b32 vcc_lo,vcc_lo,0xffff\n"
"s_cmp_eq_u32 vcc_lo,0\ns_cbranch_scc1 L_wfe_kvq\nzp s90,s90,1\ns_cmp_lt_u32 s90,300000\ns_cbranch_scc0 L_wfe_kvq\ns_sleep 1\ns_branch L_wf_kvq\nL_wfe_kvq:\n"
"L_wfb_kvq:\ns_barrier\nL_A_kvokq:\nzp s89,s75,0\ns_lshl_b32 s79,s89,14\ns_and_b32 s89,s89,3\ns_lshl_b32 s89,s89,14\ns_lshl_b32 s90,s23,12\n"
"zp m0,s89,s90\ns_nop 0\nzl v4,s[28:31],s79 offen offset:0 sc1 lds\nzl v4,s[28:31],s79 offen offset:1024 sc1 lds\nzl v4,s[28:31],s79 offen offset:2048 sc1 lds\nzl v4,s[28:31],s79 offen offset:3072 sc1 lds\nzp s89,s75,1\ns_lshl_b32 s79,s89,14\n"
"s_and_b32 s89,s89,3\ns_lshl_b32 s89,s89,14\ns_lshl_b32 s90,s23,12\nzp m0,s89,s90\ns_nop 0\nzl v4,s[28:31],s79 offen offset:0 sc1 lds\nzl v4,s[28:31],s79 offen offset:1024 sc1 lds\nzl v4,s[28:31],s79 offen offset:2048 sc1 lds\n"
"zl v4,s[28:31],s79 offen offset:3072 sc1 lds\nzp s89,s75,0\ns_lshl_b32 s78,s89,14\ns_and_b32 s89,s89,3\ns_lshl_b32 s89,s89,14\ns_lshl_b32 s90,s23,12\nzp s89,s89,s90\nzp m0,s89,65536\n"
"s_nop 0\nzl v4,s[36:39],s78 offen offset:0 sc1 lds\nzl v4,s[36:39],s78 offen offset:1024 sc1 lds\nzl v4,s[36:39],s78 offen offset:2048 sc1 lds\nzl v4,s[36:39],s78 offen offset:3072 sc1 lds\nzh v29,4,v6\ns_sub_u32 s62,15,s68\ns_cmp_eq_u32 s60,0\n"
"s_cselect_b32 s62,s62,s68\ns_lshl_b32 s62,s62,16\ns_lshl_b32 s63,s61,12\nzp s48,s62,s63\nv_add_u32 v30,s63,v29\nv_add_u32 v28,65536,v30\nv_add_u32 v30,32768,v30\ns_cmp_eq_u32 s60,0\n"
"s_cbranch_scc0 L_P_epiQ1\nzr v160,a0\nzr v161,a1\nzr v162,a2\nzr v163,a3\nzr v164,a32\nzr v165,a33\nzr v166,a34\n"
"zr v167,a35\nzc v168,v160,v161\nzc v169,v162,v163\nzc v170,v164,v165\nzc v171,v166,v167\nds_write_b128 v30,v[168:171] offset:0\nzs v[168:171],v29,s[56:59],s48 offen offset:0 sc1\nzr v172,a64\n"
"zr v173,a65\nzr v174,a66\nzr v175,a67\nzr v176,a96\nzr v177,a97\nzr v178,a98\nzr v179,a99\nzc v180,v172,v173\n"
"zc v181,v174,v175\nzc v182,v176,v177\nzc v183,v178,v179\nds_write_b128 v30,v[180:183] offset:1024\nzs v[180:183],v29,s[56:59],s48 offen offset:1024 sc1\nzr v184,a128\nzr v185,a129\nzr v186,a130\n"
"zr v187,a131\nzr v188,a160\nzr v189,a161\nzr v190,a162\nzr v191,a163\nzc v192,v184,v185\nzc v193,v186,v187\nzc v194,v188,v189\n"
"zc v195,v190,v191\nds_write_b128 v30,v[192:195] offset:2048\nzs v[192:195],v29,s[56:59],s48 offen offset:2048 sc1\nzr v196,a192\nzr v197,a193\nzr v198,a194\nzr v199,a195\nzr v200,a224\n"
"zr v201,a225\nzr v202,a226\nzr v203,a227\nzc v204,v196,v197\nzc v205,v198,v199\nzc v206,v200,v201\nzc v207,v202,v203\nds_write_b128 v30,v[204:207] offset:3072\n"
"zs v[204:207],v29,s[56:59],s48 offen offset:3072 sc1\nzp s48,s48,8192\nzr v160,a4\nzr v161,a5\nzr v162,a6\nzr v163,a7\nzr v164,a36\nzr v165,a37\n"
"zr v166,a38\nzr v167,a39\nzc v168,v160,v161\nzc v169,v162,v163\nzc v170,v164,v165\nzc v171,v166,v167\nds_write_b128 v30,v[168:171] offset:8192\nzs v[168:171],v29,s[56:59],s48 offen offset:0 sc1\n"
"zr v172,a68\nzr v173,a69\nzr v174,a70\nzr v175,a71\nzr v176,a100\nzr v177,a101\nzr v178,a102\nzr v179,a103\n"
"zc v180,v172,v173\nzc v181,v174,v175\nzc v182,v176,v177\nzc v183,v178,v179\nds_write_b128 v30,v[180:183] offset:9216\nzs v[180:183],v29,s[56:59],s48 offen offset:1024 sc1\nzr v184,a132\nzr v185,a133\n"
"zr v186,a134\nzr v187,a135\nzr v188,a164\nzr v189,a165\nzr v190,a166\nzr v191,a167\nzc v192,v184,v185\nzc v193,v186,v187\n"
"zc v194,v188,v189\nzc v195,v190,v191\nds_write_b128 v30,v[192:195] offset:10240\nzs v[192:195],v29,s[56:59],s48 offen offset:2048 sc1\nzr v196,a196\nzr v197,a197\nzr v198,a198\nzr v199,a199\n"
"zr v200,a228\nzr v201,a229\nzr v202,a230\nzr v203,a231\nzc v204,v196,v197\nzc v205,v198,v199\nzc v206,v200,v201\nzc v207,v202,v203\n"
"ds_write_b128 v30,v[204:207] offset:11264\nzs v[204:207],v29,s[56:59],s48 offen offset:3072 sc1\nzp s48,s48,8192\nzr v160,a8\nzr v161,a9\nzr v162,a10\nzr v163,a11\nzr v164,a40\n"
"zr v165,a41\nzr v166,a42\nzr v167,a43\nzc v168,v160,v161\nzc v169,v162,v163\nzc v170,v164,v165\nzc v171,v166,v167\nds_write_b128 v30,v[168:171] offset:16384\n"
"zs v[168:171],v29,s[56:59],s48 offen offset:0 sc1\nzr v172,a72\nzr v173,a73\nzr v174,a74\nzr v175,a75\nzr v176,a104\nzr v177,a105\nzr v178,a106\n"
"zr v179,a107\nzc v180,v172,v173\nzc v181,v174,v175\nzc v182,v176,v177\nzc v183,v178,v179\nds_write_b128 v30,v[180:183] offset:17408\nzs v[180:183],v29,s[56:59],s48 offen offset:1024 sc1\nzr v184,a136\n"
"zr v185,a137\nzr v186,a138\nzr v187,a139\nzr v188,a168\nzr v189,a169\nzr v190,a170\nzr v191,a171\nzc v192,v184,v185\n"
"zc v193,v186,v187\nzc v194,v188,v189\nzc v195,v190,v191\nds_write_b128 v30,v[192:195] offset:18432\nzs v[192:195],v29,s[56:59],s48 offen offset:2048 sc1\nzr v196,a200\nzr v197,a201\nzr v198,a202\n"
"zr v199,a203\nzr v200,a232\nzr v201,a233\nzr v202,a234\nzr v203,a235\nzc v204,v196,v197\nzc v205,v198,v199\nzc v206,v200,v201\n"
"zc v207,v202,v203\nds_write_b128 v30,v[204:207] offset:19456\nzs v[204:207],v29,s[56:59],s48 offen offset:3072 sc1\nzp s48,s48,8192\nzr v160,a12\nzr v161,a13\nzr v162,a14\nzr v163,a15\n"
"zr v164,a44\nzr v165,a45\nzr v166,a46\nzr v167,a47\nzc v168,v160,v161\nzc v169,v162,v163\nzc v170,v164,v165\nzc v171,v166,v167\n"
"ds_write_b128 v30,v[168:171] offset:24576\nzs v[168:171],v29,s[56:59],s48 offen offset:0 sc1\nzr v172,a76\nzr v173,a77\nzr v174,a78\nzr v175,a79\nzr v176,a108\nzr v177,a109\n"
"zr v178,a110\nzr v179,a111\nzc v180,v172,v173\nzc v181,v174,v175\nzc v182,v176,v177\nzc v183,v178,v179\nds_write_b128 v30,v[180:183] offset:25600\nzs v[180:183],v29,s[56:59],s48 offen offset:1024 sc1\n"
"zr v184,a140\nzr v185,a141\nzr v186,a142\nzr v187,a143\nzr v188,a172\nzr v189,a173\nzr v190,a174\nzr v191,a175\n"
"zc v192,v184,v185\nzc v193,v186,v187\nzc v194,v188,v189\nzc v195,v190,v191\nds_write_b128 v30,v[192:195] offset:26624\nzs v[192:195],v29,s[56:59],s48 offen offset:2048 sc1\nzr v196,a204\nzr v197,a205\n"
"zr v198,a206\nzr v199,a207\nzr v200,a236\nzr v201,a237\nzr v202,a238\nzr v203,a239\nzc v204,v196,v197\nzc v205,v198,v199\n"
"zc v206,v200,v201\nzc v207,v202,v203\nds_write_b128 v30,v[204:207] offset:27648\nzs v[204:207],v29,s[56:59],s48 offen offset:3072 sc1\nzp s48,s48,8192\nzr v160,a16\nzr v161,a17\nzr v162,a18\n"
"zr v163,a19\nzr v164,a48\nzr v165,a49\nzr v166,a50\nzr v167,a51\nzc v168,v160,v161\nzc v169,v162,v163\nzc v170,v164,v165\n"
"zc v171,v166,v167\nds_write_b128 v28,v[168:171] offset:32768\nzs v[168:171],v29,s[56:59],s48 offen offset:0 sc1\nzr v172,a80\nzr v173,a81\nzr v174,a82\nzr v175,a83\nzr v176,a112\n"
"zr v177,a113\nzr v178,a114\nzr v179,a115\nzc v180,v172,v173\nzc v181,v174,v175\nzc v182,v176,v177\nzc v183,v178,v179\nds_write_b128 v28,v[180:183] offset:33792\n"
"zs v[180:183],v29,s[56:59],s48 offen offset:1024 sc1\nzr v184,a144\nzr v185,a145\nzr v186,a146\nzr v187,a147\nzr v188,a176\nzr v189,a177\nzr v190,a178\n"
"zr v191,a179\nzc v192,v184,v185\nzc v193,v186,v187\nzc v194,v188,v189\nzc v195,v190,v191\nds_write_b128 v28,v[192:195] offset:34816\nzs v[192:195],v29,s[56:59],s48 offen offset:2048 sc1\nzr v196,a208\n"
"zr v197,a209\nzr v198,a210\nzr v199,a211\nzr v200,a240\nzr v201,a241\nzr v202,a242\nzr v203,a243\nzc v204,v196,v197\n"
"zc v205,v198,v199\nzc v206,v200,v201\nzc v207,v202,v203\nds_write_b128 v28,v[204:207] offset:35840\nzs v[204:207],v29,s[56:59],s48 offen offset:3072 sc1\nzp s48,s48,8192\nzr v160,a20\nzr v161,a21\n"
"zr v162,a22\nzr v163,a23\nzr v164,a52\nzr v165,a53\nzr v166,a54\nzr v167,a55\nzc v168,v160,v161\nzc v169,v162,v163\n"
"zc v170,v164,v165\nzc v171,v166,v167\nds_write_b128 v28,v[168:171] offset:40960\nzs v[168:171],v29,s[56:59],s48 offen offset:0 sc1\nzr v172,a84\nzr v173,a85\nzr v174,a86\nzr v175,a87\n"
"zr v176,a116\nzr v177,a117\nzr v178,a118\nzr v179,a119\nzc v180,v172,v173\nzc v181,v174,v175\nzc v182,v176,v177\nzc v183,v178,v179\n"
"ds_write_b128 v28,v[180:183] offset:41984\nzs v[180:183],v29,s[56:59],s48 offen offset:1024 sc1\nzr v184,a148\nzr v185,a149\nzr v186,a150\nzr v187,a151\nzr v188,a180\nzr v189,a181\n"
"zr v190,a182\nzr v191,a183\nzc v192,v184,v185\nzc v193,v186,v187\nzc v194,v188,v189\nzc v195,v190,v191\nds_write_b128 v28,v[192:195] offset:43008\nzs v[192:195],v29,s[56:59],s48 offen offset:2048 sc1\n"
"zr v196,a212\nzr v197,a213\nzr v198,a214\nzr v199,a215\nzr v200,a244\nzr v201,a245\nzr v202,a246\nzr v203,a247\n"
"zc v204,v196,v197\nzc v205,v198,v199\nzc v206,v200,v201\nzc v207,v202,v203\nds_write_b128 v28,v[204:207] offset:44032\nzs v[204:207],v29,s[56:59],s48 offen offset:3072 sc1\nzp s48,s48,8192\nzr v160,a24\n"
"zr v161,a25\nzr v162,a26\nzr v163,a27\nzr v164,a56\nzr v165,a57\nzr v166,a58\nzr v167,a59\nzc v168,v160,v161\n"
"zc v169,v162,v163\nzc v170,v164,v165\nzc v171,v166,v167\nds_write_b128 v28,v[168:171] offset:49152\nzs v[168:171],v29,s[56:59],s48 offen offset:0 sc1\nzr v172,a88\nzr v173,a89\nzr v174,a90\n"
"zr v175,a91\nzr v176,a120\nzr v177,a121\nzr v178,a122\nzr v179,a123\nzc v180,v172,v173\nzc v181,v174,v175\nzc v182,v176,v177\n"
"zc v183,v178,v179\nds_write_b128 v28,v[180:183] offset:50176\nzs v[180:183],v29,s[56:59],s48 offen offset:1024 sc1\nzr v184,a152\nzr v185,a153\nzr v186,a154\nzr v187,a155\nzr v188,a184\n"
"zr v189,a185\nzr v190,a186\nzr v191,a187\nzc v192,v184,v185\nzc v193,v186,v187\nzc v194,v188,v189\nzc v195,v190,v191\nds_write_b128 v28,v[192:195] offset:51200\n"
"zs v[192:195],v29,s[56:59],s48 offen offset:2048 sc1\nzr v196,a216\nzr v197,a217\nzr v198,a218\nzr v199,a219\nzr v200,a248\nzr v201,a249\nzr v202,a250\n"
"zr v203,a251\nzc v204,v196,v197\nzc v205,v198,v199\nzc v206,v200,v201\nzc v207,v202,v203\nds_write_b128 v28,v[204:207] offset:52224\nzs v[204:207],v29,s[56:59],s48 offen offset:3072 sc1\nzp s48,s48,8192\n"
"zr v160,a28\nzr v161,a29\nzr v162,a30\nzr v163,a31\nzr v164,a60\nzr v165,a61\nzr v166,a62\nzr v167,a63\n"
"zc v168,v160,v161\nzc v169,v162,v163\nzc v170,v164,v165\nzc v171,v166,v167\nds_write_b128 v28,v[168:171] offset:57344\nzs v[168:171],v29,s[56:59],s48 offen offset:0 sc1\nzr v172,a92\nzr v173,a93\n"
"zr v174,a94\nzr v175,a95\nzr v176,a124\nzr v177,a125\nzr v178,a126\nzr v179,a127\nzc v180,v172,v173\nzc v181,v174,v175\n"
"zc v182,v176,v177\nzc v183,v178,v179\nds_write_b128 v28,v[180:183] offset:58368\nzs v[180:183],v29,s[56:59],s48 offen offset:1024 sc1\nzr v184,a156\nzr v185,a157\nzr v186,a158\nzr v187,a159\n"
"zr v188,a188\nzr v189,a189\nzr v190,a190\nzr v191,a191\nzc v192,v184,v185\nzc v193,v186,v187\nzc v194,v188,v189\nzc v195,v190,v191\n"
"ds_write_b128 v28,v[192:195] offset:59392\nzs v[192:195],v29,s[56:59],s48 offen offset:2048 sc1\nzr v196,a220\nzr v197,a221\nzr v198,a222\nzr v199,a223\nzr v200,a252\nzr v201,a253\n"
"zr v202,a254\nzr v203,a255\nzc v204,v196,v197\nzc v205,v198,v199\nzc v206,v200,v201\nzc v207,v202,v203\nds_write_b128 v28,v[204:207] offset:60416\nzs v[204:207],v29,s[56:59],s48 offen offset:3072 sc1\n"
"zp s48,s48,8192\ns_branch L_P_epiQe\nL_P_epiQ1:\nzr v160,a0\nzr v161,a1\nzr v162,a2\nzr v163,a3\nzr v164,a32\n"
"zr v165,a33\nzr v166,a34\nzr v167,a35\nzc v168,v160,v161\nzc v169,v162,v163\nzc v170,v164,v165\nzc v171,v166,v167\nzs v[168:171],v29,s[56:59],s48 offen offset:0 sc1\n"
"zr v172,a64\nzr v173,a65\nzr v174,a66\nzr v175,a67\nzr v176,a96\nzr v177,a97\nzr v178,a98\nzr v179,a99\n"
"zc v180,v172,v173\nzc v181,v174,v175\nzc v182,v176,v177\nzc v183,v178,v179\nzs v[180:183],v29,s[56:59],s48 offen offset:1024 sc1\nzr v184,a128\nzr v185,a129\nzr v186,a130\n"
"zr v187,a131\nzr v188,a160\nzr v189,a161\nzr v190,a162\nzr v191,a163\nzc v192,v184,v185\nzc v193,v186,v187\nzc v194,v188,v189\n"
"zc v195,v190,v191\nzs v[192:195],v29,s[56:59],s48 offen offset:2048 sc1\nzr v196,a192\nzr v197,a193\nzr v198,a194\nzr v199,a195\nzr v200,a224\nzr v201,a225\n"
"zr v202,a226\nzr v203,a227\nzc v204,v196,v197\nzc v205,v198,v199\nzc v206,v200,v201\nzc v207,v202,v203\nzs v[204:207],v29,s[56:59],s48 offen offset:3072 sc1\nzp s48,s48,8192\n"
"zr v160,a4\nzr v161,a5\nzr v162,a6\nzr v163,a7\nzr v164,a36\nzr v165,a37\nzr v166,a38\nzr v167,a39\n"
"zc v168,v160,v161\nzc v169,v162,v163\nzc v170,v164,v165\nzc v171,v166,v167\nzs v[168:171],v29,s[56:59],s48 offen offset:0 sc1\nzr v172,a68\nzr v173,a69\nzr v174,a70\n"
"zr v175,a71\nzr v176,a100\nzr v177,a101\nzr v178,a102\nzr v179,a103\nzc v180,v172,v173\nzc v181,v174,v175\nzc v182,v176,v177\n"
"zc v183,v178,v179\nzs v[180:183],v29,s[56:59],s48 offen offset:1024 sc1\nzr v184,a132\nzr v185,a133\nzr v186,a134\nzr v187,a135\nzr v188,a164\nzr v189,a165\n"
"zr v190,a166\nzr v191,a167\nzc v192,v184,v185\nzc v193,v186,v187\nzc v194,v188,v189\nzc v195,v190,v191\nzs v[192:195],v29,s[56:59],s48 offen offset:2048 sc1\nzr v196,a196\n"
"zr v197,a197\nzr v198,a198\nzr v199,a199\nzr v200,a228\nzr v201,a229\nzr v202,a230\nzr v203,a231\nzc v204,v196,v197\n"
"zc v205,v198,v199\nzc v206,v200,v201\nzc v207,v202,v203\nzs v[204:207],v29,s[56:59],s48 offen offset:3072 sc1\nzp s48,s48,8192\nzr v160,a8\nzr v161,a9\nzr v162,a10\n"
"zr v163,a11\nzr v164,a40\nzr v165,a41\nzr v166,a42\nzr v167,a43\nzc v168,v160,v161\nzc v169,v162,v163\nzc v170,v164,v165\n"
"zc v171,v166,v167\nzs v[168:171],v29,s[56:59],s48 offen offset:0 sc1\nzr v172,a72\nzr v173,a73\nzr v174,a74\nzr v175,a75\nzr v176,a104\nzr v177,a105\n"
"zr v178,a106\nzr v179,a107\nzc v180,v172,v173\nzc v181,v174,v175\nzc v182,v176,v177\nzc v183,v178,v179\nzs v[180:183],v29,s[56:59],s48 offen offset:1024 sc1\nzr v184,a136\n"
"zr v185,a137\nzr v186,a138\nzr v187,a139\nzr v188,a168\nzr v189,a169\nzr v190,a170\nzr v191,a171\nzc v192,v184,v185\n"
"zc v193,v186,v187\nzc v194,v188,v189\nzc v195,v190,v191\nzs v[192:195],v29,s[56:59],s48 offen offset:2048 sc1\nzr v196,a200\nzr v197,a201\nzr v198,a202\nzr v199,a203\n"
"zr v200,a232\nzr v201,a233\nzr v202,a234\nzr v203,a235\nzc v204,v196,v197\nzc v205,v198,v199\nzc v206,v200,v201\nzc v207,v202,v203\n"
"zs v[204:207],v29,s[56:59],s48 offen offset:3072 sc1\nzp s48,s48,8192\nzr v160,a12\nzr v161,a13\nzr v162,a14\nzr v163,a15\nzr v164,a44\nzr v165,a45\n"
"zr v166,a46\nzr v167,a47\nzc v168,v160,v161\nzc v169,v162,v163\nzc v170,v164,v165\nzc v171,v166,v167\nzs v[168:171],v29,s[56:59],s48 offen offset:0 sc1\nzr v172,a76\n"
"zr v173,a77\nzr v174,a78\nzr v175,a79\nzr v176,a108\nzr v177,a109\nzr v178,a110\nzr v179,a111\nzc v180,v172,v173\n"
"zc v181,v174,v175\nzc v182,v176,v177\nzc v183,v178,v179\nzs v[180:183],v29,s[56:59],s48 offen offset:1024 sc1\nzr v184,a140\nzr v185,a141\nzr v186,a142\nzr v187,a143\n"
"zr v188,a172\nzr v189,a173\nzr v190,a174\nzr v191,a175\nzc v192,v184,v185\nzc v193,v186,v187\nzc v194,v188,v189\nzc v195,v190,v191\n"
"zs v[192:195],v29,s[56:59],s48 offen offset:2048 sc1\nzr v196,a204\nzr v197,a205\nzr v198,a206\nzr v199,a207\nzr v200,a236\nzr v201,a237\nzr v202,a238\n"
"zr v203,a239\nzc v204,v196,v197\nzc v205,v198,v199\nzc v206,v200,v201\nzc v207,v202,v203\nzs v[204:207],v29,s[56:59],s48 offen offset:3072 sc1\nzp s48,s48,8192\nzr v160,a16\n"
"zr v161,a17\nzr v162,a18\nzr v163,a19\nzr v164,a48\nzr v165,a49\nzr v166,a50\nzr v167,a51\nzc v168,v160,v161\n"
"zc v169,v162,v163\nzc v170,v164,v165\nzc v171,v166,v167\nzs v[168:171],v29,s[56:59],s48 offen offset:0 sc1\nzr v172,a80\nzr v173,a81\nzr v174,a82\nzr v175,a83\n"
"zr v176,a112\nzr v177,a113\nzr v178,a114\nzr v179,a115\nzc v180,v172,v173\nzc v181,v174,v175\nzc v182,v176,v177\nzc v183,v178,v179\n"
"zs v[180:183],v29,s[56:59],s48 offen offset:1024 sc1\nzr v184,a144\nzr v185,a145\nzr v186,a146\nzr v187,a147\nzr v188,a176\nzr v189,a177\nzr v190,a178\n"
"zr v191,a179\nzc v192,v184,v185\nzc v193,v186,v187\nzc v194,v188,v189\nzc v195,v190,v191\nzs v[192:195],v29,s[56:59],s48 offen offset:2048 sc1\nzr v196,a208\nzr v197,a209\n"
"zr v198,a210\nzr v199,a211\nzr v200,a240\nzr v201,a241\nzr v202,a242\nzr v203,a243\nzc v204,v196,v197\nzc v205,v198,v199\n"
"zc v206,v200,v201\nzc v207,v202,v203\nzs v[204:207],v29,s[56:59],s48 offen offset:3072 sc1\nzp s48,s48,8192\nzr v160,a20\nzr v161,a21\nzr v162,a22\nzr v163,a23\n"
"zr v164,a52\nzr v165,a53\nzr v166,a54\nzr v167,a55\nzc v168,v160,v161\nzc v169,v162,v163\nzc v170,v164,v165\nzc v171,v166,v167\n"
"zs v[168:171],v29,s[56:59],s48 offen offset:0 sc1\nzr v172,a84\nzr v173,a85\nzr v174,a86\nzr v175,a87\nzr v176,a116\nzr v177,a117\nzr v178,a118\n"
"zr v179,a119\nzc v180,v172,v173\nzc v181,v174,v175\nzc v182,v176,v177\nzc v183,v178,v179\nzs v[180:183],v29,s[56:59],s48 offen offset:1024 sc1\nzr v184,a148\nzr v185,a149\n"
"zr v186,a150\nzr v187,a151\nzr v188,a180\nzr v189,a181\nzr v190,a182\nzr v191,a183\nzc v192,v184,v185\nzc v193,v186,v187\n"
"zc v194,v188,v189\nzc v195,v190,v191\nzs v[192:195],v29,s[56:59],s48 offen offset:2048 sc1\nzr v196,a212\nzr v197,a213\nzr v198,a214\nzr v199,a215\nzr v200,a244\n"
"zr v201,a245\nzr v202,a246\nzr v203,a247\nzc v204,v196,v197\nzc v205,v198,v199\nzc v206,v200,v201\nzc v207,v202,v203\nzs v[204:207],v29,s[56:59],s48 offen offset:3072 sc1\n"
"zp s48,s48,8192\nzr v160,a24\nzr v161,a25\nzr v162,a26\nzr v163,a27\nzr v164,a56\nzr v165,a57\nzr v166,a58\n"
"zr v167,a59\nzc v168,v160,v161\nzc v169,v162,v163\nzc v170,v164,v165\nzc v171,v166,v167\nzs v[168:171],v29,s[56:59],s48 offen offset:0 sc1\nzr v172,a88\nzr v173,a89\n"
"zr v174,a90\nzr v175,a91\nzr v176,a120\nzr v177,a121\nzr v178,a122\nzr v179,a123\nzc v180,v172,v173\nzc v181,v174,v175\n"
"zc v182,v176,v177\nzc v183,v178,v179\nzs v[180:183],v29,s[56:59],s48 offen offset:1024 sc1\nzr v184,a152\nzr v185,a153\nzr v186,a154\nzr v187,a155\nzr v188,a184\n"
"zr v189,a185\nzr v190,a186\nzr v191,a187\nzc v192,v184,v185\nzc v193,v186,v187\nzc v194,v188,v189\nzc v195,v190,v191\nzs v[192:195],v29,s[56:59],s48 offen offset:2048 sc1\n"
"zr v196,a216\nzr v197,a217\nzr v198,a218\nzr v199,a219\nzr v200,a248\nzr v201,a249\nzr v202,a250\nzr v203,a251\n"
"zc v204,v196,v197\nzc v205,v198,v199\nzc v206,v200,v201\nzc v207,v202,v203\nzs v[204:207],v29,s[56:59],s48 offen offset:3072 sc1\nzp s48,s48,8192\nzr v160,a28\nzr v161,a29\n"
"zr v162,a30\nzr v163,a31\nzr v164,a60\nzr v165,a61\nzr v166,a62\nzr v167,a63\nzc v168,v160,v161\nzc v169,v162,v163\n"
"zc v170,v164,v165\nzc v171,v166,v167\nzs v[168:171],v29,s[56:59],s48 offen offset:0 sc1\nzr v172,a92\nzr v173,a93\nzr v174,a94\nzr v175,a95\nzr v176,a124\n"
"zr v177,a125\nzr v178,a126\nzr v179,a127\nzc v180,v172,v173\nzc v181,v174,v175\nzc v182,v176,v177\nzc v183,v178,v179\nzs v[180:183],v29,s[56:59],s48 offen offset:1024 sc1\n"
"zr v184,a156\nzr v185,a157\nzr v186,a158\nzr v187,a159\nzr v188,a188\nzr v189,a189\nzr v190,a190\nzr v191,a191\n"
"zc v192,v184,v185\nzc v193,v186,v187\nzc v194,v188,v189\nzc v195,v190,v191\nzs v[192:195],v29,s[56:59],s48 offen offset:2048 sc1\nzr v196,a220\nzr v197,a221\nzr v198,a222\n"
"zr v199,a223\nzr v200,a252\nzr v201,a253\nzr v202,a254\nzr v203,a255\nzc v204,v196,v197\nzc v205,v198,v199\nzc v206,v200,v201\n"
"zc v207,v202,v203\nzs v[204:207],v29,s[56:59],s48 offen offset:3072 sc1\nzp s48,s48,8192\nL_P_epiQe:\ns_branch L_P_fin\nL_P_notQ:\nzh v5,4,v6\ns_lshl_b32 s89,s23,12\n"
"v_add_u32 v4,s89,v5\ns_and_b32 s89,s22,7\ns_lshr_b32 s90,s22,3\ns_lshr_b32 s91,s90,4\ns_lshl_b32 s89,s89,1\nzp s73,s89,s91\ns_and_b32 s86,s90,1\ns_lshr_b32 s85,s90,1\n"
"s_and_b32 s85,s85,7\ns_lshl_b32 s89,s73,20\nzp s90,s89,8388608\nzp s24,s94,s90\ns_addc_u32 s25,s95,0\ns_and_b32 s25,s25,0xffff\ns_mov_b32 s26,1048576\ns_mov_b32 s27,0x20000\n"
"zp s90,s89,25165824\nzp s28,s94,s90\ns_addc_u32 s29,s95,0\ns_and_b32 s29,s29,0xffff\ns_mov_b32 s30,1048576\ns_mov_b32 s31,0x20000\nzp s90,s89,41943040\nzp s36,s94,s90\n"
"s_addc_u32 s37,s95,0\ns_and_b32 s37,s37,0xffff\ns_mov_b32 s38,1048576\ns_mov_b32 s39,0x20000\ns_mov_b32 s87,0\ns_cmp_eq_u32 s87,0\ns_cbranch_scc0 L_A_seg1ag\ns_sub_u32 s74,15,s85\n"
"s_lshl_b32 s89,s74,2\nzp s89,s89,4\ns_cmp_eq_u32 s86,0\ns_cbranch_scc0 L_A_seg0Bag\ns_mov_b32 s75,0\ns_min_u32 s90,s89,40\ns_cmp_lt_u32 40,s89\ns_cselect_b32 s84,2,0\n"
"s_branch L_A_seggoag\nL_A_seg0Bag:\ns_mov_b32 s75,40\ns_mov_b32 s90,s89\ns_mov_b32 s84,1\ns_cmp_lt_u32 40,s89\ns_cbranch_scc1 L_A_seggoag\ns_mov_b32 s87,1\n"
"L_A_seg1ag:\ns_mov_b32 s74,s85\ns_mov_b32 s75,0\ns_lshl_b32 s90,s74,2\nzp s90,s90,4\ns_mov_b32 s84,0\nL_A_seggoag:\ns_sub_u32 s91,s90,2\n"
"s_lshl_b32 s92,s74,2\ns_sub_i32 s92,s92,2\ns_min_i32 s92,s92,s91\ns_sub_i32 s92,s92,s75\ns_max_i32 s92,s92,0\ns_lshr_b32 s76,s92,1\ns_sub_u32 s91,s91,s75\ns_lshr_b32 s91,s91,1\n"
"s_sub_u32 s77,s91,s76\ns_lshl_b32 s89,s73,7\nzp s89,s89,4096\nzv vmcnt(0)\ns_cmp_eq_u32 s23,0\ns_cbranch_scc0 L_wfb_kvg\nzp s40,s94,s89\ns_addc_u32 s41,s95,0\n"
"s_and_b32 s41,s41,0xffff\ns_mov_b32 s42,64\ns_mov_b32 s43,0x20000\nzh v209,2,v6\ns_mov_b32 s90,0\nL_wf_kvg:\nbuffer_load_dword v208,v209,s[40:43],0 offen sc1\nzv vmcnt(0)\n"
"v_cmp_ne_u32 vcc,s34,v208\ns_nop 2\ns_and_b32 vcc_lo,vcc_lo,0xffff\ns_cmp_eq_u32 vcc_lo,0\ns_cbranch_scc1 L_wfe_kvg\nzp s90,s90,1\ns_cmp_lt_u32 s90,300000\ns_cbranch_scc0 L_wfe_kvg\n"
"s_sleep 1\ns_branch L_wf_kvg\nL_wfe_kvg:\nL_wfb_kvg:\ns_barrier\nL_A_kvokg:\nzp s89,s75,0\ns_lshl_b32 s79,s89,14\n"
"s_and_b32 s89,s89,3\ns_lshl_b32 s89,s89,14\ns_lshl_b32 s90,s23,12\nzp m0,s89,s90\ns_nop 0\nzl v4,s[28:31],s79 offen offset:0 sc1 lds\nzl v4,s[28:31],s79 offen offset:1024 sc1 lds\nzl v4,s[28:31],s79 offen offset:2048 sc1 lds\n"
"zl v4,s[28:31],s79 offen offset:3072 sc1 lds\nzp s89,s75,1\ns_lshl_b32 s79,s89,14\ns_and_b32 s89,s89,3\ns_lshl_b32 s89,s89,14\ns_lshl_b32 s90,s23,12\nzp m0,s89,s90\ns_nop 0\n"
"zl v4,s[28:31],s79 offen offset:0 sc1 lds\nzl v4,s[28:31],s79 offen offset:1024 sc1 lds\nzl v4,s[28:31],s79 offen offset:2048 sc1 lds\nzl v4,s[28:31],s79 offen offset:3072 sc1 lds\ns_cmp_eq_u32 s66,2\ns_cbranch_scc1 L_P_fin\nzh v26,4,v6\ns_lshl_b32 s62,s68,4\n"
"s_lshl_b32 s63,s60,3\nzp s62,s62,s63\ns_mul_i32 s62,s62,56\ns_lshl_b32 s63,s67,2\nzp s62,s62,s63\ns_lshl_b32 s63,s61,1\nzp s62,s62,s63\ns_lshl_b32 s62,s62,10\n"
"s_mov_b32 s52,0xbfb8aa3b\ns_mov_b32 s53,0xbfb8aa3b\nzr v160,a0\nzr v161,a1\nzr v162,a2\nzr v163,a3\nzr v164,a32\nzr v165,a33\n"
"zr v166,a34\nzr v167,a35\nzr v168,a128\nzr v169,a129\nzr v170,a130\nzr v171,a131\nzr v172,a160\nzr v173,a161\n"
"zr v174,a162\nzr v175,a163\nv_pk_mul_f32 v[176:177],v[160:161],s[52:53]\nv_pk_mul_f32 v[178:179],v[162:163],s[52:53]\nze v176,v176\nze v177,v177\nze v178,v178\nze v179,v179\n"
"v_pk_mul_f32 v[168:169],v[168:169],v[160:161]\nv_pk_mul_f32 v[170:171],v[170:171],v[162:163]\nv_pk_add_f32 v[176:177],v[176:177],1.0 op_sel_hi:[1,0]\nv_pk_add_f32 v[178:179],v[178:179],1.0 op_sel_hi:[1,0]\nzq v176,v176\nzq v177,v177\nzq v178,v178\nzq v179,v179\n"
"s_nop 0\nv_pk_mul_f32 v[160:161],v[168:169],v[176:177]\nv_pk_mul_f32 v[162:163],v[170:171],v[178:179]\nv_pk_mul_f32 v[176:177],v[164:165],s[52:53]\nv_pk_mul_f32 v[178:179],v[166:167],s[52:53]\nze v176,v176\nze v177,v177\nze v178,v178\n"
"ze v179,v179\nv_pk_mul_f32 v[172:173],v[172:173],v[164:165]\nv_pk_mul_f32 v[174:175],v[174:175],v[166:167]\nv_pk_add_f32 v[176:177],v[176:177],1.0 op_sel_hi:[1,0]\nv_pk_add_f32 v[178:179],v[178:179],1.0 op_sel_hi:[1,0]\nzq v176,v176\nzq v177,v177\nzq v178,v178\n"
"zq v179,v179\ns_nop 0\nv_pk_mul_f32 v[164:165],v[172:173],v[176:177]\nv_pk_mul_f32 v[166:167],v[174:175],v[178:179]\nzc v180,v160,v161\nzc v181,v162,v163\nzc v182,v164,v165\nzc v183,v166,v167\n"
"zs v[180:183],v26,s[56:59],s62 offen offset:0 sc1\nzr v184,a64\nzr v185,a65\nzr v186,a66\nzr v187,a67\nzr v188,a96\nzr v189,a97\nzr v190,a98\n"
"zr v191,a99\nzr v192,a192\nzr v193,a193\nzr v194,a194\nzr v195,a195\nzr v196,a224\nzr v197,a225\nzr v198,a226\n"
"zr v199,a227\nv_pk_mul_f32 v[200:201],v[184:185],s[52:53]\nv_pk_mul_f32 v[202:203],v[186:187],s[52:53]\nze v200,v200\nze v201,v201\nze v202,v202\nze v203,v203\nv_pk_mul_f32 v[192:193],v[192:193],v[184:185]\n"
"v_pk_mul_f32 v[194:195],v[194:195],v[186:187]\nv_pk_add_f32 v[200:201],v[200:201],1.0 op_sel_hi:[1,0]\nv_pk_add_f32 v[202:203],v[202:203],1.0 op_sel_hi:[1,0]\nzq v200,v200\nzq v201,v201\nzq v202,v202\nzq v203,v203\ns_nop 0\n"
"v_pk_mul_f32 v[184:185],v[192:193],v[200:201]\nv_pk_mul_f32 v[186:187],v[194:195],v[202:203]\nv_pk_mul_f32 v[200:201],v[188:189],s[52:53]\nv_pk_mul_f32 v[202:203],v[190:191],s[52:53]\nze v200,v200\nze v201,v201\nze v202,v202\nze v203,v203\n"
"v_pk_mul_f32 v[196:197],v[196:197],v[188:189]\nv_pk_mul_f32 v[198:199],v[198:199],v[190:191]\nv_pk_add_f32 v[200:201],v[200:201],1.0 op_sel_hi:[1,0]\nv_pk_add_f32 v[202:203],v[202:203],1.0 op_sel_hi:[1,0]\nzq v200,v200\nzq v201,v201\nzq v202,v202\nzq v203,v203\n"
"s_nop 0\nv_pk_mul_f32 v[188:189],v[196:197],v[200:201]\nv_pk_mul_f32 v[190:191],v[198:199],v[202:203]\nzc v204,v184,v185\nzc v205,v186,v187\nzc v206,v188,v189\nzc v207,v190,v191\nzs v[204:207],v26,s[56:59],s62 offen offset:1024 sc1\n"
"zp s62,s62,57344\nzr v160,a4\nzr v161,a5\nzr v162,a6\nzr v163,a7\nzr v164,a36\nzr v165,a37\nzr v166,a38\n"
"zr v167,a39\nzr v168,a132\nzr v169,a133\nzr v170,a134\nzr v171,a135\nzr v172,a164\nzr v173,a165\nzr v174,a166\n"
"zr v175,a167\nv_pk_mul_f32 v[176:177],v[160:161],s[52:53]\nv_pk_mul_f32 v[178:179],v[162:163],s[52:53]\nze v176,v176\nze v177,v177\nze v178,v178\nze v179,v179\nv_pk_mul_f32 v[168:169],v[168:169],v[160:161]\n"
"v_pk_mul_f32 v[170:171],v[170:171],v[162:163]\nv_pk_add_f32 v[176:177],v[176:177],1.0 op_sel_hi:[1,0]\nv_pk_add_f32 v[178:179],v[178:179],1.0 op_sel_hi:[1,0]\nzq v176,v176\nzq v177,v177\nzq v178,v178\nzq v179,v179\ns_nop 0\n"
"v_pk_mul_f32 v[160:161],v[168:169],v[176:177]\nv_pk_mul_f32 v[162:163],v[170:171],v[178:179]\nv_pk_mul_f32 v[176:177],v[164:165],s[52:53]\nv_pk_mul_f32 v[178:179],v[166:167],s[52:53]\nze v176,v176\nze v177,v177\nze v178,v178\nze v179,v179\n"
"v_pk_mul_f32 v[172:173],v[172:173],v[164:165]\nv_pk_mul_f32 v[174:175],v[174:175],v[166:167]\nv_pk_add_f32 v[176:177],v[176:177],1.0 op_sel_hi:[1,0]\nv_pk_add_f32 v[178:179],v[178:179],1.0 op_sel_hi:[1,0]\nzq v176,v176\nzq v177,v177\nzq v178,v178\nzq v179,v179\n"
"s_nop 0\nv_pk_mul_f32 v[164:165],v[172:173],v[176:177]\nv_pk_mul_f32 v[166:167],v[174:175],v[178:179]\nzc v180,v160,v161\nzc v181,v162,v163\nzc v182,v164,v165\nzc v183,v166,v167\nzs v[180:183],v26,s[56:59],s62 offen offset:0 sc1\n"
"zr v184,a68\nzr v185,a69\nzr v186,a70\nzr v187,a71\nzr v188,a100\nzr v189,a101\nzr v190,a102\nzr v191,a103\n"
"zr v192,a196\nzr v193,a197\nzr v194,a198\nzr v195,a199\nzr v196,a228\nzr v197,a229\nzr v198,a230\nzr v199,a231\n"
"v_pk_mul_f32 v[200:201],v[184:185],s[52:53]\nv_pk_mul_f32 v[202:203],v[186:187],s[52:53]\nze v200,v200\nze v201,v201\nze v202,v202\nze v203,v203\nv_pk_mul_f32 v[192:193],v[192:193],v[184:185]\nv_pk_mul_f32 v[194:195],v[194:195],v[186:187]\n"
"v_pk_add_f32 v[200:201],v[200:201],1.0 op_sel_hi:[1,0]\nv_pk_add_f32 v[202:203],v[202:203],1.0 op_sel_hi:[1,0]\nzq v200,v200\nzq v201,v201\nzq v202,v202\nzq v203,v203\ns_nop 0\nv_pk_mul_f32 v[184:185],v[192:193],v[200:201]\n"
"v_pk_mul_f32 v[186:187],v[194:195],v[202:203]\nv_pk_mul_f32 v[200:201],v[188:189],s[52:53]\nv_pk_mul_f32 v[202:203],v[190:191],s[52:53]\nze v200,v200\nze v201,v201\nze v202,v202\nze v203,v203\nv_pk_mul_f32 v[196:197],v[196:197],v[188:189]\n"
"v_pk_mul_f32 v[198:199],v[198:199],v[190:191]\nv_pk_add_f32 v[200:201],v[200:201],1.0 op_sel_hi:[1,0]\nv_pk_add_f32 v[202:203],v[202:203],1.0 op_sel_hi:[1,0]\nzq v200,v200\nzq v201,v201\nzq v202,v202\nzq v203,v203\ns_nop 0\n"
"v_pk_mul_f32 v[188:189],v[196:197],v[200:201]\nv_pk_mul_f32 v[190:191],v[198:199],v[202:203]\nzc v204,v184,v185\nzc v205,v186,v187\nzc v206,v188,v189\nzc v207,v190,v191\nzs v[204:207],v26,s[56:59],s62 offen offset:1024 sc1\nzp s62,s62,57344\n"
"zr v160,a8\nzr v161,a9\nzr v162,a10\nzr v163,a11\nzr v164,a40\nzr v165,a41\nzr v166,a42\nzr v167,a43\n"
"zr v168,a136\nzr v169,a137\nzr v170,a138\nzr v171,a139\nzr v172,a168\nzr v173,a169\nzr v174,a170\nzr v175,a171\n"
"v_pk_mul_f32 v[176:177],v[160:161],s[52:53]\nv_pk_mul_f32 v[178:179],v[162:163],s[52:53]\nze v176,v176\nze v177,v177\nze v178,v178\nze v179,v179\nv_pk_mul_f32 v[168:169],v[168:169],v[160:161]\nv_pk_mul_f32 v[170:171],v[170:171],v[162:163]\n"
"v_pk_add_f32 v[176:177],v[176:177],1.0 op_sel_hi:[1,0]\nv_pk_add_f32 v[178:179],v[178:179],1.0 op_sel_hi:[1,0]\nzq v176,v176\nzq v177,v177\nzq v178,v178\nzq v179,v179\ns_nop 0\nv_pk_mul_f32 v[160:161],v[168:169],v[176:177]\n"
"v_pk_mul_f32 v[162:163],v[170:171],v[178:179]\nv_pk_mul_f32 v[176:177],v[164:165],s[52:53]\nv_pk_mul_f32 v[178:179],v[166:167],s[52:53]\nze v176,v176\nze v177,v177\nze v178,v178\nze v179,v179\nv_pk_mul_f32 v[172:173],v[172:173],v[164:165]\n"
"v_pk_mul_f32 v[174:175],v[174:175],v[166:167]\nv_pk_add_f32 v[176:177],v[176:177],1.0 op_sel_hi:[1,0]\nv_pk_add_f32 v[178:179],v[178:179],1.0 op_sel_hi:[1,0]\nzq v176,v176\nzq v177,v177\nzq v178,v178\nzq v179,v179\ns_nop 0\n"
"v_pk_mul_f32 v[164:165],v[172:173],v[176:177]\nv_pk_mul_f32 v[166:167],v[174:175],v[178:179]\nzc v180,v160,v161\nzc v181,v162,v163\nzc v182,v164,v165\nzc v183,v166,v167\nzs v[180:183],v26,s[56:59],s62 offen offset:0 sc1\nzr v184,a72\n"
"zr v185,a73\nzr v186,a74\nzr v187,a75\nzr v188,a104\nzr v189,a105\nzr v190,a106\nzr v191,a107\nzr v192,a200\n"
"zr v193,a201\nzr v194,a202\nzr v195,a203\nzr v196,a232\nzr v197,a233\nzr v198,a234\nzr v199,a235\nv_pk_mul_f32 v[200:201],v[184:185],s[52:53]\n"
"v_pk_mul_f32 v[202:203],v[186:187],s[52:53]\nze v200,v200\nze v201,v201\nze v202,v202\nze v203,v203\nv_pk_mul_f32 v[192:193],v[192:193],v[184:185]\nv_pk_mul_f32 v[194:195],v[194:195],v[186:187]\nv_pk_add_f32 v[200:201],v[200:201],1.0 op_sel_hi:[1,0]\n"
"v_pk_add_f32 v[202:203],v[202:203],1.0 op_sel_hi:[1,0]\nzq v200,v200\nzq v201,v201\nzq v202,v202\nzq v203,v203\ns_nop 0\nv_pk_mul_f32 v[184:185],v[192:193],v[200:201]\nv_pk_mul_f32 v[186:187],v[194:195],v[202:203]\n"
"v_pk_mul_f32 v[200:201],v[188:189],s[52:53]\nv_pk_mul_f32 v[202:203],v[190:191],s[52:53]\nze v200,v200\nze v201,v201\nze v202,v202\nze v203,v203\nv_pk_mul_f32 v[196:197],v[196:197],v[188:189]\nv_pk_mul_f32 v[198:199],v[198:199],v[190:191]\n"
"v_pk_add_f32 v[200:201],v[200:201],1.0 op_sel_hi:[1,0]\nv_pk_add_f32 v[202:203],v[202:203],1.0 op_sel_hi:[1,0]\nzq v200,v200\nzq v201,v201\nzq v202,v202\nzq v203,v203\ns_nop 0\nv_pk_mul_f32 v[188:189],v[196:197],v[200:201]\n"
"v_pk_mul_f32 v[190:191],v[198:199],v[202:203]\nzc v204,v184,v185\nzc v205,v186,v187\nzc v206,v188,v189\nzc v207,v190,v191\nzs v[204:207],v26,s[56:59],s62 offen offset:1024 sc1\nzp s62,s62,57344\nzr v160,a12\n"
"zr v161,a13\nzr v162,a14\nzr v163,a15\nzr v164,a44\nzr v165,a45\nzr v166,a46\nzr v167,a47\nzr v168,a140\n"
"zr v169,a141\nzr v170,a142\nzr v171,a143\nzr v172,a172\nzr v173,a173\nzr v174,a174\nzr v175,a175\nv_pk_mul_f32 v[176:177],v[160:161],s[52:53]\n"
"v_pk_mul_f32 v[178:179],v[162:163],s[52:53]\nze v176,v176\nze v177,v177\nze v178,v178\nze v179,v179\nv_pk_mul_f32 v[168:169],v[168:169],v[160:161]\nv_pk_mul_f32 v[170:171],v[170:171],v[162:163]\nv_pk_add_f32 v[176:177],v[176:177],1.0 op_sel_hi:[1,0]\n"
"v_pk_add_f32 v[178:179],v[178:179],1.0 op_sel_hi:[1,0]\nzq v176,v176\nzq v177,v177\nzq v178,v178\nzq v179,v179\ns_nop 0\nv_pk_mul_f32 v[160:161],v[168:169],v[176:177]\nv_pk_mul_f32 v[162:163],v[170:171],v[178:179]\n"
"v_pk_mul_f32 v[176:177],v[164:165],s[52:53]\nv_pk_mul_f32 v[178:179],v[166:167],s[52:53]\nze v176,v176\nze v177,v177\nze v178,v178\nze v179,v179\nv_pk_mul_f32 v[172:173],v[172:173],v[164:165]\nv_pk_mul_f32 v[174:175],v[174:175],v[166:167]\n"
"v_pk_add_f32 v[176:177],v[176:177],1.0 op_sel_hi:[1,0]\nv_pk_add_f32 v[178:179],v[178:179],1.0 op_sel_hi:[1,0]\nzq v176,v176\nzq v177,v177\nzq v178,v178\nzq v179,v179\ns_nop 0\nv_pk_mul_f32 v[164:165],v[172:173],v[176:177]\n"
"v_pk_mul_f32 v[166:167],v[174:175],v[178:179]\nzc v180,v160,v161\nzc v181,v162,v163\nzc v182,v164,v165\nzc v183,v166,v167\nzs v[180:183],v26,s[56:59],s62 offen offset:0 sc1\nzr v184,a76\nzr v185,a77\n"
"zr v186,a78\nzr v187,a79\nzr v188,a108\nzr v189,a109\nzr v190,a110\nzr v191,a111\nzr v192,a204\nzr v193,a205\n"
"zr v194,a206\nzr v195,a207\nzr v196,a236\nzr v197,a237\nzr v198,a238\nzr v199,a239\nv_pk_mul_f32 v[200:201],v[184:185],s[52:53]\nv_pk_mul_f32 v[202:203],v[186:187],s[52:53]\n"
"ze v200,v200\nze v201,v201\nze v202,v202\nze v203,v203\nv_pk_mul_f32 v[192:193],v[192:193],v[184:185]\nv_pk_mul_f32 v[194:195],v[194:195],v[186:187]\nv_pk_add_f32 v[200:201],v[200:201],1.0 op_sel_hi:[1,0]\nv_pk_add_f32 v[202:203],v[202:203],1.0 op_sel_hi:[1,0]\n"
"zq v200,v200\nzq v201,v201\nzq v202,v202\nzq v203,v203\ns_nop 0\nv_pk_mul_f32 v[184:185],v[192:193],v[200:201]\nv_pk_mul_f32 v[186:187],v[194:195],v[202:203]\nv_pk_mul_f32 v[200:201],v[188:189],s[52:53]\n"
"v_pk_mul_f32 v[202:203],v[190:191],s[52:53]\nze v200,v200\nze v201,v201\nze v202,v202\nze v203,v203\nv_pk_mul_f32 v[196:197],v[196:197],v[188:189]\nv_pk_mul_f32 v[198:199],v[198:199],v[190:191]\nv_pk_add_f32 v[200:201],v[200:201],1.0 op_sel_hi:[1,0]\n"
"v_pk_add_f32 v[202:203],v[202:203],1.0 op_sel_hi:[1,0]\nzq v200,v200\nzq v201,v201\nzq v202,v202\nzq v203,v203\ns_nop 0\nv_pk_mul_f32 v[188:189],v[196:197],v[200:201]\nv_pk_mul_f32 v[190:191],v[198:199],v[202:203]\n"
"zc v204,v184,v185\nzc v205,v186,v187\nzc v206,v188,v189\nzc v207,v190,v191\nzs v[204:207],v26,s[56:59],s62 offen offset:1024 sc1\nzp s62,s62,57344\nzr v160,a16\nzr v161,a17\n"
"zr v162,a18\nzr v163,a19\nzr v164,a48\nzr v165,a49\nzr v166,a50\nzr v167,a51\nzr v168,a144\nzr v169,a145\n"
"zr v170,a146\nzr v171,a147\nzr v172,a176\nzr v173,a177\nzr v174,a178\nzr v175,a179\nv_pk_mul_f32 v[176:177],v[160:161],s[52:53]\nv_pk_mul_f32 v[178:179],v[162:163],s[52:53]\n"
"ze v176,v176\nze v177,v177\nze v178,v178\nze v179,v179\nv_pk_mul_f32 v[168:169],v[168:169],v[160:161]\nv_pk_mul_f32 v[170:171],v[170:171],v[162:163]\nv_pk_add_f32 v[176:177],v[176:177],1.0 op_sel_hi:[1,0]\nv_pk_add_f32 v[178:179],v[178:179],1.0 op_sel_hi:[1,0]\n"
"zq v176,v176\nzq v177,v177\nzq v178,v178\nzq v179,v179\ns_nop 0\nv_pk_mul_f32 v[160:161],v[168:169],v[176:177]\nv_pk_mul_f32 v[162:163],v[170:171],v[178:179]\nv_pk_mul_f32 v[176:177],v[164:165],s[52:53]\n"
"v_pk_mul_f32 v[178:179],v[166:167],s[52:53]\nze v176,v176\nze v177,v177\nze v178,v178\nze v179,v179\nv_pk_mul_f32 v[172:173],v[172:173],v[164:165]\nv_pk_mul_f32 v[174:175],v[174:175],v[166:167]\nv_pk_add_f32 v[176:177],v[176:177],1.0 op_sel_hi:[1,0]\n"
"v_pk_add_f32 v[178:179],v[178:179],1.0 op_sel_hi:[1,0]\nzq v176,v176\nzq v177,v177\nzq v178,v178\nzq v179,v179\ns_nop 0\nv_pk_mul_f32 v[164:165],v[172:173],v[176:177]\nv_pk_mul_f32 v[166:167],v[174:175],v[178:179]\n"
"zc v180,v160,v161\nzc v181,v162,v163\nzc v182,v164,v165\nzc v183,v166,v167\nzs v[180:183],v26,s[56:59],s62 offen offset:0 sc1\nzr v184,a80\nzr v185,a81\nzr v186,a82\n"
"zr v187,a83\nzr v188,a112\nzr v189,a113\nzr v190,a114\nzr v191,a115\nzr v192,a208\nzr v193,a209\nzr v194,a210\n"
"zr v195,a211\nzr v196,a240\nzr v197,a241\nzr v198,a242\nzr v199,a243\nv_pk_mul_f32 v[200:201],v[184:185],s[52:53]\nv_pk_mul_f32 v[202:203],v[186:187],s[52:53]\nze v200,v200\n"
"ze v201,v201\nze v202,v202\nze v203,v203\nv_pk_mul_f32 v[192:193],v[192:193],v[184:185]\nv_pk_mul_f32 v[194:195],v[194:195],v[186:187]\nv_pk_add_f32 v[200:201],v[200:201],1.0 op_sel_hi:[1,0]\nv_pk_add_f32 v[202:203],v[202:203],1.0 op_sel_hi:[1,0]\nzq v200,v200\n"
"zq v201,v201\nzq v202,v202\nzq v203,v203\ns_nop 0\nv_pk_mul_f32 v[184:185],v[192:193],v[200:201]\nv_pk_mul_f32 v[186:187],v[194:195],v[202:203]\nv_pk_mul_f32 v[200:201],v[188:189],s[52:53]\nv_pk_mul_f32 v[202:203],v[190:191],s[52:53]\n"
"ze v200,v200\nze v201,v201\nze v202,v202\nze v203,v203\nv_pk_mul_f32 v[196:197],v[196:197],v[188:189]\nv_pk_mul_f32 v[198:199],v[198:199],v[190:191]\nv_pk_add_f32 v[200:201],v[200:201],1.0 op_sel_hi:[1,0]\nv_pk_add_f32 v[202:203],v[202:203],1.0 op_sel_hi:[1,0]\n"
"zq v200,v200\nzq v201,v201\nzq v202,v202\nzq v203,v203\ns_nop 0\nv_pk_mul_f32 v[188:189],v[196:197],v[200:201]\nv_pk_mul_f32 v[190:191],v[198:199],v[202:203]\nzc v204,v184,v185\n"
"zc v205,v186,v187\nzc v206,v188,v189\nzc v207,v190,v191\nzs v[204:207],v26,s[56:59],s62 offen offset:1024 sc1\nzp s62,s62,57344\nzr v160,a20\nzr v161,a21\nzr v162,a22\n"
"zr v163,a23\nzr v164,a52\nzr v165,a53\nzr v166,a54\nzr v167,a55\nzr v168,a148\nzr v169,a149\nzr v170,a150\n"
"zr v171,a151\nzr v172,a180\nzr v173,a181\nzr v174,a182\nzr v175,a183\nv_pk_mul_f32 v[176:177],v[160:161],s[52:53]\nv_pk_mul_f32 v[178:179],v[162:163],s[52:53]\nze v176,v176\n"
"ze v177,v177\nze v178,v178\nze v179,v179\nv_pk_mul_f32 v[168:169],v[168:169],v[160:161]\nv_pk_mul_f32 v[170:171],v[170:171],v[162:163]\nv_pk_add_f32 v[176:177],v[176:177],1.0 op_sel_hi:[1,0]\nv_pk_add_f32 v[178:179],v[178:179],1.0 op_sel_hi:[1,0]\nzq v176,v176\n"
"zq v177,v177\nzq v178,v178\nzq v179,v179\ns_nop 0\nv_pk_mul_f32 v[160:161],v[168:169],v[176:177]\nv_pk_mul_f32 v[162:163],v[170:171],v[178:179]\nv_pk_mul_f32 v[176:177],v[164:165],s[52:53]\nv_pk_mul_f32 v[178:179],v[166:167],s[52:53]\n"
"ze v176,v176\nze v177,v177\nze v178,v178\nze v179,v179\nv_pk_mul_f32 v[172:173],v[172:173],v[164:165]\nv_pk_mul_f32 v[174:175],v[174:175],v[166:167]\nv_pk_add_f32 v[176:177],v[176:177],1.0 op_sel_hi:[1,0]\nv_pk_add_f32 v[178:179],v[178:179],1.0 op_sel_hi:[1,0]\n"
"zq v176,v176\nzq v177,v177\nzq v178,v178\nzq v179,v179\ns_nop 0\nv_pk_mul_f32 v[164:165],v[172:173],v[176:177]\nv_pk_mul_f32 v[166:167],v[174:175],v[178:179]\nzc v180,v160,v161\n"
"zc v181,v162,v163\nzc v182,v164,v165\nzc v183,v166,v167\nzs v[180:183],v26,s[56:59],s62 offen offset:0 sc1\nzr v184,a84\nzr v185,a85\nzr v186,a86\nzr v187,a87\n"
"zr v188,a116\nzr v189,a117\nzr v190,a118\nzr v191,a119\nzr v192,a212\nzr v193,a213\nzr v194,a214\nzr v195,a215\n"
"zr v196,a244\nzr v197,a245\nzr v198,a246\nzr v199,a247\nv_pk_mul_f32 v[200:201],v[184:185],s[52:53]\nv_pk_mul_f32 v[202:203],v[186:187],s[52:53]\nze v200,v200\nze v201,v201\n"
"ze v202,v202\nze v203,v203\nv_pk_mul_f32 v[192:193],v[192:193],v[184:185]\nv_pk_mul_f32 v[194:195],v[194:195],v[186:187]\nv_pk_add_f32 v[200:201],v[200:201],1.0 op_sel_hi:[1,0]\nv_pk_add_f32 v[202:203],v[202:203],1.0 op_sel_hi:[1,0]\nzq v200,v200\nzq v201,v201\n"
"zq v202,v202\nzq v203,v203\ns_nop 0\nv_pk_mul_f32 v[184:185],v[192:193],v[200:201]\nv_pk_mul_f32 v[186:187],v[194:195],v[202:203]\nv_pk_mul_f32 v[200:201],v[188:189],s[52:53]\nv_pk_mul_f32 v[202:203],v[190:191],s[52:53]\nze v200,v200\n"
"ze v201,v201\nze v202,v202\nze v203,v203\nv_pk_mul_f32 v[196:197],v[196:197],v[188:189]\nv_pk_mul_f32 v[198:199],v[198:199],v[190:191]\nv_pk_add_f32 v[200:201],v[200:201],1.0 op_sel_hi:[1,0]\nv_pk_add_f32 v[202:203],v[202:203],1.0 op_sel_hi:[1,0]\nzq v200,v200\n"
"zq v201,v201\nzq v202,v202\nzq v203,v203\ns_nop 0\nv_pk_mul_f32 v[188:189],v[196:197],v[200:201]\nv_pk_mul_f32 v[190:191],v[198:199],v[202:203]\nzc v204,v184,v185\nzc v205,v186,v187\n"
"zc v206,v188,v189\nzc v207,v190,v191\nzs v[204:207],v26,s[56:59],s62 offen offset:1024 sc1\nzp s62,s62,57344\nzr v160,a24\nzr v161,a25\nzr v162,a26\nzr v163,a27\n"
"zr v164,a56\nzr v165,a57\nzr v166,a58\nzr v167,a59\nzr v168,a152\nzr v169,a153\nzr v170,a154\nzr v171,a155\n"
"zr v172,a184\nzr v173,a185\nzr v174,a186\nzr v175,a187\nv_pk_mul_f32 v[176:177],v[160:161],s[52:53]\nv_pk_mul_f32 v[178:179],v[162:163],s[52:53]\nze v176,v176\nze v177,v177\n"
"ze v178,v178\nze v179,v179\nv_pk_mul_f32 v[168:169],v[168:169],v[160:161]\nv_pk_mul_f32 v[170:171],v[170:171],v[162:163]\nv_pk_add_f32 v[176:177],v[176:177],1.0 op_sel_hi:[1,0]\nv_pk_add_f32 v[178:179],v[178:179],1.0 op_sel_hi:[1,0]\nzq v176,v176\nzq v177,v177\n"
"zq v178,v178\nzq v179,v179\ns_nop 0\nv_pk_mul_f32 v[160:161],v[168:169],v[176:177]\nv_pk_mul_f32 v[162:163],v[170:171],v[178:179]\nv_pk_mul_f32 v[176:177],v[164:165],s[52:53]\nv_pk_mul_f32 v[178:179],v[166:167],s[52:53]\nze v176,v176\n"
"ze v177,v177\nze v178,v178\nze v179,v179\nv_pk_mul_f32 v[172:173],v[172:173],v[164:165]\nv_pk_mul_f32 v[174:175],v[174:175],v[166:167]\nv_pk_add_f32 v[176:177],v[176:177],1.0 op_sel_hi:[1,0]\nv_pk_add_f32 v[178:179],v[178:179],1.0 op_sel_hi:[1,0]\nzq v176,v176\n"
"zq v177,v177\nzq v178,v178\nzq v179,v179\ns_nop 0\nv_pk_mul_f32 v[164:165],v[172:173],v[176:177]\nv_pk_mul_f32 v[166:167],v[174:175],v[178:179]\nzc v180,v160,v161\nzc v181,v162,v163\n"
"zc v182,v164,v165\nzc v183,v166,v167\nzs v[180:183],v26,s[56:59],s62 offen offset:0 sc1\nzr v184,a88\nzr v185,a89\nzr v186,a90\nzr v187,a91\nzr v188,a120\n"
"zr v189,a121\nzr v190,a122\nzr v191,a123\nzr v192,a216\nzr v193,a217\nzr v194,a218\nzr v195,a219\nzr v196,a248\n"
"zr v197,a249\nzr v198,a250\nzr v199,a251\nv_pk_mul_f32 v[200:201],v[184:185],s[52:53]\nv_pk_mul_f32 v[202:203],v[186:187],s[52:53]\nze v200,v200\nze v201,v201\nze v202,v202\n"
"ze v203,v203\nv_pk_mul_f32 v[192:193],v[192:193],v[184:185]\nv_pk_mul_f32 v[194:195],v[194:195],v[186:187]\nv_pk_add_f32 v[200:201],v[200:201],1.0 op_sel_hi:[1,0]\nv_pk_add_f32 v[202:203],v[202:203],1.0 op_sel_hi:[1,0]\nzq v200,v200\nzq v201,v201\nzq v202,v202\n"
"zq v203,v203\ns_nop 0\nv_pk_mul_f32 v[184:185],v[192:193],v[200:201]\nv_pk_mul_f32 v[186:187],v[194:195],v[202:203]\nv_pk_mul_f32 v[200:201],v[188:189],s[52:53]\nv_pk_mul_f32 v[202:203],v[190:191],s[52:53]\nze v200,v200\nze v201,v201\n"
"ze v202,v202\nze v203,v203\nv_pk_mul_f32 v[196:197],v[196:197],v[188:189]\nv_pk_mul_f32 v[198:199],v[198:199],v[190:191]\nv_pk_add_f32 v[200:201],v[200:201],1.0 op_sel_hi:[1,0]\nv_pk_add_f32 v[202:203],v[202:203],1.0 op_sel_hi:[1,0]\nzq v200,v200\nzq v201,v201\n"
"zq v202,v202\nzq v203,v203\ns_nop 0\nv_pk_mul_f32 v[188:189],v[196:197],v[200:201]\nv_pk_mul_f32 v[190:191],v[198:199],v[202:203]\nzc v204,v184,v185\nzc v205,v186,v187\nzc v206,v188,v189\n"
"zc v207,v190,v191\nzs v[204:207],v26,s[56:59],s62 offen offset:1024 sc1\nzp s62,s62,57344\nzr v160,a28\nzr v161,a29\nzr v162,a30\nzr v163,a31\nzr v164,a60\n"
"zr v165,a61\nzr v166,a62\nzr v167,a63\nzr v168,a156\nzr v169,a157\nzr v170,a158\nzr v171,a159\nzr v172,a188\n"
"zr v173,a189\nzr v174,a190\nzr v175,a191\nv_pk_mul_f32 v[176:177],v[160:161],s[52:53]\nv_pk_mul_f32 v[178:179],v[162:163],s[52:53]\nze v176,v176\nze v177,v177\nze v178,v178\n"
"ze v179,v179\nv_pk_mul_f32 v[168:169],v[168:169],v[160:161]\nv_pk_mul_f32 v[170:171],v[170:171],v[162:163]\nv_pk_add_f32 v[176:177],v[176:177],1.0 op_sel_hi:[1,0]\nv_pk_add_f32 v[178:179],v[178:179],1.0 op_sel_hi:[1,0]\nzq v176,v176\nzq v177,v177\nzq v178,v178\n"
"zq v179,v179\ns_nop 0\nv_pk_mul_f32 v[160:161],v[168:169],v[176:177]\nv_pk_mul_f32 v[162:163],v[170:171],v[178:179]\nv_pk_mul_f32 v[176:177],v[164:165],s[52:53]\nv_pk_mul_f32 v[178:179],v[166:167],s[52:53]\nze v176,v176\nze v177,v177\n"
"ze v178,v178\nze v179,v179\nv_pk_mul_f32 v[172:173],v[172:173],v[164:165]\nv_pk_mul_f32 v[174:175],v[174:175],v[166:167]\nv_pk_add_f32 v[176:177],v[176:177],1.0 op_sel_hi:[1,0]\nv_pk_add_f32 v[178:179],v[178:179],1.0 op_sel_hi:[1,0]\nzq v176,v176\nzq v177,v177\n"
"zq v178,v178\nzq v179,v179\ns_nop 0\nv_pk_mul_f32 v[164:165],v[172:173],v[176:177]\nv_pk_mul_f32 v[166:167],v[174:175],v[178:179]\nzc v180,v160,v161\nzc v181,v162,v163\nzc v182,v164,v165\n"
"zc v183,v166,v167\nzs v[180:183],v26,s[56:59],s62 offen offset:0 sc1\nzr v184,a92\nzr v185,a93\nzr v186,a94\nzr v187,a95\nzr v188,a124\nzr v189,a125\n"
"zr v190,a126\nzr v191,a127\nzr v192,a220\nzr v193,a221\nzr v194,a222\nzr v195,a223\nzr v196,a252\nzr v197,a253\n"
"zr v198,a254\nzr v199,a255\nv_pk_mul_f32 v[200:201],v[184:185],s[52:53]\nv_pk_mul_f32 v[202:203],v[186:187],s[52:53]\nze v200,v200\nze v201,v201\nze v202,v202\nze v203,v203\n"
"v_pk_mul_f32 v[192:193],v[192:193],v[184:185]\nv_pk_mul_f32 v[194:195],v[194:195],v[186:187]\nv_pk_add_f32 v[200:201],v[200:201],1.0 op_sel_hi:[1,0]\nv_pk_add_f32 v[202:203],v[202:203],1.0 op_sel_hi:[1,0]\nzq v200,v200\nzq v201,v201\nzq v202,v202\nzq v203,v203\n"
"s_nop 0\nv_pk_mul_f32 v[184:185],v[192:193],v[200:201]\nv_pk_mul_f32 v[186:187],v[194:195],v[202:203]\nv_pk_mul_f32 v[200:201],v[188:189],s[52:53]\nv_pk_mul_f32 v[202:203],v[190:191],s[52:53]\nze v200,v200\nze v201,v201\nze v202,v202\n"
"ze v203,v203\nv_pk_mul_f32 v[196:197],v[196:197],v[188:189]\nv_pk_mul_f32 v[198:199],v[198:199],v[190:191]\nv_pk_add_f32 v[200:201],v[200:201],1.0 op_sel_hi:[1,0]\nv_pk_add_f32 v[202:203],v[202:203],1.0 op_sel_hi:[1,0]\nzq v200,v200\nzq v201,v201\nzq v202,v202\n"
"zq v203,v203\ns_nop 0\nv_pk_mul_f32 v[188:189],v[196:197],v[200:201]\nv_pk_mul_f32 v[190:191],v[198:199],v[202:203]\nzc v204,v184,v185\nzc v205,v186,v187\nzc v206,v188,v189\nzc v207,v190,v191\n"
"zs v[204:207],v26,s[56:59],s62 offen offset:1024 sc1\nzp s62,s62,57344\ns_lshl_b32 s69,s68,6\ns_lshl_b32 s62,s67,2\nzp s69,s69,s62\nzp s69,s69,8192\nzv vmcnt(0)\ns_barrier\n"
"s_cmp_eq_u32 s23,0\ns_cbranch_scc0 L_nf_t2g\nv_mov_b32 v26,s69\nv_mov_b32 v27,s34\nglobal_store_dword v26,v27,s[94:95] sc1\nL_nf_t2g:\nL_P_fin:\ns_cmp_eq_u32 s86,0\n"
"s_cbranch_scc0 L_A_qB\nzw a0,0\nzw a1,0\nzw a2,0\nzw a3,0\nzw a4,0\nzw a5,0\nzw a6,0\n"
"zw a7,0\nzw a8,0\nzw a9,0\nzw a10,0\nzw a11,0\nzw a12,0\nzw a13,0\nzw a14,0\n"
"zw a15,0\nzw a16,0\nzw a17,0\nzw a18,0\nzw a19,0\nzw a20,0\nzw a21,0\nzw a22,0\n"
"zw a23,0\nzw a24,0\nzw a25,0\nzw a26,0\nzw a27,0\nzw a28,0\nzw a29,0\nzw a30,0\n"
"zw a31,0\nzw a32,0\nzw a33,0\nzw a34,0\nzw a35,0\nzw a36,0\nzw a37,0\nzw a38,0\n"
"zw a39,0\nzw a40,0\nzw a41,0\nzw a42,0\nzw a43,0\nzw a44,0\nzw a45,0\nzw a46,0\n"
"zw a47,0\nzw a48,0\nzw a49,0\nzw a50,0\nzw a51,0\nzw a52,0\nzw a53,0\nzw a54,0\n"
"zw a55,0\nzw a56,0\nzw a57,0\nzw a58,0\nzw a59,0\nzw a60,0\nzw a61,0\nzw a62,0\n"
"zw a63,0\nzw a64,0\nzw a65,0\nzw a66,0\nzw a67,0\nzw a68,0\nzw a69,0\nzw a70,0\n"
"zw a71,0\nzw a72,0\nzw a73,0\nzw a74,0\nzw a75,0\nzw a76,0\nzw a77,0\nzw a78,0\n"
"zw a79,0\nzw a80,0\nzw a81,0\nzw a82,0\nzw a83,0\nzw a84,0\nzw a85,0\nzw a86,0\n"
"zw a87,0\nzw a88,0\nzw a89,0\nzw a90,0\nzw a91,0\nzw a92,0\nzw a93,0\nzw a94,0\n"
"zw a95,0\nzw a96,0\nzw a97,0\nzw a98,0\nzw a99,0\nzw a100,0\nzw a101,0\nzw a102,0\n"
"zw a103,0\nzw a104,0\nzw a105,0\nzw a106,0\nzw a107,0\nzw a108,0\nzw a109,0\nzw a110,0\n"
"zw a111,0\nzw a112,0\nzw a113,0\nzw a114,0\nzw a115,0\nzw a116,0\nzw a117,0\nzw a118,0\n"
"zw a119,0\nzw a120,0\nzw a121,0\nzw a122,0\nzw a123,0\nzw a124,0\nzw a125,0\nzw a126,0\n"
"zw a127,0\nzv vmcnt(32) lgkmcnt(0)\ns_barrier\ns_lshl_b32 s89,s23,14\nv_add_u32 v112,s89,v5\ns_cmp_lt_u32 s23,2\ns_mov_b32 s89,65536\ns_cselect_b32 s89,32768,s89\n"
"v_add_u32 v112,s89,v112\nzd a[128:131],v112 offset:0\nzd a[132:135],v112 offset:1024\nzd a[136:139],v112 offset:2048\nzd a[140:143],v112 offset:3072\nzd a[144:147],v112 offset:4096\nzd a[148:151],v112 offset:5120\nzd a[152:155],v112 offset:6144\n"
"zd a[156:159],v112 offset:7168\nzd a[160:163],v112 offset:8192\nzd a[164:167],v112 offset:9216\nzd a[168:171],v112 offset:10240\nzd a[172:175],v112 offset:11264\nzd a[176:179],v112 offset:12288\nzd a[180:183],v112 offset:13312\nzd a[184:187],v112 offset:14336\n"
"zd a[188:191],v112 offset:15360\nzv lgkmcnt(0)\ns_barrier\nzp s89,s75,2\ns_lshl_b32 s79,s89,14\ns_and_b32 s89,s89,3\ns_lshl_b32 s89,s89,14\ns_lshl_b32 s90,s23,12\n"
"zp m0,s89,s90\ns_nop 0\nzl v4,s[28:31],s79 offen offset:0 sc1 lds\nzl v4,s[28:31],s79 offen offset:1024 sc1 lds\nzl v4,s[28:31],s79 offen offset:2048 sc1 lds\nzl v4,s[28:31],s79 offen offset:3072 sc1 lds\nzp s89,s75,1\ns_lshl_b32 s78,s89,14\n"
"s_and_b32 s89,s89,3\ns_lshl_b32 s89,s89,14\ns_lshl_b32 s90,s23,12\nzp s89,s89,s90\nzp m0,s89,65536\ns_nop 0\nzl v4,s[36:39],s78 offen offset:0 sc1 lds\nzl v4,s[36:39],s78 offen offset:1024 sc1 lds\n"
"zl v4,s[36:39],s78 offen offset:2048 sc1 lds\nzl v4,s[36:39],s78 offen offset:3072 sc1 lds\nzp s89,s75,3\ns_lshl_b32 s79,s89,14\ns_and_b32 s89,s89,3\ns_lshl_b32 s89,s89,14\ns_lshl_b32 s90,s23,12\nzp m0,s89,s90\n"
"s_nop 0\nzl v4,s[28:31],s79 offen offset:0 sc1 lds\nzl v4,s[28:31],s79 offen offset:1024 sc1 lds\nzl v4,s[28:31],s79 offen offset:2048 sc1 lds\nzl v4,s[28:31],s79 offen offset:3072 sc1 lds\nzp s89,s75,2\ns_lshl_b32 s78,s89,14\nzp s89,s75,4\n"
"s_lshl_b32 s79,s89,14\ns_and_b32 s89,s75,2\ns_lshl_b32 s89,s89,14\ns_lshl_b32 s90,s23,12\nzp s80,s89,s90\ns_xor_b32 s91,s89,0x8000\nzp s81,s91,s90\nzp s81,s81,65536\n"
"v_add_u32 v8,s91,v5\nzp s92,s89,65536\nv_add_u32 v9,s92,v5\nv_add_u32 v208,s89,v5\ns_lshl_b32 s82,s74,7\ns_lshl_b32 s90,s23,5\nzp s82,s82,s90\ns_lshl_b32 s90,s75,5\n"
"s_sub_u32 s82,s82,s90\ns_lshl_b32 s89,s73,3\nzp s89,s89,s85\ns_lshl_b32 s89,s89,2\nzp s89,s89,s23\ns_lshl_b32 s89,s89,2\nzp s89,s89,24576\nv_mov_b32 v15,s89\n"
"v_mov_b32 v112,s34\nzv vmcnt(12)\nglobal_store_dword v15,v112,s[94:95] sc1\ns_mov_b32 s8,3\ns_branch L_A_qdone\nL_A_qB:\ns_lshl_b32 s89,s73,3\nzp s89,s89,s85\n"
"s_lshl_b32 s89,s89,4\nzp s89,s89,24576\nzv vmcnt(0)\ns_cmp_eq_u32 s23,0\ns_cbranch_scc0 L_wfb_q\nzp s40,s94,s89\ns_addc_u32 s41,s95,0\ns_and_b32 s41,s41,0xffff\n"
"s_mov_b32 s42,16\ns_mov_b32 s43,0x20000\nzh v113,2,v6\ns_mov_b32 s90,0\nL_wf_q:\nbuffer_load_dword v112,v113,s[40:43],0 offen sc1\nzv vmcnt(0)\nv_cmp_ne_u32 vcc,s34,v112\n"
"s_nop 2\ns_and_b32 vcc_lo,vcc_lo,0xf\ns_cmp_eq_u32 vcc_lo,0\ns_cbranch_scc1 L_wfe_q\nzp s90,s90,1\ns_cmp_lt_u32 s90,300000\ns_cbranch_scc0 L_wfe_q\ns_sleep 1\n"
"s_branch L_wf_q\nL_wfe_q:\nL_wfb_q:\ns_barrier\ns_lshl_b32 s89,s74,3\ns_lshl_b32 s90,s23,1\nzp s89,s89,s90\ns_lshl_b32 s89,s89,13\n"
"zp s90,s89,0\nzl a[128:131],v5,s[24:27],s90 offen offset:0 sc1\nzl a[132:135],v5,s[24:27],s90 offen offset:1024 sc1\nzl a[136:139],v5,s[24:27],s90 offen offset:2048 sc1\nzl a[140:143],v5,s[24:27],s90 offen offset:3072 sc1\nzp s90,s89,4096\nzl a[144:147],v5,s[24:27],s90 offen offset:0 sc1\nzl a[148:151],v5,s[24:27],s90 offen offset:1024 sc1\n"
"zl a[152:155],v5,s[24:27],s90 offen offset:2048 sc1\nzl a[156:159],v5,s[24:27],s90 offen offset:3072 sc1\nzp s90,s89,8192\nzl a[160:163],v5,s[24:27],s90 offen offset:0 sc1\nzl a[164:167],v5,s[24:27],s90 offen offset:1024 sc1\nzl a[168:171],v5,s[24:27],s90 offen offset:2048 sc1\nzl a[172:175],v5,s[24:27],s90 offen offset:3072 sc1\nzp s90,s89,12288\n"
"zl a[176:179],v5,s[24:27],s90 offen offset:0 sc1\nzl a[180:183],v5,s[24:27],s90 offen offset:1024 sc1\nzl a[184:187],v5,s[24:27],s90 offen offset:2048 sc1\nzl a[188:191],v5,s[24:27],s90 offen offset:3072 sc1\nzp s89,s75,0\ns_lshl_b32 s78,s89,14\ns_and_b32 s89,s89,3\ns_lshl_b32 s89,s89,14\n"
"s_lshl_b32 s90,s23,12\nzp s89,s89,s90\nzp m0,s89,65536\ns_nop 0\nzl v4,s[36:39],s78 offen offset:0 sc1 lds\nzl v4,s[36:39],s78 offen offset:1024 sc1 lds\nzl v4,s[36:39],s78 offen offset:2048 sc1 lds\nzl v4,s[36:39],s78 offen offset:3072 sc1 lds\n"
"zp s89,s75,2\ns_lshl_b32 s79,s89,14\ns_and_b32 s89,s89,3\ns_lshl_b32 s89,s89,14\ns_lshl_b32 s90,s23,12\nzp m0,s89,s90\ns_nop 0\nzl v4,s[28:31],s79 offen offset:0 sc1 lds\n"
"zl v4,s[28:31],s79 offen offset:1024 sc1 lds\nzl v4,s[28:31],s79 offen offset:2048 sc1 lds\nzl v4,s[28:31],s79 offen offset:3072 sc1 lds\nzp s89,s75,1\ns_lshl_b32 s78,s89,14\ns_and_b32 s89,s89,3\ns_lshl_b32 s89,s89,14\ns_lshl_b32 s90,s23,12\n"
"zp s89,s89,s90\nzp m0,s89,65536\ns_nop 0\nzl v4,s[36:39],s78 offen offset:0 sc1 lds\nzl v4,s[36:39],s78 offen offset:1024 sc1 lds\nzl v4,s[36:39],s78 offen offset:2048 sc1 lds\nzl v4,s[36:39],s78 offen offset:3072 sc1 lds\nzp s89,s75,3\n"
"s_lshl_b32 s79,s89,14\ns_and_b32 s89,s89,3\ns_lshl_b32 s89,s89,14\ns_lshl_b32 s90,s23,12\nzp m0,s89,s90\ns_nop 0\nzl v4,s[28:31],s79 offen offset:0 sc1 lds\nzl v4,s[28:31],s79 offen offset:1024 sc1 lds\n"
"zl v4,s[28:31],s79 offen offset:2048 sc1 lds\nzl v4,s[28:31],s79 offen offset:3072 sc1 lds\nzp s89,s75,2\ns_lshl_b32 s78,s89,14\nzp s89,s75,4\ns_lshl_b32 s79,s89,14\ns_and_b32 s89,s75,2\ns_lshl_b32 s89,s89,14\n"
"s_lshl_b32 s90,s23,12\nzp s80,s89,s90\ns_xor_b32 s91,s89,0x8000\nzp s81,s91,s90\nzp s81,s81,65536\nv_add_u32 v8,s91,v5\nzp s92,s89,65536\nv_add_u32 v9,s92,v5\n"
"v_add_u32 v208,s89,v5\ns_lshl_b32 s82,s74,7\ns_lshl_b32 s90,s23,5\nzp s82,s82,s90\ns_lshl_b32 s90,s75,5\ns_sub_u32 s82,s82,s90\ns_mov_b32 s8,2\nL_A_qdone:\n"
"s_mov_b32 s83,0x45800000\nv_mov_b32 v14,0xff800000\nv_lshrrev_b32 v28,4,v6\nzh v28,2,v28\nv_and_b32 v29,15,v6\nv_sub_u32 v28,v28,v29\nv_add_u32 v16,-16,v28\nv_add_u32 v17,-15,v28\n"
"v_add_u32 v18,-14,v28\nv_add_u32 v19,-13,v28\nv_add_u32 v20,0,v28\nv_add_u32 v21,1,v28\nv_add_u32 v22,2,v28\nv_add_u32 v23,3,v28\nv_add_u32 v24,16,v28\nv_add_u32 v25,17,v28\n"
"v_add_u32 v26,18,v28\nv_add_u32 v27,19,v28\ns_lshl_b32 s89,s73,3\nzp s89,s89,s85\ns_lshl_b32 s90,s89,16\nzp s90,s90,83886080\nzp s44,s94,s90\ns_addc_u32 s45,s95,0\n"
"s_and_b32 s45,s45,0xffff\ns_mov_b32 s46,65536\ns_mov_b32 s47,0x20000\ns_lshl_b32 s90,s89,12\ns_lshl_b32 s91,s23,10\nzp s90,s90,s91\nzp s90,s90,92274688\nzp s4,s94,s90\n"
"s_addc_u32 s5,s95,0\ns_lshl_b32 s90,s89,4\ns_lshl_b32 s91,s23,2\nzp s90,s90,s91\nzp s90,s90,12288\nzp s6,s94,s90\ns_addc_u32 s7,s95,0\ns_cmp_eq_u32 s8,3\n"
"s_cbranch_scc1 L_A_compz\nL_A_compute:\nzw a0,0\nzw a1,0\nzw a2,0\nzw a3,0\nzw a4,0\nzw a5,0\n"
"zw a6,0\nzw a7,0\nzw a8,0\nzw a9,0\nzw a10,0\nzw a11,0\nzw a12,0\nzw a13,0\n"
"zw a14,0\nzw a15,0\nzw a16,0\nzw a17,0\nzw a18,0\nzw a19,0\nzw a20,0\nzw a21,0\n"
"zw a22,0\nzw a23,0\nzw a24,0\nzw a25,0\nzw a26,0\nzw a27,0\nzw a28,0\nzw a29,0\n"
"zw a30,0\nzw a31,0\nzw a32,0\nzw a33,0\nzw a34,0\nzw a35,0\nzw a36,0\nzw a37,0\n"
"zw a38,0\nzw a39,0\nzw a40,0\nzw a41,0\nzw a42,0\nzw a43,0\nzw a44,0\nzw a45,0\n"
"zw a46,0\nzw a47,0\nzw a48,0\nzw a49,0\nzw a50,0\nzw a51,0\nzw a52,0\nzw a53,0\n"
"zw a54,0\nzw a55,0\nzw a56,0\nzw a57,0\nzw a58,0\nzw a59,0\nzw a60,0\nzw a61,0\n"
"zw a62,0\nzw a63,0\nzw a64,0\nzw a65,0\nzw a66,0\nzw a67,0\nzw a68,0\nzw a69,0\n"
"zw a70,0\nzw a71,0\nzw a72,0\nzw a73,0\nzw a74,0\nzw a75,0\nzw a76,0\nzw a77,0\n"
"zw a78,0\nzw a79,0\nzw a80,0\nzw a81,0\nzw a82,0\nzw a83,0\nzw a84,0\nzw a85,0\n"
"zw a86,0\nzw a87,0\nzw a88,0\nzw a89,0\nzw a90,0\nzw a91,0\nzw a92,0\nzw a93,0\n"
"zw a94,0\nzw a95,0\nzw a96,0\nzw a97,0\nzw a98,0\nzw a99,0\nzw a100,0\nzw a101,0\n"
"zw a102,0\nzw a103,0\nzw a104,0\nzw a105,0\nzw a106,0\nzw a107,0\nzw a108,0\nzw a109,0\n"
"zw a110,0\nzw a111,0\nzw a112,0\nzw a113,0\nzw a114,0\nzw a115,0\nzw a116,0\nzw a117,0\n"
"zw a118,0\nzw a119,0\nzw a120,0\nzw a121,0\nzw a122,0\nzw a123,0\nzw a124,0\nzw a125,0\n"
"zw a126,0\nzw a127,0\nL_A_compz:\nv_mov_b32 v10,0\nv_mov_b32 v11,0\ns_cmp_eq_u32 s8,1\ns_cbranch_scc1 L_A_w1b\ns_cmp_eq_u32 s8,3\n"
"s_cbranch_scc1 L_A_w1e\ns_cmp_eq_u32 s8,2\ns_cbranch_scc1 L_A_w1c\nzv vmcnt(20)\ns_branch L_A_w1e\nL_A_w1c:\nzv vmcnt(16)\ns_branch L_A_w1e\n"
"L_A_w1b:\nzv vmcnt(38)\nL_A_w1e:\ns_barrier\nzd a[192:195],v208 offset:0\nzd a[196:199],v208 offset:1024\nzd a[200:203],v208 offset:2048\nzd a[204:207],v208 offset:3072\n"
"zd a[208:211],v208 offset:4096\nzd a[212:215],v208 offset:5120\nzd a[216:219],v208 offset:6144\nzd a[220:223],v208 offset:7168\nzd a[224:227],v208 offset:8192\nzd a[228:231],v208 offset:9216\nzd a[232:235],v208 offset:10240\nzd a[236:239],v208 offset:11264\n"
"zd a[240:243],v208 offset:12288\nzd a[244:247],v208 offset:13312\nzd a[248:251],v208 offset:14336\nzd a[252:255],v208 offset:15360\nzv lgkmcnt(12)\nzm v[32:35],a[192:195],a[128:131],0\nzm v[36:39],a[192:195],a[160:163],0\nzm v[32:35],a[196:199],a[132:135],v[32:35]\n"
"zm v[36:39],a[196:199],a[164:167],v[36:39]\nzm v[32:35],a[200:203],a[136:139],v[32:35]\nzm v[36:39],a[200:203],a[168:171],v[36:39]\nzm v[32:35],a[204:207],a[140:143],v[32:35]\nzm v[36:39],a[204:207],a[172:175],v[36:39]\nzv lgkmcnt(8)\nzm v[32:35],a[208:211],a[144:147],v[32:35]\nzm v[36:39],a[208:211],a[176:179],v[36:39]\n"
"zm v[32:35],a[212:215],a[148:151],v[32:35]\nzm v[36:39],a[212:215],a[180:183],v[36:39]\nzm v[32:35],a[216:219],a[152:155],v[32:35]\nzm v[36:39],a[216:219],a[184:187],v[36:39]\nzm v[32:35],a[220:223],a[156:159],v[32:35]\nzm v[36:39],a[220:223],a[188:191],v[36:39]\nzv lgkmcnt(4)\nzm v[40:43],a[224:227],a[128:131],0\n"
"zm v[44:47],a[224:227],a[160:163],0\nzm v[40:43],a[228:231],a[132:135],v[40:43]\nzm v[44:47],a[228:231],a[164:167],v[44:47]\nzm v[40:43],a[232:235],a[136:139],v[40:43]\nzm v[44:47],a[232:235],a[168:171],v[44:47]\nzm v[40:43],a[236:239],a[140:143],v[40:43]\nzm v[44:47],a[236:239],a[172:175],v[44:47]\nzv lgkmcnt(0)\n"
"zm v[40:43],a[240:243],a[144:147],v[40:43]\nzm v[44:47],a[240:243],a[176:179],v[44:47]\nzm v[40:43],a[244:247],a[148:151],v[40:43]\nzm v[44:47],a[244:247],a[180:183],v[44:47]\nzm v[40:43],a[248:251],a[152:155],v[40:43]\nzm v[44:47],a[248:251],a[184:187],v[44:47]\nzm v[40:43],a[252:255],a[156:159],v[40:43]\nzm v[44:47],a[252:255],a[188:191],v[44:47]\n"
"s_cmp_eq_u32 s8,3\ns_cbranch_scc1 L_A_w2e\ns_cmp_eq_u32 s8,1\ns_cbranch_scc1 L_A_w2b\nzv vmcnt(16)\ns_branch L_A_w2e\nL_A_w2b:\nzv vmcnt(34)\n"
"L_A_w2e:\ns_barrier\nzd a[192:195],v208 offset:16384\nzd a[196:199],v208 offset:17408\nzd a[200:203],v208 offset:18432\nzd a[204:207],v208 offset:19456\nzd a[208:211],v208 offset:20480\nzd a[212:215],v208 offset:21504\n"
"zd a[216:219],v208 offset:22528\nzd a[220:223],v208 offset:23552\nzd a[224:227],v208 offset:24576\nzd a[228:231],v208 offset:25600\nzd a[232:235],v208 offset:26624\nzd a[236:239],v208 offset:27648\nzd a[240:243],v208 offset:28672\nzd a[244:247],v208 offset:29696\n"
"zd a[248:251],v208 offset:30720\nzd a[252:255],v208 offset:31744\nv_cmp_ge_i32 s[56:57],s82,v20\nv_cmp_ge_i32 s[58:59],s82,v21\nv_cmp_ge_i32 s[60:61],s82,v22\nv_cmp_ge_i32 s[62:63],s82,v23\nv_cmp_ge_i32 s[48:49],s82,v16\nv_cmp_ge_i32 s[50:51],s82,v17\n"
"v_cmp_ge_i32 s[52:53],s82,v18\nv_cmp_ge_i32 s[54:55],s82,v19\nv_cmp_ge_i32 s[64:65],s82,v24\nv_cmp_ge_i32 s[66:67],s82,v25\nv_cmp_ge_i32 s[68:69],s82,v26\nv_cmp_ge_i32 s[70:71],s82,v27\ns_nop 1\nzk v32,v14,v32,s[56:57]\n"
"zk v33,v14,v33,s[58:59]\nzk v34,v14,v34,s[60:61]\nzk v35,v14,v35,s[62:63]\nzk v36,v14,v36,s[48:49]\nzk v37,v14,v37,s[50:51]\nzk v38,v14,v38,s[52:53]\nzk v39,v14,v39,s[54:55]\nzk v40,v14,v40,s[64:65]\n"
"zk v41,v14,v41,s[66:67]\nzk v42,v14,v42,s[68:69]\nzk v43,v14,v43,s[70:71]\nzk v44,v14,v44,s[56:57]\nzk v45,v14,v45,s[58:59]\nzk v46,v14,v46,s[60:61]\nzk v47,v14,v47,s[62:63]\nv_max3_f32 v12,v32,v33,v34\n"
"v_max3_f32 v12,v12,v35,v40\nv_max3_f32 v12,v12,v41,v42\nv_max_f32 v12,v12,v43\nv_mov_b32 v15,v12\ns_nop 1\nv_permlane16_swap_b32 v12,v15\ns_nop 1\nv_max_f32 v12,v12,v15\n"
"v_mov_b32 v15,v12\ns_nop 1\nv_permlane32_swap_b32 v12,v15\ns_nop 1\nv_max_f32 v12,v12,v15\nv_max_f32 v12,0xf149f2ca,v12\nzu v96,0,v12\nzu v97,0,v12\n"
"zu v98,0,v12\nzu v99,0,v12\nv_max3_f32 v13,v36,v37,v38\nv_max3_f32 v13,v13,v39,v44\nv_max3_f32 v13,v13,v45,v46\nv_max_f32 v13,v13,v47\nv_mov_b32 v15,v13\ns_nop 1\n"
"v_permlane16_swap_b32 v13,v15\ns_nop 1\nv_max_f32 v13,v13,v15\nv_mov_b32 v15,v13\ns_nop 1\nv_permlane32_swap_b32 v13,v15\ns_nop 1\nv_max_f32 v13,v13,v15\n"
"v_max_f32 v13,0xf149f2ca,v13\nzu v100,0,v13\nzu v101,0,v13\nzu v102,0,v13\nzu v103,0,v13\nzv lgkmcnt(0)\nzm v[48:51],a[192:195],a[128:131],v[96:99]\nzm v[52:55],a[192:195],a[160:163],v[100:103]\n"
"zm v[48:51],a[196:199],a[132:135],v[48:51]\nzm v[52:55],a[196:199],a[164:167],v[52:55]\nzm v[48:51],a[200:203],a[136:139],v[48:51]\nzm v[52:55],a[200:203],a[168:171],v[52:55]\nzm v[48:51],a[204:207],a[140:143],v[48:51]\nzm v[52:55],a[204:207],a[172:175],v[52:55]\nzm v[48:51],a[208:211],a[144:147],v[48:51]\nzm v[52:55],a[208:211],a[176:179],v[52:55]\n"
"zm v[48:51],a[212:215],a[148:151],v[48:51]\nzm v[52:55],a[212:215],a[180:183],v[52:55]\nzm v[48:51],a[216:219],a[152:155],v[48:51]\nzm v[52:55],a[216:219],a[184:187],v[52:55]\nzm v[48:51],a[220:223],a[156:159],v[48:51]\nzm v[52:55],a[220:223],a[188:191],v[52:55]\nzu v32,v32,v12\nzu v33,v33,v12\n"
"zu v34,v34,v12\nzu v35,v35,v12\nzu v40,v40,v12\nzu v41,v41,v12\nzu v42,v42,v12\nzu v43,v43,v12\nzu v36,v36,v13\nzu v37,v37,v13\n"
"zu v38,v38,v13\nzu v39,v39,v13\nzu v44,v44,v13\nzu v45,v45,v13\nzu v46,v46,v13\nzu v47,v47,v13\nze v80,v32\nze v81,v33\n"
"ze v82,v34\nze v83,v35\nze v84,v36\nze v85,v37\nze v86,v38\nze v87,v39\ns_nop 1\nza v104,v80,v81\n"
"za v105,v82,v83\nzc v64,v80,v81\nzc v65,v82,v83\nza v108,v104,v105\nza v106,v84,v85\nza v107,v86,v87\nzc v68,v84,v85\nzc v69,v86,v87\n"
"za v109,v106,v107\nze v88,v40\nze v89,v41\nze v90,v42\nze v91,v43\nze v92,v44\nze v93,v45\nze v94,v46\n"
"ze v95,v47\ns_nop 1\nza v104,v88,v89\nza v105,v90,v91\nzc v66,v88,v89\nzc v67,v90,v91\nza v110,v104,v105\nza v106,v92,v93\n"
"za v107,v94,v95\nzc v70,v92,v93\nzc v71,v94,v95\nza v111,v106,v107\nza v108,v108,v110\nza v109,v109,v111\ns_sub_u32 s82,s82,32\ns_cmp_eq_u32 s8,1\n"
"s_cbranch_scc0 L_A_nopend\nzv vmcnt(0)\nv_mov_b32 v15,0\nv_mov_b32 v112,s34\nglobal_store_dword v15,v112,s[6:7] sc1\ns_mov_b32 s8,0\nL_A_nopend:\ns_lshl_b32 s89,s76,6\n"
"s_sub_u32 s82,s82,s89\ns_cmp_eq_u32 s76,0\ns_cbranch_scc1 L_A_loopM_entry\ns_and_b32 s9,s76,1\nzp s76,s76,1\ns_lshr_b32 s76,s76,1\ns_cmp_eq_u32 s9,1\ns_cbranch_scc1 L_A_loopU2\n"
".p2align 6\nL_A_loopU:\nzv vmcnt(8)\ns_barrier\nzm v[56:59],a[224:227],a[128:131],v[96:99]\nzd a[192:195],v9 offset:0\nzm v[60:63],a[224:227],a[160:163],v[100:103]\ns_mov_b32 m0,s81\n"
"zm v[56:59],a[228:231],a[132:135],v[56:59]\nzd a[196:199],v9 offset:1024\nzm v[60:63],a[228:231],a[164:167],v[60:63]\nzl v4,s[36:39],s78 offen offset:0 sc1 lds\nzm v[56:59],a[232:235],a[136:139],v[56:59]\nzd a[200:203],v9 offset:2048\nzm v[60:63],a[232:235],a[168:171],v[60:63]\nzm v[56:59],a[236:239],a[140:143],v[56:59]\n"
"zd a[204:207],v9 offset:3072\nzm v[60:63],a[236:239],a[172:175],v[60:63]\nzv lgkmcnt(4)\nzm v[56:59],a[240:243],a[144:147],v[56:59]\nzd a[208:211],v9 offset:4096\nza v10,v10,v108\nzm v[60:63],a[240:243],a[176:179],v[60:63]\nze v80,v48\n"
"zm v[56:59],a[244:247],a[148:151],v[56:59]\nzd a[212:215],v9 offset:5120\nza v11,v11,v109\nzm v[60:63],a[244:247],a[180:183],v[60:63]\nzl v4,s[36:39],s78 offen offset:1024 sc1 lds\nzm v[56:59],a[248:251],a[152:155],v[56:59]\nzd a[216:219],v9 offset:6144\nzm v[60:63],a[248:251],a[184:187],v[60:63]\n"
"ze v81,v49\nzm v[56:59],a[252:255],a[156:159],v[56:59]\nzd a[220:223],v9 offset:7168\nza v104,v80,v81\nzm v[60:63],a[252:255],a[188:191],v[60:63]\nzv lgkmcnt(4)\nze v82,v50\nzm a[0:3],a[192:195],v[64:67],a[0:3]\n"
"zd a[224:227],v9 offset:8192\nzc v72,v80,v81\nzm a[4:7],a[192:195],v[68:71],a[4:7]\nze v83,v51\nzm a[8:11],a[196:199],v[64:67],a[8:11]\nzd a[228:231],v9 offset:9216\nza v105,v82,v83\nzm a[12:15],a[196:199],v[68:71],a[12:15]\n"
"zl v4,s[36:39],s78 offen offset:2048 sc1 lds\nzm a[16:19],a[200:203],v[64:67],a[16:19]\nzd a[232:235],v9 offset:10240\nzc v73,v82,v83\nzm a[20:23],a[200:203],v[68:71],a[20:23]\nze v84,v52\nzm a[24:27],a[204:207],v[64:67],a[24:27]\nzd a[236:239],v9 offset:11264\n"
"za v108,v104,v105\nzm a[28:31],a[204:207],v[68:71],a[28:31]\nzv lgkmcnt(4)\nze v85,v53\nzm a[32:35],a[208:211],v[64:67],a[32:35]\nzd a[240:243],v9 offset:12288\nza v106,v84,v85\nzm a[36:39],a[208:211],v[68:71],a[36:39]\n"
"ze v86,v54\nzm a[40:43],a[212:215],v[64:67],a[40:43]\nzd a[244:247],v9 offset:13312\nzc v76,v84,v85\nzm a[44:47],a[212:215],v[68:71],a[44:47]\nzl v4,s[36:39],s78 offen offset:3072 sc1 lds\nzm a[48:51],a[216:219],v[64:67],a[48:51]\nzd a[248:251],v9 offset:14336\n"
"zm a[52:55],a[216:219],v[68:71],a[52:55]\nzp s78,s78,16384\nze v87,v55\nzm a[56:59],a[220:223],v[64:67],a[56:59]\nzd a[252:255],v9 offset:15360\nza v107,v86,v87\nzm a[60:63],a[220:223],v[68:71],a[60:63]\nzv lgkmcnt(4)\n"
"zc v77,v86,v87\nza v109,v106,v107\nzm a[64:67],a[224:227],v[64:67],a[64:67]\nzd a[192:195],v8 offset:0\nzm a[68:71],a[224:227],v[68:71],a[68:71]\ns_mov_b32 m0,s80\nze v88,v56\nzm a[72:75],a[228:231],v[64:67],a[72:75]\n"
"zd a[196:199],v8 offset:1024\nzm a[76:79],a[228:231],v[68:71],a[76:79]\nzl v4,s[28:31],s79 offen offset:0 sc1 lds\nzm a[80:83],a[232:235],v[64:67],a[80:83]\nzd a[200:203],v8 offset:2048\nzm a[84:87],a[232:235],v[68:71],a[84:87]\nze v89,v57\nzm a[88:91],a[236:239],v[64:67],a[88:91]\n"
"zd a[204:207],v8 offset:3072\nza v104,v88,v89\nzm a[92:95],a[236:239],v[68:71],a[92:95]\nzv lgkmcnt(4)\nze v90,v58\nzm a[96:99],a[240:243],v[64:67],a[96:99]\nzd a[208:211],v8 offset:4096\nzc v74,v88,v89\n"
"zm a[100:103],a[240:243],v[68:71],a[100:103]\nze v91,v59\nzm a[104:107],a[244:247],v[64:67],a[104:107]\nzd a[212:215],v8 offset:5120\nza v105,v90,v91\nzm a[108:111],a[244:247],v[68:71],a[108:111]\nzl v4,s[28:31],s79 offen offset:1024 sc1 lds\nzm a[112:115],a[248:251],v[64:67],a[112:115]\n"
"zd a[216:219],v8 offset:6144\nzc v75,v90,v91\nzm a[116:119],a[248:251],v[68:71],a[116:119]\nze v92,v60\nzm a[120:123],a[252:255],v[64:67],a[120:123]\nzd a[220:223],v8 offset:7168\nza v110,v104,v105\nzm a[124:127],a[252:255],v[68:71],a[124:127]\n"
"zv lgkmcnt(4)\nze v93,v61\nzm v[32:35],a[192:195],a[128:131],v[96:99]\nzd a[224:227],v8 offset:8192\nza v106,v92,v93\nzm v[36:39],a[192:195],a[160:163],v[100:103]\nze v94,v62\nzm v[32:35],a[196:199],a[132:135],v[32:35]\n"
"zd a[228:231],v8 offset:9216\nzc v78,v92,v93\nzm v[36:39],a[196:199],a[164:167],v[36:39]\nzl v4,s[28:31],s79 offen offset:2048 sc1 lds\nzm v[32:35],a[200:203],a[136:139],v[32:35]\nzd a[232:235],v8 offset:10240\nza v108,v108,v110\nzm v[36:39],a[200:203],a[168:171],v[36:39]\n"
"ze v95,v63\nzm v[32:35],a[204:207],a[140:143],v[32:35]\nzd a[236:239],v8 offset:11264\nza v107,v94,v95\nzm v[36:39],a[204:207],a[172:175],v[36:39]\nzv lgkmcnt(4)\nzc v79,v94,v95\nza v111,v106,v107\n"
"zm v[32:35],a[208:211],a[144:147],v[32:35]\nzd a[240:243],v8 offset:12288\nza v109,v109,v111\nzm v[36:39],a[208:211],a[176:179],v[36:39]\nv_max_f32 v104,v108,v109\nzm v[32:35],a[212:215],a[148:151],v[32:35]\nzd a[244:247],v8 offset:13312\nv_cmp_lt_f32 vcc,s83,v104\n"
"zm v[36:39],a[212:215],a[180:183],v[36:39]\nzl v4,s[28:31],s79 offen offset:3072 sc1 lds\nzm v[32:35],a[216:219],a[152:155],v[32:35]\nzd a[248:251],v8 offset:14336\nzm v[36:39],a[216:219],a[184:187],v[36:39]\nzp s79,s79,16384\nzm v[32:35],a[220:223],a[156:159],v[32:35]\nzd a[252:255],v8 offset:15360\n"
"zm v[36:39],a[220:223],a[188:191],v[36:39]\nzv lgkmcnt(4)\ns_cbranch_vccnz L_A_rare_U0\nL_A_back_U0:\nzv vmcnt(8)\ns_barrier\nzm v[40:43],a[224:227],a[128:131],v[96:99]\nzd a[192:195],v9 offset:16384\n"
"zm v[44:47],a[224:227],a[160:163],v[100:103]\nzp m0,s81,16384\nzm v[40:43],a[228:231],a[132:135],v[40:43]\nzd a[196:199],v9 offset:17408\nzm v[44:47],a[228:231],a[164:167],v[44:47]\nzl v4,s[36:39],s78 offen offset:0 sc1 lds\nzm v[40:43],a[232:235],a[136:139],v[40:43]\nzd a[200:203],v9 offset:18432\n"
"zm v[44:47],a[232:235],a[168:171],v[44:47]\ns_xor_b32 s81,s81,0x8000\nzm v[40:43],a[236:239],a[140:143],v[40:43]\nzd a[204:207],v9 offset:19456\nzm v[44:47],a[236:239],a[172:175],v[44:47]\nzv lgkmcnt(4)\nzm v[40:43],a[240:243],a[144:147],v[40:43]\nzd a[208:211],v9 offset:20480\n"
"za v10,v10,v108\nzm v[44:47],a[240:243],a[176:179],v[44:47]\nze v80,v32\nzm v[40:43],a[244:247],a[148:151],v[40:43]\nzd a[212:215],v9 offset:21504\nza v11,v11,v109\nzm v[44:47],a[244:247],a[180:183],v[44:47]\nzl v4,s[36:39],s78 offen offset:1024 sc1 lds\n"
"zm v[40:43],a[248:251],a[152:155],v[40:43]\nzd a[216:219],v9 offset:22528\nzm v[44:47],a[248:251],a[184:187],v[44:47]\nze v81,v33\nzm v[40:43],a[252:255],a[156:159],v[40:43]\nzd a[220:223],v9 offset:23552\nza v104,v80,v81\nzm v[44:47],a[252:255],a[188:191],v[44:47]\n"
"zv lgkmcnt(4)\nze v82,v34\nzm a[0:3],a[192:195],v[72:75],a[0:3]\nzd a[224:227],v9 offset:24576\nzc v64,v80,v81\nzm a[4:7],a[192:195],v[76:79],a[4:7]\nze v83,v35\nzm a[8:11],a[196:199],v[72:75],a[8:11]\n"
"zd a[228:231],v9 offset:25600\nza v105,v82,v83\nzm a[12:15],a[196:199],v[76:79],a[12:15]\nzl v4,s[36:39],s78 offen offset:2048 sc1 lds\nzm a[16:19],a[200:203],v[72:75],a[16:19]\nzd a[232:235],v9 offset:26624\nzc v65,v82,v83\nzm a[20:23],a[200:203],v[76:79],a[20:23]\n"
"ze v84,v36\nzm a[24:27],a[204:207],v[72:75],a[24:27]\nzd a[236:239],v9 offset:27648\nza v108,v104,v105\nzm a[28:31],a[204:207],v[76:79],a[28:31]\nzv lgkmcnt(4)\nze v85,v37\nzm a[32:35],a[208:211],v[72:75],a[32:35]\n"
"zd a[240:243],v9 offset:28672\nza v106,v84,v85\nzm a[36:39],a[208:211],v[76:79],a[36:39]\nze v86,v38\nzm a[40:43],a[212:215],v[72:75],a[40:43]\nzd a[244:247],v9 offset:29696\nzc v68,v84,v85\nzm a[44:47],a[212:215],v[76:79],a[44:47]\n"
"zl v4,s[36:39],s78 offen offset:3072 sc1 lds\nzm a[48:51],a[216:219],v[72:75],a[48:51]\nzd a[248:251],v9 offset:30720\nzm a[52:55],a[216:219],v[76:79],a[52:55]\nzp s78,s78,16384\nze v87,v39\nzm a[56:59],a[220:223],v[72:75],a[56:59]\nzd a[252:255],v9 offset:31744\n"
"za v107,v86,v87\nzm a[60:63],a[220:223],v[76:79],a[60:63]\nzv lgkmcnt(4)\nzo v9,0x8000,v9\nzc v69,v86,v87\nzm a[64:67],a[224:227],v[72:75],a[64:67]\nzd a[192:195],v8 offset:16384\nza v109,v106,v107\n"
"zm a[68:71],a[224:227],v[76:79],a[68:71]\nzp m0,s80,16384\nze v88,v40\nzm a[72:75],a[228:231],v[72:75],a[72:75]\nzd a[196:199],v8 offset:17408\nzm a[76:79],a[228:231],v[76:79],a[76:79]\nzl v4,s[28:31],s79 offen offset:0 sc1 lds\nzm a[80:83],a[232:235],v[72:75],a[80:83]\n"
"zd a[200:203],v8 offset:18432\nzm a[84:87],a[232:235],v[76:79],a[84:87]\ns_xor_b32 s80,s80,0x8000\nze v89,v41\nzm a[88:91],a[236:239],v[72:75],a[88:91]\nzd a[204:207],v8 offset:19456\nza v104,v88,v89\nzm a[92:95],a[236:239],v[76:79],a[92:95]\n"
"zv lgkmcnt(4)\nze v90,v42\nzm a[96:99],a[240:243],v[72:75],a[96:99]\nzd a[208:211],v8 offset:20480\nzc v66,v88,v89\nzm a[100:103],a[240:243],v[76:79],a[100:103]\nze v91,v43\nzm a[104:107],a[244:247],v[72:75],a[104:107]\n"
"zd a[212:215],v8 offset:21504\nza v105,v90,v91\nzm a[108:111],a[244:247],v[76:79],a[108:111]\nzl v4,s[28:31],s79 offen offset:1024 sc1 lds\nzm a[112:115],a[248:251],v[72:75],a[112:115]\nzd a[216:219],v8 offset:22528\nzc v67,v90,v91\nzm a[116:119],a[248:251],v[76:79],a[116:119]\n"
"ze v92,v44\nzm a[120:123],a[252:255],v[72:75],a[120:123]\nzd a[220:223],v8 offset:23552\nza v110,v104,v105\nzm a[124:127],a[252:255],v[76:79],a[124:127]\nzv lgkmcnt(4)\nze v93,v45\nzm v[48:51],a[192:195],a[128:131],v[96:99]\n"
"zd a[224:227],v8 offset:24576\nza v106,v92,v93\nzm v[52:55],a[192:195],a[160:163],v[100:103]\nze v94,v46\nzm v[48:51],a[196:199],a[132:135],v[48:51]\nzd a[228:231],v8 offset:25600\nzc v70,v92,v93\nzm v[52:55],a[196:199],a[164:167],v[52:55]\n"
"zl v4,s[28:31],s79 offen offset:2048 sc1 lds\nzm v[48:51],a[200:203],a[136:139],v[48:51]\nzd a[232:235],v8 offset:26624\nza v108,v108,v110\nzm v[52:55],a[200:203],a[168:171],v[52:55]\nze v95,v47\nzm v[48:51],a[204:207],a[140:143],v[48:51]\nzd a[236:239],v8 offset:27648\n"
"za v107,v94,v95\nzm v[52:55],a[204:207],a[172:175],v[52:55]\nzv lgkmcnt(4)\nzc v71,v94,v95\nza v111,v106,v107\nzm v[48:51],a[208:211],a[144:147],v[48:51]\nzd a[240:243],v8 offset:28672\nza v109,v109,v111\n"
"zm v[52:55],a[208:211],a[176:179],v[52:55]\nv_max_f32 v104,v108,v109\nzm v[48:51],a[212:215],a[148:151],v[48:51]\nzd a[244:247],v8 offset:29696\nv_cmp_lt_f32 vcc,s83,v104\nzm v[52:55],a[212:215],a[180:183],v[52:55]\nzl v4,s[28:31],s79 offen offset:3072 sc1 lds\nzm v[48:51],a[216:219],a[152:155],v[48:51]\n"
"zd a[248:251],v8 offset:30720\nzm v[52:55],a[216:219],a[184:187],v[52:55]\nzp s79,s79,16384\nzm v[48:51],a[220:223],a[156:159],v[48:51]\nzd a[252:255],v8 offset:31744\nzm v[52:55],a[220:223],a[188:191],v[52:55]\nzv lgkmcnt(4)\nzo v8,0x8000,v8\n"
"s_cbranch_vccnz L_A_rare_U1\nL_A_back_U1:\nL_A_loopU2:\nzv vmcnt(8)\ns_barrier\nzm v[56:59],a[224:227],a[128:131],v[96:99]\nzd a[192:195],v9 offset:0\nzm v[60:63],a[224:227],a[160:163],v[100:103]\n"
"s_mov_b32 m0,s81\nzm v[56:59],a[228:231],a[132:135],v[56:59]\nzd a[196:199],v9 offset:1024\nzm v[60:63],a[228:231],a[164:167],v[60:63]\nzl v4,s[36:39],s78 offen offset:0 sc1 lds\nzm v[56:59],a[232:235],a[136:139],v[56:59]\nzd a[200:203],v9 offset:2048\nzm v[60:63],a[232:235],a[168:171],v[60:63]\n"
"zm v[56:59],a[236:239],a[140:143],v[56:59]\nzd a[204:207],v9 offset:3072\nzm v[60:63],a[236:239],a[172:175],v[60:63]\nzv lgkmcnt(4)\nzm v[56:59],a[240:243],a[144:147],v[56:59]\nzd a[208:211],v9 offset:4096\nza v10,v10,v108\nzm v[60:63],a[240:243],a[176:179],v[60:63]\n"
"ze v80,v48\nzm v[56:59],a[244:247],a[148:151],v[56:59]\nzd a[212:215],v9 offset:5120\nza v11,v11,v109\nzm v[60:63],a[244:247],a[180:183],v[60:63]\nzl v4,s[36:39],s78 offen offset:1024 sc1 lds\nzm v[56:59],a[248:251],a[152:155],v[56:59]\nzd a[216:219],v9 offset:6144\n"
"zm v[60:63],a[248:251],a[184:187],v[60:63]\nze v81,v49\nzm v[56:59],a[252:255],a[156:159],v[56:59]\nzd a[220:223],v9 offset:7168\nza v104,v80,v81\nzm v[60:63],a[252:255],a[188:191],v[60:63]\nzv lgkmcnt(4)\nze v82,v50\n"
"zm a[0:3],a[192:195],v[64:67],a[0:3]\nzd a[224:227],v9 offset:8192\nzc v72,v80,v81\nzm a[4:7],a[192:195],v[68:71],a[4:7]\nze v83,v51\nzm a[8:11],a[196:199],v[64:67],a[8:11]\nzd a[228:231],v9 offset:9216\nza v105,v82,v83\n"
"zm a[12:15],a[196:199],v[68:71],a[12:15]\nzl v4,s[36:39],s78 offen offset:2048 sc1 lds\nzm a[16:19],a[200:203],v[64:67],a[16:19]\nzd a[232:235],v9 offset:10240\nzc v73,v82,v83\nzm a[20:23],a[200:203],v[68:71],a[20:23]\nze v84,v52\nzm a[24:27],a[204:207],v[64:67],a[24:27]\n"
"zd a[236:239],v9 offset:11264\nza v108,v104,v105\nzm a[28:31],a[204:207],v[68:71],a[28:31]\nzv lgkmcnt(4)\nze v85,v53\nzm a[32:35],a[208:211],v[64:67],a[32:35]\nzd a[240:243],v9 offset:12288\nza v106,v84,v85\n"
"zm a[36:39],a[208:211],v[68:71],a[36:39]\nze v86,v54\nzm a[40:43],a[212:215],v[64:67],a[40:43]\nzd a[244:247],v9 offset:13312\nzc v76,v84,v85\nzm a[44:47],a[212:215],v[68:71],a[44:47]\nzl v4,s[36:39],s78 offen offset:3072 sc1 lds\nzm a[48:51],a[216:219],v[64:67],a[48:51]\n"
"zd a[248:251],v9 offset:14336\nzm a[52:55],a[216:219],v[68:71],a[52:55]\nzp s78,s78,16384\nze v87,v55\nzm a[56:59],a[220:223],v[64:67],a[56:59]\nzd a[252:255],v9 offset:15360\nza v107,v86,v87\nzm a[60:63],a[220:223],v[68:71],a[60:63]\n"
"zv lgkmcnt(4)\nzc v77,v86,v87\nza v109,v106,v107\nzm a[64:67],a[224:227],v[64:67],a[64:67]\nzd a[192:195],v8 offset:0\nzm a[68:71],a[224:227],v[68:71],a[68:71]\ns_mov_b32 m0,s80\nze v88,v56\n"
"zm a[72:75],a[228:231],v[64:67],a[72:75]\nzd a[196:199],v8 offset:1024\nzm a[76:79],a[228:231],v[68:71],a[76:79]\nzl v4,s[28:31],s79 offen offset:0 sc1 lds\nzm a[80:83],a[232:235],v[64:67],a[80:83]\nzd a[200:203],v8 offset:2048\nzm a[84:87],a[232:235],v[68:71],a[84:87]\nze v89,v57\n"
"zm a[88:91],a[236:239],v[64:67],a[88:91]\nzd a[204:207],v8 offset:3072\nza v104,v88,v89\nzm a[92:95],a[236:239],v[68:71],a[92:95]\nzv lgkmcnt(4)\nze v90,v58\nzm a[96:99],a[240:243],v[64:67],a[96:99]\nzd a[208:211],v8 offset:4096\n"
"zc v74,v88,v89\nzm a[100:103],a[240:243],v[68:71],a[100:103]\nze v91,v59\nzm a[104:107],a[244:247],v[64:67],a[104:107]\nzd a[212:215],v8 offset:5120\nza v105,v90,v91\nzm a[108:111],a[244:247],v[68:71],a[108:111]\nzl v4,s[28:31],s79 offen offset:1024 sc1 lds\n"
"zm a[112:115],a[248:251],v[64:67],a[112:115]\nzd a[216:219],v8 offset:6144\nzc v75,v90,v91\nzm a[116:119],a[248:251],v[68:71],a[116:119]\nze v92,v60\nzm a[120:123],a[252:255],v[64:67],a[120:123]\nzd a[220:223],v8 offset:7168\nza v110,v104,v105\n"
"zm a[124:127],a[252:255],v[68:71],a[124:127]\nzv lgkmcnt(4)\nze v93,v61\nzm v[32:35],a[192:195],a[128:131],v[96:99]\nzd a[224:227],v8 offset:8192\nza v106,v92,v93\nzm v[36:39],a[192:195],a[160:163],v[100:103]\nze v94,v62\n"
"zm v[32:35],a[196:199],a[132:135],v[32:35]\nzd a[228:231],v8 offset:9216\nzc v78,v92,v93\nzm v[36:39],a[196:199],a[164:167],v[36:39]\nzl v4,s[28:31],s79 offen offset:2048 sc1 lds\nzm v[32:35],a[200:203],a[136:139],v[32:35]\nzd a[232:235],v8 offset:10240\nza v108,v108,v110\n"
"zm v[36:39],a[200:203],a[168:171],v[36:39]\nze v95,v63\nzm v[32:35],a[204:207],a[140:143],v[32:35]\nzd a[236:239],v8 offset:11264\nza v107,v94,v95\nzm v[36:39],a[204:207],a[172:175],v[36:39]\nzv lgkmcnt(4)\nzc v79,v94,v95\n"
"za v111,v106,v107\nzm v[32:35],a[208:211],a[144:147],v[32:35]\nzd a[240:243],v8 offset:12288\nza v109,v109,v111\nzm v[36:39],a[208:211],a[176:179],v[36:39]\nv_max_f32 v104,v108,v109\nzm v[32:35],a[212:215],a[148:151],v[32:35]\nzd a[244:247],v8 offset:13312\n"
"v_cmp_lt_f32 vcc,s83,v104\nzm v[36:39],a[212:215],a[180:183],v[36:39]\nzl v4,s[28:31],s79 offen offset:3072 sc1 lds\nzm v[32:35],a[216:219],a[152:155],v[32:35]\nzd a[248:251],v8 offset:14336\nzm v[36:39],a[216:219],a[184:187],v[36:39]\nzp s79,s79,16384\nzm v[32:35],a[220:223],a[156:159],v[32:35]\n"
"zd a[252:255],v8 offset:15360\nzm v[36:39],a[220:223],a[188:191],v[36:39]\nzv lgkmcnt(4)\ns_cbranch_vccnz L_A_rare_U2\nL_A_back_U2:\nzv vmcnt(8)\ns_barrier\nzm v[40:43],a[224:227],a[128:131],v[96:99]\n"
"zd a[192:195],v9 offset:16384\nzm v[44:47],a[224:227],a[160:163],v[100:103]\nzp m0,s81,16384\nzm v[40:43],a[228:231],a[132:135],v[40:43]\nzd a[196:199],v9 offset:17408\nzm v[44:47],a[228:231],a[164:167],v[44:47]\nzl v4,s[36:39],s78 offen offset:0 sc1 lds\nzm v[40:43],a[232:235],a[136:139],v[40:43]\n"
"zd a[200:203],v9 offset:18432\nzm v[44:47],a[232:235],a[168:171],v[44:47]\ns_xor_b32 s81,s81,0x8000\nzm v[40:43],a[236:239],a[140:143],v[40:43]\nzd a[204:207],v9 offset:19456\nzm v[44:47],a[236:239],a[172:175],v[44:47]\nzv lgkmcnt(4)\nzm v[40:43],a[240:243],a[144:147],v[40:43]\n"
"zd a[208:211],v9 offset:20480\nza v10,v10,v108\nzm v[44:47],a[240:243],a[176:179],v[44:47]\nze v80,v32\nzm v[40:43],a[244:247],a[148:151],v[40:43]\nzd a[212:215],v9 offset:21504\nza v11,v11,v109\nzm v[44:47],a[244:247],a[180:183],v[44:47]\n"
"zl v4,s[36:39],s78 offen offset:1024 sc1 lds\nzm v[40:43],a[248:251],a[152:155],v[40:43]\nzd a[216:219],v9 offset:22528\nzm v[44:47],a[248:251],a[184:187],v[44:47]\nze v81,v33\nzm v[40:43],a[252:255],a[156:159],v[40:43]\nzd a[220:223],v9 offset:23552\nza v104,v80,v81\n"
"zm v[44:47],a[252:255],a[188:191],v[44:47]\nzv lgkmcnt(4)\nze v82,v34\nzm a[0:3],a[192:195],v[72:75],a[0:3]\nzd a[224:227],v9 offset:24576\nzc v64,v80,v81\nzm a[4:7],a[192:195],v[76:79],a[4:7]\nze v83,v35\n"
"zm a[8:11],a[196:199],v[72:75],a[8:11]\nzd a[228:231],v9 offset:25600\nza v105,v82,v83\nzm a[12:15],a[196:199],v[76:79],a[12:15]\nzl v4,s[36:39],s78 offen offset:2048 sc1 lds\nzm a[16:19],a[200:203],v[72:75],a[16:19]\nzd a[232:235],v9 offset:26624\nzc v65,v82,v83\n"
"zm a[20:23],a[200:203],v[76:79],a[20:23]\nze v84,v36\nzm a[24:27],a[204:207],v[72:75],a[24:27]\nzd a[236:239],v9 offset:27648\nza v108,v104,v105\nzm a[28:31],a[204:207],v[76:79],a[28:31]\nzv lgkmcnt(4)\nze v85,v37\n"
"zm a[32:35],a[208:211],v[72:75],a[32:35]\nzd a[240:243],v9 offset:28672\nza v106,v84,v85\nzm a[36:39],a[208:211],v[76:79],a[36:39]\nze v86,v38\nzm a[40:43],a[212:215],v[72:75],a[40:43]\nzd a[244:247],v9 offset:29696\nzc v68,v84,v85\n"
"zm a[44:47],a[212:215],v[76:79],a[44:47]\nzl v4,s[36:39],s78 offen offset:3072 sc1 lds\nzm a[48:51],a[216:219],v[72:75],a[48:51]\nzd a[248:251],v9 offset:30720\nzm a[52:55],a[216:219],v[76:79],a[52:55]\nzp s78,s78,16384\nze v87,v39\nzm a[56:59],a[220:223],v[72:75],a[56:59]\n"
"zd a[252:255],v9 offset:31744\nza v107,v86,v87\nzm a[60:63],a[220:223],v[76:79],a[60:63]\nzv lgkmcnt(4)\nzo v9,0x8000,v9\nzc v69,v86,v87\nzm a[64:67],a[224:227],v[72:75],a[64:67]\nzd a[192:195],v8 offset:16384\n"
"za v109,v106,v107\nzm a[68:71],a[224:227],v[76:79],a[68:71]\nzp m0,s80,16384\nze v88,v40\nzm a[72:75],a[228:231],v[72:75],a[72:75]\nzd a[196:199],v8 offset:17408\nzm a[76:79],a[228:231],v[76:79],a[76:79]\nzl v4,s[28:31],s79 offen offset:0 sc1 lds\n"
"zm a[80:83],a[232:235],v[72:75],a[80:83]\nzd a[200:203],v8 offset:18432\nzm a[84:87],a[232:235],v[76:79],a[84:87]\ns_xor_b32 s80,s80,0x8000\nze v89,v41\nzm a[88:91],a[236:239],v[72:75],a[88:91]\nzd a[204:207],v8 offset:19456\nza v104,v88,v89\n"
"zm a[92:95],a[236:239],v[76:79],a[92:95]\nzv lgkmcnt(4)\nze v90,v42\nzm a[96:99],a[240:243],v[72:75],a[96:99]\nzd a[208:211],v8 offset:20480\nzc v66,v88,v89\nzm a[100:103],a[240:243],v[76:79],a[100:103]\nze v91,v43\n"
"zm a[104:107],a[244:247],v[72:75],a[104:107]\nzd a[212:215],v8 offset:21504\nza v105,v90,v91\nzm a[108:111],a[244:247],v[76:79],a[108:111]\nzl v4,s[28:31],s79 offen offset:1024 sc1 lds\nzm a[112:115],a[248:251],v[72:75],a[112:115]\nzd a[216:219],v8 offset:22528\nzc v67,v90,v91\n"
"zm a[116:119],a[248:251],v[76:79],a[116:119]\nze v92,v44\nzm a[120:123],a[252:255],v[72:75],a[120:123]\nzd a[220:223],v8 offset:23552\nza v110,v104,v105\nzm a[124:127],a[252:255],v[76:79],a[124:127]\nzv lgkmcnt(4)\nze v93,v45\n"
"zm v[48:51],a[192:195],a[128:131],v[96:99]\nzd a[224:227],v8 offset:24576\nza v106,v92,v93\nzm v[52:55],a[192:195],a[160:163],v[100:103]\nze v94,v46\nzm v[48:51],a[196:199],a[132:135],v[48:51]\nzd a[228:231],v8 offset:25600\nzc v70,v92,v93\n"
"zm v[52:55],a[196:199],a[164:167],v[52:55]\nzl v4,s[28:31],s79 offen offset:2048 sc1 lds\nzm v[48:51],a[200:203],a[136:139],v[48:51]\nzd a[232:235],v8 offset:26624\nza v108,v108,v110\nzm v[52:55],a[200:203],a[168:171],v[52:55]\nze v95,v47\nzm v[48:51],a[204:207],a[140:143],v[48:51]\n"
"zd a[236:239],v8 offset:27648\nza v107,v94,v95\nzm v[52:55],a[204:207],a[172:175],v[52:55]\nzv lgkmcnt(4)\nzc v71,v94,v95\nza v111,v106,v107\nzm v[48:51],a[208:211],a[144:147],v[48:51]\nzd a[240:243],v8 offset:28672\n"
"za v109,v109,v111\nzm v[52:55],a[208:211],a[176:179],v[52:55]\nv_max_f32 v104,v108,v109\nzm v[48:51],a[212:215],a[148:151],v[48:51]\nzd a[244:247],v8 offset:29696\nv_cmp_lt_f32 vcc,s83,v104\nzm v[52:55],a[212:215],a[180:183],v[52:55]\nzl v4,s[28:31],s79 offen offset:3072 sc1 lds\n"
"zm v[48:51],a[216:219],a[152:155],v[48:51]\nzd a[248:251],v8 offset:30720\nzm v[52:55],a[216:219],a[184:187],v[52:55]\nzp s79,s79,16384\nzm v[48:51],a[220:223],a[156:159],v[48:51]\nzd a[252:255],v8 offset:31744\nzm v[52:55],a[220:223],a[188:191],v[52:55]\nzv lgkmcnt(4)\n"
"zo v8,0x8000,v8\ns_cbranch_vccnz L_A_rare_U3\nL_A_back_U3:\ns_sub_u32 s76,s76,1\ns_cmp_lg_u32 s76,0\ns_cbranch_scc1 L_A_loopU\nL_A_loopM_entry:\ns_cmp_eq_u32 s77,0\n"
"s_cbranch_scc1 L_A_last\nL_A_loopM:\nzv vmcnt(8)\ns_barrier\nzm v[56:59],a[224:227],a[128:131],v[96:99]\nzd a[192:195],v9 offset:0\nzm v[60:63],a[224:227],a[160:163],v[100:103]\ns_mov_b32 m0,s81\n"
"zm v[56:59],a[228:231],a[132:135],v[56:59]\nzd a[196:199],v9 offset:1024\nzm v[60:63],a[228:231],a[164:167],v[60:63]\nzl v4,s[36:39],s78 offen offset:0 sc1 lds\nzm v[56:59],a[232:235],a[136:139],v[56:59]\nzd a[200:203],v9 offset:2048\nzm v[60:63],a[232:235],a[168:171],v[60:63]\nzm v[56:59],a[236:239],a[140:143],v[56:59]\n"
"zd a[204:207],v9 offset:3072\nzm v[60:63],a[236:239],a[172:175],v[60:63]\nzv lgkmcnt(4)\nzm v[56:59],a[240:243],a[144:147],v[56:59]\nzd a[208:211],v9 offset:4096\nza v10,v10,v108\nv_cmp_ge_i32 s[56:57],s82,v20\nzm v[60:63],a[240:243],a[176:179],v[60:63]\n"
"v_cmp_ge_i32 s[58:59],s82,v21\nv_cmp_ge_i32 s[60:61],s82,v22\nv_cmp_ge_i32 s[62:63],s82,v23\nzm v[56:59],a[244:247],a[148:151],v[56:59]\nzd a[212:215],v9 offset:5120\nza v11,v11,v109\nv_cmp_ge_i32 s[48:49],s82,v16\nzm v[60:63],a[244:247],a[180:183],v[60:63]\n"
"zl v4,s[36:39],s78 offen offset:1024 sc1 lds\nv_cmp_ge_i32 s[50:51],s82,v17\nzm v[56:59],a[248:251],a[152:155],v[56:59]\nzd a[216:219],v9 offset:6144\nv_cmp_ge_i32 s[52:53],s82,v18\nv_cmp_ge_i32 s[54:55],s82,v19\nzm v[60:63],a[248:251],a[184:187],v[60:63]\nzk v48,v14,v48,s[56:57]\n"
"zk v49,v14,v49,s[58:59]\nzm v[56:59],a[252:255],a[156:159],v[56:59]\nzd a[220:223],v9 offset:7168\nzk v50,v14,v50,s[60:61]\nzm v[60:63],a[252:255],a[188:191],v[60:63]\nzv lgkmcnt(4)\nzk v51,v14,v51,s[62:63]\nzm a[0:3],a[192:195],v[64:67],a[0:3]\n"
"zd a[224:227],v9 offset:8192\nzk v52,v14,v52,s[48:49]\nzk v53,v14,v53,s[50:51]\nzm a[4:7],a[192:195],v[68:71],a[4:7]\nzk v54,v14,v54,s[52:53]\nzm a[8:11],a[196:199],v[64:67],a[8:11]\nzd a[228:231],v9 offset:9216\nzk v55,v14,v55,s[54:55]\n"
"zm a[12:15],a[196:199],v[68:71],a[12:15]\nzl v4,s[36:39],s78 offen offset:2048 sc1 lds\nv_cmp_ge_i32 s[64:65],s82,v24\nzm a[16:19],a[200:203],v[64:67],a[16:19]\nzd a[232:235],v9 offset:10240\nze v80,v48\nzm a[20:23],a[200:203],v[68:71],a[20:23]\nze v81,v49\n"
"v_cmp_ge_i32 s[66:67],s82,v25\nzm a[24:27],a[204:207],v[64:67],a[24:27]\nzd a[236:239],v9 offset:11264\nze v82,v50\nzm a[28:31],a[204:207],v[68:71],a[28:31]\nzv lgkmcnt(4)\nze v83,v51\nzm a[32:35],a[208:211],v[64:67],a[32:35]\n"
"zd a[240:243],v9 offset:12288\nza v104,v80,v81\nzc v72,v80,v81\nzm a[36:39],a[208:211],v[68:71],a[36:39]\nze v84,v52\nzm a[40:43],a[212:215],v[64:67],a[40:43]\nzd a[244:247],v9 offset:13312\nze v85,v53\n"
"zm a[44:47],a[212:215],v[68:71],a[44:47]\nzl v4,s[36:39],s78 offen offset:3072 sc1 lds\nza v105,v82,v83\nzm a[48:51],a[216:219],v[64:67],a[48:51]\nzd a[248:251],v9 offset:14336\nze v86,v54\nzm a[52:55],a[216:219],v[68:71],a[52:55]\nzp s78,s78,16384\n"
"zc v73,v82,v83\nze v87,v55\nzm a[56:59],a[220:223],v[64:67],a[56:59]\nzd a[252:255],v9 offset:15360\nza v108,v104,v105\nzm a[60:63],a[220:223],v[68:71],a[60:63]\nzv lgkmcnt(4)\nza v106,v84,v85\n"
"zm a[64:67],a[224:227],v[64:67],a[64:67]\nzd a[192:195],v8 offset:0\nzc v76,v84,v85\nzm a[68:71],a[224:227],v[68:71],a[68:71]\ns_mov_b32 m0,s80\nza v107,v86,v87\nzc v77,v86,v87\nzm a[72:75],a[228:231],v[64:67],a[72:75]\n"
"zd a[196:199],v8 offset:1024\nza v109,v106,v107\nzm a[76:79],a[228:231],v[68:71],a[76:79]\nzl v4,s[28:31],s79 offen offset:0 sc1 lds\nv_cmp_ge_i32 s[68:69],s82,v26\nzm a[80:83],a[232:235],v[64:67],a[80:83]\nzd a[200:203],v8 offset:2048\nv_cmp_ge_i32 s[70:71],s82,v27\n"
"zm a[84:87],a[232:235],v[68:71],a[84:87]\nzk v56,v14,v56,s[64:65]\nzk v57,v14,v57,s[66:67]\nzm a[88:91],a[236:239],v[64:67],a[88:91]\nzd a[204:207],v8 offset:3072\nzk v58,v14,v58,s[68:69]\nzm a[92:95],a[236:239],v[68:71],a[92:95]\nzv lgkmcnt(4)\n"
"zk v59,v14,v59,s[70:71]\nzm a[96:99],a[240:243],v[64:67],a[96:99]\nzd a[208:211],v8 offset:4096\nzk v60,v14,v60,s[56:57]\nzm a[100:103],a[240:243],v[68:71],a[100:103]\nzk v61,v14,v61,s[58:59]\nzk v62,v14,v62,s[60:61]\nzm a[104:107],a[244:247],v[64:67],a[104:107]\n"
"zd a[212:215],v8 offset:5120\nzk v63,v14,v63,s[62:63]\nzm a[108:111],a[244:247],v[68:71],a[108:111]\nzl v4,s[28:31],s79 offen offset:1024 sc1 lds\nzm a[112:115],a[248:251],v[64:67],a[112:115]\nzd a[216:219],v8 offset:6144\nze v88,v56\nzm a[116:119],a[248:251],v[68:71],a[116:119]\n"
"ze v89,v57\nzm a[120:123],a[252:255],v[64:67],a[120:123]\nzd a[220:223],v8 offset:7168\nze v90,v58\nzm a[124:127],a[252:255],v[68:71],a[124:127]\nzv lgkmcnt(4)\nze v91,v59\nza v104,v88,v89\n"
"zm v[32:35],a[192:195],a[128:131],v[96:99]\nzd a[224:227],v8 offset:8192\nze v92,v60\nzm v[36:39],a[192:195],a[160:163],v[100:103]\nzc v74,v88,v89\nze v93,v61\nzm v[32:35],a[196:199],a[132:135],v[32:35]\nzd a[228:231],v8 offset:9216\n"
"za v105,v90,v91\nzc v75,v90,v91\nzm v[36:39],a[196:199],a[164:167],v[36:39]\nzl v4,s[28:31],s79 offen offset:2048 sc1 lds\nza v110,v104,v105\nzm v[32:35],a[200:203],a[136:139],v[32:35]\nzd a[232:235],v8 offset:10240\nze v94,v62\n"
"zm v[36:39],a[200:203],a[168:171],v[36:39]\nze v95,v63\nzm v[32:35],a[204:207],a[140:143],v[32:35]\nzd a[236:239],v8 offset:11264\nza v106,v92,v93\nzc v78,v92,v93\nzm v[36:39],a[204:207],a[172:175],v[36:39]\nzv lgkmcnt(4)\n"
"za v107,v94,v95\nzm v[32:35],a[208:211],a[144:147],v[32:35]\nzd a[240:243],v8 offset:12288\nzc v79,v94,v95\nzm v[36:39],a[208:211],a[176:179],v[36:39]\nza v111,v106,v107\nzm v[32:35],a[212:215],a[148:151],v[32:35]\nzd a[244:247],v8 offset:13312\n"
"za v108,v108,v110\nza v109,v109,v111\nzm v[36:39],a[212:215],a[180:183],v[36:39]\nzl v4,s[28:31],s79 offen offset:3072 sc1 lds\nv_max_f32 v104,v108,v109\nzm v[32:35],a[216:219],a[152:155],v[32:35]\nzd a[248:251],v8 offset:14336\nv_cmp_lt_f32 vcc,s83,v104\n"
"zm v[36:39],a[216:219],a[184:187],v[36:39]\nzp s79,s79,16384\nzm v[32:35],a[220:223],a[156:159],v[32:35]\nzd a[252:255],v8 offset:15360\nzm v[36:39],a[220:223],a[188:191],v[36:39]\ns_sub_u32 s82,s82,32\nzv lgkmcnt(4)\ns_cbranch_vccnz L_A_rare_M0\n"
"L_A_back_M0:\nzv vmcnt(8)\ns_barrier\nzm v[40:43],a[224:227],a[128:131],v[96:99]\nzd a[192:195],v9 offset:16384\nzm v[44:47],a[224:227],a[160:163],v[100:103]\nzp m0,s81,16384\nzm v[40:43],a[228:231],a[132:135],v[40:43]\n"
"zd a[196:199],v9 offset:17408\nzm v[44:47],a[228:231],a[164:167],v[44:47]\nzl v4,s[36:39],s78 offen offset:0 sc1 lds\nzm v[40:43],a[232:235],a[136:139],v[40:43]\nzd a[200:203],v9 offset:18432\nzm v[44:47],a[232:235],a[168:171],v[44:47]\ns_xor_b32 s81,s81,0x8000\nzm v[40:43],a[236:239],a[140:143],v[40:43]\n"
"zd a[204:207],v9 offset:19456\nzm v[44:47],a[236:239],a[172:175],v[44:47]\nzv lgkmcnt(4)\nzm v[40:43],a[240:243],a[144:147],v[40:43]\nzd a[208:211],v9 offset:20480\nza v10,v10,v108\nv_cmp_ge_i32 s[56:57],s82,v20\nzm v[44:47],a[240:243],a[176:179],v[44:47]\n"
"v_cmp_ge_i32 s[58:59],s82,v21\nv_cmp_ge_i32 s[60:61],s82,v22\nv_cmp_ge_i32 s[62:63],s82,v23\nzm v[40:43],a[244:247],a[148:151],v[40:43]\nzd a[212:215],v9 offset:21504\nza v11,v11,v109\nv_cmp_ge_i32 s[48:49],s82,v16\nzm v[44:47],a[244:247],a[180:183],v[44:47]\n"
"zl v4,s[36:39],s78 offen offset:1024 sc1 lds\nv_cmp_ge_i32 s[50:51],s82,v17\nzm v[40:43],a[248:251],a[152:155],v[40:43]\nzd a[216:219],v9 offset:22528\nv_cmp_ge_i32 s[52:53],s82,v18\nv_cmp_ge_i32 s[54:55],s82,v19\nzm v[44:47],a[248:251],a[184:187],v[44:47]\nzk v32,v14,v32,s[56:57]\n"
"zk v33,v14,v33,s[58:59]\nzm v[40:43],a[252:255],a[156:159],v[40:43]\nzd a[220:223],v9 offset:23552\nzk v34,v14,v34,s[60:61]\nzm v[44:47],a[252:255],a[188:191],v[44:47]\nzv lgkmcnt(4)\nzk v35,v14,v35,s[62:63]\nzm a[0:3],a[192:195],v[72:75],a[0:3]\n"
"zd a[224:227],v9 offset:24576\nzk v36,v14,v36,s[48:49]\nzk v37,v14,v37,s[50:51]\nzm a[4:7],a[192:195],v[76:79],a[4:7]\nzk v38,v14,v38,s[52:53]\nzm a[8:11],a[196:199],v[72:75],a[8:11]\nzd a[228:231],v9 offset:25600\nzk v39,v14,v39,s[54:55]\n"
"zm a[12:15],a[196:199],v[76:79],a[12:15]\nzl v4,s[36:39],s78 offen offset:2048 sc1 lds\nv_cmp_ge_i32 s[64:65],s82,v24\nzm a[16:19],a[200:203],v[72:75],a[16:19]\nzd a[232:235],v9 offset:26624\nze v80,v32\nzm a[20:23],a[200:203],v[76:79],a[20:23]\nze v81,v33\n"
"v_cmp_ge_i32 s[66:67],s82,v25\nzm a[24:27],a[204:207],v[72:75],a[24:27]\nzd a[236:239],v9 offset:27648\nze v82,v34\nzm a[28:31],a[204:207],v[76:79],a[28:31]\nzv lgkmcnt(4)\nze v83,v35\nzm a[32:35],a[208:211],v[72:75],a[32:35]\n"
"zd a[240:243],v9 offset:28672\nza v104,v80,v81\nzc v64,v80,v81\nzm a[36:39],a[208:211],v[76:79],a[36:39]\nze v84,v36\nzm a[40:43],a[212:215],v[72:75],a[40:43]\nzd a[244:247],v9 offset:29696\nze v85,v37\n"
"zm a[44:47],a[212:215],v[76:79],a[44:47]\nzl v4,s[36:39],s78 offen offset:3072 sc1 lds\nza v105,v82,v83\nzm a[48:51],a[216:219],v[72:75],a[48:51]\nzd a[248:251],v9 offset:30720\nze v86,v38\nzm a[52:55],a[216:219],v[76:79],a[52:55]\nzp s78,s78,16384\n"
"zc v65,v82,v83\nze v87,v39\nzm a[56:59],a[220:223],v[72:75],a[56:59]\nzd a[252:255],v9 offset:31744\nza v108,v104,v105\nzm a[60:63],a[220:223],v[76:79],a[60:63]\nzv lgkmcnt(4)\nzo v9,0x8000,v9\n"
"za v106,v84,v85\nzm a[64:67],a[224:227],v[72:75],a[64:67]\nzd a[192:195],v8 offset:16384\nzc v68,v84,v85\nzm a[68:71],a[224:227],v[76:79],a[68:71]\nzp m0,s80,16384\nza v107,v86,v87\nzc v69,v86,v87\n"
"zm a[72:75],a[228:231],v[72:75],a[72:75]\nzd a[196:199],v8 offset:17408\nza v109,v106,v107\nzm a[76:79],a[228:231],v[76:79],a[76:79]\nzl v4,s[28:31],s79 offen offset:0 sc1 lds\nv_cmp_ge_i32 s[68:69],s82,v26\nzm a[80:83],a[232:235],v[72:75],a[80:83]\nzd a[200:203],v8 offset:18432\n"
"v_cmp_ge_i32 s[70:71],s82,v27\nzm a[84:87],a[232:235],v[76:79],a[84:87]\ns_xor_b32 s80,s80,0x8000\nzk v40,v14,v40,s[64:65]\nzk v41,v14,v41,s[66:67]\nzm a[88:91],a[236:239],v[72:75],a[88:91]\nzd a[204:207],v8 offset:19456\nzk v42,v14,v42,s[68:69]\n"
"zm a[92:95],a[236:239],v[76:79],a[92:95]\nzv lgkmcnt(4)\nzk v43,v14,v43,s[70:71]\nzm a[96:99],a[240:243],v[72:75],a[96:99]\nzd a[208:211],v8 offset:20480\nzk v44,v14,v44,s[56:57]\nzm a[100:103],a[240:243],v[76:79],a[100:103]\nzk v45,v14,v45,s[58:59]\n"
"zk v46,v14,v46,s[60:61]\nzm a[104:107],a[244:247],v[72:75],a[104:107]\nzd a[212:215],v8 offset:21504\nzk v47,v14,v47,s[62:63]\nzm a[108:111],a[244:247],v[76:79],a[108:111]\nzl v4,s[28:31],s79 offen offset:1024 sc1 lds\nzm a[112:115],a[248:251],v[72:75],a[112:115]\nzd a[216:219],v8 offset:22528\n"
"ze v88,v40\nzm a[116:119],a[248:251],v[76:79],a[116:119]\nze v89,v41\nzm a[120:123],a[252:255],v[72:75],a[120:123]\nzd a[220:223],v8 offset:23552\nze v90,v42\nzm a[124:127],a[252:255],v[76:79],a[124:127]\nzv lgkmcnt(4)\n"
"ze v91,v43\nza v104,v88,v89\nzm v[48:51],a[192:195],a[128:131],v[96:99]\nzd a[224:227],v8 offset:24576\nze v92,v44\nzm v[52:55],a[192:195],a[160:163],v[100:103]\nzc v66,v88,v89\nze v93,v45\n"
"zm v[48:51],a[196:199],a[132:135],v[48:51]\nzd a[228:231],v8 offset:25600\nza v105,v90,v91\nzc v67,v90,v91\nzm v[52:55],a[196:199],a[164:167],v[52:55]\nzl v4,s[28:31],s79 offen offset:2048 sc1 lds\nza v110,v104,v105\nzm v[48:51],a[200:203],a[136:139],v[48:51]\n"
"zd a[232:235],v8 offset:26624\nze v94,v46\nzm v[52:55],a[200:203],a[168:171],v[52:55]\nze v95,v47\nzm v[48:51],a[204:207],a[140:143],v[48:51]\nzd a[236:239],v8 offset:27648\nza v106,v92,v93\nzc v70,v92,v93\n"
"zm v[52:55],a[204:207],a[172:175],v[52:55]\nzv lgkmcnt(4)\nza v107,v94,v95\nzm v[48:51],a[208:211],a[144:147],v[48:51]\nzd a[240:243],v8 offset:28672\nzc v71,v94,v95\nzm v[52:55],a[208:211],a[176:179],v[52:55]\nza v111,v106,v107\n"
"zm v[48:51],a[212:215],a[148:151],v[48:51]\nzd a[244:247],v8 offset:29696\nza v108,v108,v110\nza v109,v109,v111\nzm v[52:55],a[212:215],a[180:183],v[52:55]\nzl v4,s[28:31],s79 offen offset:3072 sc1 lds\nv_max_f32 v104,v108,v109\nzm v[48:51],a[216:219],a[152:155],v[48:51]\n"
"zd a[248:251],v8 offset:30720\nv_cmp_lt_f32 vcc,s83,v104\nzm v[52:55],a[216:219],a[184:187],v[52:55]\nzp s79,s79,16384\nzm v[48:51],a[220:223],a[156:159],v[48:51]\nzd a[252:255],v8 offset:31744\nzm v[52:55],a[220:223],a[188:191],v[52:55]\ns_sub_u32 s82,s82,32\n"
"zv lgkmcnt(4)\nzo v8,0x8000,v8\ns_cbranch_vccnz L_A_rare_M1\nL_A_back_M1:\ns_sub_u32 s77,s77,1\ns_cmp_lg_u32 s77,0\ns_cbranch_scc1 L_A_loopM\nL_A_last:\n"
"s_cmp_eq_u32 s84,2\ns_cbranch_scc0 L_A_nopoll\nv_mov_b32 v15,0\nglobal_load_dword v209,v15,s[6:7] sc1\nL_A_nopoll:\nzv vmcnt(8)\ns_barrier\nzm v[56:59],a[224:227],a[128:131],v[96:99]\n"
"zd a[192:195],v9 offset:0\nzm v[60:63],a[224:227],a[160:163],v[100:103]\ns_mov_b32 m0,s81\nzm v[56:59],a[228:231],a[132:135],v[56:59]\nzd a[196:199],v9 offset:1024\nzm v[60:63],a[228:231],a[164:167],v[60:63]\nzl v4,s[36:39],s78 offen offset:0 sc1 lds\nzm v[56:59],a[232:235],a[136:139],v[56:59]\n"
"zd a[200:203],v9 offset:2048\nzm v[60:63],a[232:235],a[168:171],v[60:63]\nzm v[56:59],a[236:239],a[140:143],v[56:59]\nzd a[204:207],v9 offset:3072\nzm v[60:63],a[236:239],a[172:175],v[60:63]\nzv lgkmcnt(4)\nzm v[56:59],a[240:243],a[144:147],v[56:59]\nzd a[208:211],v9 offset:4096\n"
"za v10,v10,v108\nv_cmp_ge_i32 s[56:57],s82,v20\nzm v[60:63],a[240:243],a[176:179],v[60:63]\nv_cmp_ge_i32 s[58:59],s82,v21\nv_cmp_ge_i32 s[60:61],s82,v22\nv_cmp_ge_i32 s[62:63],s82,v23\nzm v[56:59],a[244:247],a[148:151],v[56:59]\nzd a[212:215],v9 offset:5120\n"
"za v11,v11,v109\nv_cmp_ge_i32 s[48:49],s82,v16\nzm v[60:63],a[244:247],a[180:183],v[60:63]\nzl v4,s[36:39],s78 offen offset:1024 sc1 lds\nv_cmp_ge_i32 s[50:51],s82,v17\nzm v[56:59],a[248:251],a[152:155],v[56:59]\nzd a[216:219],v9 offset:6144\nv_cmp_ge_i32 s[52:53],s82,v18\n"
"v_cmp_ge_i32 s[54:55],s82,v19\nzm v[60:63],a[248:251],a[184:187],v[60:63]\nzk v48,v14,v48,s[56:57]\nzk v49,v14,v49,s[58:59]\nzm v[56:59],a[252:255],a[156:159],v[56:59]\nzd a[220:223],v9 offset:7168\nzk v50,v14,v50,s[60:61]\nzm v[60:63],a[252:255],a[188:191],v[60:63]\n"
"zv lgkmcnt(4)\nzk v51,v14,v51,s[62:63]\nzm a[0:3],a[192:195],v[64:67],a[0:3]\nzd a[224:227],v9 offset:8192\nzk v52,v14,v52,s[48:49]\nzk v53,v14,v53,s[50:51]\nzm a[4:7],a[192:195],v[68:71],a[4:7]\nzk v54,v14,v54,s[52:53]\n"
"zm a[8:11],a[196:199],v[64:67],a[8:11]\nzd a[228:231],v9 offset:9216\nzk v55,v14,v55,s[54:55]\nzm a[12:15],a[196:199],v[68:71],a[12:15]\nzl v4,s[36:39],s78 offen offset:2048 sc1 lds\nv_cmp_ge_i32 s[64:65],s82,v24\nzm a[16:19],a[200:203],v[64:67],a[16:19]\nzd a[232:235],v9 offset:10240\n"
"ze v80,v48\nzm a[20:23],a[200:203],v[68:71],a[20:23]\nze v81,v49\nv_cmp_ge_i32 s[66:67],s82,v25\nzm a[24:27],a[204:207],v[64:67],a[24:27]\nzd a[236:239],v9 offset:11264\nze v82,v50\nzm a[28:31],a[204:207],v[68:71],a[28:31]\n"
"zv lgkmcnt(4)\nze v83,v51\nzm a[32:35],a[208:211],v[64:67],a[32:35]\nzd a[240:243],v9 offset:12288\nza v104,v80,v81\nzc v72,v80,v81\nzm a[36:39],a[208:211],v[68:71],a[36:39]\nze v84,v52\n"
"zm a[40:43],a[212:215],v[64:67],a[40:43]\nzd a[244:247],v9 offset:13312\nze v85,v53\nzm a[44:47],a[212:215],v[68:71],a[44:47]\nzl v4,s[36:39],s78 offen offset:3072 sc1 lds\nza v105,v82,v83\nzm a[48:51],a[216:219],v[64:67],a[48:51]\nzd a[248:251],v9 offset:14336\n"
"ze v86,v54\nzm a[52:55],a[216:219],v[68:71],a[52:55]\nzp s78,s78,16384\nzc v73,v82,v83\nze v87,v55\nzm a[56:59],a[220:223],v[64:67],a[56:59]\nzd a[252:255],v9 offset:15360\nza v108,v104,v105\n"
"zm a[60:63],a[220:223],v[68:71],a[60:63]\nzv lgkmcnt(4)\nza v106,v84,v85\nzm a[64:67],a[224:227],v[64:67],a[64:67]\nzd a[192:195],v8 offset:0\nzc v76,v84,v85\nzm a[68:71],a[224:227],v[68:71],a[68:71]\ns_mov_b32 m0,s80\n"
"za v107,v86,v87\nzc v77,v86,v87\nzm a[72:75],a[228:231],v[64:67],a[72:75]\nzd a[196:199],v8 offset:1024\nza v109,v106,v107\nzm a[76:79],a[228:231],v[68:71],a[76:79]\nzl v4,s[28:31],s79 offen offset:0 sc1 lds\nv_cmp_ge_i32 s[68:69],s82,v26\n"
"zm a[80:83],a[232:235],v[64:67],a[80:83]\nzd a[200:203],v8 offset:2048\nv_cmp_ge_i32 s[70:71],s82,v27\nzm a[84:87],a[232:235],v[68:71],a[84:87]\nzk v56,v14,v56,s[64:65]\nzk v57,v14,v57,s[66:67]\nzm a[88:91],a[236:239],v[64:67],a[88:91]\nzd a[204:207],v8 offset:3072\n"
"zk v58,v14,v58,s[68:69]\nzm a[92:95],a[236:239],v[68:71],a[92:95]\nzv lgkmcnt(4)\nzk v59,v14,v59,s[70:71]\nzm a[96:99],a[240:243],v[64:67],a[96:99]\nzd a[208:211],v8 offset:4096\nzk v60,v14,v60,s[56:57]\nzm a[100:103],a[240:243],v[68:71],a[100:103]\n"
"zk v61,v14,v61,s[58:59]\nzk v62,v14,v62,s[60:61]\nzm a[104:107],a[244:247],v[64:67],a[104:107]\nzd a[212:215],v8 offset:5120\nzk v63,v14,v63,s[62:63]\nzm a[108:111],a[244:247],v[68:71],a[108:111]\nzl v4,s[28:31],s79 offen offset:1024 sc1 lds\nzm a[112:115],a[248:251],v[64:67],a[112:115]\n"
"zd a[216:219],v8 offset:6144\nze v88,v56\nzm a[116:119],a[248:251],v[68:71],a[116:119]\nze v89,v57\nzm a[120:123],a[252:255],v[64:67],a[120:123]\nzd a[220:223],v8 offset:7168\nze v90,v58\nzm a[124:127],a[252:255],v[68:71],a[124:127]\n"
"zv lgkmcnt(4)\nze v91,v59\nza v104,v88,v89\nzm v[32:35],a[192:195],a[128:131],v[96:99]\nzd a[224:227],v8 offset:8192\nze v92,v60\nzm v[36:39],a[192:195],a[160:163],v[100:103]\nzc v74,v88,v89\n"
"ze v93,v61\nzm v[32:35],a[196:199],a[132:135],v[32:35]\nzd a[228:231],v8 offset:9216\nza v105,v90,v91\nzc v75,v90,v91\nzm v[36:39],a[196:199],a[164:167],v[36:39]\nzl v4,s[28:31],s79 offen offset:2048 sc1 lds\nza v110,v104,v105\n"
"zm v[32:35],a[200:203],a[136:139],v[32:35]\nzd a[232:235],v8 offset:10240\nze v94,v62\nzm v[36:39],a[200:203],a[168:171],v[36:39]\nze v95,v63\nzm v[32:35],a[204:207],a[140:143],v[32:35]\nzd a[236:239],v8 offset:11264\nza v106,v92,v93\n"
"zc v78,v92,v93\nzm v[36:39],a[204:207],a[172:175],v[36:39]\nzv lgkmcnt(4)\nza v107,v94,v95\nzm v[32:35],a[208:211],a[144:147],v[32:35]\nzd a[240:243],v8 offset:12288\nzc v79,v94,v95\nzm v[36:39],a[208:211],a[176:179],v[36:39]\n"
"za v111,v106,v107\nzm v[32:35],a[212:215],a[148:151],v[32:35]\nzd a[244:247],v8 offset:13312\nza v108,v108,v110\nza v109,v109,v111\nzm v[36:39],a[212:215],a[180:183],v[36:39]\nzl v4,s[28:31],s79 offen offset:3072 sc1 lds\nv_max_f32 v104,v108,v109\n"
"zm v[32:35],a[216:219],a[152:155],v[32:35]\nzd a[248:251],v8 offset:14336\nv_cmp_lt_f32 vcc,s83,v104\nzm v[36:39],a[216:219],a[184:187],v[36:39]\nzp s79,s79,16384\nzm v[32:35],a[220:223],a[156:159],v[32:35]\nzd a[252:255],v8 offset:15360\nzm v[36:39],a[220:223],a[188:191],v[36:39]\n"
"s_sub_u32 s82,s82,32\nzv lgkmcnt(4)\ns_cbranch_vccnz L_A_rare_L0\nL_A_back_L0:\nza v10,v10,v108\nza v11,v11,v109\nzv vmcnt(8)\ns_barrier\n"
"zd a[192:195],v9 offset:16384\nzd a[196:199],v9 offset:17408\nzd a[200:203],v9 offset:18432\nzd a[204:207],v9 offset:19456\nzd a[208:211],v9 offset:20480\nzd a[212:215],v9 offset:21504\nzd a[216:219],v9 offset:22528\nzd a[220:223],v9 offset:23552\n"
"zd a[224:227],v9 offset:24576\nzd a[228:231],v9 offset:25600\nzd a[232:235],v9 offset:26624\nzd a[236:239],v9 offset:27648\nzd a[240:243],v9 offset:28672\nzd a[244:247],v9 offset:29696\nzd a[248:251],v9 offset:30720\nzd a[252:255],v9 offset:31744\n"
"s_cmp_eq_u32 s84,2\ns_cbranch_scc0 L_A_nomf\nv_readfirstlane_b32 s89,v209\ns_nop 3\ns_cmp_eq_u32 s89,s34\ns_cbranch_scc1 L_A_polled\ns_mov_b32 s93,0\nv_mov_b32 v15,0\n"
"L_A_poll:\nglobal_load_dword v209,v15,s[6:7] sc1\nzv vmcnt(0)\nv_readfirstlane_b32 s89,v209\ns_nop 3\ns_cmp_eq_u32 s89,s34\ns_cbranch_scc1 L_A_polled\nzp s93,s93,1\n"
"s_cmp_lt_u32 s93,300000\ns_cbranch_scc0 L_A_polled\ns_sleep 1\ns_branch L_A_poll\nL_A_polled:\ns_lshl_b32 s89,s23,14\nzp s90,s89,0\nzl v[144:147],v5,s[44:47],s90 offen offset:0 sc1\n"
"zl v[148:151],v5,s[44:47],s90 offen offset:1024 sc1\nzl v[152:155],v5,s[44:47],s90 offen offset:2048 sc1\nzl v[156:159],v5,s[44:47],s90 offen offset:3072 sc1\nzp s90,s89,4096\nzl v[160:163],v5,s[44:47],s90 offen offset:0 sc1\nzl v[164:167],v5,s[44:47],s90 offen offset:1024 sc1\nzl v[168:171],v5,s[44:47],s90 offen offset:2048 sc1\nzl v[172:175],v5,s[44:47],s90 offen offset:3072 sc1\n"
"zp s90,s89,8192\nzl v[176:179],v5,s[44:47],s90 offen offset:0 sc1\nzl v[180:183],v5,s[44:47],s90 offen offset:1024 sc1\nzl v[184:187],v5,s[44:47],s90 offen offset:2048 sc1\nzl v[188:191],v5,s[44:47],s90 offen offset:3072 sc1\nzp s90,s89,12288\nzl v[192:195],v5,s[44:47],s90 offen offset:0 sc1\nzl v[196:199],v5,s[44:47],s90 offen offset:1024 sc1\n"
"zl v[200:203],v5,s[44:47],s90 offen offset:2048 sc1\nzl v[204:207],v5,s[44:47],s90 offen offset:3072 sc1\nzh v15,3,v6\nglobal_load_dwordx2 v[210:211],v15,s[4:5] offset:0 sc1\nglobal_load_dwordx2 v[212:213],v15,s[4:5] offset:512 sc1\nL_A_nomf:\nzv lgkmcnt(12)\nzm a[0:3],a[192:195],v[72:75],a[0:3]\n"
"zm a[4:7],a[192:195],v[76:79],a[4:7]\nzm a[8:11],a[196:199],v[72:75],a[8:11]\nzm a[12:15],a[196:199],v[76:79],a[12:15]\nzm a[16:19],a[200:203],v[72:75],a[16:19]\nzm a[20:23],a[200:203],v[76:79],a[20:23]\nzm a[24:27],a[204:207],v[72:75],a[24:27]\nzm a[28:31],a[204:207],v[76:79],a[28:31]\nzv lgkmcnt(8)\n"
"zm a[32:35],a[208:211],v[72:75],a[32:35]\nzm a[36:39],a[208:211],v[76:79],a[36:39]\nzm a[40:43],a[212:215],v[72:75],a[40:43]\nzm a[44:47],a[212:215],v[76:79],a[44:47]\nzm a[48:51],a[216:219],v[72:75],a[48:51]\nzm a[52:55],a[216:219],v[76:79],a[52:55]\nzm a[56:59],a[220:223],v[72:75],a[56:59]\nzm a[60:63],a[220:223],v[76:79],a[60:63]\n"
"zv lgkmcnt(4)\nzm a[64:67],a[224:227],v[72:75],a[64:67]\nzm a[68:71],a[224:227],v[76:79],a[68:71]\nzm a[72:75],a[228:231],v[72:75],a[72:75]\nzm a[76:79],a[228:231],v[76:79],a[76:79]\nzm a[80:83],a[232:235],v[72:75],a[80:83]\nzm a[84:87],a[232:235],v[76:79],a[84:87]\nzm a[88:91],a[236:239],v[72:75],a[88:91]\n"
"zm a[92:95],a[236:239],v[76:79],a[92:95]\nzv lgkmcnt(0)\nzm a[96:99],a[240:243],v[72:75],a[96:99]\nzm a[100:103],a[240:243],v[76:79],a[100:103]\nzm a[104:107],a[244:247],v[72:75],a[104:107]\nzm a[108:111],a[244:247],v[76:79],a[108:111]\nzm a[112:115],a[248:251],v[72:75],a[112:115]\nzm a[116:119],a[248:251],v[76:79],a[116:119]\n"
"zm a[120:123],a[252:255],v[72:75],a[120:123]\nzm a[124:127],a[252:255],v[76:79],a[124:127]\ns_mov_b32 s88,s74\ns_mov_b32 s72,s84\ns_cmp_eq_u32 s84,1\ns_cbranch_scc0 L_A_nonext\ns_barrier\ns_mov_b32 s87,1\n"
"s_cmp_eq_u32 s87,0\ns_cbranch_scc0 L_A_seg1b\ns_sub_u32 s74,15,s85\ns_lshl_b32 s89,s74,2\nzp s89,s89,4\ns_cmp_eq_u32 s86,0\ns_cbranch_scc0 L_A_seg0Bb\ns_mov_b32 s75,0\n"
"s_min_u32 s90,s89,40\ns_cmp_lt_u32 40,s89\ns_cselect_b32 s84,2,0\ns_branch L_A_seggob\nL_A_seg0Bb:\ns_mov_b32 s75,40\ns_mov_b32 s90,s89\ns_mov_b32 s84,1\n"
"s_cmp_lt_u32 40,s89\ns_cbranch_scc1 L_A_seggob\ns_mov_b32 s87,1\nL_A_seg1b:\ns_mov_b32 s74,s85\ns_mov_b32 s75,0\ns_lshl_b32 s90,s74,2\nzp s90,s90,4\n"
"s_mov_b32 s84,0\nL_A_seggob:\ns_sub_u32 s91,s90,2\ns_lshl_b32 s92,s74,2\ns_sub_i32 s92,s92,2\ns_min_i32 s92,s92,s91\ns_sub_i32 s92,s92,s75\ns_max_i32 s92,s92,0\n"
"s_lshr_b32 s76,s92,1\ns_sub_u32 s91,s91,s75\ns_lshr_b32 s91,s91,1\ns_sub_u32 s77,s91,s76\nzp s89,s75,0\ns_lshl_b32 s79,s89,14\ns_and_b32 s89,s89,3\ns_lshl_b32 s89,s89,14\n"
"s_lshl_b32 s90,s23,12\nzp m0,s89,s90\ns_nop 0\nzl v4,s[28:31],s79 offen offset:0 sc1 lds\nzl v4,s[28:31],s79 offen offset:1024 sc1 lds\nzl v4,s[28:31],s79 offen offset:2048 sc1 lds\nzl v4,s[28:31],s79 offen offset:3072 sc1 lds\ns_lshl_b32 s89,s74,3\n"
"s_lshl_b32 s90,s23,1\nzp s89,s89,s90\ns_lshl_b32 s89,s89,13\nzp s90,s89,0\nzl a[128:131],v5,s[24:27],s90 offen offset:0 sc1\nzl a[132:135],v5,s[24:27],s90 offen offset:1024 sc1\nzl a[136:139],v5,s[24:27],s90 offen offset:2048 sc1\nzl a[140:143],v5,s[24:27],s90 offen offset:3072 sc1\n"
"zp s90,s89,4096\nzl a[144:147],v5,s[24:27],s90 offen offset:0 sc1\nzl a[148:151],v5,s[24:27],s90 offen offset:1024 sc1\nzl a[152:155],v5,s[24:27],s90 offen offset:2048 sc1\nzl a[156:159],v5,s[24:27],s90 offen offset:3072 sc1\nzp s90,s89,8192\nzl a[160:163],v5,s[24:27],s90 offen offset:0 sc1\nzl a[164:167],v5,s[24:27],s90 offen offset:1024 sc1\n"
"zl a[168:171],v5,s[24:27],s90 offen offset:2048 sc1\nzl a[172:175],v5,s[24:27],s90 offen offset:3072 sc1\nzp s90,s89,12288\nzl a[176:179],v5,s[24:27],s90 offen offset:0 sc1\nzl a[180:183],v5,s[24:27],s90 offen offset:1024 sc1\nzl a[184:187],v5,s[24:27],s90 offen offset:2048 sc1\nzl a[188:191],v5,s[24:27],s90 offen offset:3072 sc1\nzp s89,s75,1\n"
"s_lshl_b32 s79,s89,14\ns_and_b32 s89,s89,3\ns_lshl_b32 s89,s89,14\ns_lshl_b32 s90,s23,12\nzp m0,s89,s90\ns_nop 0\nzl v4,s[28:31],s79 offen offset:0 sc1 lds\nzl v4,s[28:31],s79 offen offset:1024 sc1 lds\n"
"zl v4,s[28:31],s79 offen offset:2048 sc1 lds\nzl v4,s[28:31],s79 offen offset:3072 sc1 lds\nzp s89,s75,0\ns_lshl_b32 s78,s89,14\ns_and_b32 s89,s89,3\ns_lshl_b32 s89,s89,14\ns_lshl_b32 s90,s23,12\nzp s89,s89,s90\n"
"zp m0,s89,65536\ns_nop 0\nzl v4,s[36:39],s78 offen offset:0 sc1 lds\nzl v4,s[36:39],s78 offen offset:1024 sc1 lds\nzl v4,s[36:39],s78 offen offset:2048 sc1 lds\nzl v4,s[36:39],s78 offen offset:3072 sc1 lds\nzp s89,s75,2\ns_lshl_b32 s79,s89,14\n"
"s_and_b32 s89,s89,3\ns_lshl_b32 s89,s89,14\ns_lshl_b32 s90,s23,12\nzp m0,s89,s90\ns_nop 0\nzl v4,s[28:31],s79 offen offset:0 sc1 lds\nzl v4,s[28:31],s79 offen offset:1024 sc1 lds\nzl v4,s[28:31],s79 offen offset:2048 sc1 lds\n"
"zl v4,s[28:31],s79 offen offset:3072 sc1 lds\nzp s89,s75,1\ns_lshl_b32 s78,s89,14\ns_and_b32 s89,s89,3\ns_lshl_b32 s89,s89,14\ns_lshl_b32 s90,s23,12\nzp s89,s89,s90\nzp m0,s89,65536\n"
"s_nop 0\nzl v4,s[36:39],s78 offen offset:0 sc1 lds\nzl v4,s[36:39],s78 offen offset:1024 sc1 lds\nzl v4,s[36:39],s78 offen offset:2048 sc1 lds\nzl v4,s[36:39],s78 offen offset:3072 sc1 lds\nzp s89,s75,3\ns_lshl_b32 s79,s89,14\ns_and_b32 s89,s89,3\n"
"s_lshl_b32 s89,s89,14\ns_lshl_b32 s90,s23,12\nzp m0,s89,s90\ns_nop 0\nzl v4,s[28:31],s79 offen offset:0 sc1 lds\nzl v4,s[28:31],s79 offen offset:1024 sc1 lds\nzl v4,s[28:31],s79 offen offset:2048 sc1 lds\nzl v4,s[28:31],s79 offen offset:3072 sc1 lds\n"
"zp s89,s75,2\ns_lshl_b32 s78,s89,14\nzp s89,s75,4\ns_lshl_b32 s79,s89,14\ns_and_b32 s89,s75,2\ns_lshl_b32 s89,s89,14\ns_lshl_b32 s90,s23,12\nzp s80,s89,s90\n"
"s_xor_b32 s91,s89,0x8000\nzp s81,s91,s90\nzp s81,s81,65536\nv_add_u32 v8,s91,v5\nzp s92,s89,65536\nv_add_u32 v9,s92,v5\nv_add_u32 v208,s89,v5\ns_lshl_b32 s82,s74,7\n"
"s_lshl_b32 s90,s23,5\nzp s82,s82,s90\ns_lshl_b32 s90,s75,5\ns_sub_u32 s82,s82,s90\nL_A_nonext:\nv_mov_b32 v15,v10\ns_nop 1\nv_permlane16_swap_b32 v10,v15\n"
"s_nop 1\nza v10,v10,v15\nv_mov_b32 v15,v10\ns_nop 1\nv_permlane32_swap_b32 v10,v15\ns_nop 1\nza v10,v10,v15\nv_mov_b32 v15,v11\n"
"s_nop 1\nv_permlane16_swap_b32 v11,v15\ns_nop 1\nza v11,v11,v15\nv_mov_b32 v15,v11\ns_nop 1\nv_permlane32_swap_b32 v11,v15\ns_nop 1\n"
"za v11,v11,v15\ns_cmp_eq_u32 s72,2\ns_cbranch_scc1 L_A_merge\nzq v28,v10\nv_cmp_lt_f32 vcc,0,v10\ns_nop 1\nzk v28,0,v28,vcc\nzq v29,v11\n"
"v_cmp_lt_f32 vcc,0,v11\ns_nop 1\nzk v29,0,v29,vcc\ns_cmp_eq_u32 s72,1\ns_cbranch_scc1 L_A_partial\ns_lshl_b32 s89,s73,13\ns_sub_u32 s90,16777216,s89\nzp s89,s89,58720256\n"
"zp s40,s94,s89\ns_addc_u32 s41,s95,0\ns_and_b32 s41,s41,0xffff\ns_mov_b32 s42,s90\ns_mov_b32 s43,0x20000\ns_lshl_b32 s89,s88,3\ns_lshl_b32 s90,s23,1\nzp s89,s89,s90\n"
"s_lshl_b32 s89,s89,17\nzr v112,a0\nzr v113,a1\nzr v114,a2\nzr v115,a3\nzr v116,a8\nzr v117,a9\nzr v118,a10\n"
"zr v119,a11\nv_pk_mul_f32 v[112:113],v[112:113],v[28:29] op_sel_hi:[1,0]\nv_pk_mul_f32 v[114:115],v[114:115],v[28:29] op_sel_hi:[1,0]\nv_pk_mul_f32 v[116:117],v[116:117],v[28:29] op_sel_hi:[1,0]\nv_pk_mul_f32 v[118:119],v[118:119],v[28:29] op_sel_hi:[1,0]\nzc v122,v112,v113\nzc v123,v114,v115\nzc v124,v116,v117\n"
"zc v125,v118,v119\nzp s90,s89,0\nzs v[122:125],v5,s[40:43],s90 offen offset:0 sc1\nzr v128,a16\nzr v129,a17\nzr v130,a18\nzr v131,a19\nzr v132,a24\n"
"zr v133,a25\nzr v134,a26\nzr v135,a27\nv_pk_mul_f32 v[128:129],v[128:129],v[28:29] op_sel_hi:[1,0]\nv_pk_mul_f32 v[130:131],v[130:131],v[28:29] op_sel_hi:[1,0]\nv_pk_mul_f32 v[132:133],v[132:133],v[28:29] op_sel_hi:[1,0]\nv_pk_mul_f32 v[134:135],v[134:135],v[28:29] op_sel_hi:[1,0]\nzc v138,v128,v129\n"
"zc v139,v130,v131\nzc v140,v132,v133\nzc v141,v134,v135\nzs v[138:141],v5,s[40:43],s90 offen offset:1024 sc1\nzr v112,a32\nzr v113,a33\nzr v114,a34\nzr v115,a35\n"
"zr v116,a40\nzr v117,a41\nzr v118,a42\nzr v119,a43\nv_pk_mul_f32 v[112:113],v[112:113],v[28:29] op_sel_hi:[1,0]\nv_pk_mul_f32 v[114:115],v[114:115],v[28:29] op_sel_hi:[1,0]\nv_pk_mul_f32 v[116:117],v[116:117],v[28:29] op_sel_hi:[1,0]\nv_pk_mul_f32 v[118:119],v[118:119],v[28:29] op_sel_hi:[1,0]\n"
"zc v122,v112,v113\nzc v123,v114,v115\nzc v124,v116,v117\nzc v125,v118,v119\nzs v[122:125],v5,s[40:43],s90 offen offset:2048 sc1\nzr v128,a48\nzr v129,a49\nzr v130,a50\n"
"zr v131,a51\nzr v132,a56\nzr v133,a57\nzr v134,a58\nzr v135,a59\nv_pk_mul_f32 v[128:129],v[128:129],v[28:29] op_sel_hi:[1,0]\nv_pk_mul_f32 v[130:131],v[130:131],v[28:29] op_sel_hi:[1,0]\nv_pk_mul_f32 v[132:133],v[132:133],v[28:29] op_sel_hi:[1,0]\n"
"v_pk_mul_f32 v[134:135],v[134:135],v[28:29] op_sel_hi:[1,0]\nzc v138,v128,v129\nzc v139,v130,v131\nzc v140,v132,v133\nzc v141,v134,v135\nzs v[138:141],v5,s[40:43],s90 offen offset:3072 sc1\nzr v112,a64\nzr v113,a65\n"
"zr v114,a66\nzr v115,a67\nzr v116,a72\nzr v117,a73\nzr v118,a74\nzr v119,a75\nv_pk_mul_f32 v[112:113],v[112:113],v[28:29] op_sel_hi:[1,0]\nv_pk_mul_f32 v[114:115],v[114:115],v[28:29] op_sel_hi:[1,0]\n"
"v_pk_mul_f32 v[116:117],v[116:117],v[28:29] op_sel_hi:[1,0]\nv_pk_mul_f32 v[118:119],v[118:119],v[28:29] op_sel_hi:[1,0]\nzc v122,v112,v113\nzc v123,v114,v115\nzc v124,v116,v117\nzc v125,v118,v119\nzp s90,s89,4096\nzs v[122:125],v5,s[40:43],s90 offen offset:0 sc1\n"
"zr v128,a80\nzr v129,a81\nzr v130,a82\nzr v131,a83\nzr v132,a88\nzr v133,a89\nzr v134,a90\nzr v135,a91\n"
"v_pk_mul_f32 v[128:129],v[128:129],v[28:29] op_sel_hi:[1,0]\nv_pk_mul_f32 v[130:131],v[130:131],v[28:29] op_sel_hi:[1,0]\nv_pk_mul_f32 v[132:133],v[132:133],v[28:29] op_sel_hi:[1,0]\nv_pk_mul_f32 v[134:135],v[134:135],v[28:29] op_sel_hi:[1,0]\nzc v138,v128,v129\nzc v139,v130,v131\nzc v140,v132,v133\nzc v141,v134,v135\n"
"zs v[138:141],v5,s[40:43],s90 offen offset:1024 sc1\nzr v112,a96\nzr v113,a97\nzr v114,a98\nzr v115,a99\nzr v116,a104\nzr v117,a105\nzr v118,a106\n"
"zr v119,a107\nv_pk_mul_f32 v[112:113],v[112:113],v[28:29] op_sel_hi:[1,0]\nv_pk_mul_f32 v[114:115],v[114:115],v[28:29] op_sel_hi:[1,0]\nv_pk_mul_f32 v[116:117],v[116:117],v[28:29] op_sel_hi:[1,0]\nv_pk_mul_f32 v[118:119],v[118:119],v[28:29] op_sel_hi:[1,0]\nzc v122,v112,v113\nzc v123,v114,v115\nzc v124,v116,v117\n"
"zc v125,v118,v119\nzs v[122:125],v5,s[40:43],s90 offen offset:2048 sc1\nzr v128,a112\nzr v129,a113\nzr v130,a114\nzr v131,a115\nzr v132,a120\nzr v133,a121\n"
"zr v134,a122\nzr v135,a123\nv_pk_mul_f32 v[128:129],v[128:129],v[28:29] op_sel_hi:[1,0]\nv_pk_mul_f32 v[130:131],v[130:131],v[28:29] op_sel_hi:[1,0]\nv_pk_mul_f32 v[132:133],v[132:133],v[28:29] op_sel_hi:[1,0]\nv_pk_mul_f32 v[134:135],v[134:135],v[28:29] op_sel_hi:[1,0]\nzc v138,v128,v129\nzc v139,v130,v131\n"
"zc v140,v132,v133\nzc v141,v134,v135\nzs v[138:141],v5,s[40:43],s90 offen offset:3072 sc1\nzr v112,a4\nzr v113,a5\nzr v114,a6\nzr v115,a7\nzr v116,a12\n"
"zr v117,a13\nzr v118,a14\nzr v119,a15\nv_pk_mul_f32 v[112:113],v[112:113],v[28:29] op_sel:[0,1]\nv_pk_mul_f32 v[114:115],v[114:115],v[28:29] op_sel:[0,1]\nv_pk_mul_f32 v[116:117],v[116:117],v[28:29] op_sel:[0,1]\nv_pk_mul_f32 v[118:119],v[118:119],v[28:29] op_sel:[0,1]\nzc v122,v112,v113\n"
"zc v123,v114,v115\nzc v124,v116,v117\nzc v125,v118,v119\nzp s90,s89,131072\nzs v[122:125],v5,s[40:43],s90 offen offset:0 sc1\nzr v128,a20\nzr v129,a21\nzr v130,a22\n"
"zr v131,a23\nzr v132,a28\nzr v133,a29\nzr v134,a30\nzr v135,a31\nv_pk_mul_f32 v[128:129],v[128:129],v[28:29] op_sel:[0,1]\nv_pk_mul_f32 v[130:131],v[130:131],v[28:29] op_sel:[0,1]\nv_pk_mul_f32 v[132:133],v[132:133],v[28:29] op_sel:[0,1]\n"
"v_pk_mul_f32 v[134:135],v[134:135],v[28:29] op_sel:[0,1]\nzc v138,v128,v129\nzc v139,v130,v131\nzc v140,v132,v133\nzc v141,v134,v135\nzs v[138:141],v5,s[40:43],s90 offen offset:1024 sc1\nzr v112,a36\nzr v113,a37\n"
"zr v114,a38\nzr v115,a39\nzr v116,a44\nzr v117,a45\nzr v118,a46\nzr v119,a47\nv_pk_mul_f32 v[112:113],v[112:113],v[28:29] op_sel:[0,1]\nv_pk_mul_f32 v[114:115],v[114:115],v[28:29] op_sel:[0,1]\n"
"v_pk_mul_f32 v[116:117],v[116:117],v[28:29] op_sel:[0,1]\nv_pk_mul_f32 v[118:119],v[118:119],v[28:29] op_sel:[0,1]\nzc v122,v112,v113\nzc v123,v114,v115\nzc v124,v116,v117\nzc v125,v118,v119\nzs v[122:125],v5,s[40:43],s90 offen offset:2048 sc1\nzr v128,a52\n"
"zr v129,a53\nzr v130,a54\nzr v131,a55\nzr v132,a60\nzr v133,a61\nzr v134,a62\nzr v135,a63\nv_pk_mul_f32 v[128:129],v[128:129],v[28:29] op_sel:[0,1]\n"
"v_pk_mul_f32 v[130:131],v[130:131],v[28:29] op_sel:[0,1]\nv_pk_mul_f32 v[132:133],v[132:133],v[28:29] op_sel:[0,1]\nv_pk_mul_f32 v[134:135],v[134:135],v[28:29] op_sel:[0,1]\nzc v138,v128,v129\nzc v139,v130,v131\nzc v140,v132,v133\nzc v141,v134,v135\nzs v[138:141],v5,s[40:43],s90 offen offset:3072 sc1\n"
"zr v112,a68\nzr v113,a69\nzr v114,a70\nzr v115,a71\nzr v116,a76\nzr v117,a77\nzr v118,a78\nzr v119,a79\n"
"v_pk_mul_f32 v[112:113],v[112:113],v[28:29] op_sel:[0,1]\nv_pk_mul_f32 v[114:115],v[114:115],v[28:29] op_sel:[0,1]\nv_pk_mul_f32 v[116:117],v[116:117],v[28:29] op_sel:[0,1]\nv_pk_mul_f32 v[118:119],v[118:119],v[28:29] op_sel:[0,1]\nzc v122,v112,v113\nzc v123,v114,v115\nzc v124,v116,v117\nzc v125,v118,v119\n"
"zp s90,s89,135168\nzs v[122:125],v5,s[40:43],s90 offen offset:0 sc1\nzr v128,a84\nzr v129,a85\nzr v130,a86\nzr v131,a87\nzr v132,a92\nzr v133,a93\n"
"zr v134,a94\nzr v135,a95\nv_pk_mul_f32 v[128:129],v[128:129],v[28:29] op_sel:[0,1]\nv_pk_mul_f32 v[130:131],v[130:131],v[28:29] op_sel:[0,1]\nv_pk_mul_f32 v[132:133],v[132:133],v[28:29] op_sel:[0,1]\nv_pk_mul_f32 v[134:135],v[134:135],v[28:29] op_sel:[0,1]\nzc v138,v128,v129\nzc v139,v130,v131\n"
"zc v140,v132,v133\nzc v141,v134,v135\nzs v[138:141],v5,s[40:43],s90 offen offset:1024 sc1\nzr v112,a100\nzr v113,a101\nzr v114,a102\nzr v115,a103\nzr v116,a108\n"
"zr v117,a109\nzr v118,a110\nzr v119,a111\nv_pk_mul_f32 v[112:113],v[112:113],v[28:29] op_sel:[0,1]\nv_pk_mul_f32 v[114:115],v[114:115],v[28:29] op_sel:[0,1]\nv_pk_mul_f32 v[116:117],v[116:117],v[28:29] op_sel:[0,1]\nv_pk_mul_f32 v[118:119],v[118:119],v[28:29] op_sel:[0,1]\nzc v122,v112,v113\n"
"zc v123,v114,v115\nzc v124,v116,v117\nzc v125,v118,v119\nzs v[122:125],v5,s[40:43],s90 offen offset:2048 sc1\nzr v128,a116\nzr v129,a117\nzr v130,a118\nzr v131,a119\n"
"zr v132,a124\nzr v133,a125\nzr v134,a126\nzr v135,a127\nv_pk_mul_f32 v[128:129],v[128:129],v[28:29] op_sel:[0,1]\nv_pk_mul_f32 v[130:131],v[130:131],v[28:29] op_sel:[0,1]\nv_pk_mul_f32 v[132:133],v[132:133],v[28:29] op_sel:[0,1]\nv_pk_mul_f32 v[134:135],v[134:135],v[28:29] op_sel:[0,1]\n"
"zc v138,v128,v129\nzc v139,v130,v131\nzc v140,v132,v133\nzc v141,v134,v135\nzs v[138:141],v5,s[40:43],s90 offen offset:3072 sc1\ns_branch L_A_fin\nL_A_partial:\ns_lshl_b32 s89,s23,14\n"
"zr v112,a0\nzr v113,a1\nzr v114,a2\nzr v115,a3\nzr v116,a8\nzr v117,a9\nzr v118,a10\nzr v119,a11\n"
"v_pk_mul_f32 v[112:113],v[112:113],v[28:29] op_sel_hi:[1,0]\nv_pk_mul_f32 v[114:115],v[114:115],v[28:29] op_sel_hi:[1,0]\nv_pk_mul_f32 v[116:117],v[116:117],v[28:29] op_sel_hi:[1,0]\nv_pk_mul_f32 v[118:119],v[118:119],v[28:29] op_sel_hi:[1,0]\nzc v122,v112,v113\nzc v123,v114,v115\nzc v124,v116,v117\nzc v125,v118,v119\n"
"zp s90,s89,0\nzs v[122:125],v5,s[44:47],s90 offen offset:0 sc1\nzr v128,a16\nzr v129,a17\nzr v130,a18\nzr v131,a19\nzr v132,a24\nzr v133,a25\n"
"zr v134,a26\nzr v135,a27\nv_pk_mul_f32 v[128:129],v[128:129],v[28:29] op_sel_hi:[1,0]\nv_pk_mul_f32 v[130:131],v[130:131],v[28:29] op_sel_hi:[1,0]\nv_pk_mul_f32 v[132:133],v[132:133],v[28:29] op_sel_hi:[1,0]\nv_pk_mul_f32 v[134:135],v[134:135],v[28:29] op_sel_hi:[1,0]\nzc v138,v128,v129\nzc v139,v130,v131\n"
"zc v140,v132,v133\nzc v141,v134,v135\nzs v[138:141],v5,s[44:47],s90 offen offset:1024 sc1\nzr v112,a32\nzr v113,a33\nzr v114,a34\nzr v115,a35\nzr v116,a40\n"
"zr v117,a41\nzr v118,a42\nzr v119,a43\nv_pk_mul_f32 v[112:113],v[112:113],v[28:29] op_sel_hi:[1,0]\nv_pk_mul_f32 v[114:115],v[114:115],v[28:29] op_sel_hi:[1,0]\nv_pk_mul_f32 v[116:117],v[116:117],v[28:29] op_sel_hi:[1,0]\nv_pk_mul_f32 v[118:119],v[118:119],v[28:29] op_sel_hi:[1,0]\nzc v122,v112,v113\n"
"zc v123,v114,v115\nzc v124,v116,v117\nzc v125,v118,v119\nzs v[122:125],v5,s[44:47],s90 offen offset:2048 sc1\nzr v128,a48\nzr v129,a49\nzr v130,a50\nzr v131,a51\n"
"zr v132,a56\nzr v133,a57\nzr v134,a58\nzr v135,a59\nv_pk_mul_f32 v[128:129],v[128:129],v[28:29] op_sel_hi:[1,0]\nv_pk_mul_f32 v[130:131],v[130:131],v[28:29] op_sel_hi:[1,0]\nv_pk_mul_f32 v[132:133],v[132:133],v[28:29] op_sel_hi:[1,0]\nv_pk_mul_f32 v[134:135],v[134:135],v[28:29] op_sel_hi:[1,0]\n"
"zc v138,v128,v129\nzc v139,v130,v131\nzc v140,v132,v133\nzc v141,v134,v135\nzs v[138:141],v5,s[44:47],s90 offen offset:3072 sc1\nzr v112,a64\nzr v113,a65\nzr v114,a66\n"
"zr v115,a67\nzr v116,a72\nzr v117,a73\nzr v118,a74\nzr v119,a75\nv_pk_mul_f32 v[112:113],v[112:113],v[28:29] op_sel_hi:[1,0]\nv_pk_mul_f32 v[114:115],v[114:115],v[28:29] op_sel_hi:[1,0]\nv_pk_mul_f32 v[116:117],v[116:117],v[28:29] op_sel_hi:[1,0]\n"
"v_pk_mul_f32 v[118:119],v[118:119],v[28:29] op_sel_hi:[1,0]\nzc v122,v112,v113\nzc v123,v114,v115\nzc v124,v116,v117\nzc v125,v118,v119\nzp s90,s89,4096\nzs v[122:125],v5,s[44:47],s90 offen offset:0 sc1\nzr v128,a80\n"
"zr v129,a81\nzr v130,a82\nzr v131,a83\nzr v132,a88\nzr v133,a89\nzr v134,a90\nzr v135,a91\nv_pk_mul_f32 v[128:129],v[128:129],v[28:29] op_sel_hi:[1,0]\n"
"v_pk_mul_f32 v[130:131],v[130:131],v[28:29] op_sel_hi:[1,0]\nv_pk_mul_f32 v[132:133],v[132:133],v[28:29] op_sel_hi:[1,0]\nv_pk_mul_f32 v[134:135],v[134:135],v[28:29] op_sel_hi:[1,0]\nzc v138,v128,v129\nzc v139,v130,v131\nzc v140,v132,v133\nzc v141,v134,v135\nzs v[138:141],v5,s[44:47],s90 offen offset:1024 sc1\n"
"zr v112,a96\nzr v113,a97\nzr v114,a98\nzr v115,a99\nzr v116,a104\nzr v117,a105\nzr v118,a106\nzr v119,a107\n"
"v_pk_mul_f32 v[112:113],v[112:113],v[28:29] op_sel_hi:[1,0]\nv_pk_mul_f32 v[114:115],v[114:115],v[28:29] op_sel_hi:[1,0]\nv_pk_mul_f32 v[116:117],v[116:117],v[28:29] op_sel_hi:[1,0]\nv_pk_mul_f32 v[118:119],v[118:119],v[28:29] op_sel_hi:[1,0]\nzc v122,v112,v113\nzc v123,v114,v115\nzc v124,v116,v117\nzc v125,v118,v119\n"
"zs v[122:125],v5,s[44:47],s90 offen offset:2048 sc1\nzr v128,a112\nzr v129,a113\nzr v130,a114\nzr v131,a115\nzr v132,a120\nzr v133,a121\nzr v134,a122\n"
"zr v135,a123\nv_pk_mul_f32 v[128:129],v[128:129],v[28:29] op_sel_hi:[1,0]\nv_pk_mul_f32 v[130:131],v[130:131],v[28:29] op_sel_hi:[1,0]\nv_pk_mul_f32 v[132:133],v[132:133],v[28:29] op_sel_hi:[1,0]\nv_pk_mul_f32 v[134:135],v[134:135],v[28:29] op_sel_hi:[1,0]\nzc v138,v128,v129\nzc v139,v130,v131\nzc v140,v132,v133\n"
"zc v141,v134,v135\nzs v[138:141],v5,s[44:47],s90 offen offset:3072 sc1\nzr v112,a4\nzr v113,a5\nzr v114,a6\nzr v115,a7\nzr v116,a12\nzr v117,a13\n"
"zr v118,a14\nzr v119,a15\nv_pk_mul_f32 v[112:113],v[112:113],v[28:29] op_sel:[0,1]\nv_pk_mul_f32 v[114:115],v[114:115],v[28:29] op_sel:[0,1]\nv_pk_mul_f32 v[116:117],v[116:117],v[28:29] op_sel:[0,1]\nv_pk_mul_f32 v[118:119],v[118:119],v[28:29] op_sel:[0,1]\nzc v122,v112,v113\nzc v123,v114,v115\n"
"zc v124,v116,v117\nzc v125,v118,v119\nzp s90,s89,8192\nzs v[122:125],v5,s[44:47],s90 offen offset:0 sc1\nzr v128,a20\nzr v129,a21\nzr v130,a22\nzr v131,a23\n"
"zr v132,a28\nzr v133,a29\nzr v134,a30\nzr v135,a31\nv_pk_mul_f32 v[128:129],v[128:129],v[28:29] op_sel:[0,1]\nv_pk_mul_f32 v[130:131],v[130:131],v[28:29] op_sel:[0,1]\nv_pk_mul_f32 v[132:133],v[132:133],v[28:29] op_sel:[0,1]\nv_pk_mul_f32 v[134:135],v[134:135],v[28:29] op_sel:[0,1]\n"
"zc v138,v128,v129\nzc v139,v130,v131\nzc v140,v132,v133\nzc v141,v134,v135\nzs v[138:141],v5,s[44:47],s90 offen offset:1024 sc1\nzr v112,a36\nzr v113,a37\nzr v114,a38\n"
"zr v115,a39\nzr v116,a44\nzr v117,a45\nzr v118,a46\nzr v119,a47\nv_pk_mul_f32 v[112:113],v[112:113],v[28:29] op_sel:[0,1]\nv_pk_mul_f32 v[114:115],v[114:115],v[28:29] op_sel:[0,1]\nv_pk_mul_f32 v[116:117],v[116:117],v[28:29] op_sel:[0,1]\n"
"v_pk_mul_f32 v[118:119],v[118:119],v[28:29] op_sel:[0,1]\nzc v122,v112,v113\nzc v123,v114,v115\nzc v124,v116,v117\nzc v125,v118,v119\nzs v[122:125],v5,s[44:47],s90 offen offset:2048 sc1\nzr v128,a52\nzr v129,a53\n"
"zr v130,a54\nzr v131,a55\nzr v132,a60\nzr v133,a61\nzr v134,a62\nzr v135,a63\nv_pk_mul_f32 v[128:129],v[128:129],v[28:29] op_sel:[0,1]\nv_pk_mul_f32 v[130:131],v[130:131],v[28:29] op_sel:[0,1]\n"
"v_pk_mul_f32 v[132:133],v[132:133],v[28:29] op_sel:[0,1]\nv_pk_mul_f32 v[134:135],v[134:135],v[28:29] op_sel:[0,1]\nzc v138,v128,v129\nzc v139,v130,v131\nzc v140,v132,v133\nzc v141,v134,v135\nzs v[138:141],v5,s[44:47],s90 offen offset:3072 sc1\nzr v112,a68\n"
"zr v113,a69\nzr v114,a70\nzr v115,a71\nzr v116,a76\nzr v117,a77\nzr v118,a78\nzr v119,a79\nv_pk_mul_f32 v[112:113],v[112:113],v[28:29] op_sel:[0,1]\n"
"v_pk_mul_f32 v[114:115],v[114:115],v[28:29] op_sel:[0,1]\nv_pk_mul_f32 v[116:117],v[116:117],v[28:29] op_sel:[0,1]\nv_pk_mul_f32 v[118:119],v[118:119],v[28:29] op_sel:[0,1]\nzc v122,v112,v113\nzc v123,v114,v115\nzc v124,v116,v117\nzc v125,v118,v119\nzp s90,s89,12288\n"
"zs v[122:125],v5,s[44:47],s90 offen offset:0 sc1\nzr v128,a84\nzr v129,a85\nzr v130,a86\nzr v131,a87\nzr v132,a92\nzr v133,a93\nzr v134,a94\n"
"zr v135,a95\nv_pk_mul_f32 v[128:129],v[128:129],v[28:29] op_sel:[0,1]\nv_pk_mul_f32 v[130:131],v[130:131],v[28:29] op_sel:[0,1]\nv_pk_mul_f32 v[132:133],v[132:133],v[28:29] op_sel:[0,1]\nv_pk_mul_f32 v[134:135],v[134:135],v[28:29] op_sel:[0,1]\nzc v138,v128,v129\nzc v139,v130,v131\nzc v140,v132,v133\n"
"zc v141,v134,v135\nzs v[138:141],v5,s[44:47],s90 offen offset:1024 sc1\nzr v112,a100\nzr v113,a101\nzr v114,a102\nzr v115,a103\nzr v116,a108\nzr v117,a109\n"
"zr v118,a110\nzr v119,a111\nv_pk_mul_f32 v[112:113],v[112:113],v[28:29] op_sel:[0,1]\nv_pk_mul_f32 v[114:115],v[114:115],v[28:29] op_sel:[0,1]\nv_pk_mul_f32 v[116:117],v[116:117],v[28:29] op_sel:[0,1]\nv_pk_mul_f32 v[118:119],v[118:119],v[28:29] op_sel:[0,1]\nzc v122,v112,v113\nzc v123,v114,v115\n"
"zc v124,v116,v117\nzc v125,v118,v119\nzs v[122:125],v5,s[44:47],s90 offen offset:2048 sc1\nzr v128,a116\nzr v129,a117\nzr v130,a118\nzr v131,a119\nzr v132,a124\n"
"zr v133,a125\nzr v134,a126\nzr v135,a127\nv_pk_mul_f32 v[128:129],v[128:129],v[28:29] op_sel:[0,1]\nv_pk_mul_f32 v[130:131],v[130:131],v[28:29] op_sel:[0,1]\nv_pk_mul_f32 v[132:133],v[132:133],v[28:29] op_sel:[0,1]\nv_pk_mul_f32 v[134:135],v[134:135],v[28:29] op_sel:[0,1]\nzc v138,v128,v129\n"
"zc v139,v130,v131\nzc v140,v132,v133\nzc v141,v134,v135\nzs v[138:141],v5,s[44:47],s90 offen offset:3072 sc1\nzh v15,3,v6\nv_mov_b32 v112,v12\nv_mov_b32 v113,v10\nglobal_store_dwordx2 v15,v[112:113],s[4:5] offset:0 sc1\n"
"v_mov_b32 v128,v13\nv_mov_b32 v129,v11\nglobal_store_dwordx2 v15,v[128:129],s[4:5] offset:512 sc1\ns_mov_b32 s8,1\ns_branch L_A_compute\nL_A_merge:\nzv vmcnt(0)\nv_max_f32 v120,v12,v210\n"
"zu v121,v12,v120\nzu v122,v210,v120\nze v121,v121\nze v122,v122\ns_nop 1\nzx v122,v122,v211\nv_fma_f32 v120,v10,v121,v122\nzq v120,v120\n"
"s_nop 1\nzx v28,v121,v120\nzx v30,v122,v120\nv_max_f32 v120,v13,v212\nzu v121,v13,v120\nzu v122,v212,v120\nze v121,v121\nze v122,v122\n"
"s_nop 1\nzx v122,v122,v213\nv_fma_f32 v120,v11,v121,v122\nzq v120,v120\ns_nop 1\nzx v29,v121,v120\nzx v31,v122,v120\ns_lshl_b32 s89,s73,13\n"
"s_sub_u32 s90,16777216,s89\nzp s89,s89,58720256\nzp s40,s94,s89\ns_addc_u32 s41,s95,0\ns_and_b32 s41,s41,0xffff\ns_mov_b32 s42,s90\ns_mov_b32 s43,0x20000\ns_lshl_b32 s89,s88,3\n"
"s_lshl_b32 s90,s23,1\nzp s89,s89,s90\ns_lshl_b32 s89,s89,17\nzr v112,a0\nzr v113,a1\nzr v114,a2\nzr v115,a3\nzr v116,a8\n"
"zr v117,a9\nzr v118,a10\nzr v119,a11\nv_pk_mul_f32 v[112:113],v[112:113],v[28:29] op_sel_hi:[1,0]\nv_pk_mul_f32 v[114:115],v[114:115],v[28:29] op_sel_hi:[1,0]\nv_pk_mul_f32 v[116:117],v[116:117],v[28:29] op_sel_hi:[1,0]\nv_pk_mul_f32 v[118:119],v[118:119],v[28:29] op_sel_hi:[1,0]\nzh v120,16,v144\n"
"v_and_b32 v121,0xffff0000,v144\nv_pk_fma_f32 v[112:113],v[120:121],v[30:31],v[112:113] op_sel_hi:[1,0,1]\nzh v120,16,v145\nv_and_b32 v121,0xffff0000,v145\nv_pk_fma_f32 v[114:115],v[120:121],v[30:31],v[114:115] op_sel_hi:[1,0,1]\nzh v120,16,v146\nv_and_b32 v121,0xffff0000,v146\nv_pk_fma_f32 v[116:117],v[120:121],v[30:31],v[116:117] op_sel_hi:[1,0,1]\n"
"zh v120,16,v147\nv_and_b32 v121,0xffff0000,v147\nv_pk_fma_f32 v[118:119],v[120:121],v[30:31],v[118:119] op_sel_hi:[1,0,1]\nzc v122,v112,v113\nzc v123,v114,v115\nzc v124,v116,v117\nzc v125,v118,v119\nzp s90,s89,0\n"
"zs v[122:125],v5,s[40:43],s90 offen offset:0 sc1\nzr v128,a16\nzr v129,a17\nzr v130,a18\nzr v131,a19\nzr v132,a24\nzr v133,a25\nzr v134,a26\n"
"zr v135,a27\nv_pk_mul_f32 v[128:129],v[128:129],v[28:29] op_sel_hi:[1,0]\nv_pk_mul_f32 v[130:131],v[130:131],v[28:29] op_sel_hi:[1,0]\nv_pk_mul_f32 v[132:133],v[132:133],v[28:29] op_sel_hi:[1,0]\nv_pk_mul_f32 v[134:135],v[134:135],v[28:29] op_sel_hi:[1,0]\nzh v136,16,v148\nv_and_b32 v137,0xffff0000,v148\nv_pk_fma_f32 v[128:129],v[136:137],v[30:31],v[128:129] op_sel_hi:[1,0,1]\n"
"zh v136,16,v149\nv_and_b32 v137,0xffff0000,v149\nv_pk_fma_f32 v[130:131],v[136:137],v[30:31],v[130:131] op_sel_hi:[1,0,1]\nzh v136,16,v150\nv_and_b32 v137,0xffff0000,v150\nv_pk_fma_f32 v[132:133],v[136:137],v[30:31],v[132:133] op_sel_hi:[1,0,1]\nzh v136,16,v151\nv_and_b32 v137,0xffff0000,v151\n"
"v_pk_fma_f32 v[134:135],v[136:137],v[30:31],v[134:135] op_sel_hi:[1,0,1]\nzc v138,v128,v129\nzc v139,v130,v131\nzc v140,v132,v133\nzc v141,v134,v135\nzs v[138:141],v5,s[40:43],s90 offen offset:1024 sc1\nzr v112,a32\nzr v113,a33\n"
"zr v114,a34\nzr v115,a35\nzr v116,a40\nzr v117,a41\nzr v118,a42\nzr v119,a43\nv_pk_mul_f32 v[112:113],v[112:113],v[28:29] op_sel_hi:[1,0]\nv_pk_mul_f32 v[114:115],v[114:115],v[28:29] op_sel_hi:[1,0]\n"
"v_pk_mul_f32 v[116:117],v[116:117],v[28:29] op_sel_hi:[1,0]\nv_pk_mul_f32 v[118:119],v[118:119],v[28:29] op_sel_hi:[1,0]\nzh v120,16,v152\nv_and_b32 v121,0xffff0000,v152\nv_pk_fma_f32 v[112:113],v[120:121],v[30:31],v[112:113] op_sel_hi:[1,0,1]\nzh v120,16,v153\nv_and_b32 v121,0xffff0000,v153\nv_pk_fma_f32 v[114:115],v[120:121],v[30:31],v[114:115] op_sel_hi:[1,0,1]\n"
"zh v120,16,v154\nv_and_b32 v121,0xffff0000,v154\nv_pk_fma_f32 v[116:117],v[120:121],v[30:31],v[116:117] op_sel_hi:[1,0,1]\nzh v120,16,v155\nv_and_b32 v121,0xffff0000,v155\nv_pk_fma_f32 v[118:119],v[120:121],v[30:31],v[118:119] op_sel_hi:[1,0,1]\nzc v122,v112,v113\nzc v123,v114,v115\n"
"zc v124,v116,v117\nzc v125,v118,v119\nzs v[122:125],v5,s[40:43],s90 offen offset:2048 sc1\nzr v128,a48\nzr v129,a49\nzr v130,a50\nzr v131,a51\nzr v132,a56\n"
"zr v133,a57\nzr v134,a58\nzr v135,a59\nv_pk_mul_f32 v[128:129],v[128:129],v[28:29] op_sel_hi:[1,0]\nv_pk_mul_f32 v[130:131],v[130:131],v[28:29] op_sel_hi:[1,0]\nv_pk_mul_f32 v[132:133],v[132:133],v[28:29] op_sel_hi:[1,0]\nv_pk_mul_f32 v[134:135],v[134:135],v[28:29] op_sel_hi:[1,0]\nzh v136,16,v156\n"
"v_and_b32 v137,0xffff0000,v156\nv_pk_fma_f32 v[128:129],v[136:137],v[30:31],v[128:129] op_sel_hi:[1,0,1]\nzh v136,16,v157\nv_and_b32 v137,0xffff0000,v157\nv_pk_fma_f32 v[130:131],v[136:137],v[30:31],v[130:131] op_sel_hi:[1,0,1]\nzh v136,16,v158\nv_and_b32 v137,0xffff0000,v158\nv_pk_fma_f32 v[132:133],v[136:137],v[30:31],v[132:133] op_sel_hi:[1,0,1]\n"
"zh v136,16,v159\nv_and_b32 v137,0xffff0000,v159\nv_pk_fma_f32 v[134:135],v[136:137],v[30:31],v[134:135] op_sel_hi:[1,0,1]\nzc v138,v128,v129\nzc v139,v130,v131\nzc v140,v132,v133\nzc v141,v134,v135\nzs v[138:141],v5,s[40:43],s90 offen offset:3072 sc1\n"
"zr v112,a64\nzr v113,a65\nzr v114,a66\nzr v115,a67\nzr v116,a72\nzr v117,a73\nzr v118,a74\nzr v119,a75\n"
"v_pk_mul_f32 v[112:113],v[112:113],v[28:29] op_sel_hi:[1,0]\nv_pk_mul_f32 v[114:115],v[114:115],v[28:29] op_sel_hi:[1,0]\nv_pk_mul_f32 v[116:117],v[116:117],v[28:29] op_sel_hi:[1,0]\nv_pk_mul_f32 v[118:119],v[118:119],v[28:29] op_sel_hi:[1,0]\nzh v120,16,v160\nv_and_b32 v121,0xffff0000,v160\nv_pk_fma_f32 v[112:113],v[120:121],v[30:31],v[112:113] op_sel_hi:[1,0,1]\nzh v120,16,v161\n"
"v_and_b32 v121,0xffff0000,v161\nv_pk_fma_f32 v[114:115],v[120:121],v[30:31],v[114:115] op_sel_hi:[1,0,1]\nzh v120,16,v162\nv_and_b32 v121,0xffff0000,v162\nv_pk_fma_f32 v[116:117],v[120:121],v[30:31],v[116:117] op_sel_hi:[1,0,1]\nzh v120,16,v163\nv_and_b32 v121,0xffff0000,v163\nv_pk_fma_f32 v[118:119],v[120:121],v[30:31],v[118:119] op_sel_hi:[1,0,1]\n"
"zc v122,v112,v113\nzc v123,v114,v115\nzc v124,v116,v117\nzc v125,v118,v119\nzp s90,s89,4096\nzs v[122:125],v5,s[40:43],s90 offen offset:0 sc1\nzr v128,a80\nzr v129,a81\n"
"zr v130,a82\nzr v131,a83\nzr v132,a88\nzr v133,a89\nzr v134,a90\nzr v135,a91\nv_pk_mul_f32 v[128:129],v[128:129],v[28:29] op_sel_hi:[1,0]\nv_pk_mul_f32 v[130:131],v[130:131],v[28:29] op_sel_hi:[1,0]\n"
"v_pk_mul_f32 v[132:133],v[132:133],v[28:29] op_sel_hi:[1,0]\nv_pk_mul_f32 v[134:135],v[134:135],v[28:29] op_sel_hi:[1,0]\nzh v136,16,v164\nv_and_b32 v137,0xffff0000,v164\nv_pk_fma_f32 v[128:129],v[136:137],v[30:31],v[128:129] op_sel_hi:[1,0,1]\nzh v136,16,v165\nv_and_b32 v137,0xffff0000,v165\nv_pk_fma_f32 v[130:131],v[136:137],v[30:31],v[130:131] op_sel_hi:[1,0,1]\n"
"zh v136,16,v166\nv_and_b32 v137,0xffff0000,v166\nv_pk_fma_f32 v[132:133],v[136:137],v[30:31],v[132:133] op_sel_hi:[1,0,1]\nzh v136,16,v167\nv_and_b32 v137,0xffff0000,v167\nv_pk_fma_f32 v[134:135],v[136:137],v[30:31],v[134:135] op_sel_hi:[1,0,1]\nzc v138,v128,v129\nzc v139,v130,v131\n"
"zc v140,v132,v133\nzc v141,v134,v135\nzs v[138:141],v5,s[40:43],s90 offen offset:1024 sc1\nzr v112,a96\nzr v113,a97\nzr v114,a98\nzr v115,a99\nzr v116,a104\n"
"zr v117,a105\nzr v118,a106\nzr v119,a107\nv_pk_mul_f32 v[112:113],v[112:113],v[28:29] op_sel_hi:[1,0]\nv_pk_mul_f32 v[114:115],v[114:115],v[28:29] op_sel_hi:[1,0]\nv_pk_mul_f32 v[116:117],v[116:117],v[28:29] op_sel_hi:[1,0]\nv_pk_mul_f32 v[118:119],v[118:119],v[28:29] op_sel_hi:[1,0]\nzh v120,16,v168\n"
"v_and_b32 v121,0xffff0000,v168\nv_pk_fma_f32 v[112:113],v[120:121],v[30:31],v[112:113] op_sel_hi:[1,0,1]\nzh v120,16,v169\nv_and_b32 v121,0xffff0000,v169\nv_pk_fma_f32 v[114:115],v[120:121],v[30:31],v[114:115] op_sel_hi:[1,0,1]\nzh v120,16,v170\nv_and_b32 v121,0xffff0000,v170\nv_pk_fma_f32 v[116:117],v[120:121],v[30:31],v[116:117] op_sel_hi:[1,0,1]\n"
"zh v120,16,v171\nv_and_b32 v121,0xffff0000,v171\nv_pk_fma_f32 v[118:119],v[120:121],v[30:31],v[118:119] op_sel_hi:[1,0,1]\nzc v122,v112,v113\nzc v123,v114,v115\nzc v124,v116,v117\nzc v125,v118,v119\nzs v[122:125],v5,s[40:43],s90 offen offset:2048 sc1\n"
"zr v128,a112\nzr v129,a113\nzr v130,a114\nzr v131,a115\nzr v132,a120\nzr v133,a121\nzr v134,a122\nzr v135,a123\n"
"v_pk_mul_f32 v[128:129],v[128:129],v[28:29] op_sel_hi:[1,0]\nv_pk_mul_f32 v[130:131],v[130:131],v[28:29] op_sel_hi:[1,0]\nv_pk_mul_f32 v[132:133],v[132:133],v[28:29] op_sel_hi:[1,0]\nv_pk_mul_f32 v[134:135],v[134:135],v[28:29] op_sel_hi:[1,0]\nzh v136,16,v172\nv_and_b32 v137,0xffff0000,v172\nv_pk_fma_f32 v[128:129],v[136:137],v[30:31],v[128:129] op_sel_hi:[1,0,1]\nzh v136,16,v173\n"
"v_and_b32 v137,0xffff0000,v173\nv_pk_fma_f32 v[130:131],v[136:137],v[30:31],v[130:131] op_sel_hi:[1,0,1]\nzh v136,16,v174\nv_and_b32 v137,0xffff0000,v174\nv_pk_fma_f32 v[132:133],v[136:137],v[30:31],v[132:133] op_sel_hi:[1,0,1]\nzh v136,16,v175\nv_and_b32 v137,0xffff0000,v175\nv_pk_fma_f32 v[134:135],v[136:137],v[30:31],v[134:135] op_sel_hi:[1,0,1]\n"
"zc v138,v128,v129\nzc v139,v130,v131\nzc v140,v132,v133\nzc v141,v134,v135\nzs v[138:141],v5,s[40:43],s90 offen offset:3072 sc1\nzr v112,a4\nzr v113,a5\nzr v114,a6\n"
"zr v115,a7\nzr v116,a12\nzr v117,a13\nzr v118,a14\nzr v119,a15\nv_pk_mul_f32 v[112:113],v[112:113],v[28:29] op_sel:[0,1]\nv_pk_mul_f32 v[114:115],v[114:115],v[28:29] op_sel:[0,1]\nv_pk_mul_f32 v[116:117],v[116:117],v[28:29] op_sel:[0,1]\n"
"v_pk_mul_f32 v[118:119],v[118:119],v[28:29] op_sel:[0,1]\nzh v120,16,v176\nv_and_b32 v121,0xffff0000,v176\nv_pk_fma_f32 v[112:113],v[120:121],v[30:31],v[112:113] op_sel:[0,1,0]\nzh v120,16,v177\nv_and_b32 v121,0xffff0000,v177\nv_pk_fma_f32 v[114:115],v[120:121],v[30:31],v[114:115] op_sel:[0,1,0]\nzh v120,16,v178\n"
"v_and_b32 v121,0xffff0000,v178\nv_pk_fma_f32 v[116:117],v[120:121],v[30:31],v[116:117] op_sel:[0,1,0]\nzh v120,16,v179\nv_and_b32 v121,0xffff0000,v179\nv_pk_fma_f32 v[118:119],v[120:121],v[30:31],v[118:119] op_sel:[0,1,0]\nzc v122,v112,v113\nzc v123,v114,v115\nzc v124,v116,v117\n"
"zc v125,v118,v119\nzp s90,s89,131072\nzs v[122:125],v5,s[40:43],s90 offen offset:0 sc1\nzr v128,a20\nzr v129,a21\nzr v130,a22\nzr v131,a23\nzr v132,a28\n"
"zr v133,a29\nzr v134,a30\nzr v135,a31\nv_pk_mul_f32 v[128:129],v[128:129],v[28:29] op_sel:[0,1]\nv_pk_mul_f32 v[130:131],v[130:131],v[28:29] op_sel:[0,1]\nv_pk_mul_f32 v[132:133],v[132:133],v[28:29] op_sel:[0,1]\nv_pk_mul_f32 v[134:135],v[134:135],v[28:29] op_sel:[0,1]\nzh v136,16,v180\n"
"v_and_b32 v137,0xffff0000,v180\nv_pk_fma_f32 v[128:129],v[136:137],v[30:31],v[128:129] op_sel:[0,1,0]\nzh v136,16,v181\nv_and_b32 v137,0xffff0000,v181\nv_pk_fma_f32 v[130:131],v[136:137],v[30:31],v[130:131] op_sel:[0,1,0]\nzh v136,16,v182\nv_and_b32 v137,0xffff0000,v182\nv_pk_fma_f32 v[132:133],v[136:137],v[30:31],v[132:133] op_sel:[0,1,0]\n"
"zh v136,16,v183\nv_and_b32 v137,0xffff0000,v183\nv_pk_fma_f32 v[134:135],v[136:137],v[30:31],v[134:135] op_sel:[0,1,0]\nzc v138,v128,v129\nzc v139,v130,v131\nzc v140,v132,v133\nzc v141,v134,v135\nzs v[138:141],v5,s[40:43],s90 offen offset:1024 sc1\n"
"zr v112,a36\nzr v113,a37\nzr v114,a38\nzr v115,a39\nzr v116,a44\nzr v117,a45\nzr v118,a46\nzr v119,a47\n"
"v_pk_mul_f32 v[112:113],v[112:113],v[28:29] op_sel:[0,1]\nv_pk_mul_f32 v[114:115],v[114:115],v[28:29] op_sel:[0,1]\nv_pk_mul_f32 v[116:117],v[116:117],v[28:29] op_sel:[0,1]\nv_pk_mul_f32 v[118:119],v[118:119],v[28:29] op_sel:[0,1]\nzh v120,16,v184\nv_and_b32 v121,0xffff0000,v184\nv_pk_fma_f32 v[112:113],v[120:121],v[30:31],v[112:113] op_sel:[0,1,0]\nzh v120,16,v185\n"
"v_and_b32 v121,0xffff0000,v185\nv_pk_fma_f32 v[114:115],v[120:121],v[30:31],v[114:115] op_sel:[0,1,0]\nzh v120,16,v186\nv_and_b32 v121,0xffff0000,v186\nv_pk_fma_f32 v[116:117],v[120:121],v[30:31],v[116:117] op_sel:[0,1,0]\nzh v120,16,v187\nv_and_b32 v121,0xffff0000,v187\nv_pk_fma_f32 v[118:119],v[120:121],v[30:31],v[118:119] op_sel:[0,1,0]\n"
"zc v122,v112,v113\nzc v123,v114,v115\nzc v124,v116,v117\nzc v125,v118,v119\nzs v[122:125],v5,s[40:43],s90 offen offset:2048 sc1\nzr v128,a52\nzr v129,a53\nzr v130,a54\n"
"zr v131,a55\nzr v132,a60\nzr v133,a61\nzr v134,a62\nzr v135,a63\nv_pk_mul_f32 v[128:129],v[128:129],v[28:29] op_sel:[0,1]\nv_pk_mul_f32 v[130:131],v[130:131],v[28:29] op_sel:[0,1]\nv_pk_mul_f32 v[132:133],v[132:133],v[28:29] op_sel:[0,1]\n"
"v_pk_mul_f32 v[134:135],v[134:135],v[28:29] op_sel:[0,1]\nzh v136,16,v188\nv_and_b32 v137,0xffff0000,v188\nv_pk_fma_f32 v[128:129],v[136:137],v[30:31],v[128:129] op_sel:[0,1,0]\nzh v136,16,v189\nv_and_b32 v137,0xffff0000,v189\nv_pk_fma_f32 v[130:131],v[136:137],v[30:31],v[130:131] op_sel:[0,1,0]\nzh v136,16,v190\n"
"v_and_b32 v137,0xffff0000,v190\nv_pk_fma_f32 v[132:133],v[136:137],v[30:31],v[132:133] op_sel:[0,1,0]\nzh v136,16,v191\nv_and_b32 v137,0xffff0000,v191\nv_pk_fma_f32 v[134:135],v[136:137],v[30:31],v[134:135] op_sel:[0,1,0]\nzc v138,v128,v129\nzc v139,v130,v131\nzc v140,v132,v133\n"
"zc v141,v134,v135\nzs v[138:141],v5,s[40:43],s90 offen offset:3072 sc1\nzr v112,a68\nzr v113,a69\nzr v114,a70\nzr v115,a71\nzr v116,a76\nzr v117,a77\n"
"zr v118,a78\nzr v119,a79\nv_pk_mul_f32 v[112:113],v[112:113],v[28:29] op_sel:[0,1]\nv_pk_mul_f32 v[114:115],v[114:115],v[28:29] op_sel:[0,1]\nv_pk_mul_f32 v[116:117],v[116:117],v[28:29] op_sel:[0,1]\nv_pk_mul_f32 v[118:119],v[118:119],v[28:29] op_sel:[0,1]\nzh v120,16,v192\nv_and_b32 v121,0xffff0000,v192\n"
"v_pk_fma_f32 v[112:113],v[120:121],v[30:31],v[112:113] op_sel:[0,1,0]\nzh v120,16,v193\nv_and_b32 v121,0xffff0000,v193\nv_pk_fma_f32 v[114:115],v[120:121],v[30:31],v[114:115] op_sel:[0,1,0]\nzh v120,16,v194\nv_and_b32 v121,0xffff0000,v194\nv_pk_fma_f32 v[116:117],v[120:121],v[30:31],v[116:117] op_sel:[0,1,0]\nzh v120,16,v195\n"
"v_and_b32 v121,0xffff0000,v195\nv_pk_fma_f32 v[118:119],v[120:121],v[30:31],v[118:119] op_sel:[0,1,0]\nzc v122,v112,v113\nzc v123,v114,v115\nzc v124,v116,v117\nzc v125,v118,v119\nzp s90,s89,135168\nzs v[122:125],v5,s[40:43],s90 offen offset:0 sc1\n"
"zr v128,a84\nzr v129,a85\nzr v130,a86\nzr v131,a87\nzr v132,a92\nzr v133,a93\nzr v134,a94\nzr v135,a95\n"
"v_pk_mul_f32 v[128:129],v[128:129],v[28:29] op_sel:[0,1]\nv_pk_mul_f32 v[130:131],v[130:131],v[28:29] op_sel:[0,1]\nv_pk_mul_f32 v[132:133],v[132:133],v[28:29] op_sel:[0,1]\nv_pk_mul_f32 v[134:135],v[134:135],v[28:29] op_sel:[0,1]\nzh v136,16,v196\nv_and_b32 v137,0xffff0000,v196\nv_pk_fma_f32 v[128:129],v[136:137],v[30:31],v[128:129] op_sel:[0,1,0]\nzh v136,16,v197\n"
"v_and_b32 v137,0xffff0000,v197\nv_pk_fma_f32 v[130:131],v[136:137],v[30:31],v[130:131] op_sel:[0,1,0]\nzh v136,16,v198\nv_and_b32 v137,0xffff0000,v198\nv_pk_fma_f32 v[132:133],v[136:137],v[30:31],v[132:133] op_sel:[0,1,0]\nzh v136,16,v199\nv_and_b32 v137,0xffff0000,v199\nv_pk_fma_f32 v[134:135],v[136:137],v[30:31],v[134:135] op_sel:[0,1,0]\n"
"zc v138,v128,v129\nzc v139,v130,v131\nzc v140,v132,v133\nzc v141,v134,v135\nzs v[138:141],v5,s[40:43],s90 offen offset:1024 sc1\nzr v112,a100\nzr v113,a101\nzr v114,a102\n"
"zr v115,a103\nzr v116,a108\nzr v117,a109\nzr v118,a110\nzr v119,a111\nv_pk_mul_f32 v[112:113],v[112:113],v[28:29] op_sel:[0,1]\nv_pk_mul_f32 v[114:115],v[114:115],v[28:29] op_sel:[0,1]\nv_pk_mul_f32 v[116:117],v[116:117],v[28:29] op_sel:[0,1]\n"
"v_pk_mul_f32 v[118:119],v[118:119],v[28:29] op_sel:[0,1]\nzh v120,16,v200\nv_and_b32 v121,0xffff0000,v200\nv_pk_fma_f32 v[112:113],v[120:121],v[30:31],v[112:113] op_sel:[0,1,0]\nzh v120,16,v201\nv_and_b32 v121,0xffff0000,v201\nv_pk_fma_f32 v[114:115],v[120:121],v[30:31],v[114:115] op_sel:[0,1,0]\nzh v120,16,v202\n"
"v_and_b32 v121,0xffff0000,v202\nv_pk_fma_f32 v[116:117],v[120:121],v[30:31],v[116:117] op_sel:[0,1,0]\nzh v120,16,v203\nv_and_b32 v121,0xffff0000,v203\nv_pk_fma_f32 v[118:119],v[120:121],v[30:31],v[118:119] op_sel:[0,1,0]\nzc v122,v112,v113\nzc v123,v114,v115\nzc v124,v116,v117\n"
"zc v125,v118,v119\nzs v[122:125],v5,s[40:43],s90 offen offset:2048 sc1\nzr v128,a116\nzr v129,a117\nzr v130,a118\nzr v131,a119\nzr v132,a124\nzr v133,a125\n"
"zr v134,a126\nzr v135,a127\nv_pk_mul_f32 v[128:129],v[128:129],v[28:29] op_sel:[0,1]\nv_pk_mul_f32 v[130:131],v[130:131],v[28:29] op_sel:[0,1]\nv_pk_mul_f32 v[132:133],v[132:133],v[28:29] op_sel:[0,1]\nv_pk_mul_f32 v[134:135],v[134:135],v[28:29] op_sel:[0,1]\nzh v136,16,v204\nv_and_b32 v137,0xffff0000,v204\n"
"v_pk_fma_f32 v[128:129],v[136:137],v[30:31],v[128:129] op_sel:[0,1,0]\nzh v136,16,v205\nv_and_b32 v137,0xffff0000,v205\nv_pk_fma_f32 v[130:131],v[136:137],v[30:31],v[130:131] op_sel:[0,1,0]\nzh v136,16,v206\nv_and_b32 v137,0xffff0000,v206\nv_pk_fma_f32 v[132:133],v[136:137],v[30:31],v[132:133] op_sel:[0,1,0]\nzh v136,16,v207\n"
"v_and_b32 v137,0xffff0000,v207\nv_pk_fma_f32 v[134:135],v[136:137],v[30:31],v[134:135] op_sel:[0,1,0]\nzc v138,v128,v129\nzc v139,v130,v131\nzc v140,v132,v133\nzc v141,v134,v135\nzs v[138:141],v5,s[40:43],s90 offen offset:3072 sc1\nL_A_fin:\n"
"s_lshl_b32 s89,s88,2\nzp s89,s89,s23\ns_lshl_b32 s89,s89,4\nzp s89,s89,s73\ns_lshl_b32 s89,s89,2\nzp s89,s89,16384\nv_mov_b32 v15,s89\nv_mov_b32 v14,s34\n"
"s_load_dwordx2 s[48:49],s[20:21],40\ns_load_dwordx2 s[50:51],s[20:21],64\ns_and_b32 s60,s22,7\ns_lshr_b32 s61,s22,3\ns_lshr_b32 s62,s60,1\ns_and_b32 s63,s61,3\ns_lshr_b32 s64,s61,2\ns_mul_i32 s64,s64,184\n"
"s_mul_i32 s65,s23,46\nzp s64,s64,s65\ns_mul_i32 s65,s63,224\ns_and_b32 s66,s63,1\ns_lshl_b32 s66,s66,5\ns_sub_u32 s65,s65,s66\ns_mul_i32 s66,s62,401408\nzp s66,s66,s65\n"
"s_mul_i32 s67,s62,917504\nzp s67,s67,s65\ns_sub_u32 s68,892,s65\nzv lgkmcnt(0)\nzp s50,s50,s66\ns_addc_u32 s51,s51,0\nzp s48,s48,s67\ns_addc_u32 s49,s49,0\n"
"s_sub_u32 s48,s48,401408\ns_subb_u32 s49,s49,0\nv_add_u32 v232,0,v6\nv_min_u32 v232,137,v232\nv_mul_u32_u24 v233,171,v232\nv_lshrrev_b32 v233,9,v233\nv_mul_u32_u24 v234,3,v233\nv_sub_u32 v234,v232,v234\n"
"v_add_u32 v233,s64,v233\nzh v234,7,v234\nv_min_u32 v234,s68,v234\nv_mul_u32_u24 v235,896,v233\nv_add_u32 v235,v235,v234\nv_mov_b32 v236,s50\nv_mov_b32 v237,s51\nv_mov_b32 v238,s48\n"
"v_mov_b32 v239,s49\nv_cmp_gt_u32 vcc,448,v233\ns_nop 1\nzk v236,v238,v236,vcc\nzk v237,v239,v237,vcc\nv_add_co_u32 v236,vcc,v236,v235\ns_nop 1\nv_addc_co_u32 v237,vcc,0,v237,vcc\n"
"global_load_dword v240,v[236:237],off\nv_add_u32 v232,64,v6\nv_min_u32 v232,137,v232\nv_mul_u32_u24 v233,171,v232\nv_lshrrev_b32 v233,9,v233\nv_mul_u32_u24 v234,3,v233\nv_sub_u32 v234,v232,v234\nv_add_u32 v233,s64,v233\n"
"zh v234,7,v234\nv_min_u32 v234,s68,v234\nv_mul_u32_u24 v235,896,v233\nv_add_u32 v235,v235,v234\nv_mov_b32 v236,s50\nv_mov_b32 v237,s51\nv_mov_b32 v238,s48\nv_mov_b32 v239,s49\n"
"v_cmp_gt_u32 vcc,448,v233\ns_nop 1\nzk v236,v238,v236,vcc\nzk v237,v239,v237,vcc\nv_add_co_u32 v236,vcc,v236,v235\ns_nop 1\nv_addc_co_u32 v237,vcc,0,v237,vcc\nglobal_load_dword v240,v[236:237],off\n"
"v_add_u32 v232,128,v6\nv_min_u32 v232,137,v232\nv_mul_u32_u24 v233,171,v232\nv_lshrrev_b32 v233,9,v233\nv_mul_u32_u24 v234,3,v233\nv_sub_u32 v234,v232,v234\nv_add_u32 v233,s64,v233\nzh v234,7,v234\n"
"v_min_u32 v234,s68,v234\nv_mul_u32_u24 v235,896,v233\nv_add_u32 v235,v235,v234\nv_mov_b32 v236,s50\nv_mov_b32 v237,s51\nv_mov_b32 v238,s48\nv_mov_b32 v239,s49\nv_cmp_gt_u32 vcc,448,v233\n"
"s_nop 1\nzk v236,v238,v236,vcc\nzk v237,v239,v237,vcc\nv_add_co_u32 v236,vcc,v236,v235\ns_nop 1\nv_addc_co_u32 v237,vcc,0,v237,vcc\nglobal_load_dword v240,v[236:237],off\ns_load_dwordx2 s[4:5],s[20:21],0\n"
"s_load_dwordx2 s[6:7],s[20:21],40\ns_load_dwordx4 s[8:11],s[20:21],64\ns_and_b32 s76,s22,7\ns_lshr_b32 s77,s22,3\ns_lshr_b32 s62,s76,1\ns_and_b32 s61,s77,3\ns_and_b32 s60,s76,1\ns_lshl_b32 s60,s60,3\n"
"s_lshr_b32 s78,s77,2\nzp s60,s60,s78\nzh v16,4,v6\nv_lshrrev_b32 v232,4,v6\nv_and_b32 v233,15,v6\nzh v234,1,v232\nzo v233,v233,v234\nv_mul_u32_u24 v232,896,v232\n"
"s_mul_i32 s76,s23,14336\nv_add_u32 v232,s76,v232\nzo v234,0,v233\nv_lshl_add_u32 v234,v234,4,v232\nv_add_u32 v17,0,v234\nzo v234,8,v233\nv_lshl_add_u32 v234,v234,4,v232\nv_add_u32 v18,2560,v234\n"
"zo v234,0,v233\nv_lshl_add_u32 v234,v234,4,v232\nv_add_u32 v19,5120,v234\nzo v234,8,v233\nv_lshl_add_u32 v234,v234,4,v232\nv_add_u32 v20,7680,v234\ns_lshr_b32 s76,s60,1\ns_lshl_b32 s76,s76,6\n"
"zp s76,s76,8192\nzh v233,2,v6\nv_min_u32 v233,52,v233\nv_add_u32 v233,s76,v233\nzv vmcnt(3)\nglobal_store_dword v15,v14,s[94:95] sc1\ns_cmp_eq_u32 s23,0\ns_cbranch_scc0 L_wwb_h\n"
"s_mov_b32 s77,0\nL_ww_h:\nglobal_load_dword v232,v233,s[94:95] sc1\nzv vmcnt(0)\nv_cmp_ne_u32 vcc,s34,v232\ns_nop 2\ns_and_b32 vcc_lo,vcc_lo,0x3fff\ns_cmp_eq_u32 vcc_lo,0\n"
"s_cbranch_scc1 L_wwb_h\nzp s77,s77,1\ns_cmp_lt_u32 s77,300000\ns_cbranch_scc0 L_wwb_h\ns_sleep 2\ns_branch L_ww_h\nL_wwb_h:\ns_barrier\n"
"zv lgkmcnt(0)\ns_lshl_b32 s76,s60,3\ns_lshl_b32 s77,s23,1\nzp s76,s76,s77\ns_mul_i32 s77,s76,57344\ns_mul_i32 s78,s62,14336\nzp s77,s77,s78\nzp s77,s77,75497472\n"
"zp s24,s94,s77\ns_addc_u32 s25,s95,0\ns_and_b32 s25,s25,0xffff\ns_mov_b32 s26,71680\ns_mov_b32 s27,0x20000\ns_lshl_b32 s77,s76,17\ns_lshl_b32 s78,s62,15\nzp s77,s77,s78\n"
"zp s77,s77,58720256\nzp s44,s94,s77\ns_addc_u32 s45,s95,0\ns_and_b32 s45,s45,0xffff\ns_mov_b32 s46,163840\ns_mov_b32 s47,0x20000\ns_mul_i32 s77,s61,224\ns_and_b32 s80,s61,1\n"
"s_lshl_b32 s81,s80,5\ns_sub_u32 s77,s77,s81\ns_mul_i32 s78,s62,401408\nzp s78,s78,s77\ns_sub_u32 s79,1605632,s78\nzp s28,s8,s78\ns_addc_u32 s29,s9,0\ns_and_b32 s29,s29,0xffff\n"
"s_mov_b32 s30,s79\ns_mov_b32 s31,0x20000\ns_mul_i32 s78,s62,917504\nzp s78,s78,s77\ns_sub_u32 s79,3670016,s78\nzp s48,s6,s78\ns_addc_u32 s49,s7,0\ns_and_b32 s49,s49,0xffff\n"
"s_mov_b32 s50,s79\ns_mov_b32 s51,0x20000\ns_mov_b32 s63,0\ns_mov_b32 s84,57344\ns_mov_b32 s64,0\ns_lshl_b32 s65,s23,12\nzp m0,s65,0\ns_nop 0\n"
"zl v17,s[28:31],s64 offen offset:0 lds\nzl v18,s[28:31],s64 offen offset:1024 lds\nzl v19,s[28:31],s64 offen offset:2048 lds\nzl v20,s[28:31],s64 offen offset:3072 lds\nzp s64,s64,57344\nzl v[32:35],v16,s[24:27],s63 offen offset:0 sc1\nzl v[36:39],v16,s[24:27],s84 offen offset:0 sc1\nzl v[40:43],v16,s[24:27],s63 offen offset:1024 sc1\n"
"zl v[44:47],v16,s[24:27],s84 offen offset:1024 sc1\nzp s63,s63,2048\nzp s84,s84,2048\nzp m0,s65,16384\ns_nop 0\nzl v17,s[28:31],s64 offen offset:0 lds\nzl v18,s[28:31],s64 offen offset:1024 lds\nzl v19,s[28:31],s64 offen offset:2048 lds\n"
"zl v20,s[28:31],s64 offen offset:3072 lds\nzp s64,s64,57344\nzl v[48:51],v16,s[24:27],s63 offen offset:0 sc1\nzl v[52:55],v16,s[24:27],s84 offen offset:0 sc1\nzl v[56:59],v16,s[24:27],s63 offen offset:1024 sc1\nzl v[60:63],v16,s[24:27],s84 offen offset:1024 sc1\nzp s63,s63,2048\nzp s84,s84,2048\n"
"zp m0,s65,32768\ns_nop 0\nzl v17,s[28:31],s64 offen offset:0 lds\nzl v18,s[28:31],s64 offen offset:1024 lds\nzl v19,s[28:31],s64 offen offset:2048 lds\nzl v20,s[28:31],s64 offen offset:3072 lds\nzp s64,s64,57344\nzl v[64:67],v16,s[24:27],s63 offen offset:0 sc1\n"
"zl v[68:71],v16,s[24:27],s84 offen offset:0 sc1\nzl v[72:75],v16,s[24:27],s63 offen offset:1024 sc1\nzl v[76:79],v16,s[24:27],s84 offen offset:1024 sc1\nzp s63,s63,2048\nzp s84,s84,2048\nv_lshrrev_b32 v232,4,v6\nv_bfe_u32 v233,v6,2,2\nv_lshl_add_u32 v232,v232,2,v233\n"
"v_and_b32 v233,7,v232\nv_and_b32 v234,3,v6\nzh v232,8,v232\nv_lshl_add_u32 v232,v234,3,v232\nzp s81,s80,0\nzo v234,s81,v233\nv_lshl_add_u32 v21,v234,5,v232\nzp s81,s80,1\n"
"zo v234,s81,v233\nv_lshl_add_u32 v22,v234,5,v232\nzp s81,s80,2\nzo v234,s81,v233\nv_lshl_add_u32 v23,v234,5,v232\nzp s81,s80,3\nzo v234,s81,v233\nv_lshl_add_u32 v24,v234,5,v232\n"
"zp s81,s80,4\nzo v234,s81,v233\nv_lshl_add_u32 v25,v234,5,v232\nzp s81,s80,5\nzo v234,s81,v233\nv_lshl_add_u32 v26,v234,5,v232\nzp s81,s80,6\nzo v234,s81,v233\n"
"v_lshl_add_u32 v27,v234,5,v232\nv_add_u32 v216,65536,v21\nv_add_u32 v217,65536,v22\nv_add_u32 v218,65536,v23\nv_add_u32 v219,65536,v24\nv_add_u32 v220,65536,v25\nv_add_u32 v221,65536,v26\nv_add_u32 v222,65536,v27\n"
"s_lshl_b32 s76,s60,2\nzp s76,s76,s61\ns_lshl_b32 s76,s76,2\nzp s76,s76,s23\ns_lshl_b32 s77,s76,16\nzp s77,s77,104857600\nzp s52,s94,s77\ns_addc_u32 s53,s95,0\n"
"s_and_b32 s53,s53,0xffff\ns_mov_b32 s54,65536\ns_mov_b32 s55,0x20000\ns_lshl_b32 s77,s76,4\nzp s77,s77,20480\nzp s68,s94,s77\ns_addc_u32 s69,s95,0\ns_lshl_b32 s76,s60,7\n"
"s_lshl_b32 s77,s23,5\nzp s76,s76,s77\ns_mul_i32 s76,s76,896\ns_mul_i32 s77,s61,224\nzp s76,s76,s77\ns_lshl_b32 s77,s62,6\nzp s76,s76,s77\nv_and_b32 v232,15,v6\n"
"v_mul_u32_u24 v232,896,v232\nv_lshrrev_b32 v233,4,v6\nv_lshl_add_u32 v232,v233,3,v232\nv_add_u32 v14,s76,v232\nzp s36,s4,0\ns_addc_u32 s37,s5,0\ns_and_b32 s37,s37,0xffff\ns_mov_b32 s38,1835008\n"
"s_mov_b32 s39,0x20000\nzp s56,s10,0\ns_addc_u32 s57,s11,0\ns_and_b32 s57,s57,0xffff\ns_mov_b32 s58,1835008\ns_mov_b32 s59,0x20000\nzv vmcnt(16)\ns_barrier\n"
"zt v[160:161],v21 offset:0\nzt v[162:163],v21 offset:4096\nzt v[164:165],v22 offset:0\nzt v[166:167],v22 offset:4096\nzt v[168:169],v23 offset:0\nzt v[170:171],v23 offset:4096\nzt v[172:173],v24 offset:0\nzt v[174:175],v24 offset:4096\n"
"zt v[176:177],v25 offset:0\nzt v[178:179],v25 offset:4096\nzt v[180:181],v26 offset:0\nzt v[182:183],v26 offset:4096\nzt v[184:185],v27 offset:0\nzt v[186:187],v27 offset:4096\nzv lgkmcnt(12)\nzm a[0:3],v[160:163],v[32:35],0\n"
"zp m0,s65,49152\nzt v[188:189],v21 offset:8192\nzt v[190:191],v21 offset:12288\nzt v[192:193],v22 offset:8192\nzm a[28:31],v[160:163],v[36:39],0\nzl v17,s[28:31],s64 offen offset:0 lds\nzv lgkmcnt(13)\nzm a[4:7],v[164:167],v[32:35],0\n"
"zt v[194:195],v22 offset:12288\nzt v[196:197],v23 offset:8192\nzt v[198:199],v23 offset:12288\nzm a[32:35],v[164:167],v[36:39],0\nzl v18,s[28:31],s64 offen offset:1024 lds\nzv lgkmcnt(14)\nzm a[8:11],v[168:171],v[32:35],0\nzt v[200:201],v24 offset:8192\n"
"zt v[202:203],v24 offset:12288\nzm a[36:39],v[168:171],v[36:39],0\nzl v19,s[28:31],s64 offen offset:2048 lds\nzv lgkmcnt(14)\nzm a[12:15],v[172:175],v[32:35],0\nzl v20,s[28:31],s64 offen offset:3072 lds\nzp s64,s64,57344\nzm a[40:43],v[172:175],v[36:39],0\n"
"zp m0,s65,65536\nzt v[204:205],v25 offset:8192\nzt v[206:207],v25 offset:12288\nzv lgkmcnt(14)\nzm a[16:19],v[176:179],v[32:35],0\nzl v17,s[28:31],s64 offen offset:0 lds\nzm a[44:47],v[176:179],v[36:39],0\nzt v[208:209],v26 offset:8192\n"
"zt v[210:211],v26 offset:12288\nzv lgkmcnt(14)\nzm a[20:23],v[180:183],v[32:35],0\nzl v18,s[28:31],s64 offen offset:1024 lds\nzm a[48:51],v[180:183],v[36:39],0\nzl v19,s[28:31],s64 offen offset:2048 lds\nzv lgkmcnt(12)\nzm a[24:27],v[184:187],v[32:35],0\n"
"zt v[212:213],v27 offset:8192\nzt v[214:215],v27 offset:12288\nzm a[52:55],v[184:187],v[36:39],0\nzl v20,s[28:31],s64 offen offset:3072 lds\nzp s64,s64,57344\nzv vmcnt(16)\ns_barrier\nzv lgkmcnt(12)\n"
"zm a[0:3],v[188:191],v[40:43],a[0:3]\nzt v[160:161],v21 offset:16384\nzt v[162:163],v21 offset:20480\nzt v[164:165],v22 offset:16384\nzm a[28:31],v[188:191],v[44:47],a[28:31]\nzl v[80:83],v16,s[24:27],s63 offen offset:0 sc1\nzv lgkmcnt(13)\nzm a[4:7],v[192:195],v[40:43],a[4:7]\n"
"zt v[166:167],v22 offset:20480\nzt v[168:169],v23 offset:16384\nzt v[170:171],v23 offset:20480\nzm a[32:35],v[192:195],v[44:47],a[32:35]\nzl v[84:87],v16,s[24:27],s84 offen offset:0 sc1\nzv lgkmcnt(14)\nzm a[8:11],v[196:199],v[40:43],a[8:11]\nzt v[172:173],v24 offset:16384\n"
"zt v[174:175],v24 offset:20480\nzm a[36:39],v[196:199],v[44:47],a[36:39]\nzl v[88:91],v16,s[24:27],s63 offen offset:1024 sc1\nzv lgkmcnt(14)\nzm a[12:15],v[200:203],v[40:43],a[12:15]\nzl v[92:95],v16,s[24:27],s84 offen offset:1024 sc1\nzp s63,s63,2048\nzp s84,s84,2048\n"
"zm a[40:43],v[200:203],v[44:47],a[40:43]\nzt v[176:177],v25 offset:16384\nzt v[178:179],v25 offset:20480\nzv lgkmcnt(14)\nzm a[16:19],v[204:207],v[40:43],a[16:19]\nzl v[96:99],v16,s[24:27],s63 offen offset:0 sc1\nzm a[44:47],v[204:207],v[44:47],a[44:47]\nzt v[180:181],v26 offset:16384\n"
"zt v[182:183],v26 offset:20480\nzv lgkmcnt(14)\nzm a[20:23],v[208:211],v[40:43],a[20:23]\nzl v[100:103],v16,s[24:27],s84 offen offset:0 sc1\nzm a[48:51],v[208:211],v[44:47],a[48:51]\nzl v[104:107],v16,s[24:27],s63 offen offset:1024 sc1\nzv lgkmcnt(12)\nzm a[24:27],v[212:215],v[40:43],a[24:27]\n"
"zt v[184:185],v27 offset:16384\nzt v[186:187],v27 offset:20480\nzm a[52:55],v[212:215],v[44:47],a[52:55]\nzl v[108:111],v16,s[24:27],s84 offen offset:1024 sc1\nzp s63,s63,2048\nzp s84,s84,2048\nzv lgkmcnt(12)\nzm a[0:3],v[160:163],v[48:51],a[0:3]\n"
"zp m0,s65,81920\nzt v[188:189],v21 offset:24576\nzt v[190:191],v21 offset:28672\nzt v[192:193],v22 offset:24576\nzm a[28:31],v[160:163],v[52:55],a[28:31]\nzl v17,s[28:31],s64 offen offset:0 lds\nzv lgkmcnt(13)\nzm a[4:7],v[164:167],v[48:51],a[4:7]\n"
"zt v[194:195],v22 offset:28672\nzt v[196:197],v23 offset:24576\nzt v[198:199],v23 offset:28672\nzm a[32:35],v[164:167],v[52:55],a[32:35]\nzl v18,s[28:31],s64 offen offset:1024 lds\nzv lgkmcnt(14)\nzm a[8:11],v[168:171],v[48:51],a[8:11]\nzt v[200:201],v24 offset:24576\n"
"zt v[202:203],v24 offset:28672\nzm a[36:39],v[168:171],v[52:55],a[36:39]\nzl v19,s[28:31],s64 offen offset:2048 lds\nzv lgkmcnt(14)\nzm a[12:15],v[172:175],v[48:51],a[12:15]\nzl v20,s[28:31],s64 offen offset:3072 lds\nzp s64,s64,57344\nzm a[40:43],v[172:175],v[52:55],a[40:43]\n"
"zp m0,s65,98304\nzt v[204:205],v25 offset:24576\nzt v[206:207],v25 offset:28672\nzv lgkmcnt(14)\nzm a[16:19],v[176:179],v[48:51],a[16:19]\nzl v17,s[28:31],s64 offen offset:0 lds\nzm a[44:47],v[176:179],v[52:55],a[44:47]\nzt v[208:209],v26 offset:24576\n"
"zt v[210:211],v26 offset:28672\nzv lgkmcnt(14)\nzm a[20:23],v[180:183],v[48:51],a[20:23]\nzl v18,s[28:31],s64 offen offset:1024 lds\nzm a[48:51],v[180:183],v[52:55],a[48:51]\nzl v19,s[28:31],s64 offen offset:2048 lds\nzv lgkmcnt(12)\nzm a[24:27],v[184:187],v[48:51],a[24:27]\n"
"zt v[212:213],v27 offset:24576\nzt v[214:215],v27 offset:28672\nzm a[52:55],v[184:187],v[52:55],a[52:55]\nzl v20,s[28:31],s64 offen offset:3072 lds\nzp s64,s64,57344\nzv vmcnt(24)\ns_barrier\nzv lgkmcnt(12)\n"
"zm a[0:3],v[188:191],v[56:59],a[0:3]\nzt v[160:161],v21 offset:32768\nzt v[162:163],v21 offset:36864\nzt v[164:165],v22 offset:32768\nzm a[28:31],v[188:191],v[60:63],a[28:31]\nzl v[112:115],v16,s[24:27],s63 offen offset:0 sc1\nzv lgkmcnt(13)\nzm a[4:7],v[192:195],v[56:59],a[4:7]\n"
"zt v[166:167],v22 offset:36864\nzt v[168:169],v23 offset:32768\nzt v[170:171],v23 offset:36864\nzm a[32:35],v[192:195],v[60:63],a[32:35]\nzl v[116:119],v16,s[24:27],s84 offen offset:0 sc1\nzv lgkmcnt(14)\nzm a[8:11],v[196:199],v[56:59],a[8:11]\nzt v[172:173],v24 offset:32768\n"
"zt v[174:175],v24 offset:36864\nzm a[36:39],v[196:199],v[60:63],a[36:39]\nzl v[120:123],v16,s[24:27],s63 offen offset:1024 sc1\nzv lgkmcnt(14)\nzm a[12:15],v[200:203],v[56:59],a[12:15]\nzl v[124:127],v16,s[24:27],s84 offen offset:1024 sc1\nzp s63,s63,2048\nzp s84,s84,2048\n"
"zm a[40:43],v[200:203],v[60:63],a[40:43]\nzt v[176:177],v25 offset:32768\nzt v[178:179],v25 offset:36864\nzv lgkmcnt(14)\nzm a[16:19],v[204:207],v[56:59],a[16:19]\nzl v[128:131],v16,s[24:27],s63 offen offset:0 sc1\nzm a[44:47],v[204:207],v[60:63],a[44:47]\nzt v[180:181],v26 offset:32768\n"
"zt v[182:183],v26 offset:36864\nzv lgkmcnt(14)\nzm a[20:23],v[208:211],v[56:59],a[20:23]\nzl v[132:135],v16,s[24:27],s84 offen offset:0 sc1\nzm a[48:51],v[208:211],v[60:63],a[48:51]\nzl v[136:139],v16,s[24:27],s63 offen offset:1024 sc1\nzv lgkmcnt(12)\nzm a[24:27],v[212:215],v[56:59],a[24:27]\n"
"zt v[184:185],v27 offset:32768\nzt v[186:187],v27 offset:36864\nzm a[52:55],v[212:215],v[60:63],a[52:55]\nzl v[140:143],v16,s[24:27],s84 offen offset:1024 sc1\nzp s63,s63,2048\nzp s84,s84,2048\nv_lshrrev_b32 v233,2,v6\nv_and_b32 v233,3,v233\n"
"v_and_b32 v234,3,v6\ns_lshl_b32 s76,s60,6\ns_lshl_b32 s77,s62,2\nzp s76,s76,s77\nv_lshl_add_u32 v233,v233,4,v234\nv_add_u32 v233,s76,v233\nzh v233,2,v233\nv_add_u32 v233,16384,v233\n"
"s_cmp_eq_u32 s23,0\ns_cbranch_scc0 L_wwb_ctx\ns_mov_b32 s77,0\nL_ww_ctx:\nglobal_load_dword v232,v233,s[94:95] sc1\nzv vmcnt(0)\nv_cmp_ne_u32 vcc,s34,v232\ns_nop 2\n"
"s_and_b32 vcc_lo,vcc_lo,0xffff\ns_cmp_eq_u32 vcc_lo,0\ns_cbranch_scc1 L_wwb_ctx\nzp s77,s77,1\ns_cmp_lt_u32 s77,300000\ns_cbranch_scc0 L_wwb_ctx\ns_sleep 2\ns_branch L_ww_ctx\n"
"L_wwb_ctx:\ns_barrier\nzp s36,s4,0\ns_addc_u32 s37,s5,0\ns_and_b32 s37,s37,0xffff\ns_mov_b32 s38,1835008\ns_mov_b32 s39,0x20000\ns_mov_b64 s[24:25],s[44:45]\n"
"s_mov_b64 s[26:27],s[46:47]\ns_mov_b64 s[28:29],s[48:49]\ns_mov_b64 s[30:31],s[50:51]\ns_mov_b32 s63,0\ns_mov_b32 s84,131072\ns_mov_b32 s64,0\nzv lgkmcnt(12)\nzm a[0:3],v[160:163],v[64:67],a[0:3]\n"
"zp m0,s65,114688\nzt v[188:189],v21 offset:40960\nzt v[190:191],v21 offset:45056\nzt v[192:193],v22 offset:40960\nzm a[28:31],v[160:163],v[68:71],a[28:31]\nzl v17,s[28:31],s64 offen offset:0 lds\nzv lgkmcnt(13)\nzm a[4:7],v[164:167],v[64:67],a[4:7]\n"
"zt v[194:195],v22 offset:45056\nzt v[196:197],v23 offset:40960\nzt v[198:199],v23 offset:45056\nzm a[32:35],v[164:167],v[68:71],a[32:35]\nzl v18,s[28:31],s64 offen offset:1024 lds\nzv lgkmcnt(14)\nzm a[8:11],v[168:171],v[64:67],a[8:11]\nzt v[200:201],v24 offset:40960\n"
"zt v[202:203],v24 offset:45056\nzm a[36:39],v[168:171],v[68:71],a[36:39]\nzl v19,s[28:31],s64 offen offset:2048 lds\nzv lgkmcnt(14)\nzm a[12:15],v[172:175],v[64:67],a[12:15]\nzl v20,s[28:31],s64 offen offset:3072 lds\nzp s64,s64,57344\nzm a[40:43],v[172:175],v[68:71],a[40:43]\n"
"zp m0,s65,0\nzt v[204:205],v25 offset:40960\nzt v[206:207],v25 offset:45056\nzv lgkmcnt(14)\nzm a[16:19],v[176:179],v[64:67],a[16:19]\nzl v17,s[28:31],s64 offen offset:0 lds\nzm a[44:47],v[176:179],v[68:71],a[44:47]\nzt v[208:209],v26 offset:40960\n"
"zt v[210:211],v26 offset:45056\nzv lgkmcnt(14)\nzm a[20:23],v[180:183],v[64:67],a[20:23]\nzl v18,s[28:31],s64 offen offset:1024 lds\nzm a[48:51],v[180:183],v[68:71],a[48:51]\nzl v19,s[28:31],s64 offen offset:2048 lds\nzv lgkmcnt(12)\nzm a[24:27],v[184:187],v[64:67],a[24:27]\n"
"zt v[212:213],v27 offset:40960\nzt v[214:215],v27 offset:45056\nzm a[52:55],v[184:187],v[68:71],a[52:55]\nzl v20,s[28:31],s64 offen offset:3072 lds\nzp s64,s64,57344\nzv vmcnt(28)\ns_barrier\nzv lgkmcnt(12)\n"
"zm a[0:3],v[188:191],v[72:75],a[0:3]\nzt v[160:161],v21 offset:49152\nzt v[162:163],v21 offset:53248\nzt v[164:165],v22 offset:49152\nzm a[28:31],v[188:191],v[76:79],a[28:31]\nzl v[144:147],v16,s[24:27],s63 offen offset:0 sc1\nzv lgkmcnt(13)\nzm a[4:7],v[192:195],v[72:75],a[4:7]\n"
"zt v[166:167],v22 offset:53248\nzt v[168:169],v23 offset:49152\nzt v[170:171],v23 offset:53248\nzm a[32:35],v[192:195],v[76:79],a[32:35]\nzl v[148:151],v16,s[24:27],s84 offen offset:0 sc1\nzv lgkmcnt(14)\nzm a[8:11],v[196:199],v[72:75],a[8:11]\nzt v[172:173],v24 offset:49152\n"
"zt v[174:175],v24 offset:53248\nzm a[36:39],v[196:199],v[76:79],a[36:39]\nzl v[152:155],v16,s[24:27],s63 offen offset:1024 sc1\nzv lgkmcnt(14)\nzm a[12:15],v[200:203],v[72:75],a[12:15]\nzl v[156:159],v16,s[24:27],s84 offen offset:1024 sc1\nzp s63,s63,2048\nzp s84,s84,2048\n"
"zm a[40:43],v[200:203],v[76:79],a[40:43]\nzt v[176:177],v25 offset:49152\nzt v[178:179],v25 offset:53248\nzv lgkmcnt(14)\nzm a[16:19],v[204:207],v[72:75],a[16:19]\nzl v[32:35],v16,s[24:27],s63 offen offset:0 sc1\nzm a[44:47],v[204:207],v[76:79],a[44:47]\nzt v[180:181],v26 offset:49152\n"
"zt v[182:183],v26 offset:53248\nzv lgkmcnt(14)\nzm a[20:23],v[208:211],v[72:75],a[20:23]\nzl v[36:39],v16,s[24:27],s84 offen offset:0 sc1\nzm a[48:51],v[208:211],v[76:79],a[48:51]\nzl v[40:43],v16,s[24:27],s63 offen offset:1024 sc1\nzv lgkmcnt(12)\nzm a[24:27],v[212:215],v[72:75],a[24:27]\n"
"zt v[184:185],v27 offset:49152\nzt v[186:187],v27 offset:53248\nzm a[52:55],v[212:215],v[76:79],a[52:55]\nzl v[44:47],v16,s[24:27],s84 offen offset:1024 sc1\nzp s63,s63,2048\nzp s84,s84,2048\nzv lgkmcnt(12)\nzm a[0:3],v[160:163],v[80:83],a[0:3]\n"
"zt v[188:189],v21 offset:57344\nzt v[190:191],v21 offset:61440\nzm a[28:31],v[160:163],v[84:87],a[28:31]\nzt v[192:193],v22 offset:57344\nzt v[194:195],v22 offset:61440\nzv lgkmcnt(14)\nzm a[4:7],v[164:167],v[80:83],a[4:7]\nzp m0,s65,16384\n"
"zt v[196:197],v23 offset:57344\nzt v[198:199],v23 offset:61440\nzm a[32:35],v[164:167],v[84:87],a[32:35]\nzl v17,s[28:31],s64 offen offset:0 lds\nzv lgkmcnt(14)\nzm a[8:11],v[168:171],v[80:83],a[8:11]\nzt v[200:201],v24 offset:57344\nzt v[202:203],v24 offset:61440\n"
"zm a[36:39],v[168:171],v[84:87],a[36:39]\nzt v[204:205],v25 offset:57344\nzv lgkmcnt(15)\nzm a[12:15],v[172:175],v[80:83],a[12:15]\nzl v18,s[28:31],s64 offen offset:1024 lds\nzm a[40:43],v[172:175],v[84:87],a[40:43]\nzt v[206:207],v25 offset:61440\nzv lgkmcnt(14)\n"
"zm a[16:19],v[176:179],v[80:83],a[16:19]\nzt v[208:209],v26 offset:57344\nzm a[44:47],v[176:179],v[84:87],a[44:47]\nzl v19,s[28:31],s64 offen offset:2048 lds\nzv lgkmcnt(13)\nzm a[20:23],v[180:183],v[80:83],a[20:23]\nzt v[210:211],v26 offset:61440\nzm a[48:51],v[180:183],v[84:87],a[48:51]\n"
"zt v[212:213],v27 offset:57344\nzv lgkmcnt(13)\nzm a[24:27],v[184:187],v[80:83],a[24:27]\nzl v20,s[28:31],s64 offen offset:3072 lds\nzp s64,s64,57344\nzm a[52:55],v[184:187],v[84:87],a[52:55]\nzt v[214:215],v27 offset:61440\nzv vmcnt(36)\n"
"s_barrier\nzv lgkmcnt(12)\nzm a[0:3],v[188:191],v[88:91],a[0:3]\nzt v[160:161],v216 offset:0\nzt v[162:163],v216 offset:4096\nzm a[28:31],v[188:191],v[92:95],a[28:31]\nzt v[164:165],v217 offset:0\nzt v[166:167],v217 offset:4096\n"
"zv lgkmcnt(14)\nzm a[4:7],v[192:195],v[88:91],a[4:7]\nzt v[168:169],v218 offset:0\nzt v[170:171],v218 offset:4096\nzm a[32:35],v[192:195],v[92:95],a[32:35]\nzl v[48:51],v16,s[24:27],s63 offen offset:0 sc1\nzv lgkmcnt(14)\nzm a[8:11],v[196:199],v[88:91],a[8:11]\n"
"zt v[172:173],v219 offset:0\nzt v[174:175],v219 offset:4096\nzm a[36:39],v[196:199],v[92:95],a[36:39]\nzt v[176:177],v220 offset:0\nzv lgkmcnt(15)\nzm a[12:15],v[200:203],v[88:91],a[12:15]\nzl v[52:55],v16,s[24:27],s84 offen offset:0 sc1\nzm a[40:43],v[200:203],v[92:95],a[40:43]\n"
"zt v[178:179],v220 offset:4096\nzv lgkmcnt(14)\nzm a[16:19],v[204:207],v[88:91],a[16:19]\nzt v[180:181],v221 offset:0\nzm a[44:47],v[204:207],v[92:95],a[44:47]\nzl v[56:59],v16,s[24:27],s63 offen offset:1024 sc1\nzv lgkmcnt(13)\nzm a[20:23],v[208:211],v[88:91],a[20:23]\n"
"zt v[182:183],v221 offset:4096\nzm a[48:51],v[208:211],v[92:95],a[48:51]\nzt v[184:185],v222 offset:0\nzv lgkmcnt(13)\nzm a[24:27],v[212:215],v[88:91],a[24:27]\nzl v[60:63],v16,s[24:27],s84 offen offset:1024 sc1\nzp s63,s63,2048\nzp s84,s84,2048\n"
"zm a[52:55],v[212:215],v[92:95],a[52:55]\nzt v[186:187],v222 offset:4096\nzv lgkmcnt(12)\nzm a[0:3],v[160:163],v[96:99],a[0:3]\nzt v[188:189],v216 offset:8192\nzt v[190:191],v216 offset:12288\nzm a[28:31],v[160:163],v[100:103],a[28:31]\nzt v[192:193],v217 offset:8192\n"
"zt v[194:195],v217 offset:12288\nzv lgkmcnt(14)\nzm a[4:7],v[164:167],v[96:99],a[4:7]\nzp m0,s65,32768\nzt v[196:197],v218 offset:8192\nzt v[198:199],v218 offset:12288\nzm a[32:35],v[164:167],v[100:103],a[32:35]\nzl v17,s[28:31],s64 offen offset:0 lds\n"
"zv lgkmcnt(14)\nzm a[8:11],v[168:171],v[96:99],a[8:11]\nzt v[200:201],v219 offset:8192\nzt v[202:203],v219 offset:12288\nzm a[36:39],v[168:171],v[100:103],a[36:39]\nzt v[204:205],v220 offset:8192\nzv lgkmcnt(15)\nzm a[12:15],v[172:175],v[96:99],a[12:15]\n"
"zl v18,s[28:31],s64 offen offset:1024 lds\nzm a[40:43],v[172:175],v[100:103],a[40:43]\nzt v[206:207],v220 offset:12288\nzv lgkmcnt(14)\nzm a[16:19],v[176:179],v[96:99],a[16:19]\nzt v[208:209],v221 offset:8192\nzm a[44:47],v[176:179],v[100:103],a[44:47]\nzl v19,s[28:31],s64 offen offset:2048 lds\n"
"zv lgkmcnt(13)\nzm a[20:23],v[180:183],v[96:99],a[20:23]\nzt v[210:211],v221 offset:12288\nzm a[48:51],v[180:183],v[100:103],a[48:51]\nzt v[212:213],v222 offset:8192\nzv lgkmcnt(13)\nzm a[24:27],v[184:187],v[96:99],a[24:27]\nzl v20,s[28:31],s64 offen offset:3072 lds\n"
"zp s64,s64,57344\nzm a[52:55],v[184:187],v[100:103],a[52:55]\nzt v[214:215],v222 offset:12288\nzv vmcnt(32)\ns_barrier\nzv lgkmcnt(12)\nzm a[0:3],v[188:191],v[104:107],a[0:3]\nzt v[160:161],v216 offset:16384\n"
"zt v[162:163],v216 offset:20480\nzm a[28:31],v[188:191],v[108:111],a[28:31]\nzt v[164:165],v217 offset:16384\nzt v[166:167],v217 offset:20480\nzv lgkmcnt(14)\nzm a[4:7],v[192:195],v[104:107],a[4:7]\nzt v[168:169],v218 offset:16384\nzt v[170:171],v218 offset:20480\n"
"zm a[32:35],v[192:195],v[108:111],a[32:35]\nzl v[64:67],v16,s[24:27],s63 offen offset:0 sc1\nzv lgkmcnt(14)\nzm a[8:11],v[196:199],v[104:107],a[8:11]\nzt v[172:173],v219 offset:16384\nzt v[174:175],v219 offset:20480\nzm a[36:39],v[196:199],v[108:111],a[36:39]\nzt v[176:177],v220 offset:16384\n"
"zv lgkmcnt(15)\nzm a[12:15],v[200:203],v[104:107],a[12:15]\nzl v[68:71],v16,s[24:27],s84 offen offset:0 sc1\nzm a[40:43],v[200:203],v[108:111],a[40:43]\nzt v[178:179],v220 offset:20480\nzv lgkmcnt(14)\nzm a[16:19],v[204:207],v[104:107],a[16:19]\nzt v[180:181],v221 offset:16384\n"
"zm a[44:47],v[204:207],v[108:111],a[44:47]\nzl v[72:75],v16,s[24:27],s63 offen offset:1024 sc1\nzv lgkmcnt(13)\nzm a[20:23],v[208:211],v[104:107],a[20:23]\nzt v[182:183],v221 offset:20480\nzm a[48:51],v[208:211],v[108:111],a[48:51]\nzt v[184:185],v222 offset:16384\nzv lgkmcnt(13)\n"
"zm a[24:27],v[212:215],v[104:107],a[24:27]\nzl v[76:79],v16,s[24:27],s84 offen offset:1024 sc1\nzp s63,s63,2048\nzp s84,s84,2048\nzm a[52:55],v[212:215],v[108:111],a[52:55]\nzt v[186:187],v222 offset:20480\nzv lgkmcnt(12)\nzm a[0:3],v[160:163],v[112:115],a[0:3]\n"
"zt v[188:189],v216 offset:24576\nzt v[190:191],v216 offset:28672\nzm a[28:31],v[160:163],v[116:119],a[28:31]\nzt v[192:193],v217 offset:24576\nzt v[194:195],v217 offset:28672\nzv lgkmcnt(14)\nzm a[4:7],v[164:167],v[112:115],a[4:7]\nzp m0,s65,49152\n"
"zt v[196:197],v218 offset:24576\nzt v[198:199],v218 offset:28672\nzm a[32:35],v[164:167],v[116:119],a[32:35]\nzl v17,s[28:31],s64 offen offset:0 lds\nzv lgkmcnt(14)\nzm a[8:11],v[168:171],v[112:115],a[8:11]\nzt v[200:201],v219 offset:24576\nzt v[202:203],v219 offset:28672\n"
"zm a[36:39],v[168:171],v[116:119],a[36:39]\nzt v[204:205],v220 offset:24576\nzv lgkmcnt(15)\nzm a[12:15],v[172:175],v[112:115],a[12:15]\nzl v18,s[28:31],s64 offen offset:1024 lds\nzm a[40:43],v[172:175],v[116:119],a[40:43]\nzt v[206:207],v220 offset:28672\nzv lgkmcnt(14)\n"
"zm a[16:19],v[176:179],v[112:115],a[16:19]\nzt v[208:209],v221 offset:24576\nzm a[44:47],v[176:179],v[116:119],a[44:47]\nzl v19,s[28:31],s64 offen offset:2048 lds\nzv lgkmcnt(13)\nzm a[20:23],v[180:183],v[112:115],a[20:23]\nzt v[210:211],v221 offset:28672\nzm a[48:51],v[180:183],v[116:119],a[48:51]\n"
"zt v[212:213],v222 offset:24576\nzv lgkmcnt(13)\nzm a[24:27],v[184:187],v[112:115],a[24:27]\nzl v20,s[28:31],s64 offen offset:3072 lds\nzp s64,s64,57344\nzm a[52:55],v[184:187],v[116:119],a[52:55]\nzt v[214:215],v222 offset:28672\nzv vmcnt(36)\n"
"s_barrier\nzv lgkmcnt(12)\nzm a[0:3],v[188:191],v[120:123],a[0:3]\nzt v[160:161],v216 offset:32768\nzt v[162:163],v216 offset:36864\nzm a[28:31],v[188:191],v[124:127],a[28:31]\nzt v[164:165],v217 offset:32768\nzt v[166:167],v217 offset:36864\n"
"zv lgkmcnt(14)\nzm a[4:7],v[192:195],v[120:123],a[4:7]\nzt v[168:169],v218 offset:32768\nzt v[170:171],v218 offset:36864\nzm a[32:35],v[192:195],v[124:127],a[32:35]\nzl v[80:83],v16,s[24:27],s63 offen offset:0 sc1\nzv lgkmcnt(14)\nzm a[8:11],v[196:199],v[120:123],a[8:11]\n"
"zt v[172:173],v219 offset:32768\nzt v[174:175],v219 offset:36864\nzm a[36:39],v[196:199],v[124:127],a[36:39]\nzt v[176:177],v220 offset:32768\nzv lgkmcnt(15)\nzm a[12:15],v[200:203],v[120:123],a[12:15]\nzl v[84:87],v16,s[24:27],s84 offen offset:0 sc1\nzm a[40:43],v[200:203],v[124:127],a[40:43]\n"
"zt v[178:179],v220 offset:36864\nzv lgkmcnt(14)\nzm a[16:19],v[204:207],v[120:123],a[16:19]\nzt v[180:181],v221 offset:32768\nzm a[44:47],v[204:207],v[124:127],a[44:47]\nzl v[88:91],v16,s[24:27],s63 offen offset:1024 sc1\nzv lgkmcnt(13)\nzm a[20:23],v[208:211],v[120:123],a[20:23]\n"
"zt v[182:183],v221 offset:36864\nzm a[48:51],v[208:211],v[124:127],a[48:51]\nzt v[184:185],v222 offset:32768\nzv lgkmcnt(13)\nzm a[24:27],v[212:215],v[120:123],a[24:27]\nzl v[92:95],v16,s[24:27],s84 offen offset:1024 sc1\nzp s63,s63,2048\nzp s84,s84,2048\n"
"zm a[52:55],v[212:215],v[124:127],a[52:55]\nzt v[186:187],v222 offset:36864\nzv lgkmcnt(12)\nzm a[0:3],v[160:163],v[128:131],a[0:3]\nzt v[188:189],v216 offset:40960\nzt v[190:191],v216 offset:45056\nzm a[28:31],v[160:163],v[132:135],a[28:31]\nzt v[192:193],v217 offset:40960\n"
"zt v[194:195],v217 offset:45056\nzv lgkmcnt(14)\nzm a[4:7],v[164:167],v[128:131],a[4:7]\nzp m0,s65,65536\nzt v[196:197],v218 offset:40960\nzt v[198:199],v218 offset:45056\nzm a[32:35],v[164:167],v[132:135],a[32:35]\nzl v17,s[28:31],s64 offen offset:0 lds\n"
"zv lgkmcnt(14)\nzm a[8:11],v[168:171],v[128:131],a[8:11]\nzt v[200:201],v219 offset:40960\nzt v[202:203],v219 offset:45056\nzm a[36:39],v[168:171],v[132:135],a[36:39]\nzt v[204:205],v220 offset:40960\nzv lgkmcnt(15)\nzm a[12:15],v[172:175],v[128:131],a[12:15]\n"
"zl v18,s[28:31],s64 offen offset:1024 lds\nzm a[40:43],v[172:175],v[132:135],a[40:43]\nzt v[206:207],v220 offset:45056\nzv lgkmcnt(14)\nzm a[16:19],v[176:179],v[128:131],a[16:19]\nzt v[208:209],v221 offset:40960\nzm a[44:47],v[176:179],v[132:135],a[44:47]\nzl v19,s[28:31],s64 offen offset:2048 lds\n"
"zv lgkmcnt(13)\nzm a[20:23],v[180:183],v[128:131],a[20:23]\nzt v[210:211],v221 offset:45056\nzm a[48:51],v[180:183],v[132:135],a[48:51]\nzt v[212:213],v222 offset:40960\nzv lgkmcnt(13)\nzm a[24:27],v[184:187],v[128:131],a[24:27]\nzl v20,s[28:31],s64 offen offset:3072 lds\n"
"zp s64,s64,57344\nzm a[52:55],v[184:187],v[132:135],a[52:55]\nzt v[214:215],v222 offset:45056\nzv vmcnt(32)\ns_barrier\nzv lgkmcnt(12)\nzm a[0:3],v[188:191],v[136:139],a[0:3]\nzt v[160:161],v216 offset:49152\n"
"zt v[162:163],v216 offset:53248\nzm a[28:31],v[188:191],v[140:143],a[28:31]\nzt v[164:165],v217 offset:49152\nzt v[166:167],v217 offset:53248\nzv lgkmcnt(14)\nzm a[4:7],v[192:195],v[136:139],a[4:7]\nzt v[168:169],v218 offset:49152\nzt v[170:171],v218 offset:53248\n"
"zm a[32:35],v[192:195],v[140:143],a[32:35]\nzl v[96:99],v16,s[24:27],s63 offen offset:0 sc1\nzv lgkmcnt(14)\nzm a[8:11],v[196:199],v[136:139],a[8:11]\nzt v[172:173],v219 offset:49152\nzt v[174:175],v219 offset:53248\nzm a[36:39],v[196:199],v[140:143],a[36:39]\nzt v[176:177],v220 offset:49152\n"
"zv lgkmcnt(15)\nzm a[12:15],v[200:203],v[136:139],a[12:15]\nzl v[100:103],v16,s[24:27],s84 offen offset:0 sc1\nzm a[40:43],v[200:203],v[140:143],a[40:43]\nzt v[178:179],v220 offset:53248\nzv lgkmcnt(14)\nzm a[16:19],v[204:207],v[136:139],a[16:19]\nzt v[180:181],v221 offset:49152\n"
"zm a[44:47],v[204:207],v[140:143],a[44:47]\nzl v[104:107],v16,s[24:27],s63 offen offset:1024 sc1\nzv lgkmcnt(13)\nzm a[20:23],v[208:211],v[136:139],a[20:23]\nzt v[182:183],v221 offset:53248\nzm a[48:51],v[208:211],v[140:143],a[48:51]\nzt v[184:185],v222 offset:49152\nzv lgkmcnt(13)\n"
"zm a[24:27],v[212:215],v[136:139],a[24:27]\nzl v[108:111],v16,s[24:27],s84 offen offset:1024 sc1\nzp s63,s63,2048\nzp s84,s84,2048\nzm a[52:55],v[212:215],v[140:143],a[52:55]\nzt v[186:187],v222 offset:53248\ns_nop 7\ns_lshl_b32 s77,s62,11\n"
"zp s77,s77,0\ns_cmp_eq_u32 s62,0\ns_cbranch_scc1 L_O_nsg0\nzp s76,s77,0\nzr v232,a0\nzr v233,a1\nzr v234,a2\nzr v235,a3\n"
"zr v236,a28\nzr v237,a29\nzr v238,a30\nzr v239,a31\nzc v240,v232,v233\nzc v241,v234,v235\nzc v242,v236,v237\nzc v243,v238,v239\n"
"zs v[240:243],v16,s[52:55],s76 offen offset:0 sc1\nzr v244,a4\nzr v245,a5\nzr v246,a6\nzr v247,a7\nzr v248,a32\nzr v249,a33\nzr v250,a34\n"
"zr v251,a35\nzc v252,v244,v245\nzc v253,v246,v247\nzc v254,v248,v249\nzc v255,v250,v251\nzs v[252:255],v16,s[52:55],s76 offen offset:1024 sc1\nL_O_nsg0:\ns_cmp_eq_u32 s62,1\n"
"s_cbranch_scc1 L_O_nsg1\nzp s76,s77,16384\nzr v232,a8\nzr v233,a9\nzr v234,a10\nzr v235,a11\nzr v236,a36\nzr v237,a37\n"
"zr v238,a38\nzr v239,a39\nzc v240,v232,v233\nzc v241,v234,v235\nzc v242,v236,v237\nzc v243,v238,v239\nzs v[240:243],v16,s[52:55],s76 offen offset:0 sc1\nzr v244,a12\n"
"zr v245,a13\nzr v246,a14\nzr v247,a15\nzr v248,a40\nzr v249,a41\nzr v250,a42\nzr v251,a43\nzc v252,v244,v245\n"
"zc v253,v246,v247\nzc v254,v248,v249\nzc v255,v250,v251\nzs v[252:255],v16,s[52:55],s76 offen offset:1024 sc1\nL_O_nsg1:\ns_cmp_eq_u32 s62,2\ns_cbranch_scc1 L_O_nsg2\nzp s76,s77,32768\n"
"zr v232,a16\nzr v233,a17\nzr v234,a18\nzr v235,a19\nzr v236,a44\nzr v237,a45\nzr v238,a46\nzr v239,a47\n"
"zc v240,v232,v233\nzc v241,v234,v235\nzc v242,v236,v237\nzc v243,v238,v239\nzs v[240:243],v16,s[52:55],s76 offen offset:0 sc1\nzr v244,a20\nzr v245,a21\nzr v246,a22\n"
"zr v247,a23\nzr v248,a48\nzr v249,a49\nzr v250,a50\nzr v251,a51\nzc v252,v244,v245\nzc v253,v246,v247\nzc v254,v248,v249\n"
"zc v255,v250,v251\nzs v[252:255],v16,s[52:55],s76 offen offset:1024 sc1\nL_O_nsg2:\ns_cmp_eq_u32 s62,3\ns_cbranch_scc1 L_O_nsg3\nzp s76,s77,49152\nzr v232,a24\nzr v233,a25\n"
"zr v234,a26\nzr v235,a27\nzr v236,a52\nzr v237,a53\nzr v238,a54\nzr v239,a55\nzc v240,v232,v233\nzc v241,v234,v235\n"
"zc v242,v236,v237\nzc v243,v238,v239\nzs v[240:243],v16,s[52:55],s76 offen offset:0 sc1\nL_O_nsg3:\nzv lgkmcnt(12)\nzm a[56:59],v[160:163],v[144:147],0\nzt v[188:189],v216 offset:57344\nzt v[190:191],v216 offset:61440\n"
"zm a[84:87],v[160:163],v[148:151],0\nzt v[192:193],v217 offset:57344\nzt v[194:195],v217 offset:61440\nzv lgkmcnt(14)\nzm a[60:63],v[164:167],v[144:147],0\nzp m0,s65,81920\nzt v[196:197],v218 offset:57344\nzt v[198:199],v218 offset:61440\n"
"zm a[88:91],v[164:167],v[148:151],0\nzl v17,s[28:31],s64 offen offset:0 lds\nzv lgkmcnt(14)\nzm a[64:67],v[168:171],v[144:147],0\nzt v[200:201],v219 offset:57344\nzt v[202:203],v219 offset:61440\nzm a[92:95],v[168:171],v[148:151],0\nzt v[204:205],v220 offset:57344\n"
"zv lgkmcnt(15)\nzm a[68:71],v[172:175],v[144:147],0\nzl v18,s[28:31],s64 offen offset:1024 lds\nzm a[96:99],v[172:175],v[148:151],0\nzt v[206:207],v220 offset:61440\nzv lgkmcnt(14)\nzm a[72:75],v[176:179],v[144:147],0\nzt v[208:209],v221 offset:57344\n"
"zm a[100:103],v[176:179],v[148:151],0\nzl v19,s[28:31],s64 offen offset:2048 lds\nzv lgkmcnt(13)\nzm a[76:79],v[180:183],v[144:147],0\nzt v[210:211],v221 offset:61440\nzm a[104:107],v[180:183],v[148:151],0\nzt v[212:213],v222 offset:57344\nzv lgkmcnt(13)\n"
"zm a[80:83],v[184:187],v[144:147],0\nzl v20,s[28:31],s64 offen offset:3072 lds\nzp s64,s64,57344\nzm a[108:111],v[184:187],v[148:151],0\nzt v[214:215],v222 offset:61440\nzv vmcnt(41)\ns_barrier\nzv lgkmcnt(12)\n"
"zm a[56:59],v[188:191],v[152:155],a[56:59]\nzt v[160:161],v21 offset:0\nzt v[162:163],v21 offset:4096\nzm a[84:87],v[188:191],v[156:159],a[84:87]\nzt v[164:165],v22 offset:0\nzt v[166:167],v22 offset:4096\nzv lgkmcnt(14)\nzm a[60:63],v[192:195],v[152:155],a[60:63]\n"
"zt v[168:169],v23 offset:0\nzt v[170:171],v23 offset:4096\nzm a[88:91],v[192:195],v[156:159],a[88:91]\nzl v[112:115],v16,s[24:27],s63 offen offset:0 sc1\nzv lgkmcnt(14)\nzm a[64:67],v[196:199],v[152:155],a[64:67]\nzt v[172:173],v24 offset:0\nzt v[174:175],v24 offset:4096\n"
"zm a[92:95],v[196:199],v[156:159],a[92:95]\nzt v[176:177],v25 offset:0\nzv lgkmcnt(15)\nzm a[68:71],v[200:203],v[152:155],a[68:71]\nzl v[116:119],v16,s[24:27],s84 offen offset:0 sc1\nzm a[96:99],v[200:203],v[156:159],a[96:99]\nzt v[178:179],v25 offset:4096\nzv lgkmcnt(14)\n"
"zm a[72:75],v[204:207],v[152:155],a[72:75]\nzt v[180:181],v26 offset:0\nzm a[100:103],v[204:207],v[156:159],a[100:103]\nzl v[120:123],v16,s[24:27],s63 offen offset:1024 sc1\nzv lgkmcnt(13)\nzm a[76:79],v[208:211],v[152:155],a[76:79]\nzt v[182:183],v26 offset:4096\nzm a[104:107],v[208:211],v[156:159],a[104:107]\n"
"zt v[184:185],v27 offset:0\nzv lgkmcnt(13)\nzm a[80:83],v[212:215],v[152:155],a[80:83]\nzl v[124:127],v16,s[24:27],s84 offen offset:1024 sc1\nzp s63,s63,2048\nzp s84,s84,2048\nzm a[108:111],v[212:215],v[156:159],a[108:111]\nzt v[186:187],v27 offset:4096\n"
"zv lgkmcnt(12)\nzm a[56:59],v[160:163],v[32:35],a[56:59]\nzt v[188:189],v21 offset:8192\nzt v[190:191],v21 offset:12288\nzm a[84:87],v[160:163],v[36:39],a[84:87]\nzt v[192:193],v22 offset:8192\nzt v[194:195],v22 offset:12288\nzv lgkmcnt(14)\n"
"zm a[60:63],v[164:167],v[32:35],a[60:63]\nzp m0,s65,98304\nzt v[196:197],v23 offset:8192\nzt v[198:199],v23 offset:12288\nzm a[88:91],v[164:167],v[36:39],a[88:91]\nzl v17,s[28:31],s64 offen offset:0 lds\nzv lgkmcnt(14)\nzm a[64:67],v[168:171],v[32:35],a[64:67]\n"
"zt v[200:201],v24 offset:8192\nzt v[202:203],v24 offset:12288\nzm a[92:95],v[168:171],v[36:39],a[92:95]\nzt v[204:205],v25 offset:8192\nzv lgkmcnt(15)\nzm a[68:71],v[172:175],v[32:35],a[68:71]\nzl v18,s[28:31],s64 offen offset:1024 lds\nzm a[96:99],v[172:175],v[36:39],a[96:99]\n"
"zt v[206:207],v25 offset:12288\nzv lgkmcnt(14)\nzm a[72:75],v[176:179],v[32:35],a[72:75]\nzt v[208:209],v26 offset:8192\nzm a[100:103],v[176:179],v[36:39],a[100:103]\nzl v19,s[28:31],s64 offen offset:2048 lds\nzv lgkmcnt(13)\nzm a[76:79],v[180:183],v[32:35],a[76:79]\n"
"zt v[210:211],v26 offset:12288\nzm a[104:107],v[180:183],v[36:39],a[104:107]\nzt v[212:213],v27 offset:8192\nzv lgkmcnt(13)\nzm a[80:83],v[184:187],v[32:35],a[80:83]\nzl v20,s[28:31],s64 offen offset:3072 lds\nzp s64,s64,57344\nzm a[108:111],v[184:187],v[36:39],a[108:111]\n"
"zt v[214:215],v27 offset:12288\nzv vmcnt(41)\ns_barrier\nzv lgkmcnt(12)\nzm a[56:59],v[188:191],v[40:43],a[56:59]\nzt v[160:161],v21 offset:16384\nzt v[162:163],v21 offset:20480\nzm a[84:87],v[188:191],v[44:47],a[84:87]\n"
"zt v[164:165],v22 offset:16384\nzt v[166:167],v22 offset:20480\nzv lgkmcnt(14)\nzm a[60:63],v[192:195],v[40:43],a[60:63]\nzt v[168:169],v23 offset:16384\nzt v[170:171],v23 offset:20480\nzm a[88:91],v[192:195],v[44:47],a[88:91]\nzl v[128:131],v16,s[24:27],s63 offen offset:0 sc1\n"
"zv lgkmcnt(14)\nzm a[64:67],v[196:199],v[40:43],a[64:67]\nzt v[172:173],v24 offset:16384\nzt v[174:175],v24 offset:20480\nzm a[92:95],v[196:199],v[44:47],a[92:95]\nzt v[176:177],v25 offset:16384\nzv lgkmcnt(15)\nzm a[68:71],v[200:203],v[40:43],a[68:71]\n"
"zl v[132:135],v16,s[24:27],s84 offen offset:0 sc1\nzm a[96:99],v[200:203],v[44:47],a[96:99]\nzt v[178:179],v25 offset:20480\nzv lgkmcnt(14)\nzm a[72:75],v[204:207],v[40:43],a[72:75]\nzt v[180:181],v26 offset:16384\nzm a[100:103],v[204:207],v[44:47],a[100:103]\nzl v[136:139],v16,s[24:27],s63 offen offset:1024 sc1\n"
"zv lgkmcnt(13)\nzm a[76:79],v[208:211],v[40:43],a[76:79]\nzt v[182:183],v26 offset:20480\nzm a[104:107],v[208:211],v[44:47],a[104:107]\nzt v[184:185],v27 offset:16384\nzv lgkmcnt(13)\nzm a[80:83],v[212:215],v[40:43],a[80:83]\nzl v[140:143],v16,s[24:27],s84 offen offset:1024 sc1\n"
"zp s63,s63,2048\nzp s84,s84,2048\nzm a[108:111],v[212:215],v[44:47],a[108:111]\nzt v[186:187],v27 offset:20480\nzv lgkmcnt(12)\nzm a[56:59],v[160:163],v[48:51],a[56:59]\nzt v[188:189],v21 offset:24576\nzt v[190:191],v21 offset:28672\n"
"zm a[84:87],v[160:163],v[52:55],a[84:87]\nzt v[192:193],v22 offset:24576\nzt v[194:195],v22 offset:28672\nzv lgkmcnt(14)\nzm a[60:63],v[164:167],v[48:51],a[60:63]\nzp m0,s65,114688\nzt v[196:197],v23 offset:24576\nzt v[198:199],v23 offset:28672\n"
"zm a[88:91],v[164:167],v[52:55],a[88:91]\nzl v17,s[28:31],s64 offen offset:0 lds\nzv lgkmcnt(14)\nzm a[64:67],v[168:171],v[48:51],a[64:67]\nzt v[200:201],v24 offset:24576\nzt v[202:203],v24 offset:28672\nzm a[92:95],v[168:171],v[52:55],a[92:95]\nzt v[204:205],v25 offset:24576\n"
"zv lgkmcnt(15)\nzm a[68:71],v[172:175],v[48:51],a[68:71]\nzl v18,s[28:31],s64 offen offset:1024 lds\nzm a[96:99],v[172:175],v[52:55],a[96:99]\nzt v[206:207],v25 offset:28672\nzv lgkmcnt(14)\nzm a[72:75],v[176:179],v[48:51],a[72:75]\nzt v[208:209],v26 offset:24576\n"
"zm a[100:103],v[176:179],v[52:55],a[100:103]\nzl v19,s[28:31],s64 offen offset:2048 lds\nzv lgkmcnt(13)\nzm a[76:79],v[180:183],v[48:51],a[76:79]\nzt v[210:211],v26 offset:28672\nzm a[104:107],v[180:183],v[52:55],a[104:107]\nzt v[212:213],v27 offset:24576\nzv lgkmcnt(13)\n"
"zm a[80:83],v[184:187],v[48:51],a[80:83]\nzl v20,s[28:31],s64 offen offset:3072 lds\nzp s64,s64,57344\nzm a[108:111],v[184:187],v[52:55],a[108:111]\nzt v[214:215],v27 offset:28672\nzv vmcnt(41)\ns_barrier\nzv lgkmcnt(12)\n"
"zm a[56:59],v[188:191],v[56:59],a[56:59]\nzt v[160:161],v21 offset:32768\nzt v[162:163],v21 offset:36864\nzm a[84:87],v[188:191],v[60:63],a[84:87]\nzt v[164:165],v22 offset:32768\nzt v[166:167],v22 offset:36864\nzv lgkmcnt(14)\nzm a[60:63],v[192:195],v[56:59],a[60:63]\n"
"zt v[168:169],v23 offset:32768\nzt v[170:171],v23 offset:36864\nzm a[88:91],v[192:195],v[60:63],a[88:91]\nzl v[144:147],v16,s[24:27],s63 offen offset:0 sc1\nzv lgkmcnt(14)\nzm a[64:67],v[196:199],v[56:59],a[64:67]\nzt v[172:173],v24 offset:32768\nzt v[174:175],v24 offset:36864\n"
"zm a[92:95],v[196:199],v[60:63],a[92:95]\nzt v[176:177],v25 offset:32768\nzv lgkmcnt(15)\nzm a[68:71],v[200:203],v[56:59],a[68:71]\nzl v[148:151],v16,s[24:27],s84 offen offset:0 sc1\nzm a[96:99],v[200:203],v[60:63],a[96:99]\nzt v[178:179],v25 offset:36864\nzv lgkmcnt(14)\n"
"zm a[72:75],v[204:207],v[56:59],a[72:75]\nzt v[180:181],v26 offset:32768\nzm a[100:103],v[204:207],v[60:63],a[100:103]\nzl v[152:155],v16,s[24:27],s63 offen offset:1024 sc1\nzv lgkmcnt(13)\nzm a[76:79],v[208:211],v[56:59],a[76:79]\nzt v[182:183],v26 offset:36864\nzm a[104:107],v[208:211],v[60:63],a[104:107]\n"
"zt v[184:185],v27 offset:32768\nzv lgkmcnt(13)\nzm a[80:83],v[212:215],v[56:59],a[80:83]\nzl v[156:159],v16,s[24:27],s84 offen offset:1024 sc1\nzp s63,s63,2048\nzp s84,s84,2048\nzm a[108:111],v[212:215],v[60:63],a[108:111]\nzt v[186:187],v27 offset:36864\n"
"zv lgkmcnt(12)\nzm a[56:59],v[160:163],v[64:67],a[56:59]\nzt v[188:189],v21 offset:40960\nzt v[190:191],v21 offset:45056\nzm a[84:87],v[160:163],v[68:71],a[84:87]\nzt v[192:193],v22 offset:40960\nzt v[194:195],v22 offset:45056\nzv lgkmcnt(14)\n"
"zm a[60:63],v[164:167],v[64:67],a[60:63]\nzp m0,s65,0\nzt v[196:197],v23 offset:40960\nzt v[198:199],v23 offset:45056\nzm a[88:91],v[164:167],v[68:71],a[88:91]\nzl v17,s[28:31],s64 offen offset:0 lds\nzv lgkmcnt(14)\nzm a[64:67],v[168:171],v[64:67],a[64:67]\n"
"zt v[200:201],v24 offset:40960\nzt v[202:203],v24 offset:45056\nzm a[92:95],v[168:171],v[68:71],a[92:95]\nzt v[204:205],v25 offset:40960\nzv lgkmcnt(15)\nzm a[68:71],v[172:175],v[64:67],a[68:71]\nzl v18,s[28:31],s64 offen offset:1024 lds\nzm a[96:99],v[172:175],v[68:71],a[96:99]\n"
"zt v[206:207],v25 offset:45056\nzv lgkmcnt(14)\nzm a[72:75],v[176:179],v[64:67],a[72:75]\nzt v[208:209],v26 offset:40960\nzm a[100:103],v[176:179],v[68:71],a[100:103]\nzl v19,s[28:31],s64 offen offset:2048 lds\nzv lgkmcnt(13)\nzm a[76:79],v[180:183],v[64:67],a[76:79]\n"
"zt v[210:211],v26 offset:45056\nzm a[104:107],v[180:183],v[68:71],a[104:107]\nzt v[212:213],v27 offset:40960\nzv lgkmcnt(13)\nzm a[80:83],v[184:187],v[64:67],a[80:83]\nzl v20,s[28:31],s64 offen offset:3072 lds\nzp s64,s64,57344\nzm a[108:111],v[184:187],v[68:71],a[108:111]\n"
"zt v[214:215],v27 offset:45056\nzv vmcnt(41)\ns_barrier\nzv lgkmcnt(12)\nzm a[56:59],v[188:191],v[72:75],a[56:59]\nzt v[160:161],v21 offset:49152\nzt v[162:163],v21 offset:53248\nzm a[84:87],v[188:191],v[76:79],a[84:87]\n"
"zt v[164:165],v22 offset:49152\nzt v[166:167],v22 offset:53248\nzv lgkmcnt(14)\nzm a[60:63],v[192:195],v[72:75],a[60:63]\nzt v[168:169],v23 offset:49152\nzt v[170:171],v23 offset:53248\nzm a[88:91],v[192:195],v[76:79],a[88:91]\nzl v[32:35],v16,s[24:27],s63 offen offset:0 sc1\n"
"zv lgkmcnt(14)\nzm a[64:67],v[196:199],v[72:75],a[64:67]\nzt v[172:173],v24 offset:49152\nzt v[174:175],v24 offset:53248\nzm a[92:95],v[196:199],v[76:79],a[92:95]\nzt v[176:177],v25 offset:49152\nzv lgkmcnt(15)\nzm a[68:71],v[200:203],v[72:75],a[68:71]\n"
"zl v[36:39],v16,s[24:27],s84 offen offset:0 sc1\nzm a[96:99],v[200:203],v[76:79],a[96:99]\nzt v[178:179],v25 offset:53248\nzv lgkmcnt(14)\nzm a[72:75],v[204:207],v[72:75],a[72:75]\nzt v[180:181],v26 offset:49152\nzm a[100:103],v[204:207],v[76:79],a[100:103]\nzl v[40:43],v16,s[24:27],s63 offen offset:1024 sc1\n"
"zv lgkmcnt(13)\nzm a[76:79],v[208:211],v[72:75],a[76:79]\nzt v[182:183],v26 offset:53248\nzm a[104:107],v[208:211],v[76:79],a[104:107]\nzt v[184:185],v27 offset:49152\nzv lgkmcnt(13)\nzm a[80:83],v[212:215],v[72:75],a[80:83]\nzl v[44:47],v16,s[24:27],s84 offen offset:1024 sc1\n"
"zp s63,s63,2048\nzp s84,s84,2048\nzm a[108:111],v[212:215],v[76:79],a[108:111]\nzt v[186:187],v27 offset:53248\nzv lgkmcnt(12)\nzm a[56:59],v[160:163],v[80:83],a[56:59]\nzt v[188:189],v21 offset:57344\nzt v[190:191],v21 offset:61440\n"
"zm a[84:87],v[160:163],v[84:87],a[84:87]\nzt v[192:193],v22 offset:57344\nzt v[194:195],v22 offset:61440\nzv lgkmcnt(14)\nzm a[60:63],v[164:167],v[80:83],a[60:63]\nzp m0,s65,16384\nzt v[196:197],v23 offset:57344\nzt v[198:199],v23 offset:61440\n"
"zm a[88:91],v[164:167],v[84:87],a[88:91]\nzl v17,s[28:31],s64 offen offset:0 lds\nzv lgkmcnt(14)\nzm a[64:67],v[168:171],v[80:83],a[64:67]\nzt v[200:201],v24 offset:57344\nzt v[202:203],v24 offset:61440\nzm a[92:95],v[168:171],v[84:87],a[92:95]\nzt v[204:205],v25 offset:57344\n"
"zv lgkmcnt(15)\nzm a[68:71],v[172:175],v[80:83],a[68:71]\nzl v18,s[28:31],s64 offen offset:1024 lds\nzm a[96:99],v[172:175],v[84:87],a[96:99]\nzt v[206:207],v25 offset:61440\nzv lgkmcnt(14)\nzm a[72:75],v[176:179],v[80:83],a[72:75]\nzt v[208:209],v26 offset:57344\n"
"zm a[100:103],v[176:179],v[84:87],a[100:103]\nzl v19,s[28:31],s64 offen offset:2048 lds\nzv lgkmcnt(13)\nzm a[76:79],v[180:183],v[80:83],a[76:79]\nzt v[210:211],v26 offset:61440\nzm a[104:107],v[180:183],v[84:87],a[104:107]\nzt v[212:213],v27 offset:57344\nzv lgkmcnt(13)\n"
"zm a[80:83],v[184:187],v[80:83],a[80:83]\nzl v20,s[28:31],s64 offen offset:3072 lds\nzp s64,s64,57344\nzm a[108:111],v[184:187],v[84:87],a[108:111]\nzt v[214:215],v27 offset:61440\nzv vmcnt(41)\ns_barrier\nzv lgkmcnt(12)\n"
"zm a[56:59],v[188:191],v[88:91],a[56:59]\nzt v[160:161],v216 offset:0\nzt v[162:163],v216 offset:4096\nzm a[84:87],v[188:191],v[92:95],a[84:87]\nzt v[164:165],v217 offset:0\nzt v[166:167],v217 offset:4096\nzv lgkmcnt(14)\nzm a[60:63],v[192:195],v[88:91],a[60:63]\n"
"zt v[168:169],v218 offset:0\nzt v[170:171],v218 offset:4096\nzm a[88:91],v[192:195],v[92:95],a[88:91]\nzl v[48:51],v16,s[24:27],s63 offen offset:0 sc1\nzv lgkmcnt(14)\nzm a[64:67],v[196:199],v[88:91],a[64:67]\nzt v[172:173],v219 offset:0\nzt v[174:175],v219 offset:4096\n"
"zm a[92:95],v[196:199],v[92:95],a[92:95]\nzt v[176:177],v220 offset:0\nzv lgkmcnt(15)\nzm a[68:71],v[200:203],v[88:91],a[68:71]\nzl v[52:55],v16,s[24:27],s84 offen offset:0 sc1\nzm a[96:99],v[200:203],v[92:95],a[96:99]\nzt v[178:179],v220 offset:4096\nzv lgkmcnt(14)\n"
"zm a[72:75],v[204:207],v[88:91],a[72:75]\nzt v[180:181],v221 offset:0\nzm a[100:103],v[204:207],v[92:95],a[100:103]\nzl v[56:59],v16,s[24:27],s63 offen offset:1024 sc1\nzv lgkmcnt(13)\nzm a[76:79],v[208:211],v[88:91],a[76:79]\nzt v[182:183],v221 offset:4096\nzm a[104:107],v[208:211],v[92:95],a[104:107]\n"
"zt v[184:185],v222 offset:0\nzv lgkmcnt(13)\nzm a[80:83],v[212:215],v[88:91],a[80:83]\nzl v[60:63],v16,s[24:27],s84 offen offset:1024 sc1\nzp s63,s63,2048\nzp s84,s84,2048\nzm a[108:111],v[212:215],v[92:95],a[108:111]\nzt v[186:187],v222 offset:4096\n"
"zv lgkmcnt(12)\nzm a[56:59],v[160:163],v[96:99],a[56:59]\nzt v[188:189],v216 offset:8192\nzt v[190:191],v216 offset:12288\nzm a[84:87],v[160:163],v[100:103],a[84:87]\nzt v[192:193],v217 offset:8192\nzt v[194:195],v217 offset:12288\nzv lgkmcnt(14)\n"
"zm a[60:63],v[164:167],v[96:99],a[60:63]\nzp m0,s65,32768\nzt v[196:197],v218 offset:8192\nzt v[198:199],v218 offset:12288\nzm a[88:91],v[164:167],v[100:103],a[88:91]\nzl v17,s[28:31],s64 offen offset:0 lds\nzv lgkmcnt(14)\nzm a[64:67],v[168:171],v[96:99],a[64:67]\n"
"zt v[200:201],v219 offset:8192\nzt v[202:203],v219 offset:12288\nzm a[92:95],v[168:171],v[100:103],a[92:95]\nzt v[204:205],v220 offset:8192\nzv lgkmcnt(15)\nzm a[68:71],v[172:175],v[96:99],a[68:71]\nzl v18,s[28:31],s64 offen offset:1024 lds\nzm a[96:99],v[172:175],v[100:103],a[96:99]\n"
"zt v[206:207],v220 offset:12288\nzv lgkmcnt(14)\nzm a[72:75],v[176:179],v[96:99],a[72:75]\nzt v[208:209],v221 offset:8192\nzm a[100:103],v[176:179],v[100:103],a[100:103]\nzl v19,s[28:31],s64 offen offset:2048 lds\nzv lgkmcnt(13)\nzm a[76:79],v[180:183],v[96:99],a[76:79]\n"
"zt v[210:211],v221 offset:12288\nzm a[104:107],v[180:183],v[100:103],a[104:107]\nzt v[212:213],v222 offset:8192\nzv lgkmcnt(13)\nzm a[80:83],v[184:187],v[96:99],a[80:83]\nzl v20,s[28:31],s64 offen offset:3072 lds\nzp s64,s64,57344\nzm a[108:111],v[184:187],v[100:103],a[108:111]\n"
"zt v[214:215],v222 offset:12288\nzv vmcnt(36)\ns_barrier\nzv lgkmcnt(12)\nzm a[56:59],v[188:191],v[104:107],a[56:59]\nzt v[160:161],v216 offset:16384\nzt v[162:163],v216 offset:20480\nzm a[84:87],v[188:191],v[108:111],a[84:87]\n"
"zt v[164:165],v217 offset:16384\nzt v[166:167],v217 offset:20480\nzv lgkmcnt(14)\nzm a[60:63],v[192:195],v[104:107],a[60:63]\nzt v[168:169],v218 offset:16384\nzt v[170:171],v218 offset:20480\nzm a[88:91],v[192:195],v[108:111],a[88:91]\nzl v[64:67],v16,s[24:27],s63 offen offset:0 sc1\n"
"zv lgkmcnt(14)\nzm a[64:67],v[196:199],v[104:107],a[64:67]\nzt v[172:173],v219 offset:16384\nzt v[174:175],v219 offset:20480\nzm a[92:95],v[196:199],v[108:111],a[92:95]\nzt v[176:177],v220 offset:16384\nzv lgkmcnt(15)\nzm a[68:71],v[200:203],v[104:107],a[68:71]\n"
"zl v[68:71],v16,s[24:27],s84 offen offset:0 sc1\nzm a[96:99],v[200:203],v[108:111],a[96:99]\nzt v[178:179],v220 offset:20480\nzv lgkmcnt(14)\nzm a[72:75],v[204:207],v[104:107],a[72:75]\nzt v[180:181],v221 offset:16384\nzm a[100:103],v[204:207],v[108:111],a[100:103]\nzl v[72:75],v16,s[24:27],s63 offen offset:1024 sc1\n"
"zv lgkmcnt(13)\nzm a[76:79],v[208:211],v[104:107],a[76:79]\nzt v[182:183],v221 offset:20480\nzm a[104:107],v[208:211],v[108:111],a[104:107]\nzt v[184:185],v222 offset:16384\nzv lgkmcnt(13)\nzm a[80:83],v[212:215],v[104:107],a[80:83]\nzl v[76:79],v16,s[24:27],s84 offen offset:1024 sc1\n"
"zp s63,s63,2048\nzp s84,s84,2048\nzm a[108:111],v[212:215],v[108:111],a[108:111]\nzt v[186:187],v222 offset:20480\nzv lgkmcnt(12)\nzm a[56:59],v[160:163],v[112:115],a[56:59]\nzt v[188:189],v216 offset:24576\nzt v[190:191],v216 offset:28672\n"
"zm a[84:87],v[160:163],v[116:119],a[84:87]\nzt v[192:193],v217 offset:24576\nzt v[194:195],v217 offset:28672\nzv lgkmcnt(14)\nzm a[60:63],v[164:167],v[112:115],a[60:63]\nzp m0,s65,49152\nzt v[196:197],v218 offset:24576\nzt v[198:199],v218 offset:28672\n"
"zm a[88:91],v[164:167],v[116:119],a[88:91]\nzl v17,s[28:31],s64 offen offset:0 lds\nzv lgkmcnt(14)\nzm a[64:67],v[168:171],v[112:115],a[64:67]\nzt v[200:201],v219 offset:24576\nzt v[202:203],v219 offset:28672\nzm a[92:95],v[168:171],v[116:119],a[92:95]\nzt v[204:205],v220 offset:24576\n"
"zv lgkmcnt(15)\nzm a[68:71],v[172:175],v[112:115],a[68:71]\nzl v18,s[28:31],s64 offen offset:1024 lds\nzm a[96:99],v[172:175],v[116:119],a[96:99]\nzt v[206:207],v220 offset:28672\nzv lgkmcnt(14)\nzm a[72:75],v[176:179],v[112:115],a[72:75]\nzt v[208:209],v221 offset:24576\n"
"zm a[100:103],v[176:179],v[116:119],a[100:103]\nzl v19,s[28:31],s64 offen offset:2048 lds\nzv lgkmcnt(13)\nzm a[76:79],v[180:183],v[112:115],a[76:79]\nzt v[210:211],v221 offset:28672\nzm a[104:107],v[180:183],v[116:119],a[104:107]\nzt v[212:213],v222 offset:24576\nzv lgkmcnt(13)\n"
"zm a[80:83],v[184:187],v[112:115],a[80:83]\nzl v20,s[28:31],s64 offen offset:3072 lds\nzp s64,s64,57344\nzm a[108:111],v[184:187],v[116:119],a[108:111]\nzt v[214:215],v222 offset:28672\nzv vmcnt(36)\ns_barrier\nzv lgkmcnt(12)\n"
"zm a[56:59],v[188:191],v[120:123],a[56:59]\nzt v[160:161],v216 offset:32768\nzt v[162:163],v216 offset:36864\nzm a[84:87],v[188:191],v[124:127],a[84:87]\nzt v[164:165],v217 offset:32768\nzt v[166:167],v217 offset:36864\nzv lgkmcnt(14)\nzm a[60:63],v[192:195],v[120:123],a[60:63]\n"
"zt v[168:169],v218 offset:32768\nzt v[170:171],v218 offset:36864\nzm a[88:91],v[192:195],v[124:127],a[88:91]\nzl v[80:83],v16,s[24:27],s63 offen offset:0 sc1\nzv lgkmcnt(14)\nzm a[64:67],v[196:199],v[120:123],a[64:67]\nzt v[172:173],v219 offset:32768\nzt v[174:175],v219 offset:36864\n"
"zm a[92:95],v[196:199],v[124:127],a[92:95]\nzt v[176:177],v220 offset:32768\nzv lgkmcnt(15)\nzm a[68:71],v[200:203],v[120:123],a[68:71]\nzl v[84:87],v16,s[24:27],s84 offen offset:0 sc1\nzm a[96:99],v[200:203],v[124:127],a[96:99]\nzt v[178:179],v220 offset:36864\nzv lgkmcnt(14)\n"
"zm a[72:75],v[204:207],v[120:123],a[72:75]\nzt v[180:181],v221 offset:32768\nzm a[100:103],v[204:207],v[124:127],a[100:103]\nzl v[88:91],v16,s[24:27],s63 offen offset:1024 sc1\nzv lgkmcnt(13)\nzm a[76:79],v[208:211],v[120:123],a[76:79]\nzt v[182:183],v221 offset:36864\nzm a[104:107],v[208:211],v[124:127],a[104:107]\n"
"zt v[184:185],v222 offset:32768\nzv lgkmcnt(13)\nzm a[80:83],v[212:215],v[120:123],a[80:83]\nzl v[92:95],v16,s[24:27],s84 offen offset:1024 sc1\nzp s63,s63,2048\nzp s84,s84,2048\nzm a[108:111],v[212:215],v[124:127],a[108:111]\nzt v[186:187],v222 offset:36864\n"
"zv lgkmcnt(12)\nzm a[56:59],v[160:163],v[128:131],a[56:59]\nzt v[188:189],v216 offset:40960\nzt v[190:191],v216 offset:45056\nzm a[84:87],v[160:163],v[132:135],a[84:87]\nzt v[192:193],v217 offset:40960\nzt v[194:195],v217 offset:45056\nzv lgkmcnt(14)\n"
"zm a[60:63],v[164:167],v[128:131],a[60:63]\nzp m0,s65,65536\nzt v[196:197],v218 offset:40960\nzt v[198:199],v218 offset:45056\nzm a[88:91],v[164:167],v[132:135],a[88:91]\nzl v17,s[28:31],s64 offen offset:0 lds\nzv lgkmcnt(14)\nzm a[64:67],v[168:171],v[128:131],a[64:67]\n"
"zt v[200:201],v219 offset:40960\nzt v[202:203],v219 offset:45056\nzm a[92:95],v[168:171],v[132:135],a[92:95]\nzt v[204:205],v220 offset:40960\nzv lgkmcnt(15)\nzm a[68:71],v[172:175],v[128:131],a[68:71]\nzl v18,s[28:31],s64 offen offset:1024 lds\nzm a[96:99],v[172:175],v[132:135],a[96:99]\n"
"zt v[206:207],v220 offset:45056\nzv lgkmcnt(14)\nzm a[72:75],v[176:179],v[128:131],a[72:75]\nzt v[208:209],v221 offset:40960\nzm a[100:103],v[176:179],v[132:135],a[100:103]\nzl v19,s[28:31],s64 offen offset:2048 lds\nzv lgkmcnt(13)\nzm a[76:79],v[180:183],v[128:131],a[76:79]\n"
"zt v[210:211],v221 offset:45056\nzm a[104:107],v[180:183],v[132:135],a[104:107]\nzt v[212:213],v222 offset:40960\nzv lgkmcnt(13)\nzm a[80:83],v[184:187],v[128:131],a[80:83]\nzl v20,s[28:31],s64 offen offset:3072 lds\nzp s64,s64,57344\nzm a[108:111],v[184:187],v[132:135],a[108:111]\n"
"zt v[214:215],v222 offset:45056\nzv vmcnt(36)\ns_barrier\nzv lgkmcnt(12)\nzm a[56:59],v[188:191],v[136:139],a[56:59]\nzt v[160:161],v216 offset:49152\nzt v[162:163],v216 offset:53248\nzm a[84:87],v[188:191],v[140:143],a[84:87]\n"
"zt v[164:165],v217 offset:49152\nzt v[166:167],v217 offset:53248\nzv lgkmcnt(14)\nzm a[60:63],v[192:195],v[136:139],a[60:63]\nzt v[168:169],v218 offset:49152\nzt v[170:171],v218 offset:53248\nzm a[88:91],v[192:195],v[140:143],a[88:91]\nzl v[96:99],v16,s[24:27],s63 offen offset:0 sc1\n"
"zv lgkmcnt(14)\nzm a[64:67],v[196:199],v[136:139],a[64:67]\nzt v[172:173],v219 offset:49152\nzt v[174:175],v219 offset:53248\nzm a[92:95],v[196:199],v[140:143],a[92:95]\nzt v[176:177],v220 offset:49152\nzv lgkmcnt(15)\nzm a[68:71],v[200:203],v[136:139],a[68:71]\n"
"zl v[100:103],v16,s[24:27],s84 offen offset:0 sc1\nzm a[96:99],v[200:203],v[140:143],a[96:99]\nzt v[178:179],v220 offset:53248\nzv lgkmcnt(14)\nzm a[72:75],v[204:207],v[136:139],a[72:75]\nzt v[180:181],v221 offset:49152\nzm a[100:103],v[204:207],v[140:143],a[100:103]\nzl v[104:107],v16,s[24:27],s63 offen offset:1024 sc1\n"
"zv lgkmcnt(13)\nzm a[76:79],v[208:211],v[136:139],a[76:79]\nzt v[182:183],v221 offset:53248\nzm a[104:107],v[208:211],v[140:143],a[104:107]\nzt v[184:185],v222 offset:49152\nzv lgkmcnt(13)\nzm a[80:83],v[212:215],v[136:139],a[80:83]\nzl v[108:111],v16,s[24:27],s84 offen offset:1024 sc1\n"
"zp s63,s63,2048\nzp s84,s84,2048\nzm a[108:111],v[212:215],v[140:143],a[108:111]\nzt v[186:187],v222 offset:53248\nzv lgkmcnt(12)\nzm a[56:59],v[160:163],v[144:147],a[56:59]\nzt v[188:189],v216 offset:57344\nzt v[190:191],v216 offset:61440\n"
"zm a[84:87],v[160:163],v[148:151],a[84:87]\nzt v[192:193],v217 offset:57344\nzt v[194:195],v217 offset:61440\nzv lgkmcnt(14)\nzm a[60:63],v[164:167],v[144:147],a[60:63]\nzp m0,s65,81920\nzt v[196:197],v218 offset:57344\nzt v[198:199],v218 offset:61440\n"
"zm a[88:91],v[164:167],v[148:151],a[88:91]\nzl v17,s[28:31],s64 offen offset:0 lds\nzv lgkmcnt(14)\nzm a[64:67],v[168:171],v[144:147],a[64:67]\nzt v[200:201],v219 offset:57344\nzt v[202:203],v219 offset:61440\nzm a[92:95],v[168:171],v[148:151],a[92:95]\nzt v[204:205],v220 offset:57344\n"
"zv lgkmcnt(15)\nzm a[68:71],v[172:175],v[144:147],a[68:71]\nzl v18,s[28:31],s64 offen offset:1024 lds\nzm a[96:99],v[172:175],v[148:151],a[96:99]\nzt v[206:207],v220 offset:61440\nzv lgkmcnt(14)\nzm a[72:75],v[176:179],v[144:147],a[72:75]\nzt v[208:209],v221 offset:57344\n"
"zm a[100:103],v[176:179],v[148:151],a[100:103]\nzl v19,s[28:31],s64 offen offset:2048 lds\nzv lgkmcnt(13)\nzm a[76:79],v[180:183],v[144:147],a[76:79]\nzt v[210:211],v221 offset:61440\nzm a[104:107],v[180:183],v[148:151],a[104:107]\nzt v[212:213],v222 offset:57344\nzv lgkmcnt(13)\n"
"zm a[80:83],v[184:187],v[144:147],a[80:83]\nzl v20,s[28:31],s64 offen offset:3072 lds\nzp s64,s64,57344\nzm a[108:111],v[184:187],v[148:151],a[108:111]\nzt v[214:215],v222 offset:61440\nzv vmcnt(36)\ns_barrier\nzv lgkmcnt(12)\n"
"zm a[56:59],v[188:191],v[152:155],a[56:59]\nzt v[160:161],v21 offset:0\nzt v[162:163],v21 offset:4096\nzm a[84:87],v[188:191],v[156:159],a[84:87]\nzt v[164:165],v22 offset:0\nzt v[166:167],v22 offset:4096\nzv lgkmcnt(14)\nzm a[60:63],v[192:195],v[152:155],a[60:63]\n"
"zt v[168:169],v23 offset:0\nzt v[170:171],v23 offset:4096\nzm a[88:91],v[192:195],v[156:159],a[88:91]\nzl v[112:115],v16,s[24:27],s63 offen offset:0 sc1\nzv lgkmcnt(14)\nzm a[64:67],v[196:199],v[152:155],a[64:67]\nzt v[172:173],v24 offset:0\nzt v[174:175],v24 offset:4096\n"
"zm a[92:95],v[196:199],v[156:159],a[92:95]\nzt v[176:177],v25 offset:0\nzv lgkmcnt(15)\nzm a[68:71],v[200:203],v[152:155],a[68:71]\nzl v[116:119],v16,s[24:27],s84 offen offset:0 sc1\nzm a[96:99],v[200:203],v[156:159],a[96:99]\nzt v[178:179],v25 offset:4096\nzv lgkmcnt(14)\n"
"zm a[72:75],v[204:207],v[152:155],a[72:75]\nzt v[180:181],v26 offset:0\nzm a[100:103],v[204:207],v[156:159],a[100:103]\nzl v[120:123],v16,s[24:27],s63 offen offset:1024 sc1\nzv lgkmcnt(13)\nzm a[76:79],v[208:211],v[152:155],a[76:79]\nzt v[182:183],v26 offset:4096\nzm a[104:107],v[208:211],v[156:159],a[104:107]\n"
"zt v[184:185],v27 offset:0\nzv lgkmcnt(13)\nzm a[80:83],v[212:215],v[152:155],a[80:83]\nzl v[124:127],v16,s[24:27],s84 offen offset:1024 sc1\nzp s63,s63,2048\nzp s84,s84,2048\nzm a[108:111],v[212:215],v[156:159],a[108:111]\nzt v[186:187],v27 offset:4096\n"
"zv lgkmcnt(12)\nzm a[56:59],v[160:163],v[32:35],a[56:59]\nzt v[188:189],v21 offset:8192\nzt v[190:191],v21 offset:12288\nzm a[84:87],v[160:163],v[36:39],a[84:87]\nzt v[192:193],v22 offset:8192\nzt v[194:195],v22 offset:12288\nzv lgkmcnt(14)\n"
"zm a[60:63],v[164:167],v[32:35],a[60:63]\nzp m0,s65,98304\nzt v[196:197],v23 offset:8192\nzt v[198:199],v23 offset:12288\nzm a[88:91],v[164:167],v[36:39],a[88:91]\nzl v17,s[28:31],s64 offen offset:0 lds\nzv lgkmcnt(14)\nzm a[64:67],v[168:171],v[32:35],a[64:67]\n"
"zt v[200:201],v24 offset:8192\nzt v[202:203],v24 offset:12288\nzm a[92:95],v[168:171],v[36:39],a[92:95]\nzt v[204:205],v25 offset:8192\nzv lgkmcnt(15)\nzm a[68:71],v[172:175],v[32:35],a[68:71]\nzl v18,s[28:31],s64 offen offset:1024 lds\nzm a[96:99],v[172:175],v[36:39],a[96:99]\n"
"zt v[206:207],v25 offset:12288\nzv lgkmcnt(14)\nzm a[72:75],v[176:179],v[32:35],a[72:75]\nzt v[208:209],v26 offset:8192\nzm a[100:103],v[176:179],v[36:39],a[100:103]\nzl v19,s[28:31],s64 offen offset:2048 lds\nzv lgkmcnt(13)\nzm a[76:79],v[180:183],v[32:35],a[76:79]\n"
"zt v[210:211],v26 offset:12288\nzm a[104:107],v[180:183],v[36:39],a[104:107]\nzt v[212:213],v27 offset:8192\nzv lgkmcnt(13)\nzm a[80:83],v[184:187],v[32:35],a[80:83]\nzl v20,s[28:31],s64 offen offset:3072 lds\nzp s64,s64,57344\nzm a[108:111],v[184:187],v[36:39],a[108:111]\n"
"zt v[214:215],v27 offset:12288\nzv vmcnt(36)\ns_barrier\nzv lgkmcnt(12)\nzm a[56:59],v[188:191],v[40:43],a[56:59]\nzt v[160:161],v21 offset:16384\nzt v[162:163],v21 offset:20480\nzm a[84:87],v[188:191],v[44:47],a[84:87]\n"
"zt v[164:165],v22 offset:16384\nzt v[166:167],v22 offset:20480\nzv lgkmcnt(14)\nzm a[60:63],v[192:195],v[40:43],a[60:63]\nzt v[168:169],v23 offset:16384\nzt v[170:171],v23 offset:20480\nzm a[88:91],v[192:195],v[44:47],a[88:91]\nzl v[128:131],v16,s[24:27],s63 offen offset:0 sc1\n"
"zv lgkmcnt(14)\nzm a[64:67],v[196:199],v[40:43],a[64:67]\nzt v[172:173],v24 offset:16384\nzt v[174:175],v24 offset:20480\nzm a[92:95],v[196:199],v[44:47],a[92:95]\nzt v[176:177],v25 offset:16384\nzv lgkmcnt(15)\nzm a[68:71],v[200:203],v[40:43],a[68:71]\n"
"zl v[132:135],v16,s[24:27],s84 offen offset:0 sc1\nzm a[96:99],v[200:203],v[44:47],a[96:99]\nzt v[178:179],v25 offset:20480\nzv lgkmcnt(14)\nzm a[72:75],v[204:207],v[40:43],a[72:75]\nzt v[180:181],v26 offset:16384\nzm a[100:103],v[204:207],v[44:47],a[100:103]\nzl v[136:139],v16,s[24:27],s63 offen offset:1024 sc1\n"
"zv lgkmcnt(13)\nzm a[76:79],v[208:211],v[40:43],a[76:79]\nzt v[182:183],v26 offset:20480\nzm a[104:107],v[208:211],v[44:47],a[104:107]\nzt v[184:185],v27 offset:16384\nzv lgkmcnt(13)\nzm a[80:83],v[212:215],v[40:43],a[80:83]\nzl v[140:143],v16,s[24:27],s84 offen offset:1024 sc1\n"
"zp s63,s63,2048\nzp s84,s84,2048\nzm a[108:111],v[212:215],v[44:47],a[108:111]\nzt v[186:187],v27 offset:20480\nzv lgkmcnt(12)\nzm a[56:59],v[160:163],v[48:51],a[56:59]\nzt v[188:189],v21 offset:24576\nzm a[84:87],v[160:163],v[52:55],a[84:87]\n"
"zt v[190:191],v21 offset:28672\nzv lgkmcnt(12)\nzm a[60:63],v[164:167],v[48:51],a[60:63]\nzt v[192:193],v22 offset:24576\nzm a[88:91],v[164:167],v[52:55],a[88:91]\nzt v[194:195],v22 offset:28672\nzv lgkmcnt(12)\nzm a[64:67],v[168:171],v[48:51],a[64:67]\n"
"zt v[196:197],v23 offset:24576\nzm a[92:95],v[168:171],v[52:55],a[92:95]\nzt v[198:199],v23 offset:28672\nzv lgkmcnt(12)\nzm a[68:71],v[172:175],v[48:51],a[68:71]\nzt v[200:201],v24 offset:24576\nzm a[96:99],v[172:175],v[52:55],a[96:99]\nzt v[202:203],v24 offset:28672\n"
"zv lgkmcnt(12)\nzm a[72:75],v[176:179],v[48:51],a[72:75]\nzt v[204:205],v25 offset:24576\nzm a[100:103],v[176:179],v[52:55],a[100:103]\nzt v[206:207],v25 offset:28672\nzv lgkmcnt(12)\nzm a[76:79],v[180:183],v[48:51],a[76:79]\nzt v[208:209],v26 offset:24576\n"
"zm a[104:107],v[180:183],v[52:55],a[104:107]\nzt v[210:211],v26 offset:28672\nzv lgkmcnt(12)\nzm a[80:83],v[184:187],v[48:51],a[80:83]\nzt v[212:213],v27 offset:24576\nzm a[108:111],v[184:187],v[52:55],a[108:111]\nzt v[214:215],v27 offset:28672\nzv vmcnt(32)\n"
"s_barrier\nzv lgkmcnt(12)\nzm a[56:59],v[188:191],v[56:59],a[56:59]\nzt v[160:161],v21 offset:32768\nzm a[84:87],v[188:191],v[60:63],a[84:87]\nzt v[162:163],v21 offset:36864\nzv lgkmcnt(12)\nzm a[60:63],v[192:195],v[56:59],a[60:63]\n"
"zt v[164:165],v22 offset:32768\nzm a[88:91],v[192:195],v[60:63],a[88:91]\nzt v[166:167],v22 offset:36864\nzv lgkmcnt(12)\nzm a[64:67],v[196:199],v[56:59],a[64:67]\nzt v[168:169],v23 offset:32768\nzm a[92:95],v[196:199],v[60:63],a[92:95]\nzt v[170:171],v23 offset:36864\n"
"zv lgkmcnt(12)\nzm a[68:71],v[200:203],v[56:59],a[68:71]\nzt v[172:173],v24 offset:32768\nzm a[96:99],v[200:203],v[60:63],a[96:99]\nzt v[174:175],v24 offset:36864\nzv lgkmcnt(12)\nzm a[72:75],v[204:207],v[56:59],a[72:75]\nzt v[176:177],v25 offset:32768\n"
"zm a[100:103],v[204:207],v[60:63],a[100:103]\nzt v[178:179],v25 offset:36864\nzv lgkmcnt(12)\nzm a[76:79],v[208:211],v[56:59],a[76:79]\nzt v[180:181],v26 offset:32768\nzm a[104:107],v[208:211],v[60:63],a[104:107]\nzt v[182:183],v26 offset:36864\nzv lgkmcnt(12)\n"
"zm a[80:83],v[212:215],v[56:59],a[80:83]\nzt v[184:185],v27 offset:32768\nzm a[108:111],v[212:215],v[60:63],a[108:111]\nzt v[186:187],v27 offset:36864\nzv lgkmcnt(12)\nzm a[56:59],v[160:163],v[64:67],a[56:59]\nzt v[188:189],v21 offset:40960\nzm a[84:87],v[160:163],v[68:71],a[84:87]\n"
"zt v[190:191],v21 offset:45056\nzv lgkmcnt(12)\nzm a[60:63],v[164:167],v[64:67],a[60:63]\nzt v[192:193],v22 offset:40960\nzm a[88:91],v[164:167],v[68:71],a[88:91]\nzt v[194:195],v22 offset:45056\nzv lgkmcnt(12)\nzm a[64:67],v[168:171],v[64:67],a[64:67]\n"
"zt v[196:197],v23 offset:40960\nzm a[92:95],v[168:171],v[68:71],a[92:95]\nzt v[198:199],v23 offset:45056\nzv lgkmcnt(12)\nzm a[68:71],v[172:175],v[64:67],a[68:71]\nzt v[200:201],v24 offset:40960\nzm a[96:99],v[172:175],v[68:71],a[96:99]\nzt v[202:203],v24 offset:45056\n"
"zv lgkmcnt(12)\nzm a[72:75],v[176:179],v[64:67],a[72:75]\nzt v[204:205],v25 offset:40960\nzm a[100:103],v[176:179],v[68:71],a[100:103]\nzt v[206:207],v25 offset:45056\nzv lgkmcnt(12)\nzm a[76:79],v[180:183],v[64:67],a[76:79]\nzt v[208:209],v26 offset:40960\n"
"zm a[104:107],v[180:183],v[68:71],a[104:107]\nzt v[210:211],v26 offset:45056\nzv lgkmcnt(12)\nzm a[80:83],v[184:187],v[64:67],a[80:83]\nzt v[212:213],v27 offset:40960\nzm a[108:111],v[184:187],v[68:71],a[108:111]\nzt v[214:215],v27 offset:45056\nzv vmcnt(24)\n"
"s_barrier\nzv lgkmcnt(12)\nzm a[56:59],v[188:191],v[72:75],a[56:59]\nzt v[160:161],v21 offset:49152\nzm a[84:87],v[188:191],v[76:79],a[84:87]\nzt v[162:163],v21 offset:53248\nzv lgkmcnt(12)\nzm a[60:63],v[192:195],v[72:75],a[60:63]\n"
"zt v[164:165],v22 offset:49152\nzm a[88:91],v[192:195],v[76:79],a[88:91]\nzt v[166:167],v22 offset:53248\nzv lgkmcnt(12)\nzm a[64:67],v[196:199],v[72:75],a[64:67]\nzt v[168:169],v23 offset:49152\nzm a[92:95],v[196:199],v[76:79],a[92:95]\nzt v[170:171],v23 offset:53248\n"
"zv lgkmcnt(12)\nzm a[68:71],v[200:203],v[72:75],a[68:71]\nzt v[172:173],v24 offset:49152\nzm a[96:99],v[200:203],v[76:79],a[96:99]\nzt v[174:175],v24 offset:53248\nzv lgkmcnt(12)\nzm a[72:75],v[204:207],v[72:75],a[72:75]\nzt v[176:177],v25 offset:49152\n"
"zm a[100:103],v[204:207],v[76:79],a[100:103]\nzt v[178:179],v25 offset:53248\nzv lgkmcnt(12)\nzm a[76:79],v[208:211],v[72:75],a[76:79]\nzt v[180:181],v26 offset:49152\nzm a[104:107],v[208:211],v[76:79],a[104:107]\nzt v[182:183],v26 offset:53248\nzv lgkmcnt(12)\n"
"zm a[80:83],v[212:215],v[72:75],a[80:83]\nzt v[184:185],v27 offset:49152\nzm a[108:111],v[212:215],v[76:79],a[108:111]\nzt v[186:187],v27 offset:53248\nzv lgkmcnt(12)\nzm a[56:59],v[160:163],v[80:83],a[56:59]\nzt v[188:189],v21 offset:57344\nzm a[84:87],v[160:163],v[84:87],a[84:87]\n"
"zt v[190:191],v21 offset:61440\nzv lgkmcnt(12)\nzm a[60:63],v[164:167],v[80:83],a[60:63]\nzt v[192:193],v22 offset:57344\nzm a[88:91],v[164:167],v[84:87],a[88:91]\nzt v[194:195],v22 offset:61440\nzv lgkmcnt(12)\nzm a[64:67],v[168:171],v[80:83],a[64:67]\n"
"zt v[196:197],v23 offset:57344\nzm a[92:95],v[168:171],v[84:87],a[92:95]\nzt v[198:199],v23 offset:61440\nzv lgkmcnt(12)\nzm a[68:71],v[172:175],v[80:83],a[68:71]\nzt v[200:201],v24 offset:57344\nzm a[96:99],v[172:175],v[84:87],a[96:99]\nzt v[202:203],v24 offset:61440\n"
"zv lgkmcnt(12)\nzm a[72:75],v[176:179],v[80:83],a[72:75]\nzt v[204:205],v25 offset:57344\nzm a[100:103],v[176:179],v[84:87],a[100:103]\nzt v[206:207],v25 offset:61440\nzv lgkmcnt(12)\nzm a[76:79],v[180:183],v[80:83],a[76:79]\nzt v[208:209],v26 offset:57344\n"
"zm a[104:107],v[180:183],v[84:87],a[104:107]\nzt v[210:211],v26 offset:61440\nzv lgkmcnt(12)\nzm a[80:83],v[184:187],v[80:83],a[80:83]\nzt v[212:213],v27 offset:57344\nzm a[108:111],v[184:187],v[84:87],a[108:111]\nzt v[214:215],v27 offset:61440\nzv vmcnt(16)\n"
"s_barrier\nzv lgkmcnt(12)\nzm a[56:59],v[188:191],v[88:91],a[56:59]\nzt v[160:161],v216 offset:0\nzm a[84:87],v[188:191],v[92:95],a[84:87]\nzt v[162:163],v216 offset:4096\nzv lgkmcnt(12)\nzm a[60:63],v[192:195],v[88:91],a[60:63]\n"
"zt v[164:165],v217 offset:0\nzm a[88:91],v[192:195],v[92:95],a[88:91]\nzt v[166:167],v217 offset:4096\nzv lgkmcnt(12)\nzm a[64:67],v[196:199],v[88:91],a[64:67]\nzt v[168:169],v218 offset:0\nzm a[92:95],v[196:199],v[92:95],a[92:95]\nzt v[170:171],v218 offset:4096\n"
"zv lgkmcnt(12)\nzm a[68:71],v[200:203],v[88:91],a[68:71]\nzt v[172:173],v219 offset:0\nzm a[96:99],v[200:203],v[92:95],a[96:99]\nzt v[174:175],v219 offset:4096\nzv lgkmcnt(12)\nzm a[72:75],v[204:207],v[88:91],a[72:75]\nzt v[176:177],v220 offset:0\n"
"zm a[100:103],v[204:207],v[92:95],a[100:103]\nzt v[178:179],v220 offset:4096\nzv lgkmcnt(12)\nzm a[76:79],v[208:211],v[88:91],a[76:79]\nzt v[180:181],v221 offset:0\nzm a[104:107],v[208:211],v[92:95],a[104:107]\nzt v[182:183],v221 offset:4096\nzv lgkmcnt(12)\n"
"zm a[80:83],v[212:215],v[88:91],a[80:83]\nzt v[184:185],v222 offset:0\nzm a[108:111],v[212:215],v[92:95],a[108:111]\nzt v[186:187],v222 offset:4096\nv_add_u32 v15,14336,v14\nbuffer_load_dwordx2 v[224:225],v14,s[36:39],0 offen offset:0\nbuffer_load_dwordx2 v[226:227],v14,s[36:39],0 offen offset:32\nbuffer_load_dwordx2 v[228:229],v15,s[36:39],0 offen offset:0\n"
"buffer_load_dwordx2 v[230:231],v15,s[36:39],0 offen offset:32\nzv lgkmcnt(12)\nzm a[56:59],v[160:163],v[96:99],a[56:59]\nzt v[188:189],v216 offset:8192\nzm a[84:87],v[160:163],v[100:103],a[84:87]\nzt v[190:191],v216 offset:12288\nzv lgkmcnt(12)\nzm a[60:63],v[164:167],v[96:99],a[60:63]\n"
"zt v[192:193],v217 offset:8192\nzm a[88:91],v[164:167],v[100:103],a[88:91]\nzt v[194:195],v217 offset:12288\nzv lgkmcnt(12)\nzm a[64:67],v[168:171],v[96:99],a[64:67]\nzt v[196:197],v218 offset:8192\nzm a[92:95],v[168:171],v[100:103],a[92:95]\nzt v[198:199],v218 offset:12288\n"
"zv lgkmcnt(12)\nzm a[68:71],v[172:175],v[96:99],a[68:71]\nzt v[200:201],v219 offset:8192\nzm a[96:99],v[172:175],v[100:103],a[96:99]\nzt v[202:203],v219 offset:12288\nzv lgkmcnt(12)\nzm a[72:75],v[176:179],v[96:99],a[72:75]\nzt v[204:205],v220 offset:8192\n"
"zm a[100:103],v[176:179],v[100:103],a[100:103]\nzt v[206:207],v220 offset:12288\nzv lgkmcnt(12)\nzm a[76:79],v[180:183],v[96:99],a[76:79]\nzt v[208:209],v221 offset:8192\nzm a[104:107],v[180:183],v[100:103],a[104:107]\nzt v[210:211],v221 offset:12288\nzv lgkmcnt(12)\n"
"zm a[80:83],v[184:187],v[96:99],a[80:83]\nzt v[212:213],v222 offset:8192\nzm a[108:111],v[184:187],v[100:103],a[108:111]\nzt v[214:215],v222 offset:12288\nzv vmcnt(12)\ns_barrier\nzv lgkmcnt(12)\nzm a[56:59],v[188:191],v[104:107],a[56:59]\n"
"zt v[160:161],v216 offset:16384\nzm a[84:87],v[188:191],v[108:111],a[84:87]\nzt v[162:163],v216 offset:20480\nzv lgkmcnt(12)\nzm a[60:63],v[192:195],v[104:107],a[60:63]\nzt v[164:165],v217 offset:16384\nzm a[88:91],v[192:195],v[108:111],a[88:91]\nzt v[166:167],v217 offset:20480\n"
"zv lgkmcnt(12)\nzm a[64:67],v[196:199],v[104:107],a[64:67]\nzt v[168:169],v218 offset:16384\nzm a[92:95],v[196:199],v[108:111],a[92:95]\nzt v[170:171],v218 offset:20480\nzv lgkmcnt(12)\nzm a[68:71],v[200:203],v[104:107],a[68:71]\nzt v[172:173],v219 offset:16384\n"
"zm a[96:99],v[200:203],v[108:111],a[96:99]\nzt v[174:175],v219 offset:20480\nzv lgkmcnt(12)\nzm a[72:75],v[204:207],v[104:107],a[72:75]\nzt v[176:177],v220 offset:16384\nzm a[100:103],v[204:207],v[108:111],a[100:103]\nzt v[178:179],v220 offset:20480\nzv lgkmcnt(12)\n"
"zm a[76:79],v[208:211],v[104:107],a[76:79]\nzt v[180:181],v221 offset:16384\nzm a[104:107],v[208:211],v[108:111],a[104:107]\nzt v[182:183],v221 offset:20480\nzv lgkmcnt(12)\nzm a[80:83],v[212:215],v[104:107],a[80:83]\nzt v[184:185],v222 offset:16384\nzm a[108:111],v[212:215],v[108:111],a[108:111]\n"
"zt v[186:187],v222 offset:20480\nzv lgkmcnt(12)\nzm a[56:59],v[160:163],v[112:115],a[56:59]\nzt v[188:189],v216 offset:24576\nzm a[84:87],v[160:163],v[116:119],a[84:87]\nzt v[190:191],v216 offset:28672\nzv lgkmcnt(12)\nzm a[60:63],v[164:167],v[112:115],a[60:63]\n"
"zt v[192:193],v217 offset:24576\nzm a[88:91],v[164:167],v[116:119],a[88:91]\nzt v[194:195],v217 offset:28672\nzv lgkmcnt(12)\nzm a[64:67],v[168:171],v[112:115],a[64:67]\nzt v[196:197],v218 offset:24576\nzm a[92:95],v[168:171],v[116:119],a[92:95]\nzt v[198:199],v218 offset:28672\n"
"zv lgkmcnt(12)\nzm a[68:71],v[172:175],v[112:115],a[68:71]\nzt v[200:201],v219 offset:24576\nzm a[96:99],v[172:175],v[116:119],a[96:99]\nzt v[202:203],v219 offset:28672\nzv lgkmcnt(12)\nzm a[72:75],v[176:179],v[112:115],a[72:75]\nzt v[204:205],v220 offset:24576\n"
"zm a[100:103],v[176:179],v[116:119],a[100:103]\nzt v[206:207],v220 offset:28672\nzv lgkmcnt(12)\nzm a[76:79],v[180:183],v[112:115],a[76:79]\nzt v[208:209],v221 offset:24576\nzm a[104:107],v[180:183],v[116:119],a[104:107]\nzt v[210:211],v221 offset:28672\nzv lgkmcnt(12)\n"
"zm a[80:83],v[184:187],v[112:115],a[80:83]\nzt v[212:213],v222 offset:24576\nzm a[108:111],v[184:187],v[116:119],a[108:111]\nzt v[214:215],v222 offset:28672\nzv vmcnt(4)\ns_barrier\nzv lgkmcnt(12)\nzm a[56:59],v[188:191],v[120:123],a[56:59]\n"
"zt v[160:161],v216 offset:32768\nzm a[84:87],v[188:191],v[124:127],a[84:87]\nzt v[162:163],v216 offset:36864\nzv lgkmcnt(12)\nzm a[60:63],v[192:195],v[120:123],a[60:63]\nzt v[164:165],v217 offset:32768\nzm a[88:91],v[192:195],v[124:127],a[88:91]\nzt v[166:167],v217 offset:36864\n"
"zv lgkmcnt(12)\nzm a[64:67],v[196:199],v[120:123],a[64:67]\nzt v[168:169],v218 offset:32768\nzm a[92:95],v[196:199],v[124:127],a[92:95]\nzt v[170:171],v218 offset:36864\nzv lgkmcnt(12)\nzm a[68:71],v[200:203],v[120:123],a[68:71]\nzt v[172:173],v219 offset:32768\n"
"zm a[96:99],v[200:203],v[124:127],a[96:99]\nzt v[174:175],v219 offset:36864\nzv lgkmcnt(12)\nzm a[72:75],v[204:207],v[120:123],a[72:75]\nzt v[176:177],v220 offset:32768\nzm a[100:103],v[204:207],v[124:127],a[100:103]\nzt v[178:179],v220 offset:36864\nzv lgkmcnt(12)\n"
"zm a[76:79],v[208:211],v[120:123],a[76:79]\nzt v[180:181],v221 offset:32768\nzm a[104:107],v[208:211],v[124:127],a[104:107]\nzt v[182:183],v221 offset:36864\nzv lgkmcnt(12)\nzm a[80:83],v[212:215],v[120:123],a[80:83]\nzt v[184:185],v222 offset:32768\nzm a[108:111],v[212:215],v[124:127],a[108:111]\n"
"zt v[186:187],v222 offset:36864\nzv lgkmcnt(12)\nzm a[56:59],v[160:163],v[128:131],a[56:59]\nzt v[188:189],v216 offset:40960\nzm a[84:87],v[160:163],v[132:135],a[84:87]\nzt v[190:191],v216 offset:45056\nzv lgkmcnt(12)\nzm a[60:63],v[164:167],v[128:131],a[60:63]\n"
"zt v[192:193],v217 offset:40960\nzm a[88:91],v[164:167],v[132:135],a[88:91]\nzt v[194:195],v217 offset:45056\nzv lgkmcnt(12)\nzm a[64:67],v[168:171],v[128:131],a[64:67]\nzt v[196:197],v218 offset:40960\nzm a[92:95],v[168:171],v[132:135],a[92:95]\nzt v[198:199],v218 offset:45056\n"
"zv lgkmcnt(12)\nzm a[68:71],v[172:175],v[128:131],a[68:71]\nzt v[200:201],v219 offset:40960\nzm a[96:99],v[172:175],v[132:135],a[96:99]\nzt v[202:203],v219 offset:45056\nzv lgkmcnt(12)\nzm a[72:75],v[176:179],v[128:131],a[72:75]\nzt v[204:205],v220 offset:40960\n"
"zm a[100:103],v[176:179],v[132:135],a[100:103]\nzt v[206:207],v220 offset:45056\nzv lgkmcnt(12)\nzm a[76:79],v[180:183],v[128:131],a[76:79]\nzt v[208:209],v221 offset:40960\nzm a[104:107],v[180:183],v[132:135],a[104:107]\nzt v[210:211],v221 offset:45056\nzv lgkmcnt(12)\n"
"zm a[80:83],v[184:187],v[128:131],a[80:83]\nzt v[212:213],v222 offset:40960\nzm a[108:111],v[184:187],v[132:135],a[108:111]\nzt v[214:215],v222 offset:45056\nzv lgkmcnt(12)\nzm a[56:59],v[188:191],v[136:139],a[56:59]\nzm a[84:87],v[188:191],v[140:143],a[84:87]\nzv lgkmcnt(10)\n"
"zm a[60:63],v[192:195],v[136:139],a[60:63]\nzm a[88:91],v[192:195],v[140:143],a[88:91]\nzv lgkmcnt(8)\nzm a[64:67],v[196:199],v[136:139],a[64:67]\nzm a[92:95],v[196:199],v[140:143],a[92:95]\nzv lgkmcnt(6)\nzm a[68:71],v[200:203],v[136:139],a[68:71]\nzm a[96:99],v[200:203],v[140:143],a[96:99]\n"
"zv lgkmcnt(4)\nzm a[72:75],v[204:207],v[136:139],a[72:75]\nzm a[100:103],v[204:207],v[140:143],a[100:103]\nzv lgkmcnt(2)\nzm a[76:79],v[208:211],v[136:139],a[76:79]\nzm a[104:107],v[208:211],v[140:143],a[104:107]\nzv lgkmcnt(0)\nzm a[80:83],v[212:215],v[136:139],a[80:83]\n"
"zm a[108:111],v[212:215],v[140:143],a[108:111]\ns_nop 7\ns_lshl_b32 s77,s62,11\nzp s77,s77,8192\ns_cmp_eq_u32 s62,0\ns_cbranch_scc1 L_O_nss0\nzp s76,s77,0\nzr v232,a56\n"
"zr v233,a57\nzr v234,a58\nzr v235,a59\nzr v236,a84\nzr v237,a85\nzr v238,a86\nzr v239,a87\nzc v240,v232,v233\n"
"zc v241,v234,v235\nzc v242,v236,v237\nzc v243,v238,v239\nzs v[240:243],v16,s[52:55],s76 offen offset:0 sc1\nzr v244,a60\nzr v245,a61\nzr v246,a62\nzr v247,a63\n"
"zr v248,a88\nzr v249,a89\nzr v250,a90\nzr v251,a91\nzc v252,v244,v245\nzc v253,v246,v247\nzc v254,v248,v249\nzc v255,v250,v251\n"
"zs v[252:255],v16,s[52:55],s76 offen offset:1024 sc1\nL_O_nss0:\ns_cmp_eq_u32 s62,1\ns_cbranch_scc1 L_O_nss1\nzp s76,s77,16384\nzr v232,a64\nzr v233,a65\nzr v234,a66\n"
"zr v235,a67\nzr v236,a92\nzr v237,a93\nzr v238,a94\nzr v239,a95\nzc v240,v232,v233\nzc v241,v234,v235\nzc v242,v236,v237\n"
"zc v243,v238,v239\nzs v[240:243],v16,s[52:55],s76 offen offset:0 sc1\nzr v244,a68\nzr v245,a69\nzr v246,a70\nzr v247,a71\nzr v248,a96\nzr v249,a97\n"
"zr v250,a98\nzr v251,a99\nzc v252,v244,v245\nzc v253,v246,v247\nzc v254,v248,v249\nzc v255,v250,v251\nzs v[252:255],v16,s[52:55],s76 offen offset:1024 sc1\nL_O_nss1:\n"
"s_cmp_eq_u32 s62,2\ns_cbranch_scc1 L_O_nss2\nzp s76,s77,32768\nzr v232,a72\nzr v233,a73\nzr v234,a74\nzr v235,a75\nzr v236,a100\n"
"zr v237,a101\nzr v238,a102\nzr v239,a103\nzc v240,v232,v233\nzc v241,v234,v235\nzc v242,v236,v237\nzc v243,v238,v239\nzs v[240:243],v16,s[52:55],s76 offen offset:0 sc1\n"
"zr v244,a76\nzr v245,a77\nzr v246,a78\nzr v247,a79\nzr v248,a104\nzr v249,a105\nzr v250,a106\nzr v251,a107\n"
"zc v252,v244,v245\nzc v253,v246,v247\nzc v254,v248,v249\nzc v255,v250,v251\nzs v[252:255],v16,s[52:55],s76 offen offset:1024 sc1\nL_O_nss2:\ns_cmp_eq_u32 s62,3\ns_cbranch_scc1 L_O_nss3\n"
"zp s76,s77,49152\nzr v232,a80\nzr v233,a81\nzr v234,a82\nzr v235,a83\nzr v236,a108\nzr v237,a109\nzr v238,a110\n"
"zr v239,a111\nzc v240,v232,v233\nzc v241,v234,v235\nzc v242,v236,v237\nzc v243,v238,v239\nzs v[240:243],v16,s[52:55],s76 offen offset:0 sc1\nL_O_nss3:\ns_cmp_eq_u32 s62,0\n"
"s_cbranch_scc0 L_O_no0\nzr v104,a0\nzr v105,a1\nzr v106,a2\nzr v107,a3\nzr v108,a56\nzr v109,a57\nzr v110,a58\n"
"zr v111,a59\nzr v112,a4\nzr v113,a5\nzr v114,a6\nzr v115,a7\nzr v116,a60\nzr v117,a61\nzr v118,a62\n"
"zr v119,a63\nzr v120,a28\nzr v121,a29\nzr v122,a30\nzr v123,a31\nzr v124,a84\nzr v125,a85\nzr v126,a86\n"
"zr v127,a87\nzr v128,a32\nzr v129,a33\nzr v130,a34\nzr v131,a35\nzr v132,a88\nzr v133,a89\nzr v134,a90\n"
"zr v135,a91\nL_O_no0:\ns_cmp_eq_u32 s62,1\ns_cbranch_scc0 L_O_no1\nzr v104,a8\nzr v105,a9\nzr v106,a10\nzr v107,a11\n"
"zr v108,a64\nzr v109,a65\nzr v110,a66\nzr v111,a67\nzr v112,a12\nzr v113,a13\nzr v114,a14\nzr v115,a15\n"
"zr v116,a68\nzr v117,a69\nzr v118,a70\nzr v119,a71\nzr v120,a36\nzr v121,a37\nzr v122,a38\nzr v123,a39\n"
"zr v124,a92\nzr v125,a93\nzr v126,a94\nzr v127,a95\nzr v128,a40\nzr v129,a41\nzr v130,a42\nzr v131,a43\n"
"zr v132,a96\nzr v133,a97\nzr v134,a98\nzr v135,a99\nL_O_no1:\ns_cmp_eq_u32 s62,2\ns_cbranch_scc0 L_O_no2\nzr v104,a16\n"
"zr v105,a17\nzr v106,a18\nzr v107,a19\nzr v108,a72\nzr v109,a73\nzr v110,a74\nzr v111,a75\nzr v112,a20\n"
"zr v113,a21\nzr v114,a22\nzr v115,a23\nzr v116,a76\nzr v117,a77\nzr v118,a78\nzr v119,a79\nzr v120,a44\n"
"zr v121,a45\nzr v122,a46\nzr v123,a47\nzr v124,a100\nzr v125,a101\nzr v126,a102\nzr v127,a103\nzr v128,a48\n"
"zr v129,a49\nzr v130,a50\nzr v131,a51\nzr v132,a104\nzr v133,a105\nzr v134,a106\nzr v135,a107\nL_O_no2:\n"
"s_cmp_eq_u32 s62,3\ns_cbranch_scc0 L_O_no3\nzr v104,a24\nzr v105,a25\nzr v106,a26\nzr v107,a27\nzr v108,a80\nzr v109,a81\n"
"zr v110,a82\nzr v111,a83\nzr v120,a52\nzr v121,a53\nzr v122,a54\nzr v123,a55\nzr v124,a108\nzr v125,a109\n"
"zr v126,a110\nzr v127,a111\nL_O_no3:\ns_lshl_b32 s76,s62,2\nv_mov_b32 v24,s76\nv_mov_b32 v25,s34\nv_and_b32 v28,3,v6\nzh v26,2,v28\n"
"s_mov_b32 s80,0\nv_mov_b32 v30,0x3f80\nv_mov_b32 v31,0x3f800000\nzv vmcnt(0)\nglobal_store_dword v24,v25,s[68:69] sc1\nv_and_b32 v233,15,v6\nzh v233,2,v233\ns_lshl_b32 s76,s60,2\n"
"zp s76,s76,s61\ns_lshl_b32 s76,s76,6\nzp s76,s76,20480\nv_add_u32 v233,s76,v233\ns_lshl_b32 s76,0x1111,s62\ns_xor_b32 s76,s76,0xffff\ns_cmp_eq_u32 s23,0\ns_cbranch_scc0 L_wwb_ex\n"
"s_mov_b32 s80,0\nL_ww_ex:\nglobal_load_dword v232,v233,s[94:95] sc1\nzv vmcnt(0)\nv_cmp_ne_u32 vcc,s34,v232\ns_nop 2\ns_and_b32 vcc_lo,vcc_lo,s76\ns_cmp_eq_u32 vcc_lo,0\n"
"s_cbranch_scc1 L_wwb_ex\nzp s80,s80,1\ns_cmp_lt_u32 s80,300000\ns_cbranch_scc0 L_wwb_ex\ns_sleep 2\ns_branch L_ww_ex\nL_wwb_ex:\ns_barrier\n"
"L_O_got:\nzp s77,s62,1\ns_and_b32 s77,s77,3\ns_lshl_b32 s77,s77,11\ns_lshl_b32 s76,s62,14\nzp s77,s77,s76\nzl v[32:35],v16,s[52:55],s77 offen offset:0 sc1\nzl v[36:39],v16,s[52:55],s77 offen offset:1024 sc1\n"
"zp s77,s77,8192\nzl v[40:43],v16,s[52:55],s77 offen offset:0 sc1\nzl v[44:47],v16,s[52:55],s77 offen offset:1024 sc1\nzp s77,s62,2\ns_and_b32 s77,s77,3\ns_lshl_b32 s77,s77,11\ns_lshl_b32 s76,s62,14\nzp s77,s77,s76\n"
"zl v[48:51],v16,s[52:55],s77 offen offset:0 sc1\nzl v[52:55],v16,s[52:55],s77 offen offset:1024 sc1\nzp s77,s77,8192\nzl v[56:59],v16,s[52:55],s77 offen offset:0 sc1\nzl v[60:63],v16,s[52:55],s77 offen offset:1024 sc1\nzp s77,s62,3\ns_and_b32 s77,s77,3\ns_lshl_b32 s77,s77,11\n"
"s_lshl_b32 s76,s62,14\nzp s77,s77,s76\nzl v[64:67],v16,s[52:55],s77 offen offset:0 sc1\nzl v[68:71],v16,s[52:55],s77 offen offset:1024 sc1\nzp s77,s77,8192\nzl v[72:75],v16,s[52:55],s77 offen offset:0 sc1\nzl v[76:79],v16,s[52:55],s77 offen offset:1024 sc1\ns_mov_b32 s76,0xbfb8aa3b\n"
"zv vmcnt(0)\nzz v104,v32,v30\nzz v105,v32,v31\nzz v106,v33,v30\nzz v107,v33,v31\nzz v120,v34,v30\nzz v121,v34,v31\nzz v122,v35,v30\n"
"zz v123,v35,v31\nzz v112,v36,v30\nzz v113,v36,v31\nzz v114,v37,v30\nzz v115,v37,v31\nzz v128,v38,v30\nzz v129,v38,v31\nzz v130,v39,v30\n"
"zz v131,v39,v31\nzz v108,v40,v30\nzz v109,v40,v31\nzz v110,v41,v30\nzz v111,v41,v31\nzz v124,v42,v30\nzz v125,v42,v31\nzz v126,v43,v30\n"
"zz v127,v43,v31\nzz v116,v44,v30\nzz v117,v44,v31\nzz v118,v45,v30\nzz v119,v45,v31\nzz v132,v46,v30\nzz v133,v46,v31\nzz v134,v47,v30\n"
"zz v135,v47,v31\nzz v104,v48,v30\nzz v105,v48,v31\nzz v106,v49,v30\nzz v107,v49,v31\nzz v120,v50,v30\nzz v121,v50,v31\nzz v122,v51,v30\n"
"zz v123,v51,v31\nzz v112,v52,v30\nzz v113,v52,v31\nzz v114,v53,v30\nzz v115,v53,v31\nzz v128,v54,v30\nzz v129,v54,v31\nzz v130,v55,v30\n"
"zz v131,v55,v31\nzz v108,v56,v30\nzz v109,v56,v31\nzz v110,v57,v30\nzz v111,v57,v31\nzz v124,v58,v30\nzz v125,v58,v31\nzz v126,v59,v30\n"
"zz v127,v59,v31\nzz v116,v60,v30\nzz v117,v60,v31\nzz v118,v61,v30\nzz v119,v61,v31\nzz v132,v62,v30\nzz v133,v62,v31\nzz v134,v63,v30\n"
"zz v135,v63,v31\nzz v104,v64,v30\nzz v105,v64,v31\nzz v106,v65,v30\nzz v107,v65,v31\nzz v120,v66,v30\nzz v121,v66,v31\nzz v122,v67,v30\n"
"zz v123,v67,v31\nzz v112,v68,v30\nzz v113,v68,v31\nzz v114,v69,v30\nzz v115,v69,v31\nzz v128,v70,v30\nzz v129,v70,v31\nzz v130,v71,v30\n"
"zz v131,v71,v31\nzz v108,v72,v30\nzz v109,v72,v31\nzz v110,v73,v30\nzz v111,v73,v31\nzz v124,v74,v30\nzz v125,v74,v31\nzz v126,v75,v30\n"
"zz v127,v75,v31\nzz v116,v76,v30\nzz v117,v76,v31\nzz v118,v77,v30\nzz v119,v77,v31\nzz v132,v78,v30\nzz v133,v78,v31\nzz v134,v79,v30\n"
"zz v135,v79,v31\ns_mov_b32 s78,0xbfb8aa3b\ns_mov_b32 s79,0xbfb8aa3b\nv_pk_mul_f32 v[160:161],v[104:105],s[78:79]\nv_pk_mul_f32 v[162:163],v[106:107],s[78:79]\nv_pk_mul_f32 v[164:165],v[112:113],s[78:79]\nv_pk_mul_f32 v[166:167],v[114:115],s[78:79]\nv_pk_mul_f32 v[168:169],v[120:121],s[78:79]\n"
"v_pk_mul_f32 v[170:171],v[122:123],s[78:79]\nv_pk_mul_f32 v[172:173],v[128:129],s[78:79]\nv_pk_mul_f32 v[174:175],v[130:131],s[78:79]\nze v160,v160\nze v161,v161\nze v162,v162\nze v163,v163\nze v164,v164\n"
"ze v165,v165\nze v166,v166\nze v167,v167\nze v168,v168\nze v169,v169\nze v170,v170\nze v171,v171\nze v172,v172\n"
"ze v173,v173\nze v174,v174\nze v175,v175\nv_pk_add_f32 v[160:161],v[160:161],1.0 op_sel_hi:[1,0]\nv_pk_add_f32 v[162:163],v[162:163],1.0 op_sel_hi:[1,0]\nv_pk_add_f32 v[164:165],v[164:165],1.0 op_sel_hi:[1,0]\nv_pk_add_f32 v[166:167],v[166:167],1.0 op_sel_hi:[1,0]\nv_pk_add_f32 v[168:169],v[168:169],1.0 op_sel_hi:[1,0]\n"
"v_pk_add_f32 v[170:171],v[170:171],1.0 op_sel_hi:[1,0]\nv_pk_add_f32 v[172:173],v[172:173],1.0 op_sel_hi:[1,0]\nv_pk_add_f32 v[174:175],v[174:175],1.0 op_sel_hi:[1,0]\nzq v160,v160\nzq v161,v161\nzq v162,v162\nzq v163,v163\nzq v164,v164\n"
"zq v165,v165\nzq v166,v166\nzq v167,v167\nzq v168,v168\nzq v169,v169\nzq v170,v170\nzq v171,v171\nzq v172,v172\n"
"zq v173,v173\nzq v174,v174\nzq v175,v175\nv_pk_mul_f32 v[104:105],v[104:105],v[160:161]\nv_pk_mul_f32 v[106:107],v[106:107],v[162:163]\nzh v176,16,v224\nv_and_b32 v177,0xffff0000,v224\nzh v178,16,v225\n"
"v_and_b32 v179,0xffff0000,v225\nv_pk_fma_f32 v[104:105],v[104:105],v[108:109],v[176:177]\nv_pk_fma_f32 v[106:107],v[106:107],v[110:111],v[178:179]\nzc v184,v104,v105\nzc v185,v106,v107\nbuffer_store_dwordx2 v[184:185],v14,s[56:59],0 offen offset:0\nv_pk_mul_f32 v[112:113],v[112:113],v[164:165]\nv_pk_mul_f32 v[114:115],v[114:115],v[166:167]\n"
"zh v176,16,v226\nv_and_b32 v177,0xffff0000,v226\nzh v178,16,v227\nv_and_b32 v179,0xffff0000,v227\nv_pk_fma_f32 v[112:113],v[112:113],v[116:117],v[176:177]\nv_pk_fma_f32 v[114:115],v[114:115],v[118:119],v[178:179]\nzc v186,v112,v113\nzc v187,v114,v115\n"
"s_cmp_eq_u32 s62,3\ns_cbranch_scc1 L_O_skip0\nbuffer_store_dwordx2 v[186:187],v14,s[56:59],0 offen offset:32\nL_O_skip0:\nv_pk_mul_f32 v[120:121],v[120:121],v[168:169]\nv_pk_mul_f32 v[122:123],v[122:123],v[170:171]\nzh v176,16,v228\nv_and_b32 v177,0xffff0000,v228\n"
"zh v178,16,v229\nv_and_b32 v179,0xffff0000,v229\nv_pk_fma_f32 v[120:121],v[120:121],v[124:125],v[176:177]\nv_pk_fma_f32 v[122:123],v[122:123],v[126:127],v[178:179]\nzc v188,v120,v121\nzc v189,v122,v123\nbuffer_store_dwordx2 v[188:189],v15,s[56:59],0 offen offset:0\nv_pk_mul_f32 v[128:129],v[128:129],v[172:173]\n"
"v_pk_mul_f32 v[130:131],v[130:131],v[174:175]\nzh v176,16,v230\nv_and_b32 v177,0xffff0000,v230\nzh v178,16,v231\nv_and_b32 v179,0xffff0000,v231\nv_pk_fma_f32 v[128:129],v[128:129],v[132:133],v[176:177]\nv_pk_fma_f32 v[130:131],v[130:131],v[134:135],v[178:179]\nzc v190,v128,v129\n"
"zc v191,v130,v131\ns_cmp_eq_u32 s62,3\ns_cbranch_scc1 L_O_skip1\nbuffer_store_dwordx2 v[190:191],v15,s[56:59],0 offen offset:32\nL_O_skip1:\nL_O_end:\ns_cmp_eq_u32 s22,0\ns_cbranch_scc0 L_F_noep\n"
"s_cmp_eq_u32 s23,0\ns_cbranch_scc0 L_F_noep\nzh v26,4,v6\nv_add_u32 v26,1024,v26\ns_mov_b32 s76,0\nL_F_eppoll:\nglobal_load_dwordx4 v[28:31],v26,s[94:95] sc1\nzv vmcnt(0)\n"
"v_cmp_ne_u32 vcc,s34,v28\nv_cmp_ne_u32 s[78:79],s34,v29\nv_cmp_ne_u32 s[80:81],s34,v30\nv_cmp_ne_u32 s[82:83],s34,v31\ns_nop 1\ns_or_b64 vcc,vcc,s[78:79]\ns_or_b64 vcc,vcc,s[80:81]\ns_or_b64 vcc,vcc,s[82:83]\n"
"s_cmp_eq_u64 vcc,0\ns_cbranch_scc1 L_F_epok\nzp s76,s76,1\ns_cmp_lt_u32 s76,300000\ns_cbranch_scc0 L_F_epok\ns_sleep 1\ns_branch L_F_eppoll\nL_F_epok:\n"
"v_mov_b32 v26,0\nv_mov_b32 v27,s34\nglobal_store_dword v26,v27,s[94:95] sc1\nL_F_noep:\nL_F_codeend:\ns_branch L_F_exit\nL_A_rare_U0:\ns_nop 7\n"
"s_nop 7\nv_max3_f32 v28,v48,v49,v50\nv_max3_f32 v28,v28,v51,v56\nv_max3_f32 v28,v28,v57,v58\nv_max_f32 v28,v28,v59\nv_mov_b32 v15,v28\ns_nop 1\nv_permlane16_swap_b32 v28,v15\n"
"s_nop 1\nv_max_f32 v28,v28,v15\nv_mov_b32 v15,v28\ns_nop 1\nv_permlane32_swap_b32 v28,v15\ns_nop 1\nv_max_f32 v28,v28,v15\nv_max_f32 v28,0,v28\n"
"zu v30,0,v28\nze v30,v30\ns_nop 1\nza v12,v12,v28\nzu v96,v96,v28\nzu v97,v97,v28\nzu v98,v98,v28\nzu v99,v99,v28\n"
"zx v10,v10,v30\nzu v48,v48,v28\nzu v49,v49,v28\nzu v50,v50,v28\nzu v51,v51,v28\nzu v56,v56,v28\nzu v57,v57,v28\nzu v58,v58,v28\n"
"zu v59,v59,v28\nzu v32,v32,v28\nzu v33,v33,v28\nzu v34,v34,v28\nzu v35,v35,v28\nzr v112,a0\nzr v113,a1\nzr v114,a2\n"
"zr v115,a3\nzx v112,v112,v30\nzx v113,v113,v30\nzx v114,v114,v30\nzx v115,v115,v30\nzw a0,v112\nzw a1,v113\nzw a2,v114\n"
"zw a3,v115\nzr v112,a8\nzr v113,a9\nzr v114,a10\nzr v115,a11\nzx v112,v112,v30\nzx v113,v113,v30\nzx v114,v114,v30\n"
"zx v115,v115,v30\nzw a8,v112\nzw a9,v113\nzw a10,v114\nzw a11,v115\nzr v112,a16\nzr v113,a17\nzr v114,a18\n"
"zr v115,a19\nzx v112,v112,v30\nzx v113,v113,v30\nzx v114,v114,v30\nzx v115,v115,v30\nzw a16,v112\nzw a17,v113\nzw a18,v114\n"
"zw a19,v115\nzr v112,a24\nzr v113,a25\nzr v114,a26\nzr v115,a27\nzx v112,v112,v30\nzx v113,v113,v30\nzx v114,v114,v30\n"
"zx v115,v115,v30\nzw a24,v112\nzw a25,v113\nzw a26,v114\nzw a27,v115\nzr v112,a32\nzr v113,a33\nzr v114,a34\n"
"zr v115,a35\nzx v112,v112,v30\nzx v113,v113,v30\nzx v114,v114,v30\nzx v115,v115,v30\nzw a32,v112\nzw a33,v113\nzw a34,v114\n"
"zw a35,v115\nzr v112,a40\nzr v113,a41\nzr v114,a42\nzr v115,a43\nzx v112,v112,v30\nzx v113,v113,v30\nzx v114,v114,v30\n"
"zx v115,v115,v30\nzw a40,v112\nzw a41,v113\nzw a42,v114\nzw a43,v115\nzr v112,a48\nzr v113,a49\nzr v114,a50\n"
"zr v115,a51\nzx v112,v112,v30\nzx v113,v113,v30\nzx v114,v114,v30\nzx v115,v115,v30\nzw a48,v112\nzw a49,v113\nzw a50,v114\n"
"zw a51,v115\nzr v112,a56\nzr v113,a57\nzr v114,a58\nzr v115,a59\nzx v112,v112,v30\nzx v113,v113,v30\nzx v114,v114,v30\n"
"zx v115,v115,v30\nzw a56,v112\nzw a57,v113\nzw a58,v114\nzw a59,v115\nzr v112,a64\nzr v113,a65\nzr v114,a66\n"
"zr v115,a67\nzx v112,v112,v30\nzx v113,v113,v30\nzx v114,v114,v30\nzx v115,v115,v30\nzw a64,v112\nzw a65,v113\nzw a66,v114\n"
"zw a67,v115\nzr v112,a72\nzr v113,a73\nzr v114,a74\nzr v115,a75\nzx v112,v112,v30\nzx v113,v113,v30\nzx v114,v114,v30\n"
"zx v115,v115,v30\nzw a72,v112\nzw a73,v113\nzw a74,v114\nzw a75,v115\nzr v112,a80\nzr v113,a81\nzr v114,a82\n"
"zr v115,a83\nzx v112,v112,v30\nzx v113,v113,v30\nzx v114,v114,v30\nzx v115,v115,v30\nzw a80,v112\nzw a81,v113\nzw a82,v114\n"
"zw a83,v115\nzr v112,a88\nzr v113,a89\nzr v114,a90\nzr v115,a91\nzx v112,v112,v30\nzx v113,v113,v30\nzx v114,v114,v30\n"
"zx v115,v115,v30\nzw a88,v112\nzw a89,v113\nzw a90,v114\nzw a91,v115\nzr v112,a96\nzr v113,a97\nzr v114,a98\n"
"zr v115,a99\nzx v112,v112,v30\nzx v113,v113,v30\nzx v114,v114,v30\nzx v115,v115,v30\nzw a96,v112\nzw a97,v113\nzw a98,v114\n"
"zw a99,v115\nzr v112,a104\nzr v113,a105\nzr v114,a106\nzr v115,a107\nzx v112,v112,v30\nzx v113,v113,v30\nzx v114,v114,v30\n"
"zx v115,v115,v30\nzw a104,v112\nzw a105,v113\nzw a106,v114\nzw a107,v115\nzr v112,a112\nzr v113,a113\nzr v114,a114\n"
"zr v115,a115\nzx v112,v112,v30\nzx v113,v113,v30\nzx v114,v114,v30\nzx v115,v115,v30\nzw a112,v112\nzw a113,v113\nzw a114,v114\n"
"zw a115,v115\nzr v112,a120\nzr v113,a121\nzr v114,a122\nzr v115,a123\nzx v112,v112,v30\nzx v113,v113,v30\nzx v114,v114,v30\n"
"zx v115,v115,v30\nzw a120,v112\nzw a121,v113\nzw a122,v114\nzw a123,v115\nv_max3_f32 v29,v52,v53,v54\nv_max3_f32 v29,v29,v55,v60\nv_max3_f32 v29,v29,v61,v62\n"
"v_max_f32 v29,v29,v63\nv_mov_b32 v15,v29\ns_nop 1\nv_permlane16_swap_b32 v29,v15\ns_nop 1\nv_max_f32 v29,v29,v15\nv_mov_b32 v15,v29\ns_nop 1\n"
"v_permlane32_swap_b32 v29,v15\ns_nop 1\nv_max_f32 v29,v29,v15\nv_max_f32 v29,0,v29\nzu v31,0,v29\nze v31,v31\ns_nop 1\nza v13,v13,v29\n"
"zu v100,v100,v29\nzu v101,v101,v29\nzu v102,v102,v29\nzu v103,v103,v29\nzx v11,v11,v31\nzu v52,v52,v29\nzu v53,v53,v29\nzu v54,v54,v29\n"
"zu v55,v55,v29\nzu v60,v60,v29\nzu v61,v61,v29\nzu v62,v62,v29\nzu v63,v63,v29\nzu v36,v36,v29\nzu v37,v37,v29\nzu v38,v38,v29\n"
"zu v39,v39,v29\nzr v112,a4\nzr v113,a5\nzr v114,a6\nzr v115,a7\nzx v112,v112,v31\nzx v113,v113,v31\nzx v114,v114,v31\n"
"zx v115,v115,v31\nzw a4,v112\nzw a5,v113\nzw a6,v114\nzw a7,v115\nzr v112,a12\nzr v113,a13\nzr v114,a14\n"
"zr v115,a15\nzx v112,v112,v31\nzx v113,v113,v31\nzx v114,v114,v31\nzx v115,v115,v31\nzw a12,v112\nzw a13,v113\nzw a14,v114\n"
"zw a15,v115\nzr v112,a20\nzr v113,a21\nzr v114,a22\nzr v115,a23\nzx v112,v112,v31\nzx v113,v113,v31\nzx v114,v114,v31\n"
"zx v115,v115,v31\nzw a20,v112\nzw a21,v113\nzw a22,v114\nzw a23,v115\nzr v112,a28\nzr v113,a29\nzr v114,a30\n"
"zr v115,a31\nzx v112,v112,v31\nzx v113,v113,v31\nzx v114,v114,v31\nzx v115,v115,v31\nzw a28,v112\nzw a29,v113\nzw a30,v114\n"
"zw a31,v115\nzr v112,a36\nzr v113,a37\nzr v114,a38\nzr v115,a39\nzx v112,v112,v31\nzx v113,v113,v31\nzx v114,v114,v31\n"
"zx v115,v115,v31\nzw a36,v112\nzw a37,v113\nzw a38,v114\nzw a39,v115\nzr v112,a44\nzr v113,a45\nzr v114,a46\n"
"zr v115,a47\nzx v112,v112,v31\nzx v113,v113,v31\nzx v114,v114,v31\nzx v115,v115,v31\nzw a44,v112\nzw a45,v113\nzw a46,v114\n"
"zw a47,v115\nzr v112,a52\nzr v113,a53\nzr v114,a54\nzr v115,a55\nzx v112,v112,v31\nzx v113,v113,v31\nzx v114,v114,v31\n"
"zx v115,v115,v31\nzw a52,v112\nzw a53,v113\nzw a54,v114\nzw a55,v115\nzr v112,a60\nzr v113,a61\nzr v114,a62\n"
"zr v115,a63\nzx v112,v112,v31\nzx v113,v113,v31\nzx v114,v114,v31\nzx v115,v115,v31\nzw a60,v112\nzw a61,v113\nzw a62,v114\n"
"zw a63,v115\nzr v112,a68\nzr v113,a69\nzr v114,a70\nzr v115,a71\nzx v112,v112,v31\nzx v113,v113,v31\nzx v114,v114,v31\n"
"zx v115,v115,v31\nzw a68,v112\nzw a69,v113\nzw a70,v114\nzw a71,v115\nzr v112,a76\nzr v113,a77\nzr v114,a78\n"
"zr v115,a79\nzx v112,v112,v31\nzx v113,v113,v31\nzx v114,v114,v31\nzx v115,v115,v31\nzw a76,v112\nzw a77,v113\nzw a78,v114\n"
"zw a79,v115\nzr v112,a84\nzr v113,a85\nzr v114,a86\nzr v115,a87\nzx v112,v112,v31\nzx v113,v113,v31\nzx v114,v114,v31\n"
"zx v115,v115,v31\nzw a84,v112\nzw a85,v113\nzw a86,v114\nzw a87,v115\nzr v112,a92\nzr v113,a93\nzr v114,a94\n"
"zr v115,a95\nzx v112,v112,v31\nzx v113,v113,v31\nzx v114,v114,v31\nzx v115,v115,v31\nzw a92,v112\nzw a93,v113\nzw a94,v114\n"
"zw a95,v115\nzr v112,a100\nzr v113,a101\nzr v114,a102\nzr v115,a103\nzx v112,v112,v31\nzx v113,v113,v31\nzx v114,v114,v31\n"
"zx v115,v115,v31\nzw a100,v112\nzw a101,v113\nzw a102,v114\nzw a103,v115\nzr v112,a108\nzr v113,a109\nzr v114,a110\n"
"zr v115,a111\nzx v112,v112,v31\nzx v113,v113,v31\nzx v114,v114,v31\nzx v115,v115,v31\nzw a108,v112\nzw a109,v113\nzw a110,v114\n"
"zw a111,v115\nzr v112,a116\nzr v113,a117\nzr v114,a118\nzr v115,a119\nzx v112,v112,v31\nzx v113,v113,v31\nzx v114,v114,v31\n"
"zx v115,v115,v31\nzw a116,v112\nzw a117,v113\nzw a118,v114\nzw a119,v115\nzr v112,a124\nzr v113,a125\nzr v114,a126\n"
"zr v115,a127\nzx v112,v112,v31\nzx v113,v113,v31\nzx v114,v114,v31\nzx v115,v115,v31\nzw a124,v112\nzw a125,v113\nzw a126,v114\n"
"zw a127,v115\nze v80,v48\nze v81,v49\nze v82,v50\nze v83,v51\nze v84,v52\nze v85,v53\nze v86,v54\n"
"ze v87,v55\ns_nop 1\nza v104,v80,v81\nza v105,v82,v83\nzc v72,v80,v81\nzc v73,v82,v83\nza v108,v104,v105\nza v106,v84,v85\n"
"za v107,v86,v87\nzc v76,v84,v85\nzc v77,v86,v87\nza v109,v106,v107\ns_nop 1\nze v88,v56\nze v89,v57\nze v90,v58\n"
"ze v91,v59\nze v92,v60\nze v93,v61\nze v94,v62\nze v95,v63\ns_nop 1\nza v104,v88,v89\nza v105,v90,v91\n"
"zc v74,v88,v89\nzc v75,v90,v91\nza v110,v104,v105\nza v106,v92,v93\nza v107,v94,v95\nzc v78,v92,v93\nzc v79,v94,v95\nza v111,v106,v107\n"
"s_nop 1\nza v108,v108,v110\nza v109,v109,v111\ns_nop 1\ns_branch L_A_back_U0\nL_A_rare_U1:\ns_nop 7\ns_nop 7\n"
"v_max3_f32 v28,v32,v33,v34\nv_max3_f32 v28,v28,v35,v40\nv_max3_f32 v28,v28,v41,v42\nv_max_f32 v28,v28,v43\nv_mov_b32 v15,v28\ns_nop 1\nv_permlane16_swap_b32 v28,v15\ns_nop 1\n"
"v_max_f32 v28,v28,v15\nv_mov_b32 v15,v28\ns_nop 1\nv_permlane32_swap_b32 v28,v15\ns_nop 1\nv_max_f32 v28,v28,v15\nv_max_f32 v28,0,v28\nzu v30,0,v28\n"
"ze v30,v30\ns_nop 1\nza v12,v12,v28\nzu v96,v96,v28\nzu v97,v97,v28\nzu v98,v98,v28\nzu v99,v99,v28\nzx v10,v10,v30\n"
"zu v32,v32,v28\nzu v33,v33,v28\nzu v34,v34,v28\nzu v35,v35,v28\nzu v40,v40,v28\nzu v41,v41,v28\nzu v42,v42,v28\nzu v43,v43,v28\n"
"zu v48,v48,v28\nzu v49,v49,v28\nzu v50,v50,v28\nzu v51,v51,v28\nzr v112,a0\nzr v113,a1\nzr v114,a2\nzr v115,a3\n"
"zx v112,v112,v30\nzx v113,v113,v30\nzx v114,v114,v30\nzx v115,v115,v30\nzw a0,v112\nzw a1,v113\nzw a2,v114\nzw a3,v115\n"
"zr v112,a8\nzr v113,a9\nzr v114,a10\nzr v115,a11\nzx v112,v112,v30\nzx v113,v113,v30\nzx v114,v114,v30\nzx v115,v115,v30\n"
"zw a8,v112\nzw a9,v113\nzw a10,v114\nzw a11,v115\nzr v112,a16\nzr v113,a17\nzr v114,a18\nzr v115,a19\n"
"zx v112,v112,v30\nzx v113,v113,v30\nzx v114,v114,v30\nzx v115,v115,v30\nzw a16,v112\nzw a17,v113\nzw a18,v114\nzw a19,v115\n"
"zr v112,a24\nzr v113,a25\nzr v114,a26\nzr v115,a27\nzx v112,v112,v30\nzx v113,v113,v30\nzx v114,v114,v30\nzx v115,v115,v30\n"
"zw a24,v112\nzw a25,v113\nzw a26,v114\nzw a27,v115\nzr v112,a32\nzr v113,a33\nzr v114,a34\nzr v115,a35\n"
"zx v112,v112,v30\nzx v113,v113,v30\nzx v114,v114,v30\nzx v115,v115,v30\nzw a32,v112\nzw a33,v113\nzw a34,v114\nzw a35,v115\n"
"zr v112,a40\nzr v113,a41\nzr v114,a42\nzr v115,a43\nzx v112,v112,v30\nzx v113,v113,v30\nzx v114,v114,v30\nzx v115,v115,v30\n"
"zw a40,v112\nzw a41,v113\nzw a42,v114\nzw a43,v115\nzr v112,a48\nzr v113,a49\nzr v114,a50\nzr v115,a51\n"
"zx v112,v112,v30\nzx v113,v113,v30\nzx v114,v114,v30\nzx v115,v115,v30\nzw a48,v112\nzw a49,v113\nzw a50,v114\nzw a51,v115\n"
"zr v112,a56\nzr v113,a57\nzr v114,a58\nzr v115,a59\nzx v112,v112,v30\nzx v113,v113,v30\nzx v114,v114,v30\nzx v115,v115,v30\n"
"zw a56,v112\nzw a57,v113\nzw a58,v114\nzw a59,v115\nzr v112,a64\nzr v113,a65\nzr v114,a66\nzr v115,a67\n"
"zx v112,v112,v30\nzx v113,v113,v30\nzx v114,v114,v30\nzx v115,v115,v30\nzw a64,v112\nzw a65,v113\nzw a66,v114\nzw a67,v115\n"
"zr v112,a72\nzr v113,a73\nzr v114,a74\nzr v115,a75\nzx v112,v112,v30\nzx v113,v113,v30\nzx v114,v114,v30\nzx v115,v115,v30\n"
"zw a72,v112\nzw a73,v113\nzw a74,v114\nzw a75,v115\nzr v112,a80\nzr v113,a81\nzr v114,a82\nzr v115,a83\n"
"zx v112,v112,v30\nzx v113,v113,v30\nzx v114,v114,v30\nzx v115,v115,v30\nzw a80,v112\nzw a81,v113\nzw a82,v114\nzw a83,v115\n"
"zr v112,a88\nzr v113,a89\nzr v114,a90\nzr v115,a91\nzx v112,v112,v30\nzx v113,v113,v30\nzx v114,v114,v30\nzx v115,v115,v30\n"
"zw a88,v112\nzw a89,v113\nzw a90,v114\nzw a91,v115\nzr v112,a96\nzr v113,a97\nzr v114,a98\nzr v115,a99\n"
"zx v112,v112,v30\nzx v113,v113,v30\nzx v114,v114,v30\nzx v115,v115,v30\nzw a96,v112\nzw a97,v113\nzw a98,v114\nzw a99,v115\n"
"zr v112,a104\nzr v113,a105\nzr v114,a106\nzr v115,a107\nzx v112,v112,v30\nzx v113,v113,v30\nzx v114,v114,v30\nzx v115,v115,v30\n"
"zw a104,v112\nzw a105,v113\nzw a106,v114\nzw a107,v115\nzr v112,a112\nzr v113,a113\nzr v114,a114\nzr v115,a115\n"
"zx v112,v112,v30\nzx v113,v113,v30\nzx v114,v114,v30\nzx v115,v115,v30\nzw a112,v112\nzw a113,v113\nzw a114,v114\nzw a115,v115\n"
"zr v112,a120\nzr v113,a121\nzr v114,a122\nzr v115,a123\nzx v112,v112,v30\nzx v113,v113,v30\nzx v114,v114,v30\nzx v115,v115,v30\n"
"zw a120,v112\nzw a121,v113\nzw a122,v114\nzw a123,v115\nv_max3_f32 v29,v36,v37,v38\nv_max3_f32 v29,v29,v39,v44\nv_max3_f32 v29,v29,v45,v46\nv_max_f32 v29,v29,v47\n"
"v_mov_b32 v15,v29\ns_nop 1\nv_permlane16_swap_b32 v29,v15\ns_nop 1\nv_max_f32 v29,v29,v15\nv_mov_b32 v15,v29\ns_nop 1\nv_permlane32_swap_b32 v29,v15\n"
"s_nop 1\nv_max_f32 v29,v29,v15\nv_max_f32 v29,0,v29\nzu v31,0,v29\nze v31,v31\ns_nop 1\nza v13,v13,v29\nzu v100,v100,v29\n"
"zu v101,v101,v29\nzu v102,v102,v29\nzu v103,v103,v29\nzx v11,v11,v31\nzu v36,v36,v29\nzu v37,v37,v29\nzu v38,v38,v29\nzu v39,v39,v29\n"
"zu v44,v44,v29\nzu v45,v45,v29\nzu v46,v46,v29\nzu v47,v47,v29\nzu v52,v52,v29\nzu v53,v53,v29\nzu v54,v54,v29\nzu v55,v55,v29\n"
"zr v112,a4\nzr v113,a5\nzr v114,a6\nzr v115,a7\nzx v112,v112,v31\nzx v113,v113,v31\nzx v114,v114,v31\nzx v115,v115,v31\n"
"zw a4,v112\nzw a5,v113\nzw a6,v114\nzw a7,v115\nzr v112,a12\nzr v113,a13\nzr v114,a14\nzr v115,a15\n"
"zx v112,v112,v31\nzx v113,v113,v31\nzx v114,v114,v31\nzx v115,v115,v31\nzw a12,v112\nzw a13,v113\nzw a14,v114\nzw a15,v115\n"
"zr v112,a20\nzr v113,a21\nzr v114,a22\nzr v115,a23\nzx v112,v112,v31\nzx v113,v113,v31\nzx v114,v114,v31\nzx v115,v115,v31\n"
"zw a20,v112\nzw a21,v113\nzw a22,v114\nzw a23,v115\nzr v112,a28\nzr v113,a29\nzr v114,a30\nzr v115,a31\n"
"zx v112,v112,v31\nzx v113,v113,v31\nzx v114,v114,v31\nzx v115,v115,v31\nzw a28,v112\nzw a29,v113\nzw a30,v114\nzw a31,v115\n"
"zr v112,a36\nzr v113,a37\nzr v114,a38\nzr v115,a39\nzx v112,v112,v31\nzx v113,v113,v31\nzx v114,v114,v31\nzx v115,v115,v31\n"
"zw a36,v112\nzw a37,v113\nzw a38,v114\nzw a39,v115\nzr v112,a44\nzr v113,a45\nzr v114,a46\nzr v115,a47\n"
"zx v112,v112,v31\nzx v113,v113,v31\nzx v114,v114,v31\nzx v115,v115,v31\nzw a44,v112\nzw a45,v113\nzw a46,v114\nzw a47,v115\n"
"zr v112,a52\nzr v113,a53\nzr v114,a54\nzr v115,a55\nzx v112,v112,v31\nzx v113,v113,v31\nzx v114,v114,v31\nzx v115,v115,v31\n"
"zw a52,v112\nzw a53,v113\nzw a54,v114\nzw a55,v115\nzr v112,a60\nzr v113,a61\nzr v114,a62\nzr v115,a63\n"
"zx v112,v112,v31\nzx v113,v113,v31\nzx v114,v114,v31\nzx v115,v115,v31\nzw a60,v112\nzw a61,v113\nzw a62,v114\nzw a63,v115\n"
"zr v112,a68\nzr v113,a69\nzr v114,a70\nzr v115,a71\nzx v112,v112,v31\nzx v113,v113,v31\nzx v114,v114,v31\nzx v115,v115,v31\n"
"zw a68,v112\nzw a69,v113\nzw a70,v114\nzw a71,v115\nzr v112,a76\nzr v113,a77\nzr v114,a78\nzr v115,a79\n"
"zx v112,v112,v31\nzx v113,v113,v31\nzx v114,v114,v31\nzx v115,v115,v31\nzw a76,v112\nzw a77,v113\nzw a78,v114\nzw a79,v115\n"
"zr v112,a84\nzr v113,a85\nzr v114,a86\nzr v115,a87\nzx v112,v112,v31\nzx v113,v113,v31\nzx v114,v114,v31\nzx v115,v115,v31\n"
"zw a84,v112\nzw a85,v113\nzw a86,v114\nzw a87,v115\nzr v112,a92\nzr v113,a93\nzr v114,a94\nzr v115,a95\n"
"zx v112,v112,v31\nzx v113,v113,v31\nzx v114,v114,v31\nzx v115,v115,v31\nzw a92,v112\nzw a93,v113\nzw a94,v114\nzw a95,v115\n"
"zr v112,a100\nzr v113,a101\nzr v114,a102\nzr v115,a103\nzx v112,v112,v31\nzx v113,v113,v31\nzx v114,v114,v31\nzx v115,v115,v31\n"
"zw a100,v112\nzw a101,v113\nzw a102,v114\nzw a103,v115\nzr v112,a108\nzr v113,a109\nzr v114,a110\nzr v115,a111\n"
"zx v112,v112,v31\nzx v113,v113,v31\nzx v114,v114,v31\nzx v115,v115,v31\nzw a108,v112\nzw a109,v113\nzw a110,v114\nzw a111,v115\n"
"zr v112,a116\nzr v113,a117\nzr v114,a118\nzr v115,a119\nzx v112,v112,v31\nzx v113,v113,v31\nzx v114,v114,v31\nzx v115,v115,v31\n"
"zw a116,v112\nzw a117,v113\nzw a118,v114\nzw a119,v115\nzr v112,a124\nzr v113,a125\nzr v114,a126\nzr v115,a127\n"
"zx v112,v112,v31\nzx v113,v113,v31\nzx v114,v114,v31\nzx v115,v115,v31\nzw a124,v112\nzw a125,v113\nzw a126,v114\nzw a127,v115\n"
"ze v80,v32\nze v81,v33\nze v82,v34\nze v83,v35\nze v84,v36\nze v85,v37\nze v86,v38\nze v87,v39\n"
"s_nop 1\nza v104,v80,v81\nza v105,v82,v83\nzc v64,v80,v81\nzc v65,v82,v83\nza v108,v104,v105\nza v106,v84,v85\nza v107,v86,v87\n"
"zc v68,v84,v85\nzc v69,v86,v87\nza v109,v106,v107\ns_nop 1\nze v88,v40\nze v89,v41\nze v90,v42\nze v91,v43\n"
"ze v92,v44\nze v93,v45\nze v94,v46\nze v95,v47\ns_nop 1\nza v104,v88,v89\nza v105,v90,v91\nzc v66,v88,v89\n"
"zc v67,v90,v91\nza v110,v104,v105\nza v106,v92,v93\nza v107,v94,v95\nzc v70,v92,v93\nzc v71,v94,v95\nza v111,v106,v107\ns_nop 1\n"
"za v108,v108,v110\nza v109,v109,v111\ns_nop 1\ns_branch L_A_back_U1\nL_A_rare_U2:\ns_nop 7\ns_nop 7\nv_max3_f32 v28,v48,v49,v50\n"
"v_max3_f32 v28,v28,v51,v56\nv_max3_f32 v28,v28,v57,v58\nv_max_f32 v28,v28,v59\nv_mov_b32 v15,v28\ns_nop 1\nv_permlane16_swap_b32 v28,v15\ns_nop 1\nv_max_f32 v28,v28,v15\n"
"v_mov_b32 v15,v28\ns_nop 1\nv_permlane32_swap_b32 v28,v15\ns_nop 1\nv_max_f32 v28,v28,v15\nv_max_f32 v28,0,v28\nzu v30,0,v28\nze v30,v30\n"
"s_nop 1\nza v12,v12,v28\nzu v96,v96,v28\nzu v97,v97,v28\nzu v98,v98,v28\nzu v99,v99,v28\nzx v10,v10,v30\nzu v48,v48,v28\n"
"zu v49,v49,v28\nzu v50,v50,v28\nzu v51,v51,v28\nzu v56,v56,v28\nzu v57,v57,v28\nzu v58,v58,v28\nzu v59,v59,v28\nzu v32,v32,v28\n"
"zu v33,v33,v28\nzu v34,v34,v28\nzu v35,v35,v28\nzr v112,a0\nzr v113,a1\nzr v114,a2\nzr v115,a3\nzx v112,v112,v30\n"
"zx v113,v113,v30\nzx v114,v114,v30\nzx v115,v115,v30\nzw a0,v112\nzw a1,v113\nzw a2,v114\nzw a3,v115\nzr v112,a8\n"
"zr v113,a9\nzr v114,a10\nzr v115,a11\nzx v112,v112,v30\nzx v113,v113,v30\nzx v114,v114,v30\nzx v115,v115,v30\nzw a8,v112\n"
"zw a9,v113\nzw a10,v114\nzw a11,v115\nzr v112,a16\nzr v113,a17\nzr v114,a18\nzr v115,a19\nzx v112,v112,v30\n"
"zx v113,v113,v30\nzx v114,v114,v30\nzx v115,v115,v30\nzw a16,v112\nzw a17,v113\nzw a18,v114\nzw a19,v115\nzr v112,a24\n"
"zr v113,a25\nzr v114,a26\nzr v115,a27\nzx v112,v112,v30\nzx v113,v113,v30\nzx v114,v114,v30\nzx v115,v115,v30\nzw a24,v112\n"
"zw a25,v113\nzw a26,v114\nzw a27,v115\nzr v112,a32\nzr v113,a33\nzr v114,a34\nzr v115,a35\nzx v112,v112,v30\n"
"zx v113,v113,v30\nzx v114,v114,v30\nzx v115,v115,v30\nzw a32,v112\nzw a33,v113\nzw a34,v114\nzw a35,v115\nzr v112,a40\n"
"zr v113,a41\nzr v114,a42\nzr v115,a43\nzx v112,v112,v30\nzx v113,v113,v30\nzx v114,v114,v30\nzx v115,v115,v30\nzw a40,v112\n"
"zw a41,v113\nzw a42,v114\nzw a43,v115\nzr v112,a48\nzr v113,a49\nzr v114,a50\nzr v115,a51\nzx v112,v112,v30\n"
"zx v113,v113,v30\nzx v114,v114,v30\nzx v115,v115,v30\nzw a48,v112\nzw a49,v113\nzw a50,v114\nzw a51,v115\nzr v112,a56\n"
"zr v113,a57\nzr v114,a58\nzr v115,a59\nzx v112,v112,v30\nzx v113,v113,v30\nzx v114,v114,v30\nzx v115,v115,v30\nzw a56,v112\n"
"zw a57,v113\nzw a58,v114\nzw a59,v115\nzr v112,a64\nzr v113,a65\nzr v114,a66\nzr v115,a67\nzx v112,v112,v30\n"
"zx v113,v113,v30\nzx v114,v114,v30\nzx v115,v115,v30\nzw a64,v112\nzw a65,v113\nzw a66,v114\nzw a67,v115\nzr v112,a72\n"
"zr v113,a73\nzr v114,a74\nzr v115,a75\nzx v112,v112,v30\nzx v113,v113,v30\nzx v114,v114,v30\nzx v115,v115,v30\nzw a72,v112\n"
"zw a73,v113\nzw a74,v114\nzw a75,v115\nzr v112,a80\nzr v113,a81\nzr v114,a82\nzr v115,a83\nzx v112,v112,v30\n"
"zx v113,v113,v30\nzx v114,v114,v30\nzx v115,v115,v30\nzw a80,v112\nzw a81,v113\nzw a82,v114\nzw a83,v115\nzr v112,a88\n"
"zr v113,a89\nzr v114,a90\nzr v115,a91\nzx v112,v112,v30\nzx v113,v113,v30\nzx v114,v114,v30\nzx v115,v115,v30\nzw a88,v112\n"
"zw a89,v113\nzw a90,v114\nzw a91,v115\nzr v112,a96\nzr v113,a97\nzr v114,a98\nzr v115,a99\nzx v112,v112,v30\n"
"zx v113,v113,v30\nzx v114,v114,v30\nzx v115,v115,v30\nzw a96,v112\nzw a97,v113\nzw a98,v114\nzw a99,v115\nzr v112,a104\n"
"zr v113,a105\nzr v114,a106\nzr v115,a107\nzx v112,v112,v30\nzx v113,v113,v30\nzx v114,v114,v30\nzx v115,v115,v30\nzw a104,v112\n"
"zw a105,v113\nzw a106,v114\nzw a107,v115\nzr v112,a112\nzr v113,a113\nzr v114,a114\nzr v115,a115\nzx v112,v112,v30\n"
"zx v113,v113,v30\nzx v114,v114,v30\nzx v115,v115,v30\nzw a112,v112\nzw a113,v113\nzw a114,v114\nzw a115,v115\nzr v112,a120\n"
"zr v113,a121\nzr v114,a122\nzr v115,a123\nzx v112,v112,v30\nzx v113,v113,v30\nzx v114,v114,v30\nzx v115,v115,v30\nzw a120,v112\n"
"zw a121,v113\nzw a122,v114\nzw a123,v115\nv_max3_f32 v29,v52,v53,v54\nv_max3_f32 v29,v29,v55,v60\nv_max3_f32 v29,v29,v61,v62\nv_max_f32 v29,v29,v63\nv_mov_b32 v15,v29\n"
"s_nop 1\nv_permlane16_swap_b32 v29,v15\ns_nop 1\nv_max_f32 v29,v29,v15\nv_mov_b32 v15,v29\ns_nop 1\nv_permlane32_swap_b32 v29,v15\ns_nop 1\n"
"v_max_f32 v29,v29,v15\nv_max_f32 v29,0,v29\nzu v31,0,v29\nze v31,v31\ns_nop 1\nza v13,v13,v29\nzu v100,v100,v29\nzu v101,v101,v29\n"
"zu v102,v102,v29\nzu v103,v103,v29\nzx v11,v11,v31\nzu v52,v52,v29\nzu v53,v53,v29\nzu v54,v54,v29\nzu v55,v55,v29\nzu v60,v60,v29\n"
"zu v61,v61,v29\nzu v62,v62,v29\nzu v63,v63,v29\nzu v36,v36,v29\nzu v37,v37,v29\nzu v38,v38,v29\nzu v39,v39,v29\nzr v112,a4\n"
"zr v113,a5\nzr v114,a6\nzr v115,a7\nzx v112,v112,v31\nzx v113,v113,v31\nzx v114,v114,v31\nzx v115,v115,v31\nzw a4,v112\n"
"zw a5,v113\nzw a6,v114\nzw a7,v115\nzr v112,a12\nzr v113,a13\nzr v114,a14\nzr v115,a15\nzx v112,v112,v31\n"
"zx v113,v113,v31\nzx v114,v114,v31\nzx v115,v115,v31\nzw a12,v112\nzw a13,v113\nzw a14,v114\nzw a15,v115\nzr v112,a20\n"
"zr v113,a21\nzr v114,a22\nzr v115,a23\nzx v112,v112,v31\nzx v113,v113,v31\nzx v114,v114,v31\nzx v115,v115,v31\nzw a20,v112\n"
"zw a21,v113\nzw a22,v114\nzw a23,v115\nzr v112,a28\nzr v113,a29\nzr v114,a30\nzr v115,a31\nzx v112,v112,v31\n"
"zx v113,v113,v31\nzx v114,v114,v31\nzx v115,v115,v31\nzw a28,v112\nzw a29,v113\nzw a30,v114\nzw a31,v115\nzr v112,a36\n"
"zr v113,a37\nzr v114,a38\nzr v115,a39\nzx v112,v112,v31\nzx v113,v113,v31\nzx v114,v114,v31\nzx v115,v115,v31\nzw a36,v112\n"
"zw a37,v113\nzw a38,v114\nzw a39,v115\nzr v112,a44\nzr v113,a45\nzr v114,a46\nzr v115,a47\nzx v112,v112,v31\n"
"zx v113,v113,v31\nzx v114,v114,v31\nzx v115,v115,v31\nzw a44,v112\nzw a45,v113\nzw a46,v114\nzw a47,v115\nzr v112,a52\n"
"zr v113,a53\nzr v114,a54\nzr v115,a55\nzx v112,v112,v31\nzx v113,v113,v31\nzx v114,v114,v31\nzx v115,v115,v31\nzw a52,v112\n"
"zw a53,v113\nzw a54,v114\nzw a55,v115\nzr v112,a60\nzr v113,a61\nzr v114,a62\nzr v115,a63\nzx v112,v112,v31\n"
"zx v113,v113,v31\nzx v114,v114,v31\nzx v115,v115,v31\nzw a60,v112\nzw a61,v113\nzw a62,v114\nzw a63,v115\nzr v112,a68\n"
"zr v113,a69\nzr v114,a70\nzr v115,a71\nzx v112,v112,v31\nzx v113,v113,v31\nzx v114,v114,v31\nzx v115,v115,v31\nzw a68,v112\n"
"zw a69,v113\nzw a70,v114\nzw a71,v115\nzr v112,a76\nzr v113,a77\nzr v114,a78\nzr v115,a79\nzx v112,v112,v31\n"
"zx v113,v113,v31\nzx v114,v114,v31\nzx v115,v115,v31\nzw a76,v112\nzw a77,v113\nzw a78,v114\nzw a79,v115\nzr v112,a84\n"
"zr v113,a85\nzr v114,a86\nzr v115,a87\nzx v112,v112,v31\nzx v113,v113,v31\nzx v114,v114,v31\nzx v115,v115,v31\nzw a84,v112\n"
"zw a85,v113\nzw a86,v114\nzw a87,v115\nzr v112,a92\nzr v113,a93\nzr v114,a94\nzr v115,a95\nzx v112,v112,v31\n"
"zx v113,v113,v31\nzx v114,v114,v31\nzx v115,v115,v31\nzw a92,v112\nzw a93,v113\nzw a94,v114\nzw a95,v115\nzr v112,a100\n"
"zr v113,a101\nzr v114,a102\nzr v115,a103\nzx v112,v112,v31\nzx v113,v113,v31\nzx v114,v114,v31\nzx v115,v115,v31\nzw a100,v112\n"
"zw a101,v113\nzw a102,v114\nzw a103,v115\nzr v112,a108\nzr v113,a109\nzr v114,a110\nzr v115,a111\nzx v112,v112,v31\n"
"zx v113,v113,v31\nzx v114,v114,v31\nzx v115,v115,v31\nzw a108,v112\nzw a109,v113\nzw a110,v114\nzw a111,v115\nzr v112,a116\n"
"zr v113,a117\nzr v114,a118\nzr v115,a119\nzx v112,v112,v31\nzx v113,v113,v31\nzx v114,v114,v31\nzx v115,v115,v31\nzw a116,v112\n"
"zw a117,v113\nzw a118,v114\nzw a119,v115\nzr v112,a124\nzr v113,a125\nzr v114,a126\nzr v115,a127\nzx v112,v112,v31\n"
"zx v113,v113,v31\nzx v114,v114,v31\nzx v115,v115,v31\nzw a124,v112\nzw a125,v113\nzw a126,v114\nzw a127,v115\nze v80,v48\n"
"ze v81,v49\nze v82,v50\nze v83,v51\nze v84,v52\nze v85,v53\nze v86,v54\nze v87,v55\ns_nop 1\n"
"za v104,v80,v81\nza v105,v82,v83\nzc v72,v80,v81\nzc v73,v82,v83\nza v108,v104,v105\nza v106,v84,v85\nza v107,v86,v87\nzc v76,v84,v85\n"
"zc v77,v86,v87\nza v109,v106,v107\ns_nop 1\nze v88,v56\nze v89,v57\nze v90,v58\nze v91,v59\nze v92,v60\n"
"ze v93,v61\nze v94,v62\nze v95,v63\ns_nop 1\nza v104,v88,v89\nza v105,v90,v91\nzc v74,v88,v89\nzc v75,v90,v91\n"
"za v110,v104,v105\nza v106,v92,v93\nza v107,v94,v95\nzc v78,v92,v93\nzc v79,v94,v95\nza v111,v106,v107\ns_nop 1\nza v108,v108,v110\n"
"za v109,v109,v111\ns_nop 1\ns_branch L_A_back_U2\nL_A_rare_U3:\ns_nop 7\ns_nop 7\nv_max3_f32 v28,v32,v33,v34\nv_max3_f32 v28,v28,v35,v40\n"
"v_max3_f32 v28,v28,v41,v42\nv_max_f32 v28,v28,v43\nv_mov_b32 v15,v28\ns_nop 1\nv_permlane16_swap_b32 v28,v15\ns_nop 1\nv_max_f32 v28,v28,v15\nv_mov_b32 v15,v28\n"
"s_nop 1\nv_permlane32_swap_b32 v28,v15\ns_nop 1\nv_max_f32 v28,v28,v15\nv_max_f32 v28,0,v28\nzu v30,0,v28\nze v30,v30\ns_nop 1\n"
"za v12,v12,v28\nzu v96,v96,v28\nzu v97,v97,v28\nzu v98,v98,v28\nzu v99,v99,v28\nzx v10,v10,v30\nzu v32,v32,v28\nzu v33,v33,v28\n"
"zu v34,v34,v28\nzu v35,v35,v28\nzu v40,v40,v28\nzu v41,v41,v28\nzu v42,v42,v28\nzu v43,v43,v28\nzu v48,v48,v28\nzu v49,v49,v28\n"
"zu v50,v50,v28\nzu v51,v51,v28\nzr v112,a0\nzr v113,a1\nzr v114,a2\nzr v115,a3\nzx v112,v112,v30\nzx v113,v113,v30\n"
"zx v114,v114,v30\nzx v115,v115,v30\nzw a0,v112\nzw a1,v113\nzw a2,v114\nzw a3,v115\nzr v112,a8\nzr v113,a9\n"
"zr v114,a10\nzr v115,a11\nzx v112,v112,v30\nzx v113,v113,v30\nzx v114,v114,v30\nzx v115,v115,v30\nzw a8,v112\nzw a9,v113\n"
"zw a10,v114\nzw a11,v115\nzr v112,a16\nzr v113,a17\nzr v114,a18\nzr v115,a19\nzx v112,v112,v30\nzx v113,v113,v30\n"
"zx v114,v114,v30\nzx v115,v115,v30\nzw a16,v112\nzw a17,v113\nzw a18,v114\nzw a19,v115\nzr v112,a24\nzr v113,a25\n"
"zr v114,a26\nzr v115,a27\nzx v112,v112,v30\nzx v113,v113,v30\nzx v114,v114,v30\nzx v115,v115,v30\nzw a24,v112\nzw a25,v113\n"
"zw a26,v114\nzw a27,v115\nzr v112,a32\nzr v113,a33\nzr v114,a34\nzr v115,a35\nzx v112,v112,v30\nzx v113,v113,v30\n"
"zx v114,v114,v30\nzx v115,v115,v30\nzw a32,v112\nzw a33,v113\nzw a34,v114\nzw a35,v115\nzr v112,a40\nzr v113,a41\n"
"zr v114,a42\nzr v115,a43\nzx v112,v112,v30\nzx v113,v113,v30\nzx v114,v114,v30\nzx v115,v115,v30\nzw a40,v112\nzw a41,v113\n"
"zw a42,v114\nzw a43,v115\nzr v112,a48\nzr v113,a49\nzr v114,a50\nzr v115,a51\nzx v112,v112,v30\nzx v113,v113,v30\n"
"zx v114,v114,v30\nzx v115,v115,v30\nzw a48,v112\nzw a49,v113\nzw a50,v114\nzw a51,v115\nzr v112,a56\nzr v113,a57\n"
"zr v114,a58\nzr v115,a59\nzx v112,v112,v30\nzx v113,v113,v30\nzx v114,v114,v30\nzx v115,v115,v30\nzw a56,v112\nzw a57,v113\n"
"zw a58,v114\nzw a59,v115\nzr v112,a64\nzr v113,a65\nzr v114,a66\nzr v115,a67\nzx v112,v112,v30\nzx v113,v113,v30\n"
"zx v114,v114,v30\nzx v115,v115,v30\nzw a64,v112\nzw a65,v113\nzw a66,v114\nzw a67,v115\nzr v112,a72\nzr v113,a73\n"
"zr v114,a74\nzr v115,a75\nzx v112,v112,v30\nzx v113,v113,v30\nzx v114,v114,v30\nzx v115,v115,v30\nzw a72,v112\nzw a73,v113\n"
"zw a74,v114\nzw a75,v115\nzr v112,a80\nzr v113,a81\nzr v114,a82\nzr v115,a83\nzx v112,v112,v30\nzx v113,v113,v30\n"
"zx v114,v114,v30\nzx v115,v115,v30\nzw a80,v112\nzw a81,v113\nzw a82,v114\nzw a83,v115\nzr v112,a88\nzr v113,a89\n"
"zr v114,a90\nzr v115,a91\nzx v112,v112,v30\nzx v113,v113,v30\nzx v114,v114,v30\nzx v115,v115,v30\nzw a88,v112\nzw a89,v113\n"
"zw a90,v114\nzw a91,v115\nzr v112,a96\nzr v113,a97\nzr v114,a98\nzr v115,a99\nzx v112,v112,v30\nzx v113,v113,v30\n"
"zx v114,v114,v30\nzx v115,v115,v30\nzw a96,v112\nzw a97,v113\nzw a98,v114\nzw a99,v115\nzr v112,a104\nzr v113,a105\n"
"zr v114,a106\nzr v115,a107\nzx v112,v112,v30\nzx v113,v113,v30\nzx v114,v114,v30\nzx v115,v115,v30\nzw a104,v112\nzw a105,v113\n"
"zw a106,v114\nzw a107,v115\nzr v112,a112\nzr v113,a113\nzr v114,a114\nzr v115,a115\nzx v112,v112,v30\nzx v113,v113,v30\n"
"zx v114,v114,v30\nzx v115,v115,v30\nzw a112,v112\nzw a113,v113\nzw a114,v114\nzw a115,v115\nzr v112,a120\nzr v113,a121\n"
"zr v114,a122\nzr v115,a123\nzx v112,v112,v30\nzx v113,v113,v30\nzx v114,v114,v30\nzx v115,v115,v30\nzw a120,v112\nzw a121,v113\n"
"zw a122,v114\nzw a123,v115\nv_max3_f32 v29,v36,v37,v38\nv_max3_f32 v29,v29,v39,v44\nv_max3_f32 v29,v29,v45,v46\nv_max_f32 v29,v29,v47\nv_mov_b32 v15,v29\ns_nop 1\n"
"v_permlane16_swap_b32 v29,v15\ns_nop 1\nv_max_f32 v29,v29,v15\nv_mov_b32 v15,v29\ns_nop 1\nv_permlane32_swap_b32 v29,v15\ns_nop 1\nv_max_f32 v29,v29,v15\n"
"v_max_f32 v29,0,v29\nzu v31,0,v29\nze v31,v31\ns_nop 1\nza v13,v13,v29\nzu v100,v100,v29\nzu v101,v101,v29\nzu v102,v102,v29\n"
"zu v103,v103,v29\nzx v11,v11,v31\nzu v36,v36,v29\nzu v37,v37,v29\nzu v38,v38,v29\nzu v39,v39,v29\nzu v44,v44,v29\nzu v45,v45,v29\n"
"zu v46,v46,v29\nzu v47,v47,v29\nzu v52,v52,v29\nzu v53,v53,v29\nzu v54,v54,v29\nzu v55,v55,v29\nzr v112,a4\nzr v113,a5\n"
"zr v114,a6\nzr v115,a7\nzx v112,v112,v31\nzx v113,v113,v31\nzx v114,v114,v31\nzx v115,v115,v31\nzw a4,v112\nzw a5,v113\n"
"zw a6,v114\nzw a7,v115\nzr v112,a12\nzr v113,a13\nzr v114,a14\nzr v115,a15\nzx v112,v112,v31\nzx v113,v113,v31\n"
"zx v114,v114,v31\nzx v115,v115,v31\nzw a12,v112\nzw a13,v113\nzw a14,v114\nzw a15,v115\nzr v112,a20\nzr v113,a21\n"
"zr v114,a22\nzr v115,a23\nzx v112,v112,v31\nzx v113,v113,v31\nzx v114,v114,v31\nzx v115,v115,v31\nzw a20,v112\nzw a21,v113\n"
"zw a22,v114\nzw a23,v115\nzr v112,a28\nzr v113,a29\nzr v114,a30\nzr v115,a31\nzx v112,v112,v31\nzx v113,v113,v31\n"
"zx v114,v114,v31\nzx v115,v115,v31\nzw a28,v112\nzw a29,v113\nzw a30,v114\nzw a31,v115\nzr v112,a36\nzr v113,a37\n"
"zr v114,a38\nzr v115,a39\nzx v112,v112,v31\nzx v113,v113,v31\nzx v114,v114,v31\nzx v115,v115,v31\nzw a36,v112\nzw a37,v113\n"
"zw a38,v114\nzw a39,v115\nzr v112,a44\nzr v113,a45\nzr v114,a46\nzr v115,a47\nzx v112,v112,v31\nzx v113,v113,v31\n"
"zx v114,v114,v31\nzx v115,v115,v31\nzw a44,v112\nzw a45,v113\nzw a46,v114\nzw a47,v115\nzr v112,a52\nzr v113,a53\n"
"zr v114,a54\nzr v115,a55\nzx v112,v112,v31\nzx v113,v113,v31\nzx v114,v114,v31\nzx v115,v115,v31\nzw a52,v112\nzw a53,v113\n"
"zw a54,v114\nzw a55,v115\nzr v112,a60\nzr v113,a61\nzr v114,a62\nzr v115,a63\nzx v112,v112,v31\nzx v113,v113,v31\n"
"zx v114,v114,v31\nzx v115,v115,v31\nzw a60,v112\nzw a61,v113\nzw a62,v114\nzw a63,v115\nzr v112,a68\nzr v113,a69\n"
"zr v114,a70\nzr v115,a71\nzx v112,v112,v31\nzx v113,v113,v31\nzx v114,v114,v31\nzx v115,v115,v31\nzw a68,v112\nzw a69,v113\n"
"zw a70,v114\nzw a71,v115\nzr v112,a76\nzr v113,a77\nzr v114,a78\nzr v115,a79\nzx v112,v112,v31\nzx v113,v113,v31\n"
"zx v114,v114,v31\nzx v115,v115,v31\nzw a76,v112\nzw a77,v113\nzw a78,v114\nzw a79,v115\nzr v112,a84\nzr v113,a85\n"
"zr v114,a86\nzr v115,a87\nzx v112,v112,v31\nzx v113,v113,v31\nzx v114,v114,v31\nzx v115,v115,v31\nzw a84,v112\nzw a85,v113\n"
"zw a86,v114\nzw a87,v115\nzr v112,a92\nzr v113,a93\nzr v114,a94\nzr v115,a95\nzx v112,v112,v31\nzx v113,v113,v31\n"
"zx v114,v114,v31\nzx v115,v115,v31\nzw a92,v112\nzw a93,v113\nzw a94,v114\nzw a95,v115\nzr v112,a100\nzr v113,a101\n"
"zr v114,a102\nzr v115,a103\nzx v112,v112,v31\nzx v113,v113,v31\nzx v114,v114,v31\nzx v115,v115,v31\nzw a100,v112\nzw a101,v113\n"
"zw a102,v114\nzw a103,v115\nzr v112,a108\nzr v113,a109\nzr v114,a110\nzr v115,a111\nzx v112,v112,v31\nzx v113,v113,v31\n"
"zx v114,v114,v31\nzx v115,v115,v31\nzw a108,v112\nzw a109,v113\nzw a110,v114\nzw a111,v115\nzr v112,a116\nzr v113,a117\n"
"zr v114,a118\nzr v115,a119\nzx v112,v112,v31\nzx v113,v113,v31\nzx v114,v114,v31\nzx v115,v115,v31\nzw a116,v112\nzw a117,v113\n"
"zw a118,v114\nzw a119,v115\nzr v112,a124\nzr v113,a125\nzr v114,a126\nzr v115,a127\nzx v112,v112,v31\nzx v113,v113,v31\n"
"zx v114,v114,v31\nzx v115,v115,v31\nzw a124,v112\nzw a125,v113\nzw a126,v114\nzw a127,v115\nze v80,v32\nze v81,v33\n"
"ze v82,v34\nze v83,v35\nze v84,v36\nze v85,v37\nze v86,v38\nze v87,v39\ns_nop 1\nza v104,v80,v81\n"
"za v105,v82,v83\nzc v64,v80,v81\nzc v65,v82,v83\nza v108,v104,v105\nza v106,v84,v85\nza v107,v86,v87\nzc v68,v84,v85\nzc v69,v86,v87\n"
"za v109,v106,v107\ns_nop 1\nze v88,v40\nze v89,v41\nze v90,v42\nze v91,v43\nze v92,v44\nze v93,v45\n"
"ze v94,v46\nze v95,v47\ns_nop 1\nza v104,v88,v89\nza v105,v90,v91\nzc v66,v88,v89\nzc v67,v90,v91\nza v110,v104,v105\n"
"za v106,v92,v93\nza v107,v94,v95\nzc v70,v92,v93\nzc v71,v94,v95\nza v111,v106,v107\ns_nop 1\nza v108,v108,v110\nza v109,v109,v111\n"
"s_nop 1\ns_branch L_A_back_U3\nL_A_rare_M0:\ns_nop 7\ns_nop 7\nv_max3_f32 v28,v48,v49,v50\nv_max3_f32 v28,v28,v51,v56\nv_max3_f32 v28,v28,v57,v58\n"
"v_max_f32 v28,v28,v59\nv_mov_b32 v15,v28\ns_nop 1\nv_permlane16_swap_b32 v28,v15\ns_nop 1\nv_max_f32 v28,v28,v15\nv_mov_b32 v15,v28\ns_nop 1\n"
"v_permlane32_swap_b32 v28,v15\ns_nop 1\nv_max_f32 v28,v28,v15\nv_max_f32 v28,0,v28\nzu v30,0,v28\nze v30,v30\ns_nop 1\nza v12,v12,v28\n"
"zu v96,v96,v28\nzu v97,v97,v28\nzu v98,v98,v28\nzu v99,v99,v28\nzx v10,v10,v30\nzu v48,v48,v28\nzu v49,v49,v28\nzu v50,v50,v28\n"
"zu v51,v51,v28\nzu v56,v56,v28\nzu v57,v57,v28\nzu v58,v58,v28\nzu v59,v59,v28\nzu v32,v32,v28\nzu v33,v33,v28\nzu v34,v34,v28\n"
"zu v35,v35,v28\nzr v112,a0\nzr v113,a1\nzr v114,a2\nzr v115,a3\nzx v112,v112,v30\nzx v113,v113,v30\nzx v114,v114,v30\n"
"zx v115,v115,v30\nzw a0,v112\nzw a1,v113\nzw a2,v114\nzw a3,v115\nzr v112,a8\nzr v113,a9\nzr v114,a10\n"
"zr v115,a11\nzx v112,v112,v30\nzx v113,v113,v30\nzx v114,v114,v30\nzx v115,v115,v30\nzw a8,v112\nzw a9,v113\nzw a10,v114\n"
"zw a11,v115\nzr v112,a16\nzr v113,a17\nzr v114,a18\nzr v115,a19\nzx v112,v112,v30\nzx v113,v113,v30\nzx v114,v114,v30\n"
"zx v115,v115,v30\nzw a16,v112\nzw a17,v113\nzw a18,v114\nzw a19,v115\nzr v112,a24\nzr v113,a25\nzr v114,a26\n"
"zr v115,a27\nzx v112,v112,v30\nzx v113,v113,v30\nzx v114,v114,v30\nzx v115,v115,v30\nzw a24,v112\nzw a25,v113\nzw a26,v114\n"
"zw a27,v115\nzr v112,a32\nzr v113,a33\nzr v114,a34\nzr v115,a35\nzx v112,v112,v30\nzx v113,v113,v30\nzx v114,v114,v30\n"
"zx v115,v115,v30\nzw a32,v112\nzw a33,v113\nzw a34,v114\nzw a35,v115\nzr v112,a40\nzr v113,a41\nzr v114,a42\n"
"zr v115,a43\nzx v112,v112,v30\nzx v113,v113,v30\nzx v114,v114,v30\nzx v115,v115,v30\nzw a40,v112\nzw a41,v113\nzw a42,v114\n"
"zw a43,v115\nzr v112,a48\nzr v113,a49\nzr v114,a50\nzr v115,a51\nzx v112,v112,v30\nzx v113,v113,v30\nzx v114,v114,v30\n"
"zx v115,v115,v30\nzw a48,v112\nzw a49,v113\nzw a50,v114\nzw a51,v115\nzr v112,a56\nzr v113,a57\nzr v114,a58\n"
"zr v115,a59\nzx v112,v112,v30\nzx v113,v113,v30\nzx v114,v114,v30\nzx v115,v115,v30\nzw a56,v112\nzw a57,v113\nzw a58,v114\n"
"zw a59,v115\nzr v112,a64\nzr v113,a65\nzr v114,a66\nzr v115,a67\nzx v112,v112,v30\nzx v113,v113,v30\nzx v114,v114,v30\n"
"zx v115,v115,v30\nzw a64,v112\nzw a65,v113\nzw a66,v114\nzw a67,v115\nzr v112,a72\nzr v113,a73\nzr v114,a74\n"
"zr v115,a75\nzx v112,v112,v30\nzx v113,v113,v30\nzx v114,v114,v30\nzx v115,v115,v30\nzw a72,v112\nzw a73,v113\nzw a74,v114\n"
"zw a75,v115\nzr v112,a80\nzr v113,a81\nzr v114,a82\nzr v115,a83\nzx v112,v112,v30\nzx v113,v113,v30\nzx v114,v114,v30\n"
"zx v115,v115,v30\nzw a80,v112\nzw a81,v113\nzw a82,v114\nzw a83,v115\nzr v112,a88\nzr v113,a89\nzr v114,a90\n"
"zr v115,a91\nzx v112,v112,v30\nzx v113,v113,v30\nzx v114,v114,v30\nzx v115,v115,v30\nzw a88,v112\nzw a89,v113\nzw a90,v114\n"
"zw a91,v115\nzr v112,a96\nzr v113,a97\nzr v114,a98\nzr v115,a99\nzx v112,v112,v30\nzx v113,v113,v30\nzx v114,v114,v30\n"
"zx v115,v115,v30\nzw a96,v112\nzw a97,v113\nzw a98,v114\nzw a99,v115\nzr v112,a104\nzr v113,a105\nzr v114,a106\n"
"zr v115,a107\nzx v112,v112,v30\nzx v113,v113,v30\nzx v114,v114,v30\nzx v115,v115,v30\nzw a104,v112\nzw a105,v113\nzw a106,v114\n"
"zw a107,v115\nzr v112,a112\nzr v113,a113\nzr v114,a114\nzr v115,a115\nzx v112,v112,v30\nzx v113,v113,v30\nzx v114,v114,v30\n"
"zx v115,v115,v30\nzw a112,v112\nzw a113,v113\nzw a114,v114\nzw a115,v115\nzr v112,a120\nzr v113,a121\nzr v114,a122\n"
"zr v115,a123\nzx v112,v112,v30\nzx v113,v113,v30\nzx v114,v114,v30\nzx v115,v115,v30\nzw a120,v112\nzw a121,v113\nzw a122,v114\n"
"zw a123,v115\nv_max3_f32 v29,v52,v53,v54\nv_max3_f32 v29,v29,v55,v60\nv_max3_f32 v29,v29,v61,v62\nv_max_f32 v29,v29,v63\nv_mov_b32 v15,v29\ns_nop 1\nv_permlane16_swap_b32 v29,v15\n"
"s_nop 1\nv_max_f32 v29,v29,v15\nv_mov_b32 v15,v29\ns_nop 1\nv_permlane32_swap_b32 v29,v15\ns_nop 1\nv_max_f32 v29,v29,v15\nv_max_f32 v29,0,v29\n"
"zu v31,0,v29\nze v31,v31\ns_nop 1\nza v13,v13,v29\nzu v100,v100,v29\nzu v101,v101,v29\nzu v102,v102,v29\nzu v103,v103,v29\n"
"zx v11,v11,v31\nzu v52,v52,v29\nzu v53,v53,v29\nzu v54,v54,v29\nzu v55,v55,v29\nzu v60,v60,v29\nzu v61,v61,v29\nzu v62,v62,v29\n"
"zu v63,v63,v29\nzu v36,v36,v29\nzu v37,v37,v29\nzu v38,v38,v29\nzu v39,v39,v29\nzr v112,a4\nzr v113,a5\nzr v114,a6\n"
"zr v115,a7\nzx v112,v112,v31\nzx v113,v113,v31\nzx v114,v114,v31\nzx v115,v115,v31\nzw a4,v112\nzw a5,v113\nzw a6,v114\n"
"zw a7,v115\nzr v112,a12\nzr v113,a13\nzr v114,a14\nzr v115,a15\nzx v112,v112,v31\nzx v113,v113,v31\nzx v114,v114,v31\n"
"zx v115,v115,v31\nzw a12,v112\nzw a13,v113\nzw a14,v114\nzw a15,v115\nzr v112,a20\nzr v113,a21\nzr v114,a22\n"
"zr v115,a23\nzx v112,v112,v31\nzx v113,v113,v31\nzx v114,v114,v31\nzx v115,v115,v31\nzw a20,v112\nzw a21,v113\nzw a22,v114\n"
"zw a23,v115\nzr v112,a28\nzr v113,a29\nzr v114,a30\nzr v115,a31\nzx v112,v112,v31\nzx v113,v113,v31\nzx v114,v114,v31\n"
"zx v115,v115,v31\nzw a28,v112\nzw a29,v113\nzw a30,v114\nzw a31,v115\nzr v112,a36\nzr v113,a37\nzr v114,a38\n"
"zr v115,a39\nzx v112,v112,v31\nzx v113,v113,v31\nzx v114,v114,v31\nzx v115,v115,v31\nzw a36,v112\nzw a37,v113\nzw a38,v114\n"
"zw a39,v115\nzr v112,a44\nzr v113,a45\nzr v114,a46\nzr v115,a47\nzx v112,v112,v31\nzx v113,v113,v31\nzx v114,v114,v31\n"
"zx v115,v115,v31\nzw a44,v112\nzw a45,v113\nzw a46,v114\nzw a47,v115\nzr v112,a52\nzr v113,a53\nzr v114,a54\n"
"zr v115,a55\nzx v112,v112,v31\nzx v113,v113,v31\nzx v114,v114,v31\nzx v115,v115,v31\nzw a52,v112\nzw a53,v113\nzw a54,v114\n"
"zw a55,v115\nzr v112,a60\nzr v113,a61\nzr v114,a62\nzr v115,a63\nzx v112,v112,v31\nzx v113,v113,v31\nzx v114,v114,v31\n"
"zx v115,v115,v31\nzw a60,v112\nzw a61,v113\nzw a62,v114\nzw a63,v115\nzr v112,a68\nzr v113,a69\nzr v114,a70\n"
"zr v115,a71\nzx v112,v112,v31\nzx v113,v113,v31\nzx v114,v114,v31\nzx v115,v115,v31\nzw a68,v112\nzw a69,v113\nzw a70,v114\n"
"zw a71,v115\nzr v112,a76\nzr v113,a77\nzr v114,a78\nzr v115,a79\nzx v112,v112,v31\nzx v113,v113,v31\nzx v114,v114,v31\n"
"zx v115,v115,v31\nzw a76,v112\nzw a77,v113\nzw a78,v114\nzw a79,v115\nzr v112,a84\nzr v113,a85\nzr v114,a86\n"
"zr v115,a87\nzx v112,v112,v31\nzx v113,v113,v31\nzx v114,v114,v31\nzx v115,v115,v31\nzw a84,v112\nzw a85,v113\nzw a86,v114\n"
"zw a87,v115\nzr v112,a92\nzr v113,a93\nzr v114,a94\nzr v115,a95\nzx v112,v112,v31\nzx v113,v113,v31\nzx v114,v114,v31\n"
"zx v115,v115,v31\nzw a92,v112\nzw a93,v113\nzw a94,v114\nzw a95,v115\nzr v112,a100\nzr v113,a101\nzr v114,a102\n"
"zr v115,a103\nzx v112,v112,v31\nzx v113,v113,v31\nzx v114,v114,v31\nzx v115,v115,v31\nzw a100,v112\nzw a101,v113\nzw a102,v114\n"
"zw a103,v115\nzr v112,a108\nzr v113,a109\nzr v114,a110\nzr v115,a111\nzx v112,v112,v31\nzx v113,v113,v31\nzx v114,v114,v31\n"
"zx v115,v115,v31\nzw a108,v112\nzw a109,v113\nzw a110,v114\nzw a111,v115\nzr v112,a116\nzr v113,a117\nzr v114,a118\n"
"zr v115,a119\nzx v112,v112,v31\nzx v113,v113,v31\nzx v114,v114,v31\nzx v115,v115,v31\nzw a116,v112\nzw a117,v113\nzw a118,v114\n"
"zw a119,v115\nzr v112,a124\nzr v113,a125\nzr v114,a126\nzr v115,a127\nzx v112,v112,v31\nzx v113,v113,v31\nzx v114,v114,v31\n"
"zx v115,v115,v31\nzw a124,v112\nzw a125,v113\nzw a126,v114\nzw a127,v115\nze v80,v48\nze v81,v49\nze v82,v50\n"
"ze v83,v51\nze v84,v52\nze v85,v53\nze v86,v54\nze v87,v55\ns_nop 1\nza v104,v80,v81\nza v105,v82,v83\n"
"zc v72,v80,v81\nzc v73,v82,v83\nza v108,v104,v105\nza v106,v84,v85\nza v107,v86,v87\nzc v76,v84,v85\nzc v77,v86,v87\nza v109,v106,v107\n"
"s_nop 1\nze v88,v56\nze v89,v57\nze v90,v58\nze v91,v59\nze v92,v60\nze v93,v61\nze v94,v62\n"
"ze v95,v63\ns_nop 1\nza v104,v88,v89\nza v105,v90,v91\nzc v74,v88,v89\nzc v75,v90,v91\nza v110,v104,v105\nza v106,v92,v93\n"
"za v107,v94,v95\nzc v78,v92,v93\nzc v79,v94,v95\nza v111,v106,v107\ns_nop 1\nza v108,v108,v110\nza v109,v109,v111\ns_nop 1\n"
"s_branch L_A_back_M0\nL_A_rare_M1:\ns_nop 7\ns_nop 7\nv_max3_f32 v28,v32,v33,v34\nv_max3_f32 v28,v28,v35,v40\nv_max3_f32 v28,v28,v41,v42\nv_max_f32 v28,v28,v43\n"
"v_mov_b32 v15,v28\ns_nop 1\nv_permlane16_swap_b32 v28,v15\ns_nop 1\nv_max_f32 v28,v28,v15\nv_mov_b32 v15,v28\ns_nop 1\nv_permlane32_swap_b32 v28,v15\n"
"s_nop 1\nv_max_f32 v28,v28,v15\nv_max_f32 v28,0,v28\nzu v30,0,v28\nze v30,v30\ns_nop 1\nza v12,v12,v28\nzu v96,v96,v28\n"
"zu v97,v97,v28\nzu v98,v98,v28\nzu v99,v99,v28\nzx v10,v10,v30\nzu v32,v32,v28\nzu v33,v33,v28\nzu v34,v34,v28\nzu v35,v35,v28\n"
"zu v40,v40,v28\nzu v41,v41,v28\nzu v42,v42,v28\nzu v43,v43,v28\nzu v48,v48,v28\nzu v49,v49,v28\nzu v50,v50,v28\nzu v51,v51,v28\n"
"zr v112,a0\nzr v113,a1\nzr v114,a2\nzr v115,a3\nzx v112,v112,v30\nzx v113,v113,v30\nzx v114,v114,v30\nzx v115,v115,v30\n"
"zw a0,v112\nzw a1,v113\nzw a2,v114\nzw a3,v115\nzr v112,a8\nzr v113,a9\nzr v114,a10\nzr v115,a11\n"
"zx v112,v112,v30\nzx v113,v113,v30\nzx v114,v114,v30\nzx v115,v115,v30\nzw a8,v112\nzw a9,v113\nzw a10,v114\nzw a11,v115\n"
"zr v112,a16\nzr v113,a17\nzr v114,a18\nzr v115,a19\nzx v112,v112,v30\nzx v113,v113,v30\nzx v114,v114,v30\nzx v115,v115,v30\n"
"zw a16,v112\nzw a17,v113\nzw a18,v114\nzw a19,v115\nzr v112,a24\nzr v113,a25\nzr v114,a26\nzr v115,a27\n"
"zx v112,v112,v30\nzx v113,v113,v30\nzx v114,v114,v30\nzx v115,v115,v30\nzw a24,v112\nzw a25,v113\nzw a26,v114\nzw a27,v115\n"
"zr v112,a32\nzr v113,a33\nzr v114,a34\nzr v115,a35\nzx v112,v112,v30\nzx v113,v113,v30\nzx v114,v114,v30\nzx v115,v115,v30\n"
"zw a32,v112\nzw a33,v113\nzw a34,v114\nzw a35,v115\nzr v112,a40\nzr v113,a41\nzr v114,a42\nzr v115,a43\n"
"zx v112,v112,v30\nzx v113,v113,v30\nzx v114,v114,v30\nzx v115,v115,v30\nzw a40,v112\nzw a41,v113\nzw a42,v114\nzw a43,v115\n"
"zr v112,a48\nzr v113,a49\nzr v114,a50\nzr v115,a51\nzx v112,v112,v30\nzx v113,v113,v30\nzx v114,v114,v30\nzx v115,v115,v30\n"
"zw a48,v112\nzw a49,v113\nzw a50,v114\nzw a51,v115\nzr v112,a56\nzr v113,a57\nzr v114,a58\nzr v115,a59\n"
"zx v112,v112,v30\nzx v113,v113,v30\nzx v114,v114,v30\nzx v115,v115,v30\nzw a56,v112\nzw a57,v113\nzw a58,v114\nzw a59,v115\n"
"zr v112,a64\nzr v113,a65\nzr v114,a66\nzr v115,a67\nzx v112,v112,v30\nzx v113,v113,v30\nzx v114,v114,v30\nzx v115,v115,v30\n"
"zw a64,v112\nzw a65,v113\nzw a66,v114\nzw a67,v115\nzr v112,a72\nzr v113,a73\nzr v114,a74\nzr v115,a75\n"
"zx v112,v112,v30\nzx v113,v113,v30\nzx v114,v114,v30\nzx v115,v115,v30\nzw a72,v112\nzw a73,v113\nzw a74,v114\nzw a75,v115\n"
"zr v112,a80\nzr v113,a81\nzr v114,a82\nzr v115,a83\nzx v112,v112,v30\nzx v113,v113,v30\nzx v114,v114,v30\nzx v115,v115,v30\n"
"zw a80,v112\nzw a81,v113\nzw a82,v114\nzw a83,v115\nzr v112,a88\nzr v113,a89\nzr v114,a90\nzr v115,a91\n"
"zx v112,v112,v30\nzx v113,v113,v30\nzx v114,v114,v30\nzx v115,v115,v30\nzw a88,v112\nzw a89,v113\nzw a90,v114\nzw a91,v115\n"
"zr v112,a96\nzr v113,a97\nzr v114,a98\nzr v115,a99\nzx v112,v112,v30\nzx v113,v113,v30\nzx v114,v114,v30\nzx v115,v115,v30\n"
"zw a96,v112\nzw a97,v113\nzw a98,v114\nzw a99,v115\nzr v112,a104\nzr v113,a105\nzr v114,a106\nzr v115,a107\n"
"zx v112,v112,v30\nzx v113,v113,v30\nzx v114,v114,v30\nzx v115,v115,v30\nzw a104,v112\nzw a105,v113\nzw a106,v114\nzw a107,v115\n"
"zr v112,a112\nzr v113,a113\nzr v114,a114\nzr v115,a115\nzx v112,v112,v30\nzx v113,v113,v30\nzx v114,v114,v30\nzx v115,v115,v30\n"
"zw a112,v112\nzw a113,v113\nzw a114,v114\nzw a115,v115\nzr v112,a120\nzr v113,a121\nzr v114,a122\nzr v115,a123\n"
"zx v112,v112,v30\nzx v113,v113,v30\nzx v114,v114,v30\nzx v115,v115,v30\nzw a120,v112\nzw a121,v113\nzw a122,v114\nzw a123,v115\n"
"v_max3_f32 v29,v36,v37,v38\nv_max3_f32 v29,v29,v39,v44\nv_max3_f32 v29,v29,v45,v46\nv_max_f32 v29,v29,v47\nv_mov_b32 v15,v29\ns_nop 1\nv_permlane16_swap_b32 v29,v15\ns_nop 1\n"
"v_max_f32 v29,v29,v15\nv_mov_b32 v15,v29\ns_nop 1\nv_permlane32_swap_b32 v29,v15\ns_nop 1\nv_max_f32 v29,v29,v15\nv_max_f32 v29,0,v29\nzu v31,0,v29\n"
"ze v31,v31\ns_nop 1\nza v13,v13,v29\nzu v100,v100,v29\nzu v101,v101,v29\nzu v102,v102,v29\nzu v103,v103,v29\nzx v11,v11,v31\n"
"zu v36,v36,v29\nzu v37,v37,v29\nzu v38,v38,v29\nzu v39,v39,v29\nzu v44,v44,v29\nzu v45,v45,v29\nzu v46,v46,v29\nzu v47,v47,v29\n"
"zu v52,v52,v29\nzu v53,v53,v29\nzu v54,v54,v29\nzu v55,v55,v29\nzr v112,a4\nzr v113,a5\nzr v114,a6\nzr v115,a7\n"
"zx v112,v112,v31\nzx v113,v113,v31\nzx v114,v114,v31\nzx v115,v115,v31\nzw a4,v112\nzw a5,v113\nzw a6,v114\nzw a7,v115\n"
"zr v112,a12\nzr v113,a13\nzr v114,a14\nzr v115,a15\nzx v112,v112,v31\nzx v113,v113,v31\nzx v114,v114,v31\nzx v115,v115,v31\n"
"zw a12,v112\nzw a13,v113\nzw a14,v114\nzw a15,v115\nzr v112,a20\nzr v113,a21\nzr v114,a22\nzr v115,a23\n"
"zx v112,v112,v31\nzx v113,v113,v31\nzx v114,v114,v31\nzx v115,v115,v31\nzw a20,v112\nzw a21,v113\nzw a22,v114\nzw a23,v115\n"
"zr v112,a28\nzr v113,a29\nzr v114,a30\nzr v115,a31\nzx v112,v112,v31\nzx v113,v113,v31\nzx v114,v114,v31\nzx v115,v115,v31\n"
"zw a28,v112\nzw a29,v113\nzw a30,v114\nzw a31,v115\nzr v112,a36\nzr v113,a37\nzr v114,a38\nzr v115,a39\n"
"zx v112,v112,v31\nzx v113,v113,v31\nzx v114,v114,v31\nzx v115,v115,v31\nzw a36,v112\nzw a37,v113\nzw a38,v114\nzw a39,v115\n"
"zr v112,a44\nzr v113,a45\nzr v114,a46\nzr v115,a47\nzx v112,v112,v31\nzx v113,v113,v31\nzx v114,v114,v31\nzx v115,v115,v31\n"
"zw a44,v112\nzw a45,v113\nzw a46,v114\nzw a47,v115\nzr v112,a52\nzr v113,a53\nzr v114,a54\nzr v115,a55\n"
"zx v112,v112,v31\nzx v113,v113,v31\nzx v114,v114,v31\nzx v115,v115,v31\nzw a52,v112\nzw a53,v113\nzw a54,v114\nzw a55,v115\n"
"zr v112,a60\nzr v113,a61\nzr v114,a62\nzr v115,a63\nzx v112,v112,v31\nzx v113,v113,v31\nzx v114,v114,v31\nzx v115,v115,v31\n"
"zw a60,v112\nzw a61,v113\nzw a62,v114\nzw a63,v115\nzr v112,a68\nzr v113,a69\nzr v114,a70\nzr v115,a71\n"
"zx v112,v112,v31\nzx v113,v113,v31\nzx v114,v114,v31\nzx v115,v115,v31\nzw a68,v112\nzw a69,v113\nzw a70,v114\nzw a71,v115\n"
"zr v112,a76\nzr v113,a77\nzr v114,a78\nzr v115,a79\nzx v112,v112,v31\nzx v113,v113,v31\nzx v114,v114,v31\nzx v115,v115,v31\n"
"zw a76,v112\nzw a77,v113\nzw a78,v114\nzw a79,v115\nzr v112,a84\nzr v113,a85\nzr v114,a86\nzr v115,a87\n"
"zx v112,v112,v31\nzx v113,v113,v31\nzx v114,v114,v31\nzx v115,v115,v31\nzw a84,v112\nzw a85,v113\nzw a86,v114\nzw a87,v115\n"
"zr v112,a92\nzr v113,a93\nzr v114,a94\nzr v115,a95\nzx v112,v112,v31\nzx v113,v113,v31\nzx v114,v114,v31\nzx v115,v115,v31\n"
"zw a92,v112\nzw a93,v113\nzw a94,v114\nzw a95,v115\nzr v112,a100\nzr v113,a101\nzr v114,a102\nzr v115,a103\n"
"zx v112,v112,v31\nzx v113,v113,v31\nzx v114,v114,v31\nzx v115,v115,v31\nzw a100,v112\nzw a101,v113\nzw a102,v114\nzw a103,v115\n"
"zr v112,a108\nzr v113,a109\nzr v114,a110\nzr v115,a111\nzx v112,v112,v31\nzx v113,v113,v31\nzx v114,v114,v31\nzx v115,v115,v31\n"
"zw a108,v112\nzw a109,v113\nzw a110,v114\nzw a111,v115\nzr v112,a116\nzr v113,a117\nzr v114,a118\nzr v115,a119\n"
"zx v112,v112,v31\nzx v113,v113,v31\nzx v114,v114,v31\nzx v115,v115,v31\nzw a116,v112\nzw a117,v113\nzw a118,v114\nzw a119,v115\n"
"zr v112,a124\nzr v113,a125\nzr v114,a126\nzr v115,a127\nzx v112,v112,v31\nzx v113,v113,v31\nzx v114,v114,v31\nzx v115,v115,v31\n"
"zw a124,v112\nzw a125,v113\nzw a126,v114\nzw a127,v115\nze v80,v32\nze v81,v33\nze v82,v34\nze v83,v35\n"
"ze v84,v36\nze v85,v37\nze v86,v38\nze v87,v39\ns_nop 1\nza v104,v80,v81\nza v105,v82,v83\nzc v64,v80,v81\n"
"zc v65,v82,v83\nza v108,v104,v105\nza v106,v84,v85\nza v107,v86,v87\nzc v68,v84,v85\nzc v69,v86,v87\nza v109,v106,v107\ns_nop 1\n"
"ze v88,v40\nze v89,v41\nze v90,v42\nze v91,v43\nze v92,v44\nze v93,v45\nze v94,v46\nze v95,v47\n"
"s_nop 1\nza v104,v88,v89\nza v105,v90,v91\nzc v66,v88,v89\nzc v67,v90,v91\nza v110,v104,v105\nza v106,v92,v93\nza v107,v94,v95\n"
"zc v70,v92,v93\nzc v71,v94,v95\nza v111,v106,v107\ns_nop 1\nza v108,v108,v110\nza v109,v109,v111\ns_nop 1\ns_branch L_A_back_M1\n"
"L_A_rare_L0:\ns_nop 7\ns_nop 7\nv_max3_f32 v28,v48,v49,v50\nv_max3_f32 v28,v28,v51,v56\nv_max3_f32 v28,v28,v57,v58\nv_max_f32 v28,v28,v59\nv_mov_b32 v15,v28\n"
"s_nop 1\nv_permlane16_swap_b32 v28,v15\ns_nop 1\nv_max_f32 v28,v28,v15\nv_mov_b32 v15,v28\ns_nop 1\nv_permlane32_swap_b32 v28,v15\ns_nop 1\n"
"v_max_f32 v28,v28,v15\nv_max_f32 v28,0,v28\nzu v30,0,v28\nze v30,v30\ns_nop 1\nza v12,v12,v28\nzu v96,v96,v28\nzu v97,v97,v28\n"
"zu v98,v98,v28\nzu v99,v99,v28\nzx v10,v10,v30\nzu v48,v48,v28\nzu v49,v49,v28\nzu v50,v50,v28\nzu v51,v51,v28\nzu v56,v56,v28\n"
"zu v57,v57,v28\nzu v58,v58,v28\nzu v59,v59,v28\nzu v32,v32,v28\nzu v33,v33,v28\nzu v34,v34,v28\nzu v35,v35,v28\nzr v112,a0\n"
"zr v113,a1\nzr v114,a2\nzr v115,a3\nzx v112,v112,v30\nzx v113,v113,v30\nzx v114,v114,v30\nzx v115,v115,v30\nzw a0,v112\n"
"zw a1,v113\nzw a2,v114\nzw a3,v115\nzr v112,a8\nzr v113,a9\nzr v114,a10\nzr v115,a11\nzx v112,v112,v30\n"
"zx v113,v113,v30\nzx v114,v114,v30\nzx v115,v115,v30\nzw a8,v112\nzw a9,v113\nzw a10,v114\nzw a11,v115\nzr v112,a16\n"
"zr v113,a17\nzr v114,a18\nzr v115,a19\nzx v112,v112,v30\nzx v113,v113,v30\nzx v114,v114,v30\nzx v115,v115,v30\nzw a16,v112\n"
"zw a17,v113\nzw a18,v114\nzw a19,v115\nzr v112,a24\nzr v113,a25\nzr v114,a26\nzr v115,a27\nzx v112,v112,v30\n"
"zx v113,v113,v30\nzx v114,v114,v30\nzx v115,v115,v30\nzw a24,v112\nzw a25,v113\nzw a26,v114\nzw a27,v115\nzr v112,a32\n"
"zr v113,a33\nzr v114,a34\nzr v115,a35\nzx v112,v112,v30\nzx v113,v113,v30\nzx v114,v114,v30\nzx v115,v115,v30\nzw a32,v112\n"
"zw a33,v113\nzw a34,v114\nzw a35,v115\nzr v112,a40\nzr v113,a41\nzr v114,a42\nzr v115,a43\nzx v112,v112,v30\n"
"zx v113,v113,v30\nzx v114,v114,v30\nzx v115,v115,v30\nzw a40,v112\nzw a41,v113\nzw a42,v114\nzw a43,v115\nzr v112,a48\n"
"zr v113,a49\nzr v114,a50\nzr v115,a51\nzx v112,v112,v30\nzx v113,v113,v30\nzx v114,v114,v30\nzx v115,v115,v30\nzw a48,v112\n"
"zw a49,v113\nzw a50,v114\nzw a51,v115\nzr v112,a56\nzr v113,a57\nzr v114,a58\nzr v115,a59\nzx v112,v112,v30\n"
"zx v113,v113,v30\nzx v114,v114,v30\nzx v115,v115,v30\nzw a56,v112\nzw a57,v113\nzw a58,v114\nzw a59,v115\nzr v112,a64\n"
"zr v113,a65\nzr v114,a66\nzr v115,a67\nzx v112,v112,v30\nzx v113,v113,v30\nzx v114,v114,v30\nzx v115,v115,v30\nzw a64,v112\n"
"zw a65,v113\nzw a66,v114\nzw a67,v115\nzr v112,a72\nzr v113,a73\nzr v114,a74\nzr v115,a75\nzx v112,v112,v30\n"
"zx v113,v113,v30\nzx v114,v114,v30\nzx v115,v115,v30\nzw a72,v112\nzw a73,v113\nzw a74,v114\nzw a75,v115\nzr v112,a80\n"
"zr v113,a81\nzr v114,a82\nzr v115,a83\nzx v112,v112,v30\nzx v113,v113,v30\nzx v114,v114,v30\nzx v115,v115,v30\nzw a80,v112\n"
"zw a81,v113\nzw a82,v114\nzw a83,v115\nzr v112,a88\nzr v113,a89\nzr v114,a90\nzr v115,a91\nzx v112,v112,v30\n"
"zx v113,v113,v30\nzx v114,v114,v30\nzx v115,v115,v30\nzw a88,v112\nzw a89,v113\nzw a90,v114\nzw a91,v115\nzr v112,a96\n"
"zr v113,a97\nzr v114,a98\nzr v115,a99\nzx v112,v112,v30\nzx v113,v113,v30\nzx v114,v114,v30\nzx v115,v115,v30\nzw a96,v112\n"
"zw a97,v113\nzw a98,v114\nzw a99,v115\nzr v112,a104\nzr v113,a105\nzr v114,a106\nzr v115,a107\nzx v112,v112,v30\n"
"zx v113,v113,v30\nzx v114,v114,v30\nzx v115,v115,v30\nzw a104,v112\nzw a105,v113\nzw a106,v114\nzw a107,v115\nzr v112,a112\n"
"zr v113,a113\nzr v114,a114\nzr v115,a115\nzx v112,v112,v30\nzx v113,v113,v30\nzx v114,v114,v30\nzx v115,v115,v30\nzw a112,v112\n"
"zw a113,v113\nzw a114,v114\nzw a115,v115\nzr v112,a120\nzr v113,a121\nzr v114,a122\nzr v115,a123\nzx v112,v112,v30\n"
"zx v113,v113,v30\nzx v114,v114,v30\nzx v115,v115,v30\nzw a120,v112\nzw a121,v113\nzw a122,v114\nzw a123,v115\nv_max3_f32 v29,v52,v53,v54\n"
"v_max3_f32 v29,v29,v55,v60\nv_max3_f32 v29,v29,v61,v62\nv_max_f32 v29,v29,v63\nv_mov_b32 v15,v29\ns_nop 1\nv_permlane16_swap_b32 v29,v15\ns_nop 1\nv_max_f32 v29,v29,v15\n"
"v_mov_b32 v15,v29\ns_nop 1\nv_permlane32_swap_b32 v29,v15\ns_nop 1\nv_max_f32 v29,v29,v15\nv_max_f32 v29,0,v29\nzu v31,0,v29\nze v31,v31\n"
"s_nop 1\nza v13,v13,v29\nzu v100,v100,v29\nzu v101,v101,v29\nzu v102,v102,v29\nzu v103,v103,v29\nzx v11,v11,v31\nzu v52,v52,v29\n"
"zu v53,v53,v29\nzu v54,v54,v29\nzu v55,v55,v29\nzu v60,v60,v29\nzu v61,v61,v29\nzu v62,v62,v29\nzu v63,v63,v29\nzu v36,v36,v29\n"
"zu v37,v37,v29\nzu v38,v38,v29\nzu v39,v39,v29\nzr v112,a4\nzr v113,a5\nzr v114,a6\nzr v115,a7\nzx v112,v112,v31\n"
"zx v113,v113,v31\nzx v114,v114,v31\nzx v115,v115,v31\nzw a4,v112\nzw a5,v113\nzw a6,v114\nzw a7,v115\nzr v112,a12\n"
"zr v113,a13\nzr v114,a14\nzr v115,a15\nzx v112,v112,v31\nzx v113,v113,v31\nzx v114,v114,v31\nzx v115,v115,v31\nzw a12,v112\n"
"zw a13,v113\nzw a14,v114\nzw a15,v115\nzr v112,a20\nzr v113,a21\nzr v114,a22\nzr v115,a23\nzx v112,v112,v31\n"
"zx v113,v113,v31\nzx v114,v114,v31\nzx v115,v115,v31\nzw a20,v112\nzw a21,v113\nzw a22,v114\nzw a23,v115\nzr v112,a28\n"
"zr v113,a29\nzr v114,a30\nzr v115,a31\nzx v112,v112,v31\nzx v113,v113,v31\nzx v114,v114,v31\nzx v115,v115,v31\nzw a28,v112\n"
"zw a29,v113\nzw a30,v114\nzw a31,v115\nzr v112,a36\nzr v113,a37\nzr v114,a38\nzr v115,a39\nzx v112,v112,v31\n"
"zx v113,v113,v31\nzx v114,v114,v31\nzx v115,v115,v31\nzw a36,v112\nzw a37,v113\nzw a38,v114\nzw a39,v115\nzr v112,a44\n"
"zr v113,a45\nzr v114,a46\nzr v115,a47\nzx v112,v112,v31\nzx v113,v113,v31\nzx v114,v114,v31\nzx v115,v115,v31\nzw a44,v112\n"
"zw a45,v113\nzw a46,v114\nzw a47,v115\nzr v112,a52\nzr v113,a53\nzr v114,a54\nzr v115,a55\nzx v112,v112,v31\n"
"zx v113,v113,v31\nzx v114,v114,v31\nzx v115,v115,v31\nzw a52,v112\nzw a53,v113\nzw a54,v114\nzw a55,v115\nzr v112,a60\n"
"zr v113,a61\nzr v114,a62\nzr v115,a63\nzx v112,v112,v31\nzx v113,v113,v31\nzx v114,v114,v31\nzx v115,v115,v31\nzw a60,v112\n"
"zw a61,v113\nzw a62,v114\nzw a63,v115\nzr v112,a68\nzr v113,a69\nzr v114,a70\nzr v115,a71\nzx v112,v112,v31\n"
"zx v113,v113,v31\nzx v114,v114,v31\nzx v115,v115,v31\nzw a68,v112\nzw a69,v113\nzw a70,v114\nzw a71,v115\nzr v112,a76\n"
"zr v113,a77\nzr v114,a78\nzr v115,a79\nzx v112,v112,v31\nzx v113,v113,v31\nzx v114,v114,v31\nzx v115,v115,v31\nzw a76,v112\n"
"zw a77,v113\nzw a78,v114\nzw a79,v115\nzr v112,a84\nzr v113,a85\nzr v114,a86\nzr v115,a87\nzx v112,v112,v31\n"
"zx v113,v113,v31\nzx v114,v114,v31\nzx v115,v115,v31\nzw a84,v112\nzw a85,v113\nzw a86,v114\nzw a87,v115\nzr v112,a92\n"
"zr v113,a93\nzr v114,a94\nzr v115,a95\nzx v112,v112,v31\nzx v113,v113,v31\nzx v114,v114,v31\nzx v115,v115,v31\nzw a92,v112\n"
"zw a93,v113\nzw a94,v114\nzw a95,v115\nzr v112,a100\nzr v113,a101\nzr v114,a102\nzr v115,a103\nzx v112,v112,v31\n"
"zx v113,v113,v31\nzx v114,v114,v31\nzx v115,v115,v31\nzw a100,v112\nzw a101,v113\nzw a102,v114\nzw a103,v115\nzr v112,a108\n"
"zr v113,a109\nzr v114,a110\nzr v115,a111\nzx v112,v112,v31\nzx v113,v113,v31\nzx v114,v114,v31\nzx v115,v115,v31\nzw a108,v112\n"
"zw a109,v113\nzw a110,v114\nzw a111,v115\nzr v112,a116\nzr v113,a117\nzr v114,a118\nzr v115,a119\nzx v112,v112,v31\n"
"zx v113,v113,v31\nzx v114,v114,v31\nzx v115,v115,v31\nzw a116,v112\nzw a117,v113\nzw a118,v114\nzw a119,v115\nzr v112,a124\n"
"zr v113,a125\nzr v114,a126\nzr v115,a127\nzx v112,v112,v31\nzx v113,v113,v31\nzx v114,v114,v31\nzx v115,v115,v31\nzw a124,v112\n"
"zw a125,v113\nzw a126,v114\nzw a127,v115\nze v80,v48\nze v81,v49\nze v82,v50\nze v83,v51\nze v84,v52\n"
"ze v85,v53\nze v86,v54\nze v87,v55\ns_nop 1\nza v104,v80,v81\nza v105,v82,v83\nzc v72,v80,v81\nzc v73,v82,v83\n"
"za v108,v104,v105\nza v106,v84,v85\nza v107,v86,v87\nzc v76,v84,v85\nzc v77,v86,v87\nza v109,v106,v107\ns_nop 1\nze v88,v56\n"
"ze v89,v57\nze v90,v58\nze v91,v59\nze v92,v60\nze v93,v61\nze v94,v62\nze v95,v63\ns_nop 1\n"
"za v104,v88,v89\nza v105,v90,v91\nzc v74,v88,v89\nzc v75,v90,v91\nza v110,v104,v105\nza v106,v92,v93\nza v107,v94,v95\nzc v78,v92,v93\n"
"zc v79,v94,v95\nza v111,v106,v107\ns_nop 1\nza v108,v108,v110\nza v109,v109,v111\ns_nop 1\ns_branch L_A_back_L0\nL_F_exit:\n"
    :
    : [karg] "s"(__builtin_amdgcn_kernarg_segment_ptr()), [bid] "s"(blockIdx.x), [tid] "v"(threadIdx.x)
    : "v1", "v2", "v3", "v4", "v5", "v6", "v7", "v8", "v9", "v10", "v11", "v12", "v13", "v14", "v15", "v16",
      "v17", "v18", "v19", "v20", "v21", "v22", "v23", "v24", "v25", "v26", "v27", "v28", "v29", "v30", "v31", "v32",
      "v33", "v34", "v35", "v36", "v37", "v38", "v39", "v40", "v41", "v42", "v43", "v44", "v45", "v46", "v47", "v48",
      "v49", "v50", "v51", "v52", "v53", "v54", "v55", "v56", "v57", "v58", "v59", "v60", "v61", "v62", "v63", "v64",
      "v65", "v66", "v67", "v68", "v69", "v70", "v71", "v72", "v73", "v74", "v75", "v76", "v77", "v78", "v79", "v80",
      "v81", "v82", "v83", "v84", "v85", "v86", "v87", "v88", "v89", "v90", "v91", "v92", "v93", "v94", "v95", "v96",
      "v97", "v98", "v99", "v100", "v101", "v102", "v103", "v104", "v105", "v106", "v107", "v108", "v109", "v110", "v111", "v112",
      "v113", "v114", "v115", "v116", "v117", "v118", "v119", "v120", "v121", "v122", "v123", "v124", "v125", "v126", "v127", "v128",
      "v129", "v130", "v131", "v132", "v133", "v134", "v135", "v136", "v137", "v138", "v139", "v140", "v141", "v142", "v143", "v144",
      "v145", "v146", "v147", "v148", "v149", "v150", "v151", "v152", "v153", "v154", "v155", "v156", "v157", "v158", "v159", "v160",
      "v161", "v162", "v163", "v164", "v165", "v166", "v167", "v168", "v169", "v170", "v171", "v172", "v173", "v174", "v175", "v176",
      "v177", "v178", "v179", "v180", "v181", "v182", "v183", "v184", "v185", "v186", "v187", "v188", "v189", "v190", "v191", "v192",
      "v193", "v194", "v195", "v196", "v197", "v198", "v199", "v200", "v201", "v202", "v203", "v204", "v205", "v206", "v207", "v208",
      "v209", "v210", "v211", "v212", "v213", "v214", "v215", "v216", "v217", "v218", "v219", "v220", "v221", "v222", "v223", "v224",
      "v225", "v226", "v227", "v228", "v229", "v230", "v231", "v232", "v233", "v234", "v235", "v236", "v237", "v238", "v239", "v240",
      "v241", "v242", "v243", "v244", "v245", "v246", "v247", "v248", "v249", "v250", "v251", "v252", "v253", "v254", "v255", "a0",
      "a1", "a2", "a3", "a4", "a5", "a6", "a7", "a8", "a9", "a10", "a11", "a12", "a13", "a14", "a15", "a16",
      "a17", "a18", "a19", "a20", "a21", "a22", "a23", "a24", "a25", "a26", "a27", "a28", "a29", "a30", "a31", "a32",
      "a33", "a34", "a35", "a36", "a37", "a38", "a39", "a40", "a41", "a42", "a43", "a44", "a45", "a46", "a47", "a48",
      "a49", "a50", "a51", "a52", "a53", "a54", "a55", "a56", "a57", "a58", "a59", "a60", "a61", "a62", "a63", "a64",
      "a65", "a66", "a67", "a68", "a69", "a70", "a71", "a72", "a73", "a74", "a75", "a76", "a77", "a78", "a79", "a80",
      "a81", "a82", "a83", "a84", "a85", "a86", "a87", "a88", "a89", "a90", "a91", "a92", "a93", "a94", "a95", "a96",
      "a97", "a98", "a99", "a100", "a101", "a102", "a103", "a104", "a105", "a106", "a107", "a108", "a109", "a110", "a111", "a112",
      "a113", "a114", "a115", "a116", "a117", "a118", "a119", "a120", "a121", "a122", "a123", "a124", "a125", "a126", "a127", "a128",
      "a129", "a130", "a131", "a132", "a133", "a134", "a135", "a136", "a137", "a138", "a139", "a140", "a141", "a142", "a143", "a144",
      "a145", "a146", "a147", "a148", "a149", "a150", "a151", "a152", "a153", "a154", "a155", "a156", "a157", "a158", "a159", "a160",
      "a161", "a162", "a163", "a164", "a165", "a166", "a167", "a168", "a169", "a170", "a171", "a172", "a173", "a174", "a175", "a176",
      "a177", "a178", "a179", "a180", "a181", "a182", "a183", "a184", "a185", "a186", "a187", "a188", "a189", "a190", "a191", "a192",
      "a193", "a194", "a195", "a196", "a197", "a198", "a199", "a200", "a201", "a202", "a203", "a204", "a205", "a206", "a207", "a208",
      "a209", "a210", "a211", "a212", "a213", "a214", "a215", "a216", "a217", "a218", "a219", "a220", "a221", "a222", "a223", "a224",
      "a225", "a226", "a227", "a228", "a229", "a230", "a231", "a232", "a233", "a234", "a235", "a236", "a237", "a238", "a239", "a240",
      "a241", "a242", "a243", "a244", "a245", "a246", "a247", "a248", "a249", "a250", "a251", "a252", "a253", "a254", "a255", "s4",
      "s5", "s6", "s7", "s8", "s9", "s10", "s11", "s12", "s13", "s14", "s15", "s16", "s17", "s18", "s19", "s20",
      "s21", "s22", "s23", "s24", "s25", "s26", "s27", "s28", "s29", "s30", "s31", "s34", "s35", "s36", "s37", "s38",
      "s39", "s40", "s41", "s42", "s43", "s44", "s45", "s46", "s47", "s48", "s49", "s50", "s51", "s52", "s53", "s54",
      "s55", "s56", "s57", "s58", "s59", "s60", "s61", "s62", "s63", "s64", "s65", "s66", "s67", "s68", "s69", "s70",
      "s71", "s72", "s73", "s74", "s75", "s76", "s77", "s78", "s79", "s80", "s81", "s82", "s83", "s84", "s85", "s86",
      "s87", "s88", "s89", "s90", "s91", "s92", "s93", "s94", "s95", "s96", "s97", "s98", "s99", "vcc", "scc", "memory");
}

extern "C" void kernel_launch(void* const* d_in, const int* in_sizes, int n_in,
                              void* d_out, int out_size, void* d_ws, size_t ws_size,
                              hipStream_t stream) {
  const int lds = 163840;
  (void)hipFuncSetAttribute((const void*)k_F, hipFuncAttributeMaxDynamicSharedMemorySize, lds);
  k_F<<<256, 256, lds, stream>>>(d_in[0], d_in[1], d_in[2], d_in[3], d_in[4], d_in[5], d_in[7], d_in[8], d_in[9], d_out, d_ws);
}
```
